```python
import torch

def _softmax(x, axis=-1):
    return torch.softmax(x, dim=axis)
def _gelu(x):
    return torch.nn.functional.gelu(x, approximate="tanh")
def _silu(x):
    return torch.nn.functional.silu(x)
def _sigmoid(x):
    return torch.sigmoid(x)
def _layernorm(x, axis=-1, eps=1e-5):
    m = x.mean(dim=axis, keepdim=True)
    v = x.var(dim=axis, keepdim=True, unbiased=False)
    return (x - m) / torch.sqrt(v + eps)
def _scatter_add(tgt, idx, src, axis=0):
    return tgt.clone().index_add_(axis, idx, src)
def _segment_sum(src, seg, n):
    out = torch.zeros((n,) + src.shape[1:], dtype=src.dtype)
    return out.index_add_(0, seg, src)
def _pad(x, pw):
    flat = tuple(v for pair in reversed(pw) for v in pair)
    return torch.nn.functional.pad(x, flat)

kernel_name = 'recurrent_scan__ssm_conv_h3__dense_activation_squared_relu__prompt'

def setup_inputs():

    _w = (lambda a: (lambda t: t.to(torch.bfloat16) if t.is_floating_point() else t)(torch.as_tensor(a)))
    def _A_setup():
        B, S, D = 1, 2048, 448
        ks = 4
        return {
            "q": _w((lambda *s: torch.randn(*s, dtype=torch.bfloat16) * 0.05)(B, S, D)),
            "k": _w((lambda *s: torch.randn(*s, dtype=torch.bfloat16) * 0.2)(B, S, D)),
            "v": _w((lambda *s: torch.randn(*s, dtype=torch.bfloat16) * 0.2)(B, S, D)),
            "shift_k": _w((lambda *s: torch.randn(*s, dtype=torch.bfloat16) * 0.2)(D, ks) * 0.5),
            "ssm_k": _w((lambda *s: torch.randn(*s, dtype=torch.bfloat16) * 0.2)(D, S) * (1.0 / S ** 0.5)),
            "ssm_D": _w((lambda *s: torch.randn(*s, dtype=torch.bfloat16) * 0.2)(D)),
        }
    _A_in = _A_setup()

    def _B_setup():
        B, S, D, F = 1, 2048, 448, 1792
        return {
            "x": _w((lambda *s: torch.randn(*s, dtype=torch.bfloat16) * 0.05)(B, S, D)),
            "w1": _w((lambda *s: torch.randn(*s, dtype=torch.bfloat16) * 0.1)(D, F)),
            "b1": _w((lambda *s: torch.randn(*s, dtype=torch.bfloat16) * 0.4)(F)),
            "w2": _w((lambda *s: torch.randn(*s, dtype=torch.bfloat16) * 0.1)(F, D)),
            "b2": _w((lambda *s: torch.randn(*s, dtype=torch.bfloat16) * 12.8)(D)),
        }
    _B_in = _B_setup()

    return {
        "A_q": _A_in["q"],
        "A_k": _A_in["k"],
        "A_v": _A_in["v"],
        "A_shift_k": _A_in["shift_k"],
        "A_ssm_k": _A_in["ssm_k"],
        "A_ssm_D": _A_in["ssm_D"],
        "B_x": _B_in["x"],
        "B_w1": _B_in["w1"],
        "B_b1": _B_in["b1"],
        "B_w2": _B_in["w2"],
        "B_b2": _B_in["b2"],
    }

def reference(A_q, A_k, A_v, A_shift_k, A_ssm_k, A_ssm_D, B_x, B_w1, B_b1, B_w2, B_b2):
    def _A_ref(q, k, v, shift_k, ssm_k, ssm_D):
        B, S, D = q.shape
        ks = shift_k.shape[-1]
        k_pad = _pad(k, ((0, 0), (ks - 1, 0), (0, 0)))
        k_shift = torch.zeros_like(k)
        for t in range(ks):
            k_shift = k_shift + k_pad[:, t:t + S, :] * shift_k[:, ks - 1 - t][None, None, :]
        kv = k_shift * v
        L = 2 * S
        kv_pad = _pad(kv, ((0, 0), (0, S), (0, 0)))
        kf_pad = _pad(ssm_k, ((0, 0), (0, S)))
        kv_f = (lambda x, axes=(-2,-1): torch.fft.rfft2(x.float(), dim=axes))(kv_pad[:, None, :, :], axes=(1, 2))
        kf_f = (lambda x, axes=(-2,-1): torch.fft.rfft2(x.float(), dim=axes))(kf_pad[:, None, :], axes=(1, 2))
        kf_f = (lambda x, *d: x.permute(*d))(kf_f, 1, 2, 0)
        y_f = kv_f * kf_f[None, :, :, :]
        y = (lambda x, s, axes=(-2,-1): torch.fft.irfft2(x, s=s, dim=axes).to(torch.bfloat16))(y_f, (1, L), axes=(1, 2))[:, 0, :S, :]
        y = y + kv * ssm_D[None, None, :]
        return q * y

    def _B_ref(x, w1, b1, w2, b2):
        h = torch.einsum("bsd,df->bsf", x, w1) + b1
        zero = torch.zeros_like(h)
        r = torch.where(h > zero, h, zero)
        h = r * r
        out = torch.einsum("bsf,fd->bsd", h, w2) + b2
        return out

    x = A_q
    xn = _layernorm(x, axis=-1)
    s = _A_ref(xn, A_k, A_v, A_shift_k, A_ssm_k, A_ssm_D)
    g = _silu(_B_ref(xn, B_w1, B_b1, B_w2, B_b2))
    return x + s * g
```

```cpp
#include <hip/hip_runtime.h>
#include <hip/hip_bf16.h>
#include <stdint.h>

typedef unsigned short u16;
constexpr int S = 2048, D = 448, F = 1792;

__device__ __forceinline__ float bf2f(u16 x) { return __uint_as_float(((unsigned)x) << 16); }
__device__ __forceinline__ u16 f2bf(float f) {
    __hip_bfloat16 b = __float2bfloat16(f);
    return __builtin_bit_cast(u16, b);
}
__device__ __forceinline__ float rbf(float f) { return bf2f(f2bf(f)); }

__global__ void n_prep(const u16* x, const u16* k, const u16* v, const u16* shk, u16* xn, u16* kv) {
    int s = blockIdx.x;
    __shared__ float red[8];
    int t = threadIdx.x;
    float xv = bf2f(x[s * D + t]);
    float sum = xv;
    for (int o = 32; o; o >>= 1) sum += __shfl_xor(sum, o);
    if ((t & 63) == 0) red[t >> 6] = sum;
    __syncthreads();
    float tot = 0; for (int i = 0; i < 7; i++) tot += red[i];
    float mean = tot / D;
    __syncthreads();
    float dv = (xv - mean) * (xv - mean);
    for (int o = 32; o; o >>= 1) dv += __shfl_xor(dv, o);
    if ((t & 63) == 0) red[t >> 6] = dv;
    __syncthreads();
    float vt = 0; for (int i = 0; i < 7; i++) vt += red[i];
    float var = rbf(vt / D);
    float m = rbf(mean);
    float sd = rbf(sqrtf(rbf(var + 1e-5f)));
    xn[s * D + t] = f2bf(rbf(xv - m) / sd);
    float ks = 0.f;
    for (int tt = 0; tt < 4; tt++) {
        int ss = s - 3 + tt;
        float kk = ss >= 0 ? bf2f(k[ss * D + t]) : 0.f;
        ks = rbf(ks + rbf(kk * bf2f(shk[t * 4 + 3 - tt])));
    }
    kv[s * D + t] = f2bf(ks * bf2f(v[s * D + t]));
}

__global__ void n_conv(const u16* kv, const u16* ssmk, const u16* ssmD, u16* y) {
    int d = blockIdx.x;
    __shared__ float u[S], h[S];
    for (int i = threadIdx.x; i < S; i += blockDim.x) { u[i] = bf2f(kv[i * D + d]); h[i] = bf2f(ssmk[d * S + i]); }
    __syncthreads();
    float dd = bf2f(ssmD[d]);
    for (int s = threadIdx.x; s < S; s += blockDim.x) {
        float a = 0;
        for (int j = 0; j <= s; j++) a += h[j] * u[s - j];
        y[s * D + d] = f2bf(rbf(a) + rbf(u[s] * dd));
    }
}

__global__ void n_gemm1(const u16* xn, const u16* w1, const u16* b1, u16* h) {
    int s = blockIdx.x; int f = blockIdx.y * 256 + threadIdx.x;
    __shared__ float xs[D];
    for (int i = threadIdx.x; i < D; i += 256) xs[i] = bf2f(xn[s * D + i]);
    __syncthreads();
    float a = 0;
    for (int i = 0; i < D; i++) a += xs[i] * bf2f(w1[i * F + f]);
    float z = rbf(rbf(a) + bf2f(b1[f]));
    z = z > 0.f ? z : 0.f;
    h[s * F + f] = f2bf(z * z);
}

__global__ void n_gemm2(const u16* h, const u16* w2, const u16* b2, const u16* x, const u16* xn, const u16* y, u16* out) {
    int s = blockIdx.x; int d = threadIdx.x;
    __shared__ float hs[F];
    for (int i = threadIdx.x; i < F; i += 448) hs[i] = bf2f(h[s * F + i]);
    __syncthreads();
    float a = 0;
    for (int i = 0; i < F; i++) a += hs[i] * bf2f(w2[i * D + d]);
    float o = rbf(rbf(a) + bf2f(b2[d]));
    float g = rbf(o / (1.f + __expf(-o)));
    float sv = rbf(bf2f(xn[s * D + d]) * bf2f(y[s * D + d]));
    out[s * D + d] = f2bf(bf2f(x[s * D + d]) + rbf(sv * g));
}

extern "C" void kernel_launch(void* const* d_in, const int* in_sizes, int n_in,
                              void* d_out, int out_size, void* d_ws, size_t ws_size,
                              hipStream_t stream) {
    const u16* q = (const u16*)d_in[0];
    const u16* k = (const u16*)d_in[1];
    const u16* v = (const u16*)d_in[2];
    const u16* shk = (const u16*)d_in[3];
    const u16* ssmk = (const u16*)d_in[4];
    const u16* ssmD = (const u16*)d_in[5];
    const u16* w1 = (const u16*)d_in[7];
    const u16* b1 = (const u16*)d_in[8];
    const u16* w2 = (const u16*)d_in[9];
    const u16* b2 = (const u16*)d_in[10];
    u16* ws = (u16*)d_ws;
    u16* xn = ws; u16* kv = xn + S * D; u16* y = kv + S * D; u16* h = y + S * D;
    n_prep<<<S, D, 0, stream>>>(q, k, v, shk, xn, kv);
    n_conv<<<D, 256, 0, stream>>>(kv, ssmk, ssmD, y);
    n_gemm1<<<dim3(S, F / 256), 256, 0, stream>>>(xn, w1, b1, h);
    n_gemm2<<<S, D, 0, stream>>>(h, w2, b2, q, xn, y, (u16*)d_out);
}
```

```cpp
#include <hip/hip_runtime.h>
#include <stdint.h>

typedef unsigned short u16;
typedef unsigned int u32;
typedef unsigned long long u64;
typedef u32 u32x4 __attribute__((ext_vector_type(4)));
typedef u32 u32x2 __attribute__((ext_vector_type(2)));
typedef float f32x4 __attribute__((ext_vector_type(4)));
typedef float f32x16 __attribute__((ext_vector_type(16)));
typedef __bf16 bf16x8 __attribute__((ext_vector_type(8)));
typedef __bf16 bf16x2 __attribute__((ext_vector_type(2)));
typedef short s16x4 __attribute__((ext_vector_type(4)));
#define LAS __attribute__((address_space(3)))
#define GAS __attribute__((address_space(1)))
#define DI __device__ __forceinline__

constexpr int S_ = 2048, D_ = 448, F_ = 1792;
constexpr int NWG = 224;
constexpr u32 WS_EPOCH = 0, WS_KVF = 256, WS_HF = 4096, WS_YF = 20480;
constexpr u32 WS_H = 1u << 20, WS_KVT = 9u << 20, WS_YT = 11u << 20;
constexpr u32 STAMP_OFF = 16u << 20;
constexpr int NSTAMP = 12;
constexpr u32 L_SLOT = 32768, L_LOC0 = 131072, L_MISC = 147456, LDS_BYTES = 163840;
constexpr u32 L_KVS = L_MISC, L_TAPS = L_MISC + 9216, L_AEXT = L_MISC, L_ROWP = L_MISC + 4096, L_FLAG = L_MISC + 13312;
constexpr u32 L_CONV = 32768, TOEP_CP = 4160, TOEP_CH = 4 * TOEP_CP, L_U = L_CONV + 2 * TOEP_CH, U_SZ = 4608, L_YS = L_CONV + 52224;
constexpr u64 TAG_SALT = 0x9E3779B97F4A7C15ull;
constexpr int SPIN_CAP = 1 << 15;

#define ST(i) do {} while (0)

DI void dma16(u32 voff, const void* sbase, u32 lds) {
    asm volatile("s_mov_b32 m0, %2\n\ts_nop 0\n\tglobal_load_lds_dwordx4 %0, %1" :: "v"(voff), "s"(sbase), "s"(lds) : "memory");
}
DI u32x4 gld16(u32 voff, const void* sbase) { u32x4 r; asm volatile("global_load_dwordx4 %0, %1, %2" : "=v"(r) : "v"(voff), "s"(sbase) : "memory"); return r; }
template <int OFF> DI u32x4 gld16c(u32 voff, const void* sbase) { u32x4 r; asm volatile("global_load_dwordx4 %0, %1, %2 offset:%3 sc1" : "=v"(r) : "v"(voff), "s"(sbase), "i"(OFF) : "memory"); return r; }
DI u32x2 gld8c(u32 voff, const void* sbase) { u32x2 r; asm volatile("global_load_dwordx2 %0, %1, %2 sc1" : "=v"(r) : "v"(voff), "s"(sbase) : "memory"); return r; }
DI u32 gld2(u32 voff, const void* sbase) { u32 r; asm volatile("global_load_ushort %0, %1, %2" : "=v"(r) : "v"(voff), "s"(sbase) : "memory"); return r; }
DI u32x4 bld16(u32 voff, u32x4 srd) { u32x4 r; asm volatile("buffer_load_dwordx4 %0, %1, %2, 0 offen" : "=v"(r) : "v"(voff), "s"(srd) : "memory"); return r; }
template <int OFF> DI void gst16c(u32 voff, void* sbase, u32x4 v) { asm volatile("global_store_dwordx4 %0, %1, %2 offset:%3 sc1\n\ts_nop 1" :: "v"(voff), "v"(v), "s"(sbase), "i"(OFF) : "memory"); }
template <int OFF> DI void gst8c(u32 voff, void* sbase, u32x2 v) { asm volatile("global_store_dwordx2 %0, %1, %2 offset:%3 sc1" :: "v"(voff), "v"(v), "s"(sbase), "i"(OFF) : "memory"); }
#define WAITV(n) asm volatile("s_waitcnt vmcnt(%0)" :: "i"(n) : "memory")
#define WAITBAR(n) asm volatile("s_waitcnt vmcnt(%0) lgkmcnt(0)\n\ts_barrier" :: "i"(n) : "memory")
#define BARL() asm volatile("s_waitcnt lgkmcnt(0)\n\ts_barrier" ::: "memory")
#define PIN(x) asm volatile("" : "+v"(x))

DI u32x4 mksrd(const void* p, u32 bytes) { u64 a = (u64)p; return u32x4{(u32)a, (u32)(a >> 32) & 0xffffu, bytes, 0x00020000u}; }
DI float bflo(u32 x) { return __uint_as_float(x << 16); }
DI float bfhi(u32 x) { return __uint_as_float(x & 0xffff0000u); }
DI u32 pk2(float a, float b) { bf16x2 v = {(__bf16)a, (__bf16)b}; return __builtin_bit_cast(u32, v); }
DI float rbf(float a) { return bflo(pk2(a, a)); }
DI float dot2(u32 a, u32 b, float c) { return __builtin_amdgcn_fdot2_f32_bf16(__builtin_bit_cast(bf16x2, a), __builtin_bit_cast(bf16x2, b), c, false); }
DI u32x4 lds16(u32 a) { return *(const LAS u32x4*)a; }
DI u32x2 lds8(u32 a) { return *(const LAS u32x2*)a; }
DI void sts16(u32 a, u32x4 v) { *(LAS u32x4*)a = v; }
DI void sts8(u32 a, u32x2 v) { *(LAS u32x2*)a = v; }
DI u32x2 ldtr(u32 a) { return __builtin_bit_cast(u32x2, __builtin_amdgcn_ds_read_tr16_b64_v4i16((LAS s16x4*)a)); }
DI u32x4 cat(u32x2 a, u32x2 b) { return u32x4{a.x, a.y, b.x, b.y}; }
DI f32x16 mfma32(u32x4 a, u32x4 b, f32x16 c) { return __builtin_amdgcn_mfma_f32_32x32x16_bf16(__builtin_bit_cast(bf16x8, a), __builtin_bit_cast(bf16x8, b), c, 0, 0, 0); }
DI f32x4 mfma16(u32x4 a, u32x4 b, f32x4 c) { return __builtin_amdgcn_mfma_f32_16x16x32_bf16(__builtin_bit_cast(bf16x8, a), __builtin_bit_cast(bf16x8, b), c, 0, 0, 0); }
DI float xhalf_sum(float v) { auto r = __builtin_amdgcn_permlane32_swap(__float_as_uint(v), __float_as_uint(v), false, false); return __uint_as_float(r[0]) + __uint_as_float(r[1]); }

struct Params {
    const u16 *x, *k, *v, *shk, *ssmk, *ssmd, *w1, *b1, *w2, *b2;
    u16* out; char* ws;
};

DI void poll8(u32 voff, const void* sbase, bool need, u32 tlo, u32 thi) {
    for (int spin = 0; spin < SPIN_CAP; ++spin) {
        u32x2 f = gld8c(voff, sbase);
        asm volatile("s_waitcnt vmcnt(0)" : "+v"(f) :: "memory");
        bool ok = !need || (f.x == tlo && f.y == thi);
        if (__all(ok)) break;
        __builtin_amdgcn_s_sleep(2);
    }
}
DI void poll16(u32 voff, const void* sbase, u32 tlo, u32 thi) {
    for (int spin = 0; spin < SPIN_CAP; ++spin) {
        u32x4 f = gld16c<0>(voff, sbase);
        asm volatile("s_waitcnt vmcnt(0)" : "+v"(f) :: "memory");
        bool ok = f.x == tlo && f.y == thi && f.z == tlo && f.w == thi;
        if (__all(ok)) break;
        __builtin_amdgcn_s_sleep(2);
    }
}

constexpr u32 loc_of(int c) { return (c % 5) == 0 ? (L_LOC0) : (c % 5) <= 2 ? (3 * L_SLOT + ((c % 5) - 1) * 4096) : (((c % 5) - 3) * 4096); }
constexpr u32 loc_gstride(int c) { return (c % 5) == 0 ? 4096 : 8192; }

constexpr int g2_younger(int c) {
    int n = 0; bool seen_h = false, seen_w = (c < 5);
    int cnt_after_h = 0, cnt_after_w = 0;
    for (int i = 0; i < 8; ++i) { if (seen_h) cnt_after_h += 4; if (seen_w) cnt_after_w += 4; if (i == c) seen_h = true; }
    for (int i = 0; i < c; ++i) {
        if (i + 5 < 14) { if (seen_h) cnt_after_h += 4; if (seen_w) cnt_after_w += 4; if (i + 5 == c) { seen_w = true; } }
        if (i + 8 < 14) { if (seen_h) cnt_after_h += 4; if (seen_w) cnt_after_w += 4; if (i + 8 == c) { seen_h = true; } }
    }
    n = cnt_after_h < cnt_after_w ? cnt_after_h : cnt_after_w;
    return n;
}

template <int C> struct G2 {
    static DI void issue_w(const Params& p, int g, int crot, u32 D0, u32 voffw, u32 lds0) {
        int c = C + crot; if (c >= 14) c -= 14;
        const char* sb = (const char*)p.w2 + (size_t)(128 * c + 32 * g) * 896 + D0 * 2;
#pragma unroll
        for (int n = 0; n < 4; ++n) dma16(voffw, sb + n * 8 * 896, lds0 + loc_of(C) + g * loc_gstride(C) + n * 1024);
    }
    static DI void issue_h(const char* hb, int crot, u32 vlane, u32x4 (&hf)[8][4]) {
        int c = C + crot; if (c >= 14) c -= 14;
        const char* sb = hb + c * 8192;
        hf[C & 7][0] = gld16c<0>(vlane, sb);
        hf[C & 7][1] = gld16c<1024>(vlane, sb);
        hf[C & 7][2] = gld16c<0>(vlane, sb + 112 * 1024);
        hf[C & 7][3] = gld16c<1024>(vlane, sb + 112 * 1024);
    }
};

template <int C> DI void g2_step(const Params& p, int g, int crot, u32 D0, u32 voffw, u32 lds0, const char* hb, u32 vlane,
                                 u32x4 (&hf)[8][4], f32x16 (&acc)[2][2], u32 ta0, u32 ta1) {
    asm volatile("s_waitcnt vmcnt(%4)" : "+v"(hf[C & 7][0]), "+v"(hf[C & 7][1]), "+v"(hf[C & 7][2]), "+v"(hf[C & 7][3]) : "i"(g2_younger(C)) : "memory");
    const u32 lb = loc_of(C) + g * loc_gstride(C);
    u32x4 wf[2][2];
#pragma unroll
    for (int ks = 0; ks < 2; ++ks) {
        wf[ks][0] = cat(ldtr(ta0 + lb + ks * 2048), ldtr(ta0 + lb + ks * 2048 + 1024));
        wf[ks][1] = cat(ldtr(ta1 + lb + ks * 2048), ldtr(ta1 + lb + ks * 2048 + 1024));
    }
#pragma unroll
    for (int ks = 0; ks < 2; ++ks)
#pragma unroll
        for (int dt = 0; dt < 2; ++dt)
#pragma unroll
            for (int sh = 0; sh < 2; ++sh) acc[dt][sh] = mfma32(wf[ks][dt], hf[C & 7][sh * 2 + ks], acc[dt][sh]);
    asm volatile("s_waitcnt lgkmcnt(0)" ::: "memory");
    if constexpr (C + 5 < 14) G2<C + 5>::issue_w(p, g, crot, D0, voffw, lds0);
    if constexpr (C + 8 < 14) G2<C + 8>::issue_h(hb, crot, vlane, hf);
    if constexpr (C + 1 < 14) g2_step<C + 1>(p, g, crot, D0, voffw, lds0, hb, vlane, hf, acc, ta0, ta1);
}

template <int HALF> DI void conv_body(u32 aA0, u32 aA1, u32 aB, f32x4 (&acc)[4]) {
    constexpr int NR = HALF ? 6 : 8;
#pragma unroll
    for (int s = 0; s < 8; ++s) {
        u32x4 A[NR], B[NR];
#pragma unroll
        for (int r = 0; r < NR; ++r) {
            A[r] = cat(lds8(aA0 + r * 512 + s * 64), lds8(aA1 + r * 512 + s * 64));
            B[r] = lds16(aB + (224 - 32 * r + 4 * s) * 16);
        }
#pragma unroll
        for (int i = 0; i < 4; ++i) {
            const int q = HALF ? (i + 2) : (i < 2 ? i : i + 4);
#pragma unroll
            for (int r = 0; r <= q; ++r) acc[i] = mfma16(A[r], B[q - r], acc[i]);
        }
    }
}

template <bool GROLE>
DI void phase1(const Params& p, int wave, int lane, u32 lds0, int rot, u32 R0, u32 F0, u32 D0, int crot, int chan,
               u32 voffw2, char* kvflag, u32 tlo, u32 thi, f32x16 (&acc)[2], float& cs0, float& cs1, float& rs, float& rq, u32x4 (&sk)[6]
               ) {
    const int fh = wave & 1, sq = wave >> 1;
    const int hi = lane >> 5, s31 = lane & 31;
    const int r8 = lane >> 3, c8 = lane & 7, r4 = lane >> 4, c16 = lane & 15;
    const u32 vx0 = (u32)((16 * wave + r8) * 896 + 16 * (c8 ^ (r8 >> 1)));
    const u32 vx1 = (u32)((16 * wave + 8 + r8) * 896 + 16 * (c8 ^ (4 | (r8 >> 1))));
    const u32 vw0 = (u32)((8 * wave + r4) * 3584 + 16 * (c16 ^ (r4 << 2)));
    const u32 vw1 = vw0 + 4 * 3584;
    const char* xb = (const char*)p.x + (size_t)R0 * 896;
    const char* wb = (const char*)p.w1 + F0 * 2;
    auto stage = [&](int st) {
        int kc = st + rot; if (kc >= 7) kc -= 7;
        const u32 sl = lds0 + (st & 3) * L_SLOT + wave * 2048;
        dma16(vx0, xb + kc * 128, sl); dma16(vx1, xb + kc * 128, sl + 1024);
        dma16(vw0, wb + (size_t)kc * 64 * 3584, sl + 16384); dma16(vw1, wb + (size_t)kc * 64 * 3584, sl + 16384 + 1024);
    };
    const int rot4 = sq ^ (fh << 1);
    const int xrow = 32 * sq + s31;
    const u32 Z = (u32)(((2 * rot4) | hi) ^ ((xrow >> 1) & 7));
    u32 ax[4], aw0[4], aw1[4];
    const int blk = (lane >> 4) & 1, q = (lane & 15) >> 2, pp = lane & 3;
    const u32 wbase = lds0 + 16384 + (8 * hi + q) * 256 + 16 * ((8 * fh + 2 * blk + (pp >> 1)) ^ (q << 2)) + 8 * (pp & 1);
#pragma unroll
    for (int i = 0; i < 4; ++i) { ax[i] = lds0 + xrow * 128 + 16 * ((2 * i) ^ Z); aw0[i] = wbase + ((i ^ rot4) << 12); aw1[i] = aw0[i] ^ 64; }
    const u32 ones = 0x3f803f80u;
    auto compute = [&](int st) {
        const u32 so = (st & 3) * L_SLOT;
#pragma unroll
        for (int i = 0; i < 4; ++i) {
            u32x4 xf = lds16(ax[i] + so);
            u32x4 w0 = cat(ldtr(aw0[i] + so), ldtr(aw0[i] + so + 1024));
            u32x4 w1 = cat(ldtr(aw1[i] + so), ldtr(aw1[i] + so + 1024));
            acc[0] = mfma32(w0, xf, acc[0]);
            acc[1] = mfma32(w1, xf, acc[1]);
            if (i == 0) {
#pragma unroll
                for (int j = 0; j < 4; ++j) { cs0 = dot2(w0[j], ones, cs0); cs1 = dot2(w1[j], ones, cs1); }
            }
            if (i < 2) {
#pragma unroll
                for (int j = 0; j < 4; ++j) { rs = dot2(xf[j], ones, rs); rq = dot2(xf[j], xf[j], rq); }
            }
        }
    };
    const int g = wave;
    auto skload = [&]() {
        const u32x4 srd = mksrd((const char*)p.ssmk + (size_t)chan * 4096, 4096);
        const int half = wave & 1;
#pragma unroll
        for (int rnd = 0; rnd < 3; ++rnd) {
            const int qq = half * 129 + rnd * 64 + lane;
            sk[2 * rnd] = bld16((u32)((qq - 2) * 16), srd);
            sk[2 * rnd + 1] = bld16((u32)((qq - 1) * 16), srd);
        }
    };
    WAITBAR(13); ST(2);
    compute(0);
    WAITBAR(9);
    stage(4);
    if constexpr (GROLE) G2<0>::issue_w(p, g, crot, D0, voffw2, lds0); else skload();
    compute(1);
    WAITBAR(GROLE ? 13 : 15);
    stage(5);
    compute(2);
    WAITBAR(GROLE ? 12 : 14); ST(3);
    if constexpr (!GROLE) { if (wave == 4 && lane == 0) gst8c<0>(0u, kvflag, u32x2{tlo, thi}); }
    stage(6);
    compute(3);
    WAITBAR(GROLE ? 12 : 14);
    if constexpr (GROLE) { G2<1>::issue_w(p, g, crot, D0, voffw2, lds0); G2<2>::issue_w(p, g, crot, D0, voffw2, lds0); }
    compute(4);
    WAITBAR(GROLE ? 12 : 4);
    if constexpr (GROLE) { G2<3>::issue_w(p, g, crot, D0, voffw2, lds0); G2<4>::issue_w(p, g, crot, D0, voffw2, lds0); }
    compute(5);
    WAITBAR(GROLE ? 16 : 0); ST(4);
    if constexpr (!GROLE) { PIN(sk[0]); PIN(sk[1]); PIN(sk[2]); PIN(sk[3]); PIN(sk[4]); PIN(sk[5]); }
    compute(6);
}

__global__ __launch_bounds__(512, 2) void fused_kernel(Params p) {
    extern __shared__ __attribute__((aligned(16))) char smem[];
    ST(0);
    const u32 lds0 = (u32)(size_t)(LAS char*)smem;
    const int tid = threadIdx.x, lane = tid & 63;
    const int wave = __builtin_amdgcn_readfirstlane(tid >> 6);
    const int b = blockIdx.x, xcd = b & 7, bi = b >> 3;
    const int rq_ = xcd >> 1, fh2 = xcd & 1, rb = bi / 7, ct = bi - rb * 7;
    const int RB = 4 * rq_ + rb, CT = 7 * fh2 + ct;
    const u32 R0 = 128 * RB, F0 = 128 * CT;
    const int tb64 = 2 * RB + fh2, db = ct;
    const u32 T0 = 64 * tb64, D0 = 64 * db;
    const int rot = xcd >= 7 ? 0 : xcd;
    const int crot = (2 * xcd) % 14;
    const int chan = 2 * b + ((wave >> 1) & 1);
    char* ws = p.ws;

    u32x2 ep = gld8c(0u, ws + WS_EPOCH);
    const int kt = tid >> 3, kc = tid & 7;
    const u32x4 ksrd = mksrd(p.k, S_ * D_ * 2);
    const int krow = (int)T0 + kt;
    u32x4 kk[4];
#pragma unroll
    for (int j = 0; j < 4; ++j) kk[j] = bld16((u32)((krow - j) * 896 + D0 * 2 + kc * 16), ksrd);
    u32x4 vv = gld16((u32)(krow * 896 + kc * 16), (const char*)p.v + D0 * 2);
    u32x4 tp = gld16((u32)((lane & 31) * 16), (const char*)p.shk + D0 * 8);
    u32 b1v = gld2((u32)((64 * (wave & 1) + lane) * 2), (const char*)p.b1 + F0 * 2);
    u32x4 b2v = gld16((u32)(kc * 16), (const char*)p.b2 + D0 * 2);
    {
        const int r8 = lane >> 3, c8 = lane & 7, r4 = lane >> 4, c16 = lane & 15;
        const u32 vx0 = (u32)((16 * wave + r8) * 896 + 16 * (c8 ^ (r8 >> 1)));
        const u32 vx1 = (u32)((16 * wave + 8 + r8) * 896 + 16 * (c8 ^ (4 | (r8 >> 1))));
        const u32 vw0 = (u32)((8 * wave + r4) * 3584 + 16 * (c16 ^ (r4 << 2)));
        const u32 vw1 = vw0 + 4 * 3584;
        const char* xb = (const char*)p.x + (size_t)R0 * 896;
        const char* wb = (const char*)p.w1 + F0 * 2;
#pragma unroll
        for (int st = 0; st < 4; ++st) {
            int kq = st + rot; if (kq >= 7) kq -= 7;
            const u32 sl = lds0 + st * L_SLOT + wave * 2048;
            dma16(vx0, xb + kq * 128, sl); dma16(vx1, xb + kq * 128, sl + 1024);
            dma16(vw0, wb + (size_t)kq * 64 * 3584, sl + 16384); dma16(vw1, wb + (size_t)kq * 64 * 3584, sl + 16384 + 1024);
        }
    }
    if (tid < 16) *(LAS u32*)(lds0 + L_FLAG + tid * 4) = 0u;
    asm volatile("s_waitcnt vmcnt(16)" : "+v"(ep), "+v"(kk[0]), "+v"(kk[1]), "+v"(kk[2]), "+v"(kk[3]), "+v"(vv), "+v"(tp), "+v"(b1v), "+v"(b2v) :: "memory");
    ST(1);
    const u64 epoch = ((u64)(u32)__builtin_amdgcn_readfirstlane(ep.y) << 32) | (u32)__builtin_amdgcn_readfirstlane(ep.x);
    const u64 tag = epoch + TAG_SALT;
    const u32 tlo = (u32)tag, thi = (u32)(tag >> 32);
    {
        const u32 tb = lds0 + L_TAPS + wave * 512;
        if (lane < 32) sts16(tb + lane * 16, tp);
        u32x4 w[4];
#pragma unroll
        for (int j = 0; j < 4; ++j) w[j] = lds16(tb + kc * 64 + j * 16);
        u32x4 o;
#pragma unroll
        for (int e2 = 0; e2 < 4; ++e2) {
            u32 a01 = __builtin_amdgcn_perm(kk[1][e2], kk[0][e2], 0x05040100u), a23 = __builtin_amdgcn_perm(kk[3][e2], kk[2][e2], 0x05040100u);
            u32 b01 = __builtin_amdgcn_perm(kk[1][e2], kk[0][e2], 0x07060302u), b23 = __builtin_amdgcn_perm(kk[3][e2], kk[2][e2], 0x07060302u);
            float lo = dot2(a23, w[e2][1], dot2(a01, w[e2][0], 0.f));
            float hi_ = dot2(b23, w[e2][3], dot2(b01, w[e2][2], 0.f));
            o[e2] = pk2(rbf(lo) * bflo(vv[e2]), rbf(hi_) * bfhi(vv[e2]));
        }
        sts16(lds0 + L_KVS + kt * 144 + kc * 16, o);
        BARL();
        const int cb = wave & 3, th = wave >> 2, grp = lane >> 4, q = (lane & 15) >> 2, pp = lane & 3;
        const int tl = 32 * th + 8 * grp;
        const u32 a = lds0 + L_KVS + (tl + 7 - q) * 144 + cb * 32 + pp * 8;
        u32x4 r = cat(ldtr(a), ldtr(a - 4 * 144));
        const int d = (int)D0 + 16 * cb + (lane & 15);
        gst16c<0>((u32)(d * 4096 + (2040 - (int)T0 - tl) * 2), ws + WS_KVT, r);
    }
    f32x16 acc1[2] = {};
    float cs0 = 0.f, cs1 = 0.f, rs = 0.f, rq = 0.f;
    u32x4 sk[6];
    const u32 voffw2 = (u32)((lane >> 3) * 896 + 16 * ((lane & 7) ^ (((lane >> 4) & 1) << 2)));
    char* kvflag = ws + WS_KVF + (db * 32 + tb64) * 8;
    if (wave < 4) phase1<true>(p, wave, lane, lds0, rot, R0, F0, D0, crot, chan, voffw2, kvflag, tlo, thi, acc1, cs0, cs1, rs, rq, sk
        );
    else phase1<false>(p, wave, lane, lds0, rot, R0, F0, D0, crot, chan, voffw2, kvflag, tlo, thi, acc1, cs0, cs1, rs, rq, sk
        );
    const int fh = wave & 1, sq = wave >> 1, hi = lane >> 5, s31 = lane & 31;
    {
        cs0 = xhalf_sum(cs0); cs1 = xhalf_sum(cs1); rs = xhalf_sum(rs); rq = xhalf_sum(rq);
        float c = hi ? cs1 : cs0;
        float c0 = rbf(c), c1 = rbf(c - c0), c2 = c - c0 - c1;
        const u32 ae = lds0 + L_AEXT + (64 * fh + lane) * 32;
        *(LAS u16*)(ae + sq * 6) = (u16)(pk2(c0, c0));
        *(LAS u16*)(ae + sq * 6 + 2) = (u16)(pk2(c1, c1));
        *(LAS u16*)(ae + sq * 6 + 4) = (u16)(pk2(c2, c2));
        if (sq == 0) sts8(ae + 24, u32x2{b1v, 0u});
        if (hi == 0) sts8(lds0 + L_ROWP + (32 * sq + s31) * 16 + fh * 8, u32x2{__float_as_uint(rs), __float_as_uint(rq)});
    }
    BARL(); ST(5);
    {
        u32x4 rp = lds16(lds0 + L_ROWP + (32 * sq + s31) * 16);
        u32x4 a0 = lds16(lds0 + L_AEXT + (64 * fh + s31) * 32 + hi * 16);
        u32x4 a1 = lds16(lds0 + L_AEXT + (64 * fh + 32 + s31) * 32 + hi * 16);
        float mean = (__uint_as_float(rp.x) + __uint_as_float(rp.z)) * (1.f / 448.f);
        float var = (__uint_as_float(rp.y) + __uint_as_float(rp.w)) * (1.f / 448.f) - mean * mean;
        var = fmaxf(var, 0.f);
        float sd = rbf(__builtin_amdgcn_sqrtf(rbf(rbf(var) + 1e-5f)));
        u32 nm = pk2(-mean, -mean);
        u32x4 bx = {nm, nm, hi ? (pk2(sd, 0.f)) : nm, hi ? 0u : nm};
        acc1[0] = mfma32(a0, bx, acc1[0]);
        acc1[1] = mfma32(a1, bx, acc1[1]);
        float ri = __builtin_amdgcn_rcpf(sd); ri = ri * ri;
        char* hb = ws + WS_H + (size_t)((4 * RB + sq) * 112 + CT * 8 + fh * 4) * 1024;
        u32x4 hv[4];
#pragma unroll
        for (int mt = 0; mt < 2; ++mt)
#pragma unroll
            for (int t = 0; t < 2; ++t)
#pragma unroll
                for (int j = 0; j < 4; ++j) {
                    float u0 = fmaxf(acc1[mt][8 * t + 2 * j], 0.f), u1 = fmaxf(acc1[mt][8 * t + 2 * j + 1], 0.f);
                    hv[mt * 2 + t][j] = pk2(u0 * u0 * ri, u1 * u1 * ri);
                }
        gst16c<0>(lane * 16u, hb, hv[0]); gst16c<1024>(lane * 16u, hb, hv[1]);
        gst16c<2048>(lane * 16u, hb, hv[2]); gst16c<3072>(lane * 16u, hb, hv[3]);
    }
    ST(6);
    char* myhflag = ws + WS_HF + (size_t)((2 * RB + (sq >> 1)) * 64 + ((sq & 1) * 14 + CT) * 2 + fh) * 8;
    if (wave < 4) {
        const int g = wave;
        WAITV(0);
        if (lane == 0) gst8c<0>(0u, myhflag, u32x2{tlo, thi});
        poll8(lane * 8u, ws + WS_HF + tb64 * 512, lane < 56 && (lane & 1) == (g >> 1), tlo, thi);
        ST(7);
        const char* hb = ws + WS_H + (size_t)(tb64 * 2 * 112 + 2 * g) * 1024;
        u32x4 hf[8][4];
        const u32 vlane = lane * 16u;
        G2<0>::issue_h(hb, crot, vlane, hf); G2<1>::issue_h(hb, crot, vlane, hf); G2<2>::issue_h(hb, crot, vlane, hf); G2<3>::issue_h(hb, crot, vlane, hf);
        G2<4>::issue_h(hb, crot, vlane, hf); G2<5>::issue_h(hb, crot, vlane, hf); G2<6>::issue_h(hb, crot, vlane, hf); G2<7>::issue_h(hb, crot, vlane, hf);
        f32x16 acc[2][2] = {};
        const int blk = (lane >> 4) & 1, q = (lane & 15) >> 2, pp = lane & 3;
        const u32 ta0 = lds0 + (4 * hi + q) * 128 + 16 * ((2 * blk + (pp >> 1)) ^ ((q >> 1) << 2)) + 8 * (pp & 1);
        const u32 ta1 = ta0 ^ 64;
        g2_step<0>(p, g, crot, D0, voffw2, lds0, hb, vlane, hf, acc, ta0, ta1);
        ST(8);
#pragma unroll
        for (int sh = 0; sh < 2; ++sh)
#pragma unroll
            for (int dt = 0; dt < 2; ++dt)
#pragma unroll
                for (int gr = 0; gr < 4; ++gr) {
                    const u32 a = lds0 + (sh ? 0u : 3 * L_SLOT) + g * 8192 + s31 * 256 + 16 * ((8 * dt + 2 * gr + hi) ^ (s31 & 7));
                    sts16(a, __builtin_bit_cast(u32x4, f32x4{acc[dt][sh][4 * gr], acc[dt][sh][4 * gr + 1], acc[dt][sh][4 * gr + 2], acc[dt][sh][4 * gr + 3]}));
                }
    } else {
        const int cw = wave - 4, half = cw & 1, chl = cw >> 1;
        const u32 tbase = lds0 + L_CONV + chl * TOEP_CH;
#pragma unroll
        for (int rnd = 0; rnd < 3; ++rnd) {
            const int qq = half * 129 + rnd * 64 + lane;
            if (rnd < 2 || lane == 0) {
                u32x4 A = sk[2 * rnd], B = sk[2 * rnd + 1];
                u32 e0 = __builtin_amdgcn_alignbit(A.y, A.x, 16), e1 = __builtin_amdgcn_alignbit(A.z, A.y, 16), e2 = __builtin_amdgcn_alignbit(A.w, A.z, 16),
                    e3 = __builtin_amdgcn_alignbit(B.x, A.w, 16), e4 = __builtin_amdgcn_alignbit(B.y, B.x, 16);
                sts16(tbase + qq * 16, u32x4{e0, e1, e2, e3});
                sts16(tbase + TOEP_CP + qq * 16, u32x4{A.y, A.z, A.w, B.x});
                sts16(tbase + 2 * TOEP_CP + qq * 16, u32x4{e1, e2, e3, e4});
                sts16(tbase + 3 * TOEP_CP + qq * 16, u32x4{A.z, A.w, B.x, B.y});
            }
        }
        const u32 ub = lds0 + L_U + cw * U_SZ;
        if (lane < 32) sts16(ub + 4096 + lane * 16, u32x4{0u, 0u, 0u, 0u});
        asm volatile("s_waitcnt vmcnt(0) lgkmcnt(0)" ::: "memory");
        if (lane == 0) { gst8c<0>(0u, myhflag, u32x2{tlo, thi}); *(volatile LAS u32*)(lds0 + L_FLAG + cw * 4) = 1u; }
        poll8((lane & 31) * 8u, ws + WS_KVF + (chan >> 6) * 256, true, tlo, thi);
        ST(7);
        {
            const char* kb = ws + WS_KVT + (size_t)chan * 4096;
            u32x4 u0 = gld16c<0>(lane * 16u, kb), u1 = gld16c<1024>(lane * 16u, kb), u2 = gld16c<2048>(lane * 16u, kb), u3 = gld16c<3072>(lane * 16u, kb);
            asm volatile("s_waitcnt vmcnt(0)" : "+v"(u0), "+v"(u1), "+v"(u2), "+v"(u3) :: "memory");
            sts16(ub + lane * 16, u0); sts16(ub + 1024 + lane * 16, u1); sts16(ub + 2048 + lane * 16, u2); sts16(ub + 3072 + lane * 16, u3);
        }
        u32x4 xa = gld16((u32)(lane * 896), (const char*)p.x + (size_t)T0 * 896 + (D0 + 16 * cw) * 2);
        u32x4 xb_ = gld16((u32)(lane * 896 + 16), (const char*)p.x + (size_t)T0 * 896 + (D0 + 16 * cw) * 2);
        u32 dv = gld2(0u, (const char*)p.ssmd + chan * 2);
        for (int spin = 0; spin < (1 << 20); ++spin) {
            if (__builtin_amdgcn_readfirstlane(*(volatile LAS u32*)(lds0 + L_FLAG + (cw ^ 1) * 4))) break;
            __builtin_amdgcn_s_sleep(1);
        }
        asm volatile("" ::: "memory");
        const int bb = lane & 15, gg = lane >> 4;
        u32 aA0 = tbase + (bb & 3) * TOEP_CP + ((bb >> 2) + 2 * gg) * 8;
        u32 aA1 = aA0 + 8; PIN(aA1);
        const u32 aB = ub + (30 - 2 * bb + gg) * 16;
        f32x4 acc[4] = {};
        if (half == 0) conv_body<0>(aA0, aA1, aB, acc); else conv_body<1>(aA0, aA1, aB, acc);
        asm volatile("s_waitcnt vmcnt(0)" : "+v"(xa), "+v"(xb_), "+v"(dv) :: "memory");
        ST(8);
        {
            const float dd = bflo(dv);
            char* yb = ws + WS_YT + (size_t)chan * 4096;
#pragma unroll
            for (int i = 0; i < 4; ++i) {
                const int qt = half ? (i + 2) : (i < 2 ? i : i + 4);
                const int t = 256 * qt + 16 * bb + 4 * gg;
                u32x2 uu = lds8(ub + (2044 - t) * 2);
                float y0 = rbf(acc[i][0]) + rbf(bfhi(uu.y) * dd), y1 = rbf(acc[i][1]) + rbf(bflo(uu.y) * dd);
                float y2 = rbf(acc[i][2]) + rbf(bfhi(uu.x) * dd), y3 = rbf(acc[i][3]) + rbf(bflo(uu.x) * dd);
                gst8c<0>((u32)(t * 2), yb, u32x2{pk2(y0, y1), pk2(y2, y3)});
            }
            WAITV(0);
            if (lane == 0) gst8c<0>(0u, ws + WS_YF + (b * 4 + cw) * 8, u32x2{tlo, thi});
        }
        poll16(lane * 16u, ws + WS_YF + db * 1024, tlo, thi);
        ST(9);
        {
            const char* yb = ws + WS_YT + (size_t)(D0 + 16 * cw) * 4096 + T0 * 2;
            const u32 vo = (u32)((lane >> 3) * 4096 + (lane & 7) * 16);
            u32x4 y0 = gld16c<0>(vo, yb), y1 = gld16c<0>(vo, yb + 8 * 4096);
            asm volatile("s_waitcnt vmcnt(0)" : "+v"(y0), "+v"(y1) :: "memory");
            const u32 ys = lds0 + L_YS + cw * 2304;
            sts16(ys + (lane >> 3) * 144 + (lane & 7) * 16, y0);
            sts16(ys + (8 + (lane >> 3)) * 144 + (lane & 7) * 16, y1);
            const int q = (lane & 15) >> 2, pp = lane & 3;
            const u32 a = ys + q * 144 + gg * 32 + pp * 8;
            u32x2 t0 = ldtr(a), t1 = ldtr(a + 4 * 144), t2 = ldtr(a + 8 * 144), t3 = ldtr(a + 12 * 144);
            u32x4 rp = lds16(lds0 + L_ROWP + (64 * fh2 + lane) * 16);
            float mean = (__uint_as_float(rp.x) + __uint_as_float(rp.z)) * (1.f / 448.f);
            float var = (__uint_as_float(rp.y) + __uint_as_float(rp.w)) * (1.f / 448.f) - mean * mean;
            var = fmaxf(var, 0.f);
            float sd = rbf(__builtin_amdgcn_sqrtf(rbf(rbf(var) + 1e-5f)));
            float m = rbf(mean), ri = __builtin_amdgcn_rcpf(sd);
            u32 yy[8] = {t0.x, t0.y, t1.x, t1.y, t2.x, t2.y, t3.x, t3.y};
            u32 xx[8] = {xa.x, xa.y, xa.z, xa.w, xb_.x, xb_.y, xb_.z, xb_.w};
            u32 ss[8];
#pragma unroll
            for (int j = 0; j < 8; ++j) {
                u32 dx = pk2(bflo(xx[j]) - m, bfhi(xx[j]) - m);
                u32 xn = pk2(bflo(dx) * ri, bfhi(dx) * ri);
                ss[j] = pk2(bflo(xn) * bflo(yy[j]), bfhi(xn) * bfhi(yy[j]));
            }
            sts16(ub + lane * 32, u32x4{ss[0], ss[1], ss[2], ss[3]}); sts16(ub + lane * 32 + 16, u32x4{ss[4], ss[5], ss[6], ss[7]});
            sts16(ub + 2048 + lane * 32, xa); sts16(ub + 2048 + lane * 32 + 16, xb_);
        }
        if (b == 0 && wave == 7) {
            poll16(lane * 16u, ws + WS_KVF, tlo, thi);
            poll16((lane < 48 ? lane : 0) * 16u, ws + WS_KVF + 1024, tlo, thi);
            if (lane == 0) { const u64 e1 = epoch + 1; gst8c<0>(0u, ws + WS_EPOCH, u32x2{(u32)e1, (u32)(e1 >> 32)}); }
        }
    }
    BARL(); ST(10);
    {
        const int t = kt, c = kc;
        const u32 pb = lds0 + ((t >> 5) ? 0u : 3 * L_SLOT) + (t & 31) * 256;
        const u32 o0 = 16 * ((2 * c) ^ (t & 7)), o1 = 16 * ((2 * c + 1) ^ (t & 7));
        f32x4 s0 = {}, s1 = {};
#pragma unroll
        for (int g = 0; g < 4; ++g) {
            s0 += __builtin_bit_cast(f32x4, lds16(pb + g * 8192 + o0));
            s1 += __builtin_bit_cast(f32x4, lds16(pb + g * 8192 + o1));
        }
        const u32 sb = lds0 + L_U + (c >> 1) * U_SZ + t * 32 + (c & 1) * 16;
        u32x4 sv = lds16(sb), xv = lds16(sb + 2048);
        float o[8] = {s0[0], s0[1], s0[2], s0[3], s1[0], s1[1], s1[2], s1[3]};
        u32x4 res;
#pragma unroll
        for (int j = 0; j < 4; ++j) {
            u32 a = pk2(o[2 * j], o[2 * j + 1]);
            u32 z = pk2(bflo(a) + bflo(b2v[j]), bfhi(a) + bfhi(b2v[j]));
            float z0 = bflo(z), z1 = bfhi(z);
            float g0 = z0 * __builtin_amdgcn_rcpf(1.f + __expf(-z0)), g1 = z1 * __builtin_amdgcn_rcpf(1.f + __expf(-z1));
            u32 gq = pk2(g0, g1);
            u32 sg = pk2(bflo(sv[j]) * bflo(gq), bfhi(sv[j]) * bfhi(gq));
            res[j] = pk2(bflo(xv[j]) + bflo(sg), bfhi(xv[j]) + bfhi(sg));
        }
        *(GAS u32x4*)((GAS char*)p.out + (size_t)(T0 + t) * 896 + D0 * 2 + c * 16) = res;
    }
}

extern "C" void kernel_launch(void* const* d_in, const int* in_sizes, int n_in,
                              void* d_out, int out_size, void* d_ws, size_t ws_size,
                              hipStream_t stream) {
    Params p;
    p.x = (const u16*)d_in[0]; p.k = (const u16*)d_in[1]; p.v = (const u16*)d_in[2]; p.shk = (const u16*)d_in[3];
    p.ssmk = (const u16*)d_in[4]; p.ssmd = (const u16*)d_in[5]; p.w1 = (const u16*)d_in[7]; p.b1 = (const u16*)d_in[8];
    p.w2 = (const u16*)d_in[9]; p.b2 = (const u16*)d_in[10];
    p.out = (u16*)d_out; p.ws = (char*)d_ws;
    (void)hipFuncSetAttribute((const void*)fused_kernel, hipFuncAttributeMaxDynamicSharedMemorySize, LDS_BYTES);
    fused_kernel<<<NWG, 512, LDS_BYTES, stream>>>(p);
}
```

```cpp
#include <hip/hip_runtime.h>
#include <stdint.h>
#include <type_traits>

typedef unsigned short u16;
typedef unsigned int u32;
typedef unsigned long long u64;
typedef u32 u32x4 __attribute__((ext_vector_type(4)));
typedef u32 u32x2 __attribute__((ext_vector_type(2)));
typedef float f32x4 __attribute__((ext_vector_type(4)));
typedef float f32x2 __attribute__((ext_vector_type(2)));
typedef float f32x16 __attribute__((ext_vector_type(16)));
typedef __bf16 bf16x8 __attribute__((ext_vector_type(8)));
typedef __bf16 bf16x2 __attribute__((ext_vector_type(2)));
typedef short s16x4 __attribute__((ext_vector_type(4)));
#define LAS __attribute__((address_space(3)))
#define GAS __attribute__((address_space(1)))
#define DI __device__ __forceinline__

constexpr int S_ = 2048, D_ = 448, F_ = 1792;
constexpr int NHELP = 32, NWG = 224;
constexpr u32 MB = 256;
constexpr u32 WS_EPOCH = 0, WS_ALL = 2048, WS_KVF = 4096, WS_HF = WS_KVF + 65536, WS_YF = WS_HF + 65536, WS_RG = WS_YF + 65536, WS_CG = WS_RG + 16384;
constexpr u32 TP = 4096 + 128;
constexpr u32 WS_H = 1u << 20, WS_KVT = 9u << 20, WS_YT = 11u << 20;
constexpr u32 STAMP_OFF = 16u << 20;
constexpr int NSTAMP = 22;
constexpr u32 L_PIECE = 16384, L_W = 65536, L_LOC0 = 131072, L_MISC = 147456, LDS_BYTES = 163840;
constexpr u32 KVRAW_SZ = 5632, L_KVS = L_LOC0 + 4 * KVRAW_SZ, L_B2 = L_KVS + 9216, L_FLAG = L_B2 + 128, L_ROWF = L_FLAG + 128, L_B2F = L_ROWF + 256, L_R2 = L_B2F + 256;
constexpr u32 L_SCR = L_LOC0 + 2 * KVRAW_SZ + 9728 + 64;
static_assert(L_R2 + 256 <= LDS_BYTES, "lds");
constexpr u32 F_TOEP = L_FLAG, F_HCNT = L_FLAG + 16, F_HRDY = L_FLAG + 20, F_KVRDY = L_FLAG + 24, F_YCNT = L_FLAG + 28, F_YRDY = L_FLAG + 32, F_S0 = L_FLAG + 36, F_KVCNT = L_FLAG + 40, F_W = L_FLAG + 48, F_LAND = L_FLAG + 64, F_CONS = L_FLAG + 96, F_KVS = L_FLAG + 124;
constexpr u32 L_CONV = 16384, CONV_CH = 65536, TOEP_CP = 4160, TOEP_CH = 4 * TOEP_CP, U_SZ = 4608, YS_SZ = 2304;
constexpr u64 TAG_SALT = 0x9E3779B97F4A7C15ull;
constexpr int SPIN_CAP = 1 << 15;
#define SLP 2

#define ST(i) do {} while (0)
#define STP nullptr

DI void dma16(u32 voff, const void* sbase, u32 lds) {
    asm volatile("s_mov_b32 m0, %2\n\ts_nop 0\n\tglobal_load_lds_dwordx4 %0, %1" :: "v"(voff), "s"(sbase), "s"(lds) : "memory");
}
DI void setm0(u32 lds) { asm volatile("s_mov_b32 m0, %0\n\ts_nop 0" :: "s"(lds) : "memory"); }
template <int OFF> DI void dmai(u32 voff, const void* sbase) { asm volatile("global_load_lds_dwordx4 %0, %1 offset:%2" :: "v"(voff), "s"(sbase), "i"(OFF) : "memory"); }
DI void bdma16(u32 voff, u32x4 srd, u32 lds) {
    asm volatile("s_mov_b32 m0, %2\n\ts_nop 0\n\tbuffer_load_dwordx4 %0, %1, 0 offen lds" :: "v"(voff), "s"(srd), "s"(lds) : "memory");
}
#define SB() __builtin_amdgcn_sched_barrier(0)
template <int OFF> DI u32x2 alds8(u32 a) { u32x2 r; asm volatile("ds_read_b64 %0, %1 offset:%2" : "=v"(r) : "v"(a), "i"(OFF)); return r; }
template <int OFF> DI u32x4 alds16(u32 a) { u32x4 r; asm volatile("ds_read_b128 %0, %1 offset:%2" : "=v"(r) : "v"(a), "i"(OFF)); return r; }
DI u32x4 gld16(u32 voff, const void* sbase) { u32x4 r; asm volatile("global_load_dwordx4 %0, %1, %2" : "=v"(r) : "v"(voff), "s"(sbase) : "memory"); return r; }
template <int OFF> DI u32x4 gld16c(u32 voff, const void* sbase) { u32x4 r; asm volatile("global_load_dwordx4 %0, %1, %2 offset:%3 sc1" : "=v"(r) : "v"(voff), "s"(sbase), "i"(OFF) : "memory"); return r; }
DI u32x2 gld8c(u32 voff, const void* sbase) { u32x2 r; asm volatile("global_load_dwordx2 %0, %1, %2 sc1" : "=v"(r) : "v"(voff), "s"(sbase) : "memory"); return r; }
DI void touch(const void* addr, u32 lds) { asm volatile("s_mov_b32 m0, %1\n\ts_nop 0\n\tglobal_load_lds_dword %0, off" :: "v"(addr), "s"(lds) : "memory"); }
DI u32 gld2(u32 voff, const void* sbase) { u32 r; asm volatile("global_load_ushort %0, %1, %2" : "=v"(r) : "v"(voff), "s"(sbase) : "memory"); return r; }
DI u32x4 bld16(u32 voff, u32x4 srd) { u32x4 r; asm volatile("buffer_load_dwordx4 %0, %1, %2, 0 offen" : "=v"(r) : "v"(voff), "s"(srd) : "memory"); return r; }
template <int OFF> DI u32x4 gld16o(u32 voff, const void* sbase) { u32x4 r; asm volatile("global_load_dwordx4 %0, %1, %2 offset:%3" : "=v"(r) : "v"(voff), "s"(sbase), "i"(OFF) : "memory"); return r; }
template <int OFF> DI void gst16c(u32 voff, void* sbase, u32x4 v) { asm volatile("global_store_dwordx4 %0, %1, %2 offset:%3 sc1\n\ts_nop 1" :: "v"(voff), "v"(v), "s"(sbase), "i"(OFF) : "memory"); }
template <int OFF> DI void gst8c(u32 voff, void* sbase, u32x2 v) { asm volatile("global_store_dwordx2 %0, %1, %2 offset:%3 sc1" :: "v"(voff), "v"(v), "s"(sbase), "i"(OFF) : "memory"); }
#define WAITV(n) asm volatile("s_waitcnt vmcnt(%0)" :: "i"(n) : "memory")
#define WAITBAR(n) asm volatile("s_waitcnt vmcnt(%0) lgkmcnt(0)\n\ts_barrier" :: "i"(n) : "memory")
#define BARL() asm volatile("s_waitcnt lgkmcnt(0)\n\ts_barrier" ::: "memory")
#define PIN(x) asm volatile("" : "+v"(x))

DI u32x4 mksrd(const void* p, u32 bytes) { u64 a = (u64)p; return u32x4{(u32)a, (u32)(a >> 32) & 0xffffu, bytes, 0x00020000u}; }
DI float bflo(u32 x) { return __uint_as_float(x << 16); }
DI float bfhi(u32 x) { return __uint_as_float(x & 0xffff0000u); }
DI u32 pk2(float a, float b) { bf16x2 v = {(__bf16)a, (__bf16)b}; return __builtin_bit_cast(u32, v); }
DI float rbf(float a) { return bflo(pk2(a, a)); }
DI float relu(float a) { int i = __float_as_int(a); return __int_as_float(i > 0 ? i : 0); }
DI float fma1(float a, float b, float c) { float r; asm("v_fma_f32 %0, %1, %2, %3" : "=v"(r) : "v"(a), "v"(b), "v"(c)); return r; }
DI float mul1(float a, float b) { float r; asm("v_mul_f32 %0, %1, %2" : "=v"(r) : "v"(a), "v"(b)); return r; }
DI float add1(float a, float b) { float r; asm("v_add_f32 %0, %1, %2" : "=v"(r) : "v"(a), "v"(b)); return r; }
DI float dot2(u32 a, u32 b, float c) { return __builtin_amdgcn_fdot2_f32_bf16(__builtin_bit_cast(bf16x2, a), __builtin_bit_cast(bf16x2, b), c, false); }
DI u32x4 lds16(u32 a) { return *(const LAS u32x4*)a; }
DI u32x2 lds8(u32 a) { return *(const LAS u32x2*)a; }
DI void sts16(u32 a, u32x4 v) { *(LAS u32x4*)a = v; }
DI void sts8(u32 a, u32x2 v) { *(LAS u32x2*)a = v; }
DI u32x2 ldtr(u32 a) { return __builtin_bit_cast(u32x2, __builtin_amdgcn_ds_read_tr16_b64_v4i16((LAS s16x4*)a)); }
DI u32x4 cat(u32x2 a, u32x2 b) { return u32x4{a.x, a.y, b.x, b.y}; }
DI f32x16 mfma32(u32x4 a, u32x4 b, f32x16 c) { return __builtin_amdgcn_mfma_f32_32x32x16_bf16(__builtin_bit_cast(bf16x8, a), __builtin_bit_cast(bf16x8, b), c, 0, 0, 0); }
DI f32x4 mfma16(u32x4 a, u32x4 b, f32x4 c) { return __builtin_amdgcn_mfma_f32_16x16x32_bf16(__builtin_bit_cast(bf16x8, a), __builtin_bit_cast(bf16x8, b), c, 0, 0, 0); }
DI float xhalf_sum(float v) { auto r = __builtin_amdgcn_permlane32_swap(__float_as_uint(v), __float_as_uint(v), false, false); return __uint_as_float(r[0]) + __uint_as_float(r[1]); }

struct Params {
    const u16 *x, *k, *v, *shk, *ssmk, *ssmd, *w1, *b1, *w2, *b2;
    u16* out; char* ws;
};

DI void poll8(u32 voff, const void* sbase, bool need, u32 tlo, u32 thi) {
    _Pragma("clang loop unroll(disable)") for (int spin = 0; spin < SPIN_CAP; ++spin) {
        u32x2 f = gld8c(voff, sbase);
        asm volatile("s_waitcnt vmcnt(0)" : "+v"(f) :: "memory");
        bool ok = !need || (f.x == tlo && f.y == thi);
        if (__all(ok)) break;
        __builtin_amdgcn_s_sleep(SLP);
    }
}
DI void poll16(u32 voff, const void* sbase, u32 tlo, u32 thi) {
    _Pragma("clang loop unroll(disable)") for (int spin = 0; spin < SPIN_CAP; ++spin) {
        u32x4 f = gld16c<0>(voff, sbase);
        asm volatile("s_waitcnt vmcnt(0)" : "+v"(f) :: "memory");
        bool ok = f.x == tlo && f.y == thi && f.z == tlo && f.w == thi;
        if (__all(ok)) break;
        __builtin_amdgcn_s_sleep(SLP);
    }
}

DI bool lds_arrive_last(u32 addr, u32 n) {
    u32 old = 0;
    if ((threadIdx.x & 63) == 0) old = __hip_atomic_fetch_add((LAS u32*)addr, 1u, __ATOMIC_RELAXED, __HIP_MEMORY_SCOPE_WORKGROUP);
    return (u32)__builtin_amdgcn_readfirstlane(old) == n - 1;
}
DI void lds_wait(u32 addr) {
    _Pragma("clang loop unroll(disable)") for (int spin = 0; spin < (1 << 22); ++spin) {
        if (__builtin_amdgcn_readfirstlane(*(volatile LAS u32*)addr)) break;
        __builtin_amdgcn_s_sleep(1);
    }
    asm volatile("" ::: "memory");
}
DI void lds_add(u32 addr) { asm volatile("" ::: "memory"); __hip_atomic_fetch_add((LAS u32*)addr, 1u, __ATOMIC_RELAXED, __HIP_MEMORY_SCOPE_WORKGROUP); asm volatile("" ::: "memory"); }
DI u32 lds_peek(u32 addr) { return *(volatile LAS u32*)addr; }
DI void lds_wait_ge(u32 addr, u32 n, u32 first) {
    u32 v = first;
    _Pragma("clang loop unroll(disable)") for (int spin = 0; spin < (1 << 22) && (u32)__builtin_amdgcn_readfirstlane(v) < n; ++spin) { __builtin_amdgcn_s_sleep(1); v = lds_peek(addr); }
    asm volatile("" ::: "memory");
}
DI void lds_set(u32 addr) { asm volatile("" ::: "memory"); if ((threadIdx.x & 63) == 0) *(volatile LAS u32*)addr = 1u; }
constexpr u32 loc_of(int c) { return (c % 5) == 0 ? L_LOC0 : (c % 5) == 1 ? 3 * L_PIECE : (c % 5) == 2 ? L_W + 3 * L_PIECE : (c % 5) == 3 ? 0u : L_W; }

constexpr int HD = 3, HS = HD + 1;
constexpr int RD = 4, WS_ = RD + 1;
constexpr int g2_younger(int c) {
    const int wneed = c + RD;
    bool sh = false, sw = false; int ah = 0, aw = 0;
    const bool wdone = wneed < 5 + RD || wneed >= 14;
    for (int i = 0; i < HD; ++i) { if (sh) ah += 4; if (i == c) sh = true; }
    for (int i = 0; i < c; ++i) {
        if (i + HD < 14) { if (sh) ah += 4; if (sw) aw += 4; if (i + HD == c) sh = true; }
        if (i >= 1 && i + RD + 4 < 14) { if (sh) ah += 4; if (sw) aw += 4; if (i + RD + 4 == wneed) sw = true; }
    }
    if (wdone) return ah;
    return ah < aw ? ah : aw;
}

struct G2Ctx { const char* w2b; const char* hb; int crot; u32 voffw[4], ldsg, vlane, ta[3][2]; };
template <int C> DI const char* g2_wsrc(const G2Ctx& x) { int c = C + x.crot; if (c >= 14) c -= 14; return x.w2b + (size_t)c * (128 * 896); }
template <int C> DI u32 g2_loc(const G2Ctx& x) { return loc_of(C) + x.ldsg; }
template <int C, int N> DI void g2_w1(const G2Ctx& x, const char* sb) { if constexpr (N == 0) setm0(g2_loc<C>(x)); dmai<N * 1024>(x.voffw[N], sb); }
template <int C> DI void g2_issue_w(const G2Ctx& x) { const char* sb = g2_wsrc<C>(x); g2_w1<C, 0>(x, sb); g2_w1<C, 1>(x, sb); g2_w1<C, 2>(x, sb); g2_w1<C, 3>(x, sb); }
template <int C> DI const char* g2_hsrc(const G2Ctx& x) { int c = C + x.crot; if (c >= 14) c -= 14; return x.hb + c * 8192; }
template <int N> DI u32x4 g2_h1(const G2Ctx& x, const char* sb) { return gld16c<(N & 1) * 1024>(x.vlane + (N >> 1) * 112 * 1024, sb); }
template <int C> DI void g2_issue_h(const G2Ctx& x, u32x4 (&hf)[HS][4]) {
    const char* sb = g2_hsrc<C>(x);
    hf[C % HS][0] = g2_h1<0>(x, sb); hf[C % HS][1] = g2_h1<1>(x, sb); hf[C % HS][2] = g2_h1<2>(x, sb); hf[C % HS][3] = g2_h1<3>(x, sb);
}
template <int C> DI void g2_read(const G2Ctx& x, u32x4 (&wf)[WS_][4]) {
    const u32 a0 = x.ta[loc_of(C) >> 16][0], a1 = x.ta[loc_of(C) >> 16][1];
    constexpr u32 lo = loc_of(C) & 0xffffu;
#pragma unroll
    for (int ks = 0; ks < 2; ++ks) {
        wf[C % WS_][ks * 2] = cat(ldtr(a0 + lo + ks * 2048), ldtr(a0 + lo + ks * 2048 + 1024));
        wf[C % WS_][ks * 2 + 1] = cat(ldtr(a1 + lo + ks * 2048), ldtr(a1 + lo + ks * 2048 + 1024));
    }
}
template <int C, int I> DI void g2_rd(const G2Ctx& x, u32x4 (&wf)[WS_][4]) {
    if constexpr (C < 14) {
        const u32 a = x.ta[loc_of(C) >> 16][I & 1]; constexpr u32 lo = (loc_of(C) & 0xffffu) + (I >> 1) * 2048;
        wf[C % WS_][I] = cat(ldtr(a + lo), ldtr(a + lo + 1024));
    }
}
template <int C> DI void g2_step(const G2Ctx& x, u32x4 (&hf)[HS][4], f32x16 (&acc)[2][2], u32x4 (&wf)[WS_][4], u64* stamps) {
    asm volatile("s_waitcnt vmcnt(%4) lgkmcnt(0)" : "+v"(hf[C % HS][0]), "+v"(hf[C % HS][1]), "+v"(hf[C % HS][2]), "+v"(hf[C % HS][3]) : "i"(g2_younger(C)) : "memory");
    if constexpr (C % 3 == 0) ST(14 + C / 3);
    SB();
    constexpr bool DH = C + HD < 14, DW = C >= 1 && C + RD + 4 < 14;
    const char* hs = g2_hsrc<(DH ? C + HD : 0)>(x);
    const char* wsrc = g2_wsrc<(DW ? C + RD + 4 : 0)>(x);
#define G2M(i) acc[((i) >> 1) & 1][(i) & 1] = mfma32(wf[C % WS_][((i) >> 2) * 2 + (((i) >> 1) & 1)], hf[C % HS][((i) & 1) * 2 + ((i) >> 2)], acc[((i) >> 1) & 1][(i) & 1])
    G2M(0); g2_rd<C + RD, 0>(x, wf); if constexpr (DH) hf[(C + HD) % HS][0] = g2_h1<0>(x, hs); SB();
    G2M(1); g2_rd<C + RD, 1>(x, wf); if constexpr (DH) hf[(C + HD) % HS][1] = g2_h1<1>(x, hs); SB();
    G2M(2); g2_rd<C + RD, 2>(x, wf); if constexpr (DH) hf[(C + HD) % HS][2] = g2_h1<2>(x, hs); SB();
    G2M(3); g2_rd<C + RD, 3>(x, wf); if constexpr (DH) hf[(C + HD) % HS][3] = g2_h1<3>(x, hs); SB();
    G2M(4); if constexpr (DW) g2_w1<C + RD + 4, 0>(x, wsrc); SB();
    G2M(5); if constexpr (DW) g2_w1<C + RD + 4, 1>(x, wsrc); SB();
    G2M(6); if constexpr (DW) g2_w1<C + RD + 4, 2>(x, wsrc); SB();
    G2M(7); if constexpr (DW) g2_w1<C + RD + 4, 3>(x, wsrc); SB();
#undef G2M
    if constexpr (C + 1 < 14) g2_step<C + 1>(x, hf, acc, wf, stamps);
}

template <int NR> struct CF { u32x2 a0[NR], a1[NR]; u32x4 b[NR]; };
template <int S, int R, int NR> DI void cv_ld(CF<NR>& f, u32 aA, u32 aB) {
    if constexpr (R < NR && S < 8) {
        f.a0[R] = alds8<R * 512 + S * 64>(aA); f.a1[R] = alds8<R * 512 + S * 64 + 8>(aA);
        f.b[R] = alds16<(224 - 32 * R + 4 * S) * 16>(aB);
    }
}
template <int NR> DI void cv_wait(CF<NR>& f) {
    if constexpr (NR == 8)
        asm volatile("s_waitcnt lgkmcnt(0)" : "+v"(f.a0[0]), "+v"(f.a0[1]), "+v"(f.a0[2]), "+v"(f.a0[3]), "+v"(f.a0[4]), "+v"(f.a0[5]), "+v"(f.a0[6]), "+v"(f.a0[7]),
                     "+v"(f.a1[0]), "+v"(f.a1[1]), "+v"(f.a1[2]), "+v"(f.a1[3]), "+v"(f.a1[4]), "+v"(f.a1[5]), "+v"(f.a1[6]), "+v"(f.a1[7]),
                     "+v"(f.b[0]), "+v"(f.b[1]), "+v"(f.b[2]), "+v"(f.b[3]), "+v"(f.b[4]), "+v"(f.b[5]), "+v"(f.b[6]), "+v"(f.b[7]));
    else
        asm volatile("s_waitcnt lgkmcnt(0)" : "+v"(f.a0[0]), "+v"(f.a0[1]), "+v"(f.a0[2]), "+v"(f.a0[3]), "+v"(f.a0[4]), "+v"(f.a0[5]),
                     "+v"(f.a1[0]), "+v"(f.a1[1]), "+v"(f.a1[2]), "+v"(f.a1[3]), "+v"(f.a1[4]), "+v"(f.a1[5]),
                     "+v"(f.b[0]), "+v"(f.b[1]), "+v"(f.b[2]), "+v"(f.b[3]), "+v"(f.b[4]), "+v"(f.b[5]));
}
template <int HALF, int M> struct CvIdx {
    static constexpr int q(int i) { return HALF ? (i + 2) : (i < 2 ? i : i + 4); }
    static constexpr int tile() { int m = M; for (int r = 0; r < 8; ++r) for (int i = 3; i >= 0; --i) if (r <= q(i)) { if (m == 0) return i; --m; } return 0; }
    static constexpr int row() { int m = M; for (int r = 0; r < 8; ++r) for (int i = 3; i >= 0; --i) if (r <= q(i)) { if (m == 0) return r; --m; } return 0; }
};
template <int HALF, int S, int M, int NR> DI void cv_mm(CF<NR>& f, CF<NR>& n, u32 aA, u32 aB, f32x4 (&acc)[4]) {
    constexpr int i = CvIdx<HALF, M>::tile(), r = CvIdx<HALF, M>::row(), q = CvIdx<HALF, M>::q(i);
    acc[i] = mfma16(cat(f.a0[r], f.a1[r]), f.b[q - r], acc[i]);
    if constexpr ((M & 1) == 0) cv_ld<S + 1, M / 2, NR>(n, aA, aB);
    SB();
    if constexpr (M + 1 < 18) cv_mm<HALF, S, M + 1, NR>(f, n, aA, aB, acc);
}
template <int HALF, int S, int NR> DI void cv_step(CF<NR>& f, u32 aA, u32 aB, f32x4 (&acc)[4]) {
    cv_wait<NR>(f); SB();
    CF<NR> n;
    cv_mm<HALF, S, 0, NR>(f, n, aA, aB, acc);
    if constexpr (S + 1 < 8) cv_step<HALF, S + 1, NR>(n, aA, aB, acc);
}
template <int HALF> DI void conv_body(u32 aA, u32 aB, f32x4 (&acc)[4]) {
    constexpr int NR = HALF ? 6 : 8;
    CF<NR> f;
    asm volatile("s_waitcnt lgkmcnt(0)" ::: "memory");
    cv_ld<0, 0, NR>(f, aA, aB); cv_ld<0, 1, NR>(f, aA, aB); cv_ld<0, 2, NR>(f, aA, aB); cv_ld<0, 3, NR>(f, aA, aB);
    cv_ld<0, 4, NR>(f, aA, aB); cv_ld<0, 5, NR>(f, aA, aB); cv_ld<0, 6, NR>(f, aA, aB); cv_ld<0, 7, NR>(f, aA, aB);
    cv_step<HALF, 0, NR>(f, aA, aB, acc);
    asm volatile("" ::: "memory");
}

struct Geo { int bi, b, xcd, fh2, RB, CT, tb64, db, rot, crot; u32 R0, F0, T0, D0; };
DI Geo make_geo() {
    Geo g; g.b = blockIdx.x - NHELP; g.xcd = g.b & 7; const int bi = g.b >> 3; g.bi = bi;
    const int rq = g.xcd >> 1, rb = bi / 7, ct = bi - rb * 7;
    g.fh2 = g.xcd & 1; g.RB = 4 * rq + rb; g.CT = 7 * g.fh2 + ct; g.R0 = 128 * g.RB; g.F0 = 128 * g.CT;
    g.tb64 = 2 * g.RB + g.fh2; g.db = ct; g.T0 = 64 * g.tb64; g.D0 = 64 * g.db;
    g.rot = (ct + 2 * rb + rq * 3 + g.fh2) % 7; g.crot = (2 * ct + 7 * (rb & 1) + 3 * (rb >> 1) + rq + 5 * g.fh2) % 14;
    return g;
}
DI void make_g2(G2Ctx& g2, const Params& p, const Geo& q, int g, int lane, u32 lds0) {
    g2.w2b = (const char*)p.w2 + (size_t)(32 * g) * 896 + q.D0 * 2;
    g2.hb = p.ws + WS_H + (size_t)(q.tb64 * 2 * 112 + 2 * g) * 1024;
    g2.crot = q.crot;
#pragma unroll
    for (int n = 0; n < 4; ++n) g2.voffw[n] = (u32)((lane >> 3) * 896 + 16 * ((lane & 7) ^ (((lane >> 4) & 1) << 2)) + n * (8 * 896 - 1024));
    g2.ldsg = lds0 + g * 4096; g2.vlane = lane * 16u;
}
DI u32 slab_base(u32 lds0, int cw) { return lds0 + L_CONV + (cw >> 1) * CONV_CH + (cw & 1) * 8192; }
DI void final_epilogue(const Params& p, const Geo& q, u32 lds0) {
    int tz = threadIdx.x; PIN(tz);
    const int t = tz >> 3, c = tz & 7;
    const u32 pb = lds0 + ((t >> 5) ? 0u : 3 * L_PIECE) + ((t >> 4) & 1) * L_W + (t & 15) * 256;
    const u32 o0 = pb + 16 * ((2 * c) ^ (t & 7)), o1 = o0 ^ 16;
    f32x4 pa[4], pb_[4];
#pragma unroll
    for (int g = 0; g < 4; ++g) { pa[g] = __builtin_bit_cast(f32x4, lds16(o0 + g * 4096)); pb_[g] = __builtin_bit_cast(f32x4, lds16(o1 + g * 4096)); }
    const float r2 = *(const LAS float*)(lds0 + L_R2 + t * 4);
    const f32x4 b0 = __builtin_bit_cast(f32x4, lds16(lds0 + L_B2F + c * 32)), b1 = __builtin_bit_cast(f32x4, lds16(lds0 + L_B2F + c * 32 + 16));
    const u32 sb = slab_base(lds0, c >> 1) + t * 64 + (c & 1) * 32;
    const f32x4 sv0 = __builtin_bit_cast(f32x4, lds16(sb)), sv1 = __builtin_bit_cast(f32x4, lds16(sb + 16));
    const f32x4 xv0 = __builtin_bit_cast(f32x4, lds16(sb + 4096)), xv1 = __builtin_bit_cast(f32x4, lds16(sb + 4096 + 16));
    SB();
    const f32x4 s0 = (pa[0] + pa[1]) + (pa[2] + pa[3]), s1 = (pb_[0] + pb_[1]) + (pb_[2] + pb_[3]);
    f32x4 z0 = s0 * r2 + b0, z1 = s1 * r2 + b1;
    f32x4 e0 = z0 * -1.4426950408889634f, e1 = z1 * -1.4426950408889634f;
#pragma unroll
    for (int j = 0; j < 4; ++j) { e0[j] = __builtin_amdgcn_exp2f(e0[j]); e1[j] = __builtin_amdgcn_exp2f(e1[j]); }
    e0 += 1.f; e1 += 1.f;
#pragma unroll
    for (int j = 0; j < 4; ++j) { e0[j] = __builtin_amdgcn_rcpf(e0[j]); e1[j] = __builtin_amdgcn_rcpf(e1[j]); }
    z0 = z0 * e0; z1 = z1 * e1;
    z0 = z0 * sv0 + xv0; z1 = z1 * sv1 + xv1;
    const u32x4 res = {pk2(z0[0], z0[1]), pk2(z0[2], z0[3]), pk2(z1[0], z1[1]), pk2(z1[2], z1[3])};
    *(GAS u32x4*)((GAS char*)p.out + (size_t)(q.T0 + t) * 896 + q.D0 * 2 + c * 16) = res;
}
#define STAMP_DECL
#define STAMP_OUT(wave) do {} while (0)

extern __shared__ __attribute__((aligned(16))) char smem[];

struct CL { const char *xb, *wb; u32 vx[4], vw[4], sl; int rot; };
struct CSrc { const char *xs, *wsrc; };
template <int ST_> DI CSrc c_src(const CL& c) { int kc = ST_ + c.rot; if (kc >= 7) kc -= 7; return CSrc{c.xb + kc * 128, c.wb + (size_t)kc * 64 * 3584}; }
template <int ST_, int N> DI void c_dma1(const CL& c, const CSrc& s) {
    if constexpr (N == 0) setm0(c.sl + (ST_ & 3) * L_PIECE);
    if constexpr (N == 4) setm0(c.sl + (ST_ & 3) * L_PIECE + L_W);
    if constexpr (N < 4) dmai<N * 1024>(c.vx[N], s.xs); else dmai<(N - 4) * 1024>(c.vw[N - 4], s.wsrc);
}
template <int ST_> DI void c_dma(const CL& c) {
    const CSrc s = c_src<ST_>(c);
    c_dma1<ST_, 0>(c, s); c_dma1<ST_, 1>(c, s); c_dma1<ST_, 2>(c, s); c_dma1<ST_, 3>(c, s);
    c_dma1<ST_, 4>(c, s); c_dma1<ST_, 5>(c, s); c_dma1<ST_, 6>(c, s); c_dma1<ST_, 7>(c, s);
}
DI void make_cl(CL& c, const Params& p, const Geo& q, int cw, int lane, u32 lds0) {
    const int r8 = lane >> 3, c8 = lane & 7, r4 = lane >> 4, c16 = lane & 15;
#pragma unroll
    for (int n = 0; n < 4; ++n) {
        c.vx[n] = (u32)((32 * cw + r8) * 896 + 16 * (c8 ^ (((n & 1) << 2) | (r8 >> 1))) + n * (8 * 896 - 1024));
        c.vw[n] = (u32)((16 * cw + r4) * 3584 + 16 * (c16 ^ (r4 << 2)) + n * (4 * 3584 - 1024));
    }
    c.xb = (const char*)p.x + (size_t)q.R0 * 896; c.wb = (const char*)p.w1 + q.F0 * 2;
    c.sl = lds0 + cw * 4096; c.rot = q.rot;
}

template <int ST_> DI void l_dma(const CL& c) {
    const CSrc s = c_src<ST_>(c);
    const u32 sl = c.sl + (ST_ & 3) * L_PIECE;
    setm0(sl); dmai<0>(c.vx[0], s.xs); dmai<1024>(c.vx[1], s.xs); dmai<2048>(c.vx[2], s.xs); dmai<3072>(c.vx[3], s.xs);
    setm0(sl + L_W); dmai<0>(c.vw[0], s.wsrc); dmai<1024>(c.vw[1], s.wsrc); dmai<2048>(c.vw[2], s.wsrc); dmai<3072>(c.vw[3], s.wsrc);
    const char* x2 = s.xs + 32 * 896; const char* w2 = s.wsrc + 16 * 3584;
    setm0(sl + 4096); dmai<0>(c.vx[0], x2); dmai<1024>(c.vx[1], x2); dmai<2048>(c.vx[2], x2); dmai<3072>(c.vx[3], x2);
    setm0(sl + L_W + 4096); dmai<0>(c.vw[0], w2); dmai<1024>(c.vw[1], w2); dmai<2048>(c.vw[2], w2); dmai<3072>(c.vw[3], w2);
}

struct Fr { u32x4 x[4][2], w[4][2]; };
struct GA { u32 ax[4], aw[2]; };
template <int ST_> DI void g1_read(const GA& a, Fr& f) {
    constexpr u32 so = (ST_ & 3) * L_PIECE;
#pragma unroll
    for (int k = 0; k < 4; ++k) {
        f.w[k][0] = cat(ldtr(a.aw[0] + so + k * 4096), ldtr(a.aw[0] + so + k * 4096 + 1024));
        f.x[k][0] = lds16(a.ax[k] + so);
        f.w[k][1] = cat(ldtr(a.aw[1] + so + k * 4096), ldtr(a.aw[1] + so + k * 4096 + 1024));
        f.x[k][1] = lds16(a.ax[k] + so + 4096);
    }
}
template <int K0> DI void g1_mma(const Fr& f, f32x16 (&acc)[2][2]) {
#pragma unroll
    for (int k = K0; k < K0 + 2; ++k) {
        acc[0][0] = mfma32(f.w[k][0], f.x[k][0], acc[0][0]); acc[1][0] = mfma32(f.w[k][1], f.x[k][0], acc[1][0]);
        acc[0][1] = mfma32(f.w[k][0], f.x[k][1], acc[0][1]); acc[1][1] = mfma32(f.w[k][1], f.x[k][1], acc[1][1]);
    }
    SB();
}

template <int ST_, int I> DI void g1_rd(const GA& a, Fr& f) {
    constexpr u32 so = (ST_ & 3) * L_PIECE; constexpr int k = I >> 1, h = I & 1;
    f.w[k][h] = cat(ldtr(a.aw[h] + so + k * 4096), ldtr(a.aw[h] + so + k * 4096 + 1024));
    f.x[k][h] = lds16(a.ax[k] + so + h * 4096);
}
template <int K0, int I> DI void g1_m1(const Fr& f, f32x16 (&acc)[2][2]) {
    constexpr int k = K0 + (I >> 2), mt = I & 1, st = (I >> 1) & 1;
    acc[mt][st] = mfma32(f.w[k][mt], f.x[k][st], acc[mt][st]);
}
template <int ST_, int I = 0> DI void g1_carry(const GA& a, const Fr& fp, Fr& fn, f32x16 (&acc)[2][2]) {
    g1_m1<2, I>(fp, acc); g1_rd<ST_, I>(a, fn); SB();
    if constexpr (I + 1 < 8) g1_carry<ST_, I + 1>(a, fp, fn, acc);
}

DI void body_g(const Params& p, const int g) {
    STAMP_DECL
    const u32 lds0 = (u32)(size_t)(LAS char*)smem;
    const int lane = threadIdx.x & 63;
    const Geo q = make_geo();
    char* ws = p.ws;
    const int hi = lane >> 5, s31 = lane & 31;
    const int r8 = lane >> 3, c8 = lane & 7;
    const u32 raw = lds0 + L_LOC0 + g * KVRAW_SZ;
    {
        const u32 vkv = (u32)(r8 * 896 + c8 * 16);
        bdma16((u32)(((int)q.T0 + 16 * g - 8) * 896 + (int)q.D0 * 2) + vkv, mksrd(p.k, S_ * D_ * 2), raw);
        const char* kb = (const char*)p.k + (size_t)(q.T0 + 16 * g) * 896 + q.D0 * 2;
        const char* vb = (const char*)p.v + (size_t)(q.T0 + 16 * g) * 896 + q.D0 * 2;
        dmai<1024>(vkv, kb - 1024); dmai<2048>(vkv, kb + 8 * 896 - 2048); dmai<3072>(vkv, vb - 3072);
        setm0(raw + 4096);
        dmai<0>(vkv, vb + 8 * 896);
        if (lane < 32) dmai<1024>(lane * 16u, (const char*)p.shk + q.D0 * 8 - 1024);
        CL c; make_cl(c, p, q, g, lane, lds0);
        c_dma<0>(c);
    }
    u32x2 ep = gld8c(0u, ws + WS_EPOCH);
    const int fh = g & 1, sh = g >> 1;
    GA ga;
    {
        const int blk = (lane >> 4) & 1, qq = (lane & 15) >> 2, pp = lane & 3;
        const u32 Z = (u32)(hi ^ ((s31 >> 1) & 7));
#pragma unroll
        for (int k = 0; k < 4; ++k) ga.ax[k] = lds0 + (64 * sh + s31) * 128 + 16 * ((2 * k) ^ Z);
        ga.aw[0] = lds0 + L_W + (8 * hi + qq) * 256 + 16 * ((8 * fh + 2 * blk + (pp >> 1)) ^ (qq << 2)) + 8 * (pp & 1);
        ga.aw[1] = ga.aw[0] ^ 64;
    }
    WAITV(9);
    ST(1);
    {
        u32x4 wr[4], kr[5], vr[2];
#pragma unroll
        for (int e = 0; e < 4; ++e) wr[e] = lds16(raw + 5120 + c8 * 64 + e * 16);
#pragma unroll
        for (int j = 0; j < 5; ++j) kr[j] = lds16(raw + (5 + 2 * r8 + j) * 128 + c8 * 16);
        vr[0] = lds16(raw + 3072 + 2 * r8 * 128 + c8 * 16); vr[1] = lds16(raw + 3072 + (2 * r8 + 1) * 128 + c8 * 16);
        SB();
        u32x4 o0, o1;
#pragma unroll
        for (int e = 0; e < 4; ++e) {
            const u32x4 w = wr[e];
            const f32x2 t0 = {bflo(w.x), bflo(w.z)}, t1 = {bfhi(w.x), bfhi(w.z)}, t2 = {bflo(w.y), bflo(w.w)}, t3 = {bfhi(w.y), bfhi(w.w)};
            f32x2 k[5];
#pragma unroll
            for (int j = 0; j < 5; ++j) k[j] = f32x2{bflo(kr[j][e]), bfhi(kr[j][e])};
            f32x2 a = k[0] * t3, b = k[1] * t3;
            a = __builtin_elementwise_fma(k[1], t2, a); b = __builtin_elementwise_fma(k[2], t2, b);
            a = __builtin_elementwise_fma(k[2], t1, a); b = __builtin_elementwise_fma(k[3], t1, b);
            a = __builtin_elementwise_fma(k[3], t0, a); b = __builtin_elementwise_fma(k[4], t0, b);
            a *= f32x2{bflo(vr[0][e]), bfhi(vr[0][e])}; b *= f32x2{bflo(vr[1][e]), bfhi(vr[1][e])};
            o0[e] = pk2(a.x, a.y); o1[e] = pk2(b.x, b.y);
        }
        sts16(lds0 + L_KVS + (16 * g + 2 * r8) * 144 + c8 * 16, o0);
        sts16(lds0 + L_KVS + (16 * g + 2 * r8 + 1) * 144 + c8 * 16, o1);
        asm volatile("s_waitcnt lgkmcnt(0)" ::: "memory");
        lds_add(lds0 + F_KVS);
    }
    f32x16 acc[2][2] = {};
    Fr fa, fb;
    WAITV(1);
    lds_add(lds0 + F_LAND);
    lds_wait_ge(lds0 + F_LAND, 256, lds_peek(lds0 + F_LAND));
    ST(2);
    u32 pk;
#define G1_STAGE(T, FP, FN) do { lds_wait_ge(lds0 + F_LAND + 4 * (T), 128, pk); g1_carry<T>(ga, FP, FN, acc); pk = lds_peek(lds0 + F_LAND + 4 * (T) + 4); SB(); \
        g1_mma<0>(FN, acc); asm volatile("s_waitcnt lgkmcnt(0)" ::: "memory"); lds_add(lds0 + F_CONS + 4 * (T)); SB(); } while (0)
    g1_read<0>(ga, fa); pk = lds_peek(lds0 + F_LAND + 4); SB();
    g1_mma<0>(fa, acc); asm volatile("s_waitcnt lgkmcnt(0)" ::: "memory"); lds_add(lds0 + F_CONS); SB();
    ST(19);
    G1_STAGE(1, fa, fb);
    ST(20);
    G1_STAGE(2, fb, fa);
    asm volatile("s_waitcnt vmcnt(0)" : "+v"(ep) :: "memory");
    ST(3);
    const u64 epoch = ((u64)(u32)__builtin_amdgcn_readfirstlane(ep.y) << 32) | (u32)__builtin_amdgcn_readfirstlane(ep.x);
    const u64 tag = epoch + TAG_SALT;
    const u32 tlo = (u32)tag, thi = (u32)(tag >> 32);
    G1_STAGE(3, fa, fb);
    G1_STAGE(4, fb, fa);
    ST(21);
    const u32 rgo = WS_RG + (q.R0 + 64 * sh + lane) * 8, cgo = WS_CG + (q.F0 + 64 * fh + lane) * 8;
    u32x2 rg = gld8c(rgo, ws), cg = gld8c(cgo, ws);
    u32 b1v = gld2((u32)((64 * fh + lane) * 2), (const char*)p.b1 + q.F0 * 2);
    G1_STAGE(5, fa, fb);
    ST(4);
    G1_STAGE(6, fb, fa);
    ST(5);
    asm volatile("s_waitcnt vmcnt(0)" : "+v"(rg), "+v"(cg), "+v"(b1v) :: "memory");
    _Pragma("clang loop unroll(disable)") for (int spin = 0; spin < SPIN_CAP && !__all(rg.y == tlo && cg.y == tlo); ++spin) {
        rg = gld8c(rgo, ws); cg = gld8c(cgo, ws);
        asm volatile("s_waitcnt vmcnt(0)" : "+v"(rg), "+v"(cg) :: "memory");
    }
    if (fh == 0 && sh == q.fh2) *(LAS u32*)(lds0 + L_ROWF + lane * 4) = rg.x;
    g1_mma<2>(fa, acc);
    {
        u32x4 ax[2], bx[2];
        const auto rr = __builtin_amdgcn_permlane32_swap(rg.x, rg.x, false, false);
        const auto cc = __builtin_amdgcn_permlane32_swap(cg.x, cg.x, false, false);
        const auto bb = __builtin_amdgcn_permlane32_swap(b1v, b1v, false, false);
#pragma unroll
        for (int i = 0; i < 2; ++i) {
            const u32 nm = __builtin_amdgcn_perm(rr[i], rr[i], 0x01000100u) ^ 0x80008000u;
            ax[i] = u32x4{cc[i], bb[i], 0u, 0u};
            bx[i] = u32x4{hi ? 0u : nm, hi ? 0u : (rr[i] >> 16), 0u, 0u};
        }
#pragma unroll
        for (int st = 0; st < 2; ++st)
#pragma unroll
            for (int mt = 0; mt < 2; ++mt) acc[mt][st] = mfma32(ax[mt], bx[st], acc[mt][st]);
#pragma unroll
        for (int st = 0; st < 2; ++st) {
            char* hb = ws + WS_H + (size_t)((4 * q.RB + 2 * sh + st) * 112 + q.CT * 8 + fh * 4) * 1024;
#pragma unroll
            for (int mt = 0; mt < 2; ++mt)
#pragma unroll
                for (int t = 0; t < 2; ++t) {
                    u32x4 hv;
#pragma unroll
                    for (int j = 0; j < 4; ++j) {
                        f32x2 a = {acc[mt][st][8 * t + 2 * j], acc[mt][st][8 * t + 2 * j + 1]};
                        f32x2 u = {relu(a.x), relu(a.y)};
                        u = u * u;
                        hv[j] = pk2(u.x, u.y);
                    }
                    if (mt == 0 && t == 0) gst16c<0>(lane * 16u, hb, hv);
                    if (mt == 0 && t == 1) gst16c<1024>(lane * 16u, hb, hv);
                    if (mt == 1 && t == 0) gst16c<2048>(lane * 16u, hb, hv);
                    if (mt == 1 && t == 1) gst16c<3072>(lane * 16u, hb, hv);
                    SB();
                }
        }
    }
    ST(6);
    G2Ctx g2; make_g2(g2, p, q, g, lane, lds0);
    {
        const int blk = (lane >> 4) & 1, qq = (lane & 15) >> 2, pp = lane & 3;
        const u32 t0 = g2.ldsg + (4 * hi + qq) * 128 + 16 * ((2 * blk + (pp >> 1)) ^ ((qq >> 1) << 2)) + 8 * (pp & 1);
#pragma unroll
        for (int i = 0; i < 3; ++i) { g2.ta[i][0] = t0 + i * 65536; g2.ta[i][1] = (t0 ^ 64) + i * 65536; PIN(g2.ta[i][0]); PIN(g2.ta[i][1]); }
    }
    u32x4 wf[WS_][4];
    lds_wait_ge(lds0 + F_CONS + 16, 256, lds_peek(lds0 + F_CONS + 16));
    lds_wait(lds0 + F_W + (g >> 1) * 4);
    g2_read<0>(g2, wf); g2_read<1>(g2, wf);
    asm volatile("s_waitcnt vmcnt(0) lgkmcnt(0)" ::: "memory"); ST(9);
    if (lds_arrive_last(lds0 + F_HCNT, 4)) { if (lane < 15) gst8c<0>(lane < 14 ? WS_HF + (q.RB * 14 + lane) * MB + q.CT * 8 : WS_ALL + q.b * 8, ws, u32x2{tlo, thi}); }
    {
        const u32 po = WS_HF + (q.RB * 14 + q.CT) * MB + (lane < 14 ? lane : 0) * 8;
        g2_issue_w<2>(g2); g2_issue_w<3>(g2);
        u32x2 f = {0u, 0u};
        if (g == 0) f = gld8c(po, ws);
        g2_issue_w<4>(g2); g2_issue_w<5>(g2); g2_issue_w<6>(g2);
        asm volatile("s_waitcnt vmcnt(12)" : "+v"(f) :: "memory");
        bool rdy = __all(f.x == tlo && f.y == thi);
        if (g == 0 && rdy) lds_set(lds0 + F_HRDY);
        g2_read<2>(g2, wf); g2_read<3>(g2, wf);
        asm volatile("s_waitcnt lgkmcnt(0)" ::: "memory");
        if (g == 0 && !rdy) f = gld8c(po, ws);
        g2_issue_w<7>(g2); g2_issue_w<8>(g2);
        if (g == 0) {
            if (!rdy) {
                asm volatile("s_waitcnt vmcnt(8)" : "+v"(f) :: "memory");
                if (!__all(f.x == tlo && f.y == thi)) poll8(po, ws, true, tlo, thi);
                lds_set(lds0 + F_HRDY);
            }
        } else lds_wait(lds0 + F_HRDY);
    }
    ST(7);
    {
        u32x4 hf[HS][4];
        g2_issue_h<0>(g2, hf); g2_issue_h<1>(g2, hf); g2_issue_h<2>(g2, hf);
        f32x16 a2[2][2] = {};
        g2_step<0>(g2, hf, a2, wf, STP);
        ST(8);
        const u32 pw = g2.ldsg + (s31 >> 4) * L_W + (s31 & 15) * 256;
#pragma unroll
        for (int s2 = 0; s2 < 2; ++s2)
#pragma unroll
            for (int dt = 0; dt < 2; ++dt)
#pragma unroll
                for (int gr = 0; gr < 4; ++gr) {
                    const u32 a = pw + (s2 ? 0u : 3 * L_PIECE) + 16 * ((8 * dt + 2 * gr + hi) ^ (s31 & 7));
                    sts16(a, __builtin_bit_cast(u32x4, f32x4{a2[dt][s2][4 * gr], a2[dt][s2][4 * gr + 1], a2[dt][s2][4 * gr + 2], a2[dt][s2][4 * gr + 3]}));
                }
    }
    BARL(); ST(10);
    final_epilogue(p, q, lds0);
    STAMP_OUT(g);
}

DI void body_c(const Params& p, const int cw) {
    STAMP_DECL
    const u32 lds0 = (u32)(size_t)(LAS char*)smem;
    const int lane = threadIdx.x & 63;
    const Geo q = make_geo();
    char* ws = p.ws;
    const int hi = lane >> 5, s31 = lane & 31;
    const int half = cw & 1, chl = cw >> 1, chan = 2 * q.b + chl;
    u32x2 ep;
    if (cw < 2) {
        CL c; make_cl(c, p, q, 2 * cw, lane, lds0);
        l_dma<1>(c); l_dma<2>(c);
        WAITV(16); lds_add(lds0 + F_LAND + 4);
        l_dma<3>(c);
        WAITV(16); lds_add(lds0 + F_LAND + 8);
        lds_wait_ge(lds0 + F_CONS, 256, lds_peek(lds0 + F_CONS)); l_dma<4>(c);
        WAITV(16); lds_add(lds0 + F_LAND + 12);
        lds_wait_ge(lds0 + F_CONS + 4, 256, lds_peek(lds0 + F_CONS + 4)); l_dma<5>(c);
        WAITV(16); lds_add(lds0 + F_LAND + 16);
        lds_wait_ge(lds0 + F_CONS + 8, 256, lds_peek(lds0 + F_CONS + 8)); l_dma<6>(c);
        ep = gld8c(0u, ws + WS_EPOCH);
        WAITV(17); lds_add(lds0 + F_LAND + 20);
        lds_wait_ge(lds0 + F_CONS + 12, 256, lds_peek(lds0 + F_CONS + 12));
#pragma unroll
        for (int i = 0; i < 2; ++i) { G2Ctx g2; make_g2(g2, p, q, 2 * cw + i, lane, lds0); g2_issue_w<0>(g2); g2_issue_w<1>(g2); }
        WAITV(17); lds_add(lds0 + F_LAND + 24);
        asm volatile("s_waitcnt vmcnt(0)" : "+v"(ep) :: "memory");
        lds_set(lds0 + F_W + cw * 4);
    } else {
        const int kw = cw - 2;
        if (kw == 0 && lane < 8) dma16(lane * 16u, (const char*)p.b2 + q.D0 * 2, lds0 + L_B2);
        ep = gld8c(0u, ws + WS_EPOCH);
        {
            u32x2 d0 = gld8c(kw ? WS_YF + (q.tb64 * 7 + q.db) * MB + s31 * 8 : WS_KVF + q.b * MB + s31 * 8, ws);
            u32x2 d1 = gld8c(WS_HF + (q.RB * 14 + q.CT) * MB, ws);
            asm volatile("s_waitcnt vmcnt(0)" : "+v"(ep), "+v"(d0), "+v"(d1) :: "memory");
        }
        lds_wait_ge(lds0 + F_KVS, 256, lds_peek(lds0 + F_KVS));
        ST(2);
        {
            const int grp = lane >> 4, qq = (lane & 15) >> 2, pp = lane & 3;
            const u32 a = lds0 + L_KVS + (16 * grp + 8 * (pp >> 1) + 7 - qq) * 144 + kw * 64 + (pp & 1) * 8;
            const u32 vo = (u32)((q.D0 + 32 * kw + (lane & 7)) * TP + (2040 - (int)q.T0 - 8 * (lane >> 3)) * 2);
            u32x4 tr[4];
#pragma unroll
            for (int i = 0; i < 4; ++i) tr[i] = cat(ldtr(a + 16 * i), ldtr(a - 4 * 144 + 16 * i));
            SB();
#pragma unroll
            for (int i = 0; i < 4; ++i) gst16c<0>(vo, ws + WS_KVT + i * 8 * TP, tr[i]);
        }
        ST(20);
        u32x4 sk[10], uu[4];
        const u32x4 sksrd = mksrd((const char*)p.ssmk + (size_t)(2 * q.b + kw) * 4096, 4096);
#pragma unroll
        for (int r = 0; r < 5; ++r) { sk[2 * r] = bld16((u32)((lane - 2 + 64 * r) * 16), sksrd); sk[2 * r + 1] = bld16((u32)((lane - 1 + 64 * r) * 16), sksrd); }
        ST(3);
        WAITV(10);
        ST(19);
        const u64 tg = (((u64)ep.y << 32) | ep.x) + TAG_SALT;
        if (lds_arrive_last(lds0 + F_KVCNT, 2)) {
            if (lane < 32) gst8c<0>(WS_KVF + (q.db * 32 + lane) * MB + q.tb64 * 8, ws, u32x2{(u32)tg, (u32)(tg >> 32)});
        }
        asm volatile("s_waitcnt vmcnt(0)" : "+v"(sk[0]), "+v"(sk[1]), "+v"(sk[2]), "+v"(sk[3]), "+v"(sk[4]), "+v"(sk[5]), "+v"(sk[6]), "+v"(sk[7]), "+v"(sk[8]), "+v"(sk[9]) :: "memory");
        u32x4 tw[5][4];
#pragma unroll
        for (int r = 0; r < 5; ++r) {
            const u32x4 A = sk[2 * r], B = sk[2 * r + 1];
            const u32 e0 = __builtin_amdgcn_alignbit(A.y, A.x, 16), e1 = __builtin_amdgcn_alignbit(A.z, A.y, 16), e2 = __builtin_amdgcn_alignbit(A.w, A.z, 16),
                      e3 = __builtin_amdgcn_alignbit(B.x, A.w, 16), e4 = __builtin_amdgcn_alignbit(B.y, B.x, 16);
            tw[r][0] = u32x4{e0, e1, e2, e3}; tw[r][1] = u32x4{A.y, A.z, A.w, B.x}; tw[r][2] = u32x4{e1, e2, e3, e4}; tw[r][3] = u32x4{A.z, A.w, B.x, B.y};
            PIN(tw[r][0]); PIN(tw[r][1]); PIN(tw[r][2]); PIN(tw[r][3]);
        }
        if (kw == 0) { poll8(WS_KVF + q.b * MB + s31 * 8, ws, true, (u32)tg, (u32)(tg >> 32)); lds_set(lds0 + F_KVRDY); } else lds_wait(lds0 + F_KVRDY);
        ST(7);
        const char* kb2 = ws + WS_KVT + (size_t)(2 * q.b + kw) * TP;
        uu[0] = gld16c<0>(lane * 16u, kb2); uu[1] = gld16c<1024>(lane * 16u, kb2); uu[2] = gld16c<2048>(lane * 16u, kb2); uu[3] = gld16c<3072>(lane * 16u, kb2);
        lds_wait_ge(lds0 + F_CONS + 20, 256, lds_peek(lds0 + F_CONS + 20)); lds_wait_ge(lds0 + F_CONS + 24, 256, lds_peek(lds0 + F_CONS + 24));
        ST(5);
        const u32 tq = lds0 + L_CONV + kw * CONV_CH + lane * 16;
#pragma unroll
        for (int r = 0; r < 4; ++r)
#pragma unroll
            for (int c = 0; c < 4; ++c) sts16(tq + r * 1024 + c * TOEP_CP, tw[r][c]);
        if (lane < 2) {
#pragma unroll
            for (int c = 0; c < 4; ++c) sts16(tq + 4096 + c * TOEP_CP, tw[4][c]);
        }
        if (lane < 32) sts16(tq + TOEP_CH + 4096, u32x4{0u, 0u, 0u, 0u});
        asm volatile("s_waitcnt vmcnt(0)" : "+v"(uu[0]), "+v"(uu[1]), "+v"(uu[2]), "+v"(uu[3]) :: "memory");
        ST(14);
        sts16(tq + TOEP_CH, uu[0]); sts16(tq + TOEP_CH + 1024, uu[1]); sts16(tq + TOEP_CH + 2048, uu[2]); sts16(tq + TOEP_CH + 3072, uu[3]);
        asm volatile("s_waitcnt lgkmcnt(0)" ::: "memory");
        lds_set(lds0 + F_TOEP + kw * 4);
    }
    u32x4 xa = gld16((u32)(lane * 896), (const char*)p.x + (size_t)q.T0 * 896 + (q.D0 + 16 * cw) * 2);
    u32x4 xb_ = gld16((u32)(lane * 896 + 16), (const char*)p.x + (size_t)q.T0 * 896 + (q.D0 + 16 * cw) * 2);
    u32 dv = gld2(0u, (const char*)p.ssmd + chan * 2);
    const u64 epoch = ((u64)(u32)__builtin_amdgcn_readfirstlane(ep.y) << 32) | (u32)__builtin_amdgcn_readfirstlane(ep.x);
    const u64 tag = epoch + TAG_SALT;
    const u32 tlo = (u32)tag, thi = (u32)(tag >> 32);
    const u32 tbase = lds0 + L_CONV + chl * CONV_CH;
    const u32 ub = tbase + TOEP_CH;
    ST(6);
    lds_wait(lds0 + F_TOEP + chl * 4);
    ST(15);
    const int bb = lane & 15, gg = lane >> 4;
    const u32 aA = tbase + (bb & 3) * TOEP_CP + ((bb >> 2) + 2 * gg) * 8;
    const u32 aB = ub + (30 - 2 * bb + gg) * 16;
    f32x4 acc[4] = {};
    if (half == 0) conv_body<0>(aA, aB, acc); else conv_body<1>(aA, aB, acc);
    ST(16);
    asm volatile("s_waitcnt vmcnt(0)" : "+v"(xa), "+v"(xb_), "+v"(dv) :: "memory");
    ST(8);
    {
        const float dd = bflo(dv);
        char* yb = ws + WS_YT + (size_t)chan * TP;
        u32x2 uq[4];
#pragma unroll
        for (int i = 0; i < 4; ++i) uq[i] = lds8(ub + (2044 - (256 * (half ? (i + 2) : (i < 2 ? i : i + 4)) + 16 * bb + 4 * gg)) * 2);
        SB();
#pragma unroll
        for (int i = 0; i < 4; ++i) {
            const int qt = half ? (i + 2) : (i < 2 ? i : i + 4);
            const int t = 256 * qt + 16 * bb + 4 * gg;
            const u32x2 uu = uq[i];
            const u32 c01 = pk2(acc[i][0], acc[i][1]), c23 = pk2(acc[i][2], acc[i][3]);
            float y0 = fma1(bfhi(uu.y), dd, bflo(c01)), y1 = fma1(bflo(uu.y), dd, bfhi(c01));
            float y2 = fma1(bfhi(uu.x), dd, bflo(c23)), y3 = fma1(bflo(uu.x), dd, bfhi(c23));
            gst8c<0>((u32)(t * 2), yb, u32x2{pk2(y0, y1), pk2(y2, y3)});
        }
        WAITV(0);
        if (lds_arrive_last(lds0 + F_YCNT, 4)) { if (lane < 32) gst8c<0>(WS_YF + (lane * 7 + (q.b >> 5)) * MB + (q.b & 31) * 8, ws, u32x2{tlo, thi}); }
    }
    _Pragma("clang loop unroll(disable)") for (int spin = 0; spin < (1 << 22); ++spin) { if (__builtin_amdgcn_readfirstlane(*(volatile LAS u32*)(lds0 + F_YCNT)) >= 4) break; __builtin_amdgcn_s_sleep(1); }
    asm volatile("" ::: "memory");
    const u32 slab = slab_base(lds0, cw) + lane * 64;
    f32x4 xn[4];
    {
        _Pragma("clang loop unroll(disable)") for (int spin = 0; spin < (1 << 22); ++spin) { if (__builtin_amdgcn_readfirstlane(*(volatile LAS u32*)(lds0 + F_HCNT)) >= 4) break; __builtin_amdgcn_s_sleep(1); }
        asm volatile("" ::: "memory");
        const u32 rf = *(const LAS u32*)(lds0 + L_ROWF + lane * 4);
        const float m = bflo(rf), ri = __builtin_amdgcn_rcpf(bfhi(rf)), nmri = -m * ri;
        const u32 xx[8] = {xa.x, xa.y, xa.z, xa.w, xb_.x, xb_.y, xb_.z, xb_.w};
#pragma unroll
        for (int j = 0; j < 4; ++j) {
            f32x4 v = {bflo(xx[2 * j]), bfhi(xx[2 * j]), bflo(xx[2 * j + 1]), bfhi(xx[2 * j + 1])};
            sts16(slab + 4096 + j * 16, __builtin_bit_cast(u32x4, v));
            xn[j] = f32x4{fma1(v[0], ri, nmri), fma1(v[1], ri, nmri), fma1(v[2], ri, nmri), fma1(v[3], ri, nmri)};
        }
        if (cw == 0) *(LAS float*)(lds0 + L_R2 + lane * 4) = ri * ri;
        if (cw == 1) *(LAS float*)(lds0 + L_B2F + lane * 4) = bflo(*(const LAS u16*)(lds0 + L_B2 + lane * 2));
    }
    if (q.b == 0 && cw == 3) {
        _Pragma("clang loop unroll(disable)") for (int spin = 0; spin < SPIN_CAP; ++spin) {
            u32x4 f0 = gld16c<0>(lane * 16u, ws + WS_ALL), f1 = gld16c<1024>((lane < 48 ? lane : 0) * 16u, ws + WS_ALL);
            asm volatile("s_waitcnt vmcnt(0)" : "+v"(f0), "+v"(f1) :: "memory");
            bool ok = f0.x == tlo && f0.y == thi && f0.z == tlo && f0.w == thi && f1.x == tlo && f1.y == thi && f1.z == tlo && f1.w == thi;
            if (__all(ok)) break;
            __builtin_amdgcn_s_sleep(SLP);
        }
        if (lane == 0) { const u64 e1 = epoch + 1; gst8c<0>(0u, ws + WS_EPOCH, u32x2{(u32)e1, (u32)(e1 >> 32)}); }
    }
    if (cw == 0) { poll8(WS_YF + (q.tb64 * 7 + q.db) * MB + s31 * 8, ws, true, tlo, thi); lds_set(lds0 + F_YRDY); }
    else lds_wait(lds0 + F_YRDY);
    ST(9);
    {
        const char* yb = ws + WS_YT + (size_t)(q.D0 + 16 * cw) * TP + q.T0 * 2;
        const u32 vo = (u32)((lane >> 3) * TP + (lane & 7) * 16);
        u32x4 y0 = gld16c<0>(vo, yb), y1 = gld16c<0>(vo, yb + 8 * TP);
        asm volatile("s_waitcnt vmcnt(0)" : "+v"(y0), "+v"(y1) :: "memory");
        const u32 ys = tbase + TOEP_CH + 2 * U_SZ + half * YS_SZ;
        sts16(ys + (lane >> 3) * 144 + (lane & 7) * 16, y0);
        sts16(ys + (8 + (lane >> 3)) * 144 + (lane & 7) * 16, y1);
        const int qq = (lane & 15) >> 2, pp = lane & 3;
        const u32 a = ys + qq * 144 + gg * 32 + pp * 8;
        u32x2 t0 = ldtr(a), t1 = ldtr(a + 4 * 144), t2 = ldtr(a + 8 * 144), t3 = ldtr(a + 12 * 144);
        const u32 yy[8] = {t0.x, t0.y, t1.x, t1.y, t2.x, t2.y, t3.x, t3.y};
#pragma unroll
        for (int j = 0; j < 4; ++j) {
            f32x4 v = {bflo(yy[2 * j]), bfhi(yy[2 * j]), bflo(yy[2 * j + 1]), bfhi(yy[2 * j + 1])};
            sts16(slab + ((j ^ (lane & 3)) * 0 + j) * 16, __builtin_bit_cast(u32x4, f32x4{mul1(v[0], xn[j][0]), mul1(v[1], xn[j][1]), mul1(v[2], xn[j][2]), mul1(v[3], xn[j][3])}));
        }
    }
    BARL(); ST(10);
    final_epilogue(p, q, lds0);
    STAMP_OUT(cw + 4);
}

DI float dpp_xor_sum8(float v) {
    v += __uint_as_float(__builtin_amdgcn_mov_dpp(__float_as_uint(v), 0xB1, 0xf, 0xf, true));
    v += __uint_as_float(__builtin_amdgcn_mov_dpp(__float_as_uint(v), 0x4E, 0xf, 0xf, true));
    v += __uint_as_float(__builtin_amdgcn_mov_dpp(__float_as_uint(v), 0x141, 0xf, 0xf, true));
    return v;
}
DI void body_help(const Params& p, const int wave) {
    const u32 lds0 = (u32)(size_t)(LAS char*)smem;
    const int lane = threadIdx.x & 63, r8 = lane >> 3, c8 = lane & 7;
    const int xcd = blockIdx.x & 7, j = blockIdx.x >> 3;
    char* ws = p.ws;
    u32x2 ep = gld8c(0u, ws + WS_EPOCH);
    u32x4 d[14];
    const int ctile = (xcd >> 1) * 4 + j;
    const bool cols = wave >= 4;
    if (!cols) {
        const char* xb = (const char*)p.x + (size_t)(256 * xcd + 64 * j + 16 * wave) * 896;
#pragma unroll
        for (int i = 0; i < 14; ++i) d[i] = gld16((u32)((r8 + 8 * (i / 7)) * 896 + (i % 7) * 128 + c8 * 16), xb);
    } else {
        const char* wb = (const char*)p.w1 + (size_t)(112 * (wave - 4)) * 3584 + ((xcd & 1) * 14 + (ctile < 14 ? ctile : 0)) * 128;
#pragma unroll
        for (int i = 0; i < 14; ++i) d[i] = gld16((u32)((r8 + 8 * i) * 3584 + c8 * 16), wb);
    }
    asm volatile("s_waitcnt vmcnt(0)" : "+v"(ep), "+v"(d[0]), "+v"(d[1]), "+v"(d[2]), "+v"(d[3]), "+v"(d[4]), "+v"(d[5]), "+v"(d[6]), "+v"(d[7]), "+v"(d[8]), "+v"(d[9]),
                 "+v"(d[10]), "+v"(d[11]), "+v"(d[12]), "+v"(d[13]) :: "memory");
    const u32 tlo = (u32)((((u64)ep.y << 32) | ep.x) + TAG_SALT);
    if (!cols) {
#pragma unroll
        for (int h = 0; h < 2; ++h) {
            float s0 = 0.f, s1 = 0.f, q0 = 0.f, q1 = 0.f;
#pragma unroll
            for (int i = 0; i < 7; ++i)
#pragma unroll
                for (int e = 0; e < 4; e += 2) {
                    s0 = dot2(d[7 * h + i][e], 0x3f803f80u, s0); q0 = dot2(d[7 * h + i][e], d[7 * h + i][e], q0);
                    s1 = dot2(d[7 * h + i][e + 1], 0x3f803f80u, s1); q1 = dot2(d[7 * h + i][e + 1], d[7 * h + i][e + 1], q1);
                }
            const float mean = dpp_xor_sum8(s0 + s1) * (1.f / 448.f);
            const float var = fmaxf(dpp_xor_sum8(q0 + q1) * (1.f / 448.f) - mean * mean, 0.f);
            const float sd = __builtin_amdgcn_sqrtf(rbf(rbf(var) + 1e-5f));
            if (c8 == 0) gst8c<0>((u32)(WS_RG + (256 * xcd + 64 * j + 16 * wave + 8 * h + r8) * 8), ws, u32x2{pk2(mean, sd), tlo});
        }
    } else {
        float a[8] = {};
        u32 klo = 0x00003f80u, khi = 0x3f800000u; PIN(klo); PIN(khi);
#pragma unroll
        for (int i = 0; i < 14; ++i)
#pragma unroll
            for (int e = 0; e < 4; ++e) { a[2 * e] = dot2(d[i][e], klo, a[2 * e]); a[2 * e + 1] = dot2(d[i][e], khi, a[2 * e + 1]); }
        const u32 o = lds0 + ((wave - 4) * 8 + r8) * 272 + c8 * 32;
        sts16(o, __builtin_bit_cast(u32x4, f32x4{a[0], a[1], a[2], a[3]})); sts16(o + 16, __builtin_bit_cast(u32x4, f32x4{a[4], a[5], a[6], a[7]}));
    }
    BARL();
    if (wave == 4 && ctile < 14) {
        float t0 = 0.f, t1 = 0.f;
#pragma unroll
        for (int i = 0; i < 32; i += 2) { t0 += *(const LAS float*)(lds0 + i * 272 + lane * 4); t1 += *(const LAS float*)(lds0 + (i + 1) * 272 + lane * 4); }
        gst8c<0>((u32)(WS_CG + (((xcd & 1) * 14 + ctile) * 64 + lane) * 8), ws, u32x2{pk2(t0 + t1, (t0 + t1) - rbf(t0 + t1)), tlo});
    }
    {
        const int crot = (2 * xcd) % 14;
        setm0(lds0 + 16384 + wave * 4096);
        for (int c = 1; c < 14; ++c) {
            int cc = c + crot; if (cc >= 14) cc -= 14;
            const char* sb = (const char*)p.w2 + (size_t)cc * 114688 + (j * 8 + wave) * 3584;
            if (lane < 32) dmai<3072>(lane * 16u, sb);
            dmai<0>(lane * 16u, sb); dmai<1024>(lane * 16u, sb); dmai<2048>(lane * 16u, sb);
            WAITV(8);
        }
        WAITV(0);
    }
}

__global__ __launch_bounds__(512, 2) void fused_kernel(Params p) {
    const int wave = __builtin_amdgcn_readfirstlane(threadIdx.x >> 6);
    if (blockIdx.x < NHELP) { body_help(p, wave); return; }
    if (threadIdx.x < 32) *(LAS u32*)((u32)(size_t)(LAS char*)smem + L_FLAG + threadIdx.x * 4) = 0u;
    BARL();
    if (wave < 4) body_g(p, wave); else body_c(p, wave - 4);
}

extern "C" void kernel_launch(void* const* d_in, const int* in_sizes, int n_in,
                              void* d_out, int out_size, void* d_ws, size_t ws_size,
                              hipStream_t stream) {
    Params p;
    p.x = (const u16*)d_in[0]; p.k = (const u16*)d_in[1]; p.v = (const u16*)d_in[2]; p.shk = (const u16*)d_in[3];
    p.ssmk = (const u16*)d_in[4]; p.ssmd = (const u16*)d_in[5]; p.w1 = (const u16*)d_in[7]; p.b1 = (const u16*)d_in[8];
    p.w2 = (const u16*)d_in[9]; p.b2 = (const u16*)d_in[10];
    p.out = (u16*)d_out; p.ws = (char*)d_ws;
    (void)hipFuncSetAttribute((const void*)fused_kernel, hipFuncAttributeMaxDynamicSharedMemorySize, LDS_BYTES);
    fused_kernel<<<NWG + NHELP, 512, LDS_BYTES, stream>>>(p);
}
```

```cpp
#define NOWARM
#include <hip/hip_runtime.h>
#include <stdint.h>
#include <type_traits>

typedef unsigned short u16;
typedef unsigned int u32;
typedef unsigned long long u64;
typedef u32 u32x4 __attribute__((ext_vector_type(4)));
typedef u32 u32x2 __attribute__((ext_vector_type(2)));
typedef float f32x4 __attribute__((ext_vector_type(4)));
typedef float f32x2 __attribute__((ext_vector_type(2)));
typedef float f32x16 __attribute__((ext_vector_type(16)));
typedef __bf16 bf16x8 __attribute__((ext_vector_type(8)));
typedef __bf16 bf16x2 __attribute__((ext_vector_type(2)));
typedef short s16x4 __attribute__((ext_vector_type(4)));
#define LAS __attribute__((address_space(3)))
#define GAS __attribute__((address_space(1)))
#define DI __device__ __forceinline__

constexpr int S_ = 2048, D_ = 448, F_ = 1792;
constexpr int NHELP = 32, NWG = 224;
constexpr u32 MB = 256;
constexpr u32 WS_EPOCH = 0, WS_ALL = 2048, WS_KVF = 4096, WS_HF = WS_KVF + 65536, WS_YF = WS_HF + 65536, WS_RG = WS_YF + 65536, WS_CG = WS_RG + 16384;
constexpr u32 TP = 4096 + 128;
constexpr u32 WS_H = 1u << 20, WS_KVT = 9u << 20, WS_YT = 11u << 20;
constexpr u32 STAMP_OFF = 16u << 20;
constexpr int NSTAMP = 22;
constexpr u32 L_PIECE = 16384, L_W = 65536, L_LOC0 = 131072, L_MISC = 147456, LDS_BYTES = 163840;
constexpr u32 KVRAW_SZ = 5632, L_KVS = L_LOC0 + 4 * KVRAW_SZ, L_B2 = L_KVS + 9216, L_FLAG = L_B2 + 128, L_ROWF = L_FLAG + 128, L_B2F = L_ROWF + 256, L_R2 = L_B2F + 256;
constexpr u32 L_SCR = L_LOC0 + 2 * KVRAW_SZ + 9728 + 64;
static_assert(L_R2 + 256 <= LDS_BYTES, "lds");
constexpr u32 F_TOEP = L_FLAG, F_HCNT = L_FLAG + 16, F_HRDY = L_FLAG + 20, F_KVRDY = L_FLAG + 24, F_YCNT = L_FLAG + 28, F_YRDY = L_FLAG + 32, F_S0 = L_FLAG + 36, F_KVCNT = L_FLAG + 40, F_W = L_FLAG + 48, F_LAND = L_FLAG + 64, F_CONS = L_FLAG + 96, F_KVS = L_FLAG + 124;
constexpr u32 L_CONV = 16384, CONV_CH = 65536, TOEP_CP = 4160, TOEP_CH = 4 * TOEP_CP, U_SZ = 4608, YS_SZ = 2304;
constexpr u64 TAG_SALT = 0x9E3779B97F4A7C15ull;
constexpr int SPIN_CAP = 1 << 15;
#define SLP 2

#define ST(i) do {} while (0)
#define STP nullptr

DI void dma16(u32 voff, const void* sbase, u32 lds) {
    asm volatile("s_mov_b32 m0, %2\n\ts_nop 0\n\tglobal_load_lds_dwordx4 %0, %1" :: "v"(voff), "s"(sbase), "s"(lds) : "memory");
}
DI void setm0(u32 lds) { asm volatile("s_mov_b32 m0, %0\n\ts_nop 0" :: "s"(lds) : "memory"); }
template <int OFF> DI void dmai(u32 voff, const void* sbase) { asm volatile("global_load_lds_dwordx4 %0, %1 offset:%2" :: "v"(voff), "s"(sbase), "i"(OFF) : "memory"); }
DI void bdma16(u32 voff, u32x4 srd, u32 lds) {
    asm volatile("s_mov_b32 m0, %2\n\ts_nop 0\n\tbuffer_load_dwordx4 %0, %1, 0 offen lds" :: "v"(voff), "s"(srd), "s"(lds) : "memory");
}
#define SB() __builtin_amdgcn_sched_barrier(0)
template <int OFF> DI u32x2 alds8(u32 a) { u32x2 r; asm volatile("ds_read_b64 %0, %1 offset:%2" : "=v"(r) : "v"(a), "i"(OFF)); return r; }
template <int OFF> DI u32x4 alds16(u32 a) { u32x4 r; asm volatile("ds_read_b128 %0, %1 offset:%2" : "=v"(r) : "v"(a), "i"(OFF)); return r; }
DI u32x4 gld16(u32 voff, const void* sbase) { u32x4 r; asm volatile("global_load_dwordx4 %0, %1, %2" : "=v"(r) : "v"(voff), "s"(sbase) : "memory"); return r; }
template <int OFF> DI u32x4 gld16c(u32 voff, const void* sbase) { u32x4 r; asm volatile("global_load_dwordx4 %0, %1, %2 offset:%3 sc1" : "=v"(r) : "v"(voff), "s"(sbase), "i"(OFF) : "memory"); return r; }
DI u32x2 gld8c(u32 voff, const void* sbase) { u32x2 r; asm volatile("global_load_dwordx2 %0, %1, %2 sc1" : "=v"(r) : "v"(voff), "s"(sbase) : "memory"); return r; }
DI void touch(const void* addr, u32 lds) { asm volatile("s_mov_b32 m0, %1\n\ts_nop 0\n\tglobal_load_lds_dword %0, off" :: "v"(addr), "s"(lds) : "memory"); }
DI u32 gld2(u32 voff, const void* sbase) { u32 r; asm volatile("global_load_ushort %0, %1, %2" : "=v"(r) : "v"(voff), "s"(sbase) : "memory"); return r; }
DI u32x4 bld16(u32 voff, u32x4 srd) { u32x4 r; asm volatile("buffer_load_dwordx4 %0, %1, %2, 0 offen" : "=v"(r) : "v"(voff), "s"(srd) : "memory"); return r; }
template <int OFF> DI u32x4 gld16o(u32 voff, const void* sbase) { u32x4 r; asm volatile("global_load_dwordx4 %0, %1, %2 offset:%3" : "=v"(r) : "v"(voff), "s"(sbase), "i"(OFF) : "memory"); return r; }
template <int OFF> DI void gst16c(u32 voff, void* sbase, u32x4 v) { asm volatile("global_store_dwordx4 %0, %1, %2 offset:%3 sc1\n\ts_nop 1" :: "v"(voff), "v"(v), "s"(sbase), "i"(OFF) : "memory"); }
template <int OFF> DI void gst8c(u32 voff, void* sbase, u32x2 v) { asm volatile("global_store_dwordx2 %0, %1, %2 offset:%3 sc1" :: "v"(voff), "v"(v), "s"(sbase), "i"(OFF) : "memory"); }
#define WAITV(n) asm volatile("s_waitcnt vmcnt(%0)" :: "i"(n) : "memory")
#define WAITBAR(n) asm volatile("s_waitcnt vmcnt(%0) lgkmcnt(0)\n\ts_barrier" :: "i"(n) : "memory")
#define BARL() asm volatile("s_waitcnt lgkmcnt(0)\n\ts_barrier" ::: "memory")
#define PIN(x) asm volatile("" : "+v"(x))

DI u32x4 mksrd(const void* p, u32 bytes) { u64 a = (u64)p; return u32x4{(u32)a, (u32)(a >> 32) & 0xffffu, bytes, 0x00020000u}; }
DI float bflo(u32 x) { return __uint_as_float(x << 16); }
DI float bfhi(u32 x) { return __uint_as_float(x & 0xffff0000u); }
DI u32 pk2(float a, float b) { bf16x2 v = {(__bf16)a, (__bf16)b}; return __builtin_bit_cast(u32, v); }
DI float rbf(float a) { return bflo(pk2(a, a)); }
DI float relu(float a) { int i = __float_as_int(a); return __int_as_float(i > 0 ? i : 0); }
DI float fma1(float a, float b, float c) { float r; asm("v_fma_f32 %0, %1, %2, %3" : "=v"(r) : "v"(a), "v"(b), "v"(c)); return r; }
DI float mul1(float a, float b) { float r; asm("v_mul_f32 %0, %1, %2" : "=v"(r) : "v"(a), "v"(b)); return r; }
DI float add1(float a, float b) { float r; asm("v_add_f32 %0, %1, %2" : "=v"(r) : "v"(a), "v"(b)); return r; }
DI float dot2(u32 a, u32 b, float c) { return __builtin_amdgcn_fdot2_f32_bf16(__builtin_bit_cast(bf16x2, a), __builtin_bit_cast(bf16x2, b), c, false); }
DI u32x4 lds16(u32 a) { return *(const LAS u32x4*)a; }
DI u32x2 lds8(u32 a) { return *(const LAS u32x2*)a; }
DI void sts16(u32 a, u32x4 v) { *(LAS u32x4*)a = v; }
DI void sts8(u32 a, u32x2 v) { *(LAS u32x2*)a = v; }
DI u32x2 ldtr(u32 a) { return __builtin_bit_cast(u32x2, __builtin_amdgcn_ds_read_tr16_b64_v4i16((LAS s16x4*)a)); }
DI u32x4 cat(u32x2 a, u32x2 b) { return u32x4{a.x, a.y, b.x, b.y}; }
DI f32x16 mfma32(u32x4 a, u32x4 b, f32x16 c) { return __builtin_amdgcn_mfma_f32_32x32x16_bf16(__builtin_bit_cast(bf16x8, a), __builtin_bit_cast(bf16x8, b), c, 0, 0, 0); }
DI f32x4 mfma16(u32x4 a, u32x4 b, f32x4 c) { return __builtin_amdgcn_mfma_f32_16x16x32_bf16(__builtin_bit_cast(bf16x8, a), __builtin_bit_cast(bf16x8, b), c, 0, 0, 0); }
DI float xhalf_sum(float v) { auto r = __builtin_amdgcn_permlane32_swap(__float_as_uint(v), __float_as_uint(v), false, false); return __uint_as_float(r[0]) + __uint_as_float(r[1]); }

struct Params {
    const u16 *x, *k, *v, *shk, *ssmk, *ssmd, *w1, *b1, *w2, *b2;
    u16* out; char* ws;
};

DI void poll8(u32 voff, const void* sbase, bool need, u32 tlo, u32 thi) {
    _Pragma("clang loop unroll(disable)") for (int spin = 0; spin < SPIN_CAP; ++spin) {
        u32x2 f = gld8c(voff, sbase);
        asm volatile("s_waitcnt vmcnt(0)" : "+v"(f) :: "memory");
        bool ok = !need || (f.x == tlo && f.y == thi);
        if (__all(ok)) break;
        __builtin_amdgcn_s_sleep(SLP);
    }
}
DI void poll16(u32 voff, const void* sbase, u32 tlo, u32 thi) {
    _Pragma("clang loop unroll(disable)") for (int spin = 0; spin < SPIN_CAP; ++spin) {
        u32x4 f = gld16c<0>(voff, sbase);
        asm volatile("s_waitcnt vmcnt(0)" : "+v"(f) :: "memory");
        bool ok = f.x == tlo && f.y == thi && f.z == tlo && f.w == thi;
        if (__all(ok)) break;
        __builtin_amdgcn_s_sleep(SLP);
    }
}

DI bool lds_arrive_last(u32 addr, u32 n) {
    u32 old = 0;
    if ((threadIdx.x & 63) == 0) old = __hip_atomic_fetch_add((LAS u32*)addr, 1u, __ATOMIC_RELAXED, __HIP_MEMORY_SCOPE_WORKGROUP);
    return (u32)__builtin_amdgcn_readfirstlane(old) == n - 1;
}
DI void lds_wait(u32 addr) {
    _Pragma("clang loop unroll(disable)") for (int spin = 0; spin < (1 << 22); ++spin) {
        if (__builtin_amdgcn_readfirstlane(*(volatile LAS u32*)addr)) break;
        __builtin_amdgcn_s_sleep(1);
    }
    asm volatile("" ::: "memory");
}
DI void lds_add(u32 addr) { asm volatile("" ::: "memory"); __hip_atomic_fetch_add((LAS u32*)addr, 1u, __ATOMIC_RELAXED, __HIP_MEMORY_SCOPE_WORKGROUP); asm volatile("" ::: "memory"); }
DI u32 lds_peek(u32 addr) { return *(volatile LAS u32*)addr; }
DI void lds_wait_ge(u32 addr, u32 n, u32 first) {
    u32 v = first;
    _Pragma("clang loop unroll(disable)") for (int spin = 0; spin < (1 << 22) && (u32)__builtin_amdgcn_readfirstlane(v) < n; ++spin) { __builtin_amdgcn_s_sleep(1); v = lds_peek(addr); }
    asm volatile("" ::: "memory");
}
DI void lds_set(u32 addr) { asm volatile("" ::: "memory"); if ((threadIdx.x & 63) == 0) *(volatile LAS u32*)addr = 1u; }
constexpr u32 loc_of(int c) { return (c % 5) == 0 ? L_LOC0 : (c % 5) == 1 ? 3 * L_PIECE : (c % 5) == 2 ? L_W + 3 * L_PIECE : (c % 5) == 3 ? 0u : L_W; }

#define HDV 3
constexpr int HD = HDV, HS = HD + 1;
constexpr int RD = 4, WS_ = RD + 1;
constexpr int g2_younger(int c) {
    const int wneed = c + RD;
    bool sh = false, sw = false; int ah = 0, aw = 0;
    const bool wdone = wneed < 5 + RD || wneed >= 14;
    for (int i = 0; i < HD; ++i) { if (sh) ah += 4; if (i == c) sh = true; }
    for (int i = 0; i < c; ++i) {
        if (i + HD < 14) { if (sh) ah += 4; if (sw) aw += 4; if (i + HD == c) sh = true; }
        if (i >= 1 && i + RD + 4 < 14) { if (sh) ah += 4; if (sw) aw += 4; if (i + RD + 4 == wneed) sw = true; }
    }
    if (wdone) return ah;
    return ah < aw ? ah : aw;
}

struct G2Ctx { const char* w2b; const char* hb; int crot; u32 voffw[4], ldsg, vlane, ta[3][2]; };
template <int C> DI const char* g2_wsrc(const G2Ctx& x) { int c = C + x.crot; if (c >= 14) c -= 14; return x.w2b + (size_t)c * (128 * 896); }
template <int C> DI u32 g2_loc(const G2Ctx& x) { return loc_of(C) + x.ldsg; }
template <int C, int N> DI void g2_w1(const G2Ctx& x, const char* sb) { if constexpr (N == 0) setm0(g2_loc<C>(x)); dmai<N * 1024>(x.voffw[N], sb); }
template <int C> DI void g2_issue_w(const G2Ctx& x) { const char* sb = g2_wsrc<C>(x); g2_w1<C, 0>(x, sb); g2_w1<C, 1>(x, sb); g2_w1<C, 2>(x, sb); g2_w1<C, 3>(x, sb); }
template <int C> DI const char* g2_hsrc(const G2Ctx& x) { int c = C + x.crot; if (c >= 14) c -= 14; return x.hb + c * 8192; }
template <int N> DI u32x4 g2_h1(const G2Ctx& x, const char* sb) { return gld16c<(N & 1) * 1024>(x.vlane + (N >> 1) * 112 * 1024, sb); }
template <int C> DI void g2_issue_h(const G2Ctx& x, u32x4 (&hf)[HS][4]) {
    const char* sb = g2_hsrc<C>(x);
    hf[C % HS][0] = g2_h1<0>(x, sb); hf[C % HS][1] = g2_h1<1>(x, sb); hf[C % HS][2] = g2_h1<2>(x, sb); hf[C % HS][3] = g2_h1<3>(x, sb);
}
template <int C> DI void g2_read(const G2Ctx& x, u32x4 (&wf)[WS_][4]) {
    const u32 a0 = x.ta[loc_of(C) >> 16][0], a1 = x.ta[loc_of(C) >> 16][1];
    constexpr u32 lo = loc_of(C) & 0xffffu;
#pragma unroll
    for (int ks = 0; ks < 2; ++ks) {
        wf[C % WS_][ks * 2] = cat(ldtr(a0 + lo + ks * 2048), ldtr(a0 + lo + ks * 2048 + 1024));
        wf[C % WS_][ks * 2 + 1] = cat(ldtr(a1 + lo + ks * 2048), ldtr(a1 + lo + ks * 2048 + 1024));
    }
}
template <int C, int I> DI void g2_rd(const G2Ctx& x, u32x4 (&wf)[WS_][4]) {
    if constexpr (C < 14) {
        const u32 a = x.ta[loc_of(C) >> 16][I & 1]; constexpr u32 lo = (loc_of(C) & 0xffffu) + (I >> 1) * 2048;
        wf[C % WS_][I] = cat(ldtr(a + lo), ldtr(a + lo + 1024));
    }
}
template <int C> DI void g2_step(const G2Ctx& x, u32x4 (&hf)[HS][4], f32x16 (&acc)[2][2], u32x4 (&wf)[WS_][4], u64* stamps) {
    asm volatile("s_waitcnt vmcnt(%4) lgkmcnt(0)" : "+v"(hf[C % HS][0]), "+v"(hf[C % HS][1]), "+v"(hf[C % HS][2]), "+v"(hf[C % HS][3]) : "i"(g2_younger(C)) : "memory");
    if constexpr (C % 3 == 0) ST(14 + C / 3);
    SB();
    constexpr bool DH = C + HD < 14, DW = C >= 1 && C + RD + 4 < 14;
    const char* hs = g2_hsrc<(DH ? C + HD : 0)>(x);
    const char* wsrc = g2_wsrc<(DW ? C + RD + 4 : 0)>(x);
#define G2M(i) acc[((i) >> 1) & 1][(i) & 1] = mfma32(wf[C % WS_][((i) >> 2) * 2 + (((i) >> 1) & 1)], hf[C % HS][((i) & 1) * 2 + ((i) >> 2)], acc[((i) >> 1) & 1][(i) & 1])
    G2M(0); g2_rd<C + RD, 0>(x, wf); if constexpr (DH) hf[(C + HD) % HS][0] = g2_h1<0>(x, hs); SB();
    G2M(1); g2_rd<C + RD, 1>(x, wf); if constexpr (DH) hf[(C + HD) % HS][1] = g2_h1<1>(x, hs); SB();
    G2M(2); g2_rd<C + RD, 2>(x, wf); if constexpr (DH) hf[(C + HD) % HS][2] = g2_h1<2>(x, hs); SB();
    G2M(3); g2_rd<C + RD, 3>(x, wf); if constexpr (DH) hf[(C + HD) % HS][3] = g2_h1<3>(x, hs); SB();
    G2M(4); if constexpr (DW) g2_w1<C + RD + 4, 0>(x, wsrc); SB();
    G2M(5); if constexpr (DW) g2_w1<C + RD + 4, 1>(x, wsrc); SB();
    G2M(6); if constexpr (DW) g2_w1<C + RD + 4, 2>(x, wsrc); SB();
    G2M(7); if constexpr (DW) g2_w1<C + RD + 4, 3>(x, wsrc); SB();
#undef G2M
    if constexpr (C + 1 < 14) g2_step<C + 1>(x, hf, acc, wf, stamps);
}

template <int NR> struct CF { u32x2 a0[NR], a1[NR]; u32x4 b[NR]; };
template <int S, int R, int NR> DI void cv_ld(CF<NR>& f, u32 aA, u32 aB) {
    if constexpr (R < NR && S < 8) {
        f.a0[R] = alds8<R * 512 + S * 64>(aA); f.a1[R] = alds8<R * 512 + S * 64 + 8>(aA);
        f.b[R] = alds16<(224 - 32 * R + 4 * S) * 16>(aB);
    }
}
template <int NR> DI void cv_wait(CF<NR>& f) {
    if constexpr (NR == 8)
        asm volatile("s_waitcnt lgkmcnt(0)" : "+v"(f.a0[0]), "+v"(f.a0[1]), "+v"(f.a0[2]), "+v"(f.a0[3]), "+v"(f.a0[4]), "+v"(f.a0[5]), "+v"(f.a0[6]), "+v"(f.a0[7]),
                     "+v"(f.a1[0]), "+v"(f.a1[1]), "+v"(f.a1[2]), "+v"(f.a1[3]), "+v"(f.a1[4]), "+v"(f.a1[5]), "+v"(f.a1[6]), "+v"(f.a1[7]),
                     "+v"(f.b[0]), "+v"(f.b[1]), "+v"(f.b[2]), "+v"(f.b[3]), "+v"(f.b[4]), "+v"(f.b[5]), "+v"(f.b[6]), "+v"(f.b[7]));
    else
        asm volatile("s_waitcnt lgkmcnt(0)" : "+v"(f.a0[0]), "+v"(f.a0[1]), "+v"(f.a0[2]), "+v"(f.a0[3]), "+v"(f.a0[4]), "+v"(f.a0[5]),
                     "+v"(f.a1[0]), "+v"(f.a1[1]), "+v"(f.a1[2]), "+v"(f.a1[3]), "+v"(f.a1[4]), "+v"(f.a1[5]),
                     "+v"(f.b[0]), "+v"(f.b[1]), "+v"(f.b[2]), "+v"(f.b[3]), "+v"(f.b[4]), "+v"(f.b[5]));
}
template <int HALF, int M> struct CvIdx {
    static constexpr int q(int i) { return HALF ? (i + 2) : (i < 2 ? i : i + 4); }
    static constexpr int tile() { int m = M; for (int r = 0; r < 8; ++r) for (int i = 3; i >= 0; --i) if (r <= q(i)) { if (m == 0) return i; --m; } return 0; }
    static constexpr int row() { int m = M; for (int r = 0; r < 8; ++r) for (int i = 3; i >= 0; --i) if (r <= q(i)) { if (m == 0) return r; --m; } return 0; }
};
template <int HALF, int S, int M, int NR> DI void cv_mm(CF<NR>& f, CF<NR>& n, u32 aA, u32 aB, f32x4 (&acc)[4]) {
    constexpr int i = CvIdx<HALF, M>::tile(), r = CvIdx<HALF, M>::row(), q = CvIdx<HALF, M>::q(i);
    acc[i] = mfma16(cat(f.a0[r], f.a1[r]), f.b[q - r], acc[i]);
    if constexpr ((M & 1) == 0) cv_ld<S + 1, M / 2, NR>(n, aA, aB);
    SB();
    if constexpr (M + 1 < 18) cv_mm<HALF, S, M + 1, NR>(f, n, aA, aB, acc);
}
template <int HALF, int S, int NR> DI void cv_step(CF<NR>& f, u32 aA, u32 aB, f32x4 (&acc)[4]) {
    cv_wait<NR>(f); SB();
    CF<NR> n;
    cv_mm<HALF, S, 0, NR>(f, n, aA, aB, acc);
    if constexpr (S + 1 < 8) cv_step<HALF, S + 1, NR>(n, aA, aB, acc);
}
template <int HALF> DI void conv_body(u32 aA, u32 aB, f32x4 (&acc)[4]) {
    constexpr int NR = HALF ? 6 : 8;
    CF<NR> f;
    asm volatile("s_waitcnt lgkmcnt(0)" ::: "memory");
    cv_ld<0, 0, NR>(f, aA, aB); cv_ld<0, 1, NR>(f, aA, aB); cv_ld<0, 2, NR>(f, aA, aB); cv_ld<0, 3, NR>(f, aA, aB);
    cv_ld<0, 4, NR>(f, aA, aB); cv_ld<0, 5, NR>(f, aA, aB); cv_ld<0, 6, NR>(f, aA, aB); cv_ld<0, 7, NR>(f, aA, aB);
    cv_step<HALF, 0, NR>(f, aA, aB, acc);
    asm volatile("" ::: "memory");
}

struct Geo { int bi, b, xcd, fh2, RB, CT, tb64, db, rot, crot; u32 R0, F0, T0, D0; };
DI Geo make_geo() {
    Geo g; g.b = blockIdx.x - NHELP; g.xcd = g.b & 7; const int bi = g.b >> 3; g.bi = bi;
    const int rq = g.xcd >> 1, rb = bi / 7, ct = bi - rb * 7;
    g.fh2 = g.xcd & 1; g.RB = 4 * rq + rb; g.CT = 7 * g.fh2 + ct; g.R0 = 128 * g.RB; g.F0 = 128 * g.CT;
    g.tb64 = 2 * g.RB + g.fh2; g.db = ct; g.T0 = 64 * g.tb64; g.D0 = 64 * g.db;
    g.rot = (ct + 2 * rb + rq * 3 + g.fh2) % 7; g.crot = (2 * ct + 7 * (rb & 1) + 3 * (rb >> 1) + rq + 5 * g.fh2) % 14;
    return g;
}
DI void make_g2(G2Ctx& g2, const Params& p, const Geo& q, int g, int lane, u32 lds0) {
    g2.w2b = (const char*)p.w2 + (size_t)(32 * g) * 896 + q.D0 * 2;
    g2.hb = p.ws + WS_H + (size_t)(q.tb64 * 2 * 112 + 2 * g) * 1024;
    g2.crot = q.crot;
#pragma unroll
    for (int n = 0; n < 4; ++n) g2.voffw[n] = (u32)((lane >> 3) * 896 + 16 * ((lane & 7) ^ (((lane >> 4) & 1) << 2)) + n * (8 * 896 - 1024));
    g2.ldsg = lds0 + g * 4096; g2.vlane = lane * 16u;
}
DI u32 slab_base(u32 lds0, int cw) { return lds0 + L_CONV + (cw >> 1) * CONV_CH + (cw & 1) * 8192; }
DI void final_epilogue(const Params& p, const Geo& q, u32 lds0) {
    int tz = threadIdx.x; PIN(tz);
    const int t = tz >> 3, c = tz & 7;
    const u32 pb = lds0 + ((t >> 5) ? 0u : 3 * L_PIECE) + ((t >> 4) & 1) * L_W + (t & 15) * 256;
    const u32 o0 = pb + 16 * ((2 * c) ^ (t & 7)), o1 = o0 ^ 16;
    f32x4 pa[4], pb_[4];
#pragma unroll
    for (int g = 0; g < 4; ++g) { pa[g] = __builtin_bit_cast(f32x4, lds16(o0 + g * 4096)); pb_[g] = __builtin_bit_cast(f32x4, lds16(o1 + g * 4096)); }
    const float r2 = *(const LAS float*)(lds0 + L_R2 + t * 4);
    const f32x4 b0 = __builtin_bit_cast(f32x4, lds16(lds0 + L_B2F + c * 32)), b1 = __builtin_bit_cast(f32x4, lds16(lds0 + L_B2F + c * 32 + 16));
    const u32 sb = slab_base(lds0, c >> 1) + t * 64 + (c & 1) * 32;
    const f32x4 sv0 = __builtin_bit_cast(f32x4, lds16(sb)), sv1 = __builtin_bit_cast(f32x4, lds16(sb + 16));
    const f32x4 xv0 = __builtin_bit_cast(f32x4, lds16(sb + 4096)), xv1 = __builtin_bit_cast(f32x4, lds16(sb + 4096 + 16));
    SB();
    const f32x4 s0 = (pa[0] + pa[1]) + (pa[2] + pa[3]), s1 = (pb_[0] + pb_[1]) + (pb_[2] + pb_[3]);
    f32x4 z0 = s0 * r2 + b0, z1 = s1 * r2 + b1;
    f32x4 e0 = z0 * -1.4426950408889634f, e1 = z1 * -1.4426950408889634f;
#pragma unroll
    for (int j = 0; j < 4; ++j) { e0[j] = __builtin_amdgcn_exp2f(e0[j]); e1[j] = __builtin_amdgcn_exp2f(e1[j]); }
    e0 += 1.f; e1 += 1.f;
#pragma unroll
    for (int j = 0; j < 4; ++j) { e0[j] = __builtin_amdgcn_rcpf(e0[j]); e1[j] = __builtin_amdgcn_rcpf(e1[j]); }
    z0 = z0 * e0; z1 = z1 * e1;
    z0 = z0 * sv0 + xv0; z1 = z1 * sv1 + xv1;
    const u32x4 res = {pk2(z0[0], z0[1]), pk2(z0[2], z0[3]), pk2(z1[0], z1[1]), pk2(z1[2], z1[3])};
    *(GAS u32x4*)((GAS char*)p.out + (size_t)(q.T0 + t) * 896 + q.D0 * 2 + c * 16) = res;
}
#define STAMP_DECL
#define STAMP_OUT(wave) do {} while (0)

extern __shared__ __attribute__((aligned(16))) char smem[];

struct CL { const char *xb, *wb; u32 vx[4], vw[4], sl; int rot; };
struct CSrc { const char *xs, *wsrc; };
template <int ST_> DI CSrc c_src(const CL& c) { int kc = ST_ + c.rot; if (kc >= 7) kc -= 7; return CSrc{c.xb + kc * 128, c.wb + (size_t)kc * 64 * 3584}; }
template <int ST_, int N> DI void c_dma1(const CL& c, const CSrc& s) {
    if constexpr (N == 0) setm0(c.sl + (ST_ & 3) * L_PIECE);
    if constexpr (N == 4) setm0(c.sl + (ST_ & 3) * L_PIECE + L_W);
    if constexpr (N < 4) dmai<N * 1024>(c.vx[N], s.xs); else dmai<(N - 4) * 1024>(c.vw[N - 4], s.wsrc);
}
template <int ST_> DI void c_dma(const CL& c) {
    const CSrc s = c_src<ST_>(c);
    c_dma1<ST_, 0>(c, s); c_dma1<ST_, 1>(c, s); c_dma1<ST_, 2>(c, s); c_dma1<ST_, 3>(c, s);
    c_dma1<ST_, 4>(c, s); c_dma1<ST_, 5>(c, s); c_dma1<ST_, 6>(c, s); c_dma1<ST_, 7>(c, s);
}
DI void make_cl(CL& c, const Params& p, const Geo& q, int cw, int lane, u32 lds0) {
    const int r8 = lane >> 3, c8 = lane & 7, r4 = lane >> 4, c16 = lane & 15;
#pragma unroll
    for (int n = 0; n < 4; ++n) {
        c.vx[n] = (u32)((32 * cw + r8) * 896 + 16 * (c8 ^ (((n & 1) << 2) | (r8 >> 1))) + n * (8 * 896 - 1024));
        c.vw[n] = (u32)((16 * cw + r4) * 3584 + 16 * (c16 ^ (r4 << 2)) + n * (4 * 3584 - 1024));
    }
    c.xb = (const char*)p.x + (size_t)q.R0 * 896; c.wb = (const char*)p.w1 + q.F0 * 2;
    c.sl = lds0 + cw * 4096; c.rot = q.rot;
}

template <int ST_> DI void l_dma(const CL& c) {
    const CSrc s = c_src<ST_>(c);
    const u32 sl = c.sl + (ST_ & 3) * L_PIECE;
    setm0(sl); dmai<0>(c.vx[0], s.xs); dmai<1024>(c.vx[1], s.xs); dmai<2048>(c.vx[2], s.xs); dmai<3072>(c.vx[3], s.xs);
    setm0(sl + L_W); dmai<0>(c.vw[0], s.wsrc); dmai<1024>(c.vw[1], s.wsrc); dmai<2048>(c.vw[2], s.wsrc); dmai<3072>(c.vw[3], s.wsrc);
    const char* x2 = s.xs + 32 * 896; const char* w2 = s.wsrc + 16 * 3584;
    setm0(sl + 4096); dmai<0>(c.vx[0], x2); dmai<1024>(c.vx[1], x2); dmai<2048>(c.vx[2], x2); dmai<3072>(c.vx[3], x2);
    setm0(sl + L_W + 4096); dmai<0>(c.vw[0], w2); dmai<1024>(c.vw[1], w2); dmai<2048>(c.vw[2], w2); dmai<3072>(c.vw[3], w2);
}

struct Fr { u32x4 x[4][2], w[4][2]; };
struct GA { u32 ax[4], aw[2]; };
template <int ST_> DI void g1_read(const GA& a, Fr& f) {
    constexpr u32 so = (ST_ & 3) * L_PIECE;
#pragma unroll
    for (int k = 0; k < 4; ++k) {
        f.w[k][0] = cat(ldtr(a.aw[0] + so + k * 4096), ldtr(a.aw[0] + so + k * 4096 + 1024));
        f.x[k][0] = lds16(a.ax[k] + so);
        f.w[k][1] = cat(ldtr(a.aw[1] + so + k * 4096), ldtr(a.aw[1] + so + k * 4096 + 1024));
        f.x[k][1] = lds16(a.ax[k] + so + 4096);
    }
}
template <int K0> DI void g1_mma(const Fr& f, f32x16 (&acc)[2][2]) {
#pragma unroll
    for (int k = K0; k < K0 + 2; ++k) {
        acc[0][0] = mfma32(f.w[k][0], f.x[k][0], acc[0][0]); acc[1][0] = mfma32(f.w[k][1], f.x[k][0], acc[1][0]);
        acc[0][1] = mfma32(f.w[k][0], f.x[k][1], acc[0][1]); acc[1][1] = mfma32(f.w[k][1], f.x[k][1], acc[1][1]);
    }
    SB();
}

template <int ST_, int I> DI void g1_rd(const GA& a, Fr& f) {
    constexpr u32 so = (ST_ & 3) * L_PIECE; constexpr int k = I >> 1, h = I & 1;
    f.w[k][h] = cat(ldtr(a.aw[h] + so + k * 4096), ldtr(a.aw[h] + so + k * 4096 + 1024));
    f.x[k][h] = lds16(a.ax[k] + so + h * 4096);
}
template <int K0, int I> DI void g1_m1(const Fr& f, f32x16 (&acc)[2][2]) {
    constexpr int k = K0 + (I >> 2), mt = I & 1, st = (I >> 1) & 1;
    acc[mt][st] = mfma32(f.w[k][mt], f.x[k][st], acc[mt][st]);
}
template <int ST_, int I = 0> DI void g1_carry(const GA& a, const Fr& fp, Fr& fn, f32x16 (&acc)[2][2]) {
    g1_m1<2, I>(fp, acc); g1_rd<ST_, I>(a, fn); SB();
    if constexpr (I + 1 < 8) g1_carry<ST_, I + 1>(a, fp, fn, acc);
}

DI void body_g(const Params& p, const int g) {
    STAMP_DECL
    const u32 lds0 = (u32)(size_t)(LAS char*)smem;
    const int lane = threadIdx.x & 63;
    const Geo q = make_geo();
    char* ws = p.ws;
    const int hi = lane >> 5, s31 = lane & 31;
    const int r8 = lane >> 3, c8 = lane & 7;
    const u32 raw = lds0 + L_LOC0 + g * KVRAW_SZ;
    {
        const u32 vkv = (u32)(r8 * 896 + c8 * 16);
        bdma16((u32)(((int)q.T0 + 16 * g - 8) * 896 + (int)q.D0 * 2) + vkv, mksrd(p.k, S_ * D_ * 2), raw);
        const char* kb = (const char*)p.k + (size_t)(q.T0 + 16 * g) * 896 + q.D0 * 2;
        const char* vb = (const char*)p.v + (size_t)(q.T0 + 16 * g) * 896 + q.D0 * 2;
        dmai<1024>(vkv, kb - 1024); dmai<2048>(vkv, kb + 8 * 896 - 2048); dmai<3072>(vkv, vb - 3072);
        setm0(raw + 4096);
        dmai<0>(vkv, vb + 8 * 896);
        if (lane < 32) dmai<1024>(lane * 16u, (const char*)p.shk + q.D0 * 8 - 1024);
        CL c; make_cl(c, p, q, g, lane, lds0);
        c_dma<0>(c);
    }
    u32x2 ep = gld8c(0u, ws + WS_EPOCH);
    const int fh = g & 1, sh = g >> 1;
    GA ga;
    {
        const int blk = (lane >> 4) & 1, qq = (lane & 15) >> 2, pp = lane & 3;
        const u32 Z = (u32)(hi ^ ((s31 >> 1) & 7));
#pragma unroll
        for (int k = 0; k < 4; ++k) ga.ax[k] = lds0 + (64 * sh + s31) * 128 + 16 * ((2 * k) ^ Z);
        ga.aw[0] = lds0 + L_W + (8 * hi + qq) * 256 + 16 * ((8 * fh + 2 * blk + (pp >> 1)) ^ (qq << 2)) + 8 * (pp & 1);
        ga.aw[1] = ga.aw[0] ^ 64;
    }
    WAITV(9);
    ST(1);
    {
        u32x4 wr[4], kr[5], vr[2];
#pragma unroll
        for (int e = 0; e < 4; ++e) wr[e] = lds16(raw + 5120 + c8 * 64 + e * 16);
#pragma unroll
        for (int j = 0; j < 5; ++j) kr[j] = lds16(raw + (5 + 2 * r8 + j) * 128 + c8 * 16);
        vr[0] = lds16(raw + 3072 + 2 * r8 * 128 + c8 * 16); vr[1] = lds16(raw + 3072 + (2 * r8 + 1) * 128 + c8 * 16);
        SB();
        u32x4 o0, o1;
#pragma unroll
        for (int e = 0; e < 4; ++e) {
            const u32x4 w = wr[e];
            const f32x2 t0 = {bflo(w.x), bflo(w.z)}, t1 = {bfhi(w.x), bfhi(w.z)}, t2 = {bflo(w.y), bflo(w.w)}, t3 = {bfhi(w.y), bfhi(w.w)};
            f32x2 k[5];
#pragma unroll
            for (int j = 0; j < 5; ++j) k[j] = f32x2{bflo(kr[j][e]), bfhi(kr[j][e])};
            f32x2 a = k[0] * t3, b = k[1] * t3;
            a = __builtin_elementwise_fma(k[1], t2, a); b = __builtin_elementwise_fma(k[2], t2, b);
            a = __builtin_elementwise_fma(k[2], t1, a); b = __builtin_elementwise_fma(k[3], t1, b);
            a = __builtin_elementwise_fma(k[3], t0, a); b = __builtin_elementwise_fma(k[4], t0, b);
            a *= f32x2{bflo(vr[0][e]), bfhi(vr[0][e])}; b *= f32x2{bflo(vr[1][e]), bfhi(vr[1][e])};
            o0[e] = pk2(a.x, a.y); o1[e] = pk2(b.x, b.y);
        }
        sts16(lds0 + L_KVS + (16 * g + 2 * r8) * 144 + c8 * 16, o0);
        sts16(lds0 + L_KVS + (16 * g + 2 * r8 + 1) * 144 + c8 * 16, o1);
        asm volatile("s_waitcnt lgkmcnt(0)" ::: "memory");
        lds_add(lds0 + F_KVS);
    }
    f32x16 acc[2][2] = {};
    Fr fa, fb;
    WAITV(1);
    lds_add(lds0 + F_LAND);
    lds_wait_ge(lds0 + F_LAND, 256, lds_peek(lds0 + F_LAND));
    ST(2);
    u32 pk;
#define G1_STAGE(T, FP, FN) do { lds_wait_ge(lds0 + F_LAND + 4 * (T), (T) == 3 ? 128 : 64, pk); g1_carry<T>(ga, FP, FN, acc); pk = lds_peek(lds0 + F_LAND + 4 * (T) + 4); SB(); \
        g1_mma<0>(FN, acc); asm volatile("s_waitcnt lgkmcnt(0)" ::: "memory"); lds_add(lds0 + F_CONS + 4 * (T)); SB(); } while (0)
    g1_read<0>(ga, fa); pk = lds_peek(lds0 + F_LAND + 4); SB();
    g1_mma<0>(fa, acc); asm volatile("s_waitcnt lgkmcnt(0)" ::: "memory"); lds_add(lds0 + F_CONS); SB();
    ST(19);
    G1_STAGE(1, fa, fb);
    ST(20);
    G1_STAGE(2, fb, fa);
    asm volatile("s_waitcnt vmcnt(0)" : "+v"(ep) :: "memory");
    ST(3);
    const u64 epoch = ((u64)(u32)__builtin_amdgcn_readfirstlane(ep.y) << 32) | (u32)__builtin_amdgcn_readfirstlane(ep.x);
    const u64 tag = epoch + TAG_SALT;
    const u32 tlo = (u32)tag, thi = (u32)(tag >> 32);
    G1_STAGE(3, fa, fb);
    G1_STAGE(4, fb, fa);
    ST(21);
    const u32 rgo = WS_RG + (q.R0 + 64 * sh + lane) * 8, cgo = WS_CG + (q.F0 + 64 * fh + lane) * 8;
    u32x2 rg = gld8c(rgo, ws), cg = gld8c(cgo, ws);
    u32 b1v = gld2((u32)((64 * fh + lane) * 2), (const char*)p.b1 + q.F0 * 2);
    G1_STAGE(5, fa, fb);
    ST(4);
    G1_STAGE(6, fb, fa);
    ST(5);
    asm volatile("s_waitcnt vmcnt(0)" : "+v"(rg), "+v"(cg), "+v"(b1v) :: "memory");
    _Pragma("clang loop unroll(disable)") for (int spin = 0; spin < SPIN_CAP && !__all(rg.y == tlo && cg.y == tlo); ++spin) {
        rg = gld8c(rgo, ws); cg = gld8c(cgo, ws);
        asm volatile("s_waitcnt vmcnt(0)" : "+v"(rg), "+v"(cg) :: "memory");
    }
    if (fh == 0 && sh == q.fh2) *(LAS u32*)(lds0 + L_ROWF + lane * 4) = rg.x;
    g1_mma<2>(fa, acc);
    {
        u32x4 ax[2], bx[2];
        const auto rr = __builtin_amdgcn_permlane32_swap(rg.x, rg.x, false, false);
        const auto cc = __builtin_amdgcn_permlane32_swap(cg.x, cg.x, false, false);
        const auto bb = __builtin_amdgcn_permlane32_swap(b1v, b1v, false, false);
#pragma unroll
        for (int i = 0; i < 2; ++i) {
            const u32 nm = __builtin_amdgcn_perm(rr[i], rr[i], 0x01000100u) ^ 0x80008000u;
            ax[i] = u32x4{cc[i], bb[i], 0u, 0u};
            bx[i] = u32x4{hi ? 0u : nm, hi ? 0u : (rr[i] >> 16), 0u, 0u};
        }
#pragma unroll
        for (int st = 0; st < 2; ++st)
#pragma unroll
            for (int mt = 0; mt < 2; ++mt) acc[mt][st] = mfma32(ax[mt], bx[st], acc[mt][st]);
#pragma unroll
        for (int st = 0; st < 2; ++st) {
            char* hb = ws + WS_H + (size_t)((4 * q.RB + 2 * sh + st) * 112 + q.CT * 8 + fh * 4) * 1024;
#pragma unroll
            for (int mt = 0; mt < 2; ++mt)
#pragma unroll
                for (int t = 0; t < 2; ++t) {
                    u32x4 hv;
#pragma unroll
                    for (int j = 0; j < 4; ++j) {
                        f32x2 a = {acc[mt][st][8 * t + 2 * j], acc[mt][st][8 * t + 2 * j + 1]};
                        f32x2 u = {relu(a.x), relu(a.y)};
                        u = u * u;
                        hv[j] = pk2(u.x, u.y);
                    }
                    if (mt == 0 && t == 0) gst16c<0>(lane * 16u, hb, hv);
                    if (mt == 0 && t == 1) gst16c<1024>(lane * 16u, hb, hv);
                    if (mt == 1 && t == 0) gst16c<2048>(lane * 16u, hb, hv);
                    if (mt == 1 && t == 1) gst16c<3072>(lane * 16u, hb, hv);
                    SB();
                }
        }
    }
    ST(6);
    G2Ctx g2; make_g2(g2, p, q, g, lane, lds0);
    {
        const int blk = (lane >> 4) & 1, qq = (lane & 15) >> 2, pp = lane & 3;
        const u32 t0 = g2.ldsg + (4 * hi + qq) * 128 + 16 * ((2 * blk + (pp >> 1)) ^ ((qq >> 1) << 2)) + 8 * (pp & 1);
#pragma unroll
        for (int i = 0; i < 3; ++i) { g2.ta[i][0] = t0 + i * 65536; g2.ta[i][1] = (t0 ^ 64) + i * 65536; PIN(g2.ta[i][0]); PIN(g2.ta[i][1]); }
    }
    u32x4 wf[WS_][4];
    lds_wait_ge(lds0 + F_CONS + 16, 256, lds_peek(lds0 + F_CONS + 16));
    lds_wait(lds0 + F_W + (g >> 1) * 4);
    g2_read<0>(g2, wf); g2_read<1>(g2, wf);
    asm volatile("s_waitcnt vmcnt(0) lgkmcnt(0)" ::: "memory"); ST(9);
    if (lds_arrive_last(lds0 + F_HCNT, 4)) { if (lane < 15) gst8c<0>(lane < 14 ? WS_HF + (q.RB * 14 + lane) * MB + q.CT * 8 : WS_ALL + q.b * 8, ws, u32x2{tlo, thi}); }
    {
        const u32 po = WS_HF + (q.RB * 14 + q.CT) * MB + (lane < 14 ? lane : 0) * 8;
        g2_issue_w<2>(g2); g2_issue_w<3>(g2);
        u32x2 f = {0u, 0u};
        if (g == 0) f = gld8c(po, ws);
        g2_issue_w<4>(g2); g2_issue_w<5>(g2); g2_issue_w<6>(g2);
        asm volatile("s_waitcnt vmcnt(12)" : "+v"(f) :: "memory");
        bool rdy = __all(f.x == tlo && f.y == thi);
        if (g == 0 && rdy) lds_set(lds0 + F_HRDY);
        g2_read<2>(g2, wf); g2_read<3>(g2, wf);
        asm volatile("s_waitcnt lgkmcnt(0)" ::: "memory");
        if (g == 0 && !rdy) f = gld8c(po, ws);
        g2_issue_w<7>(g2); g2_issue_w<8>(g2);
        if (g == 0) {
            if (!rdy) {
                asm volatile("s_waitcnt vmcnt(8)" : "+v"(f) :: "memory");
                if (!__all(f.x == tlo && f.y == thi)) poll8(po, ws, true, tlo, thi);
                lds_set(lds0 + F_HRDY);
            }
        } else lds_wait(lds0 + F_HRDY);
    }
    ST(7);
    {
        u32x4 hf[HS][4];
        g2_issue_h<0>(g2, hf); g2_issue_h<1>(g2, hf); if constexpr (HD > 2) g2_issue_h<2 % HS>(g2, hf); if constexpr (HD > 3) g2_issue_h<3 % HS>(g2, hf); if constexpr (HD > 4) g2_issue_h<4 % HS>(g2, hf);
        f32x16 a2[2][2] = {};
        g2_step<0>(g2, hf, a2, wf, STP);
        ST(8);
        const u32 pw = g2.ldsg + (s31 >> 4) * L_W + (s31 & 15) * 256;
#pragma unroll
        for (int s2 = 0; s2 < 2; ++s2)
#pragma unroll
            for (int dt = 0; dt < 2; ++dt)
#pragma unroll
                for (int gr = 0; gr < 4; ++gr) {
                    const u32 a = pw + (s2 ? 0u : 3 * L_PIECE) + 16 * ((8 * dt + 2 * gr + hi) ^ (s31 & 7));
                    sts16(a, __builtin_bit_cast(u32x4, f32x4{a2[dt][s2][4 * gr], a2[dt][s2][4 * gr + 1], a2[dt][s2][4 * gr + 2], a2[dt][s2][4 * gr + 3]}));
                }
    }
    BARL(); ST(10);
    final_epilogue(p, q, lds0);
    STAMP_OUT(g);
}

DI void body_c(const Params& p, const int cw) {
    STAMP_DECL
    const u32 lds0 = (u32)(size_t)(LAS char*)smem;
    const int lane = threadIdx.x & 63;
    const Geo q = make_geo();
    char* ws = p.ws;
    const int hi = lane >> 5, s31 = lane & 31;
    const int half = cw & 1, chl = cw >> 1, chan = 2 * q.b + chl;
    u32x2 ep;
    if (cw < 2) {
        CL c, c2; make_cl(c, p, q, 0, lane, lds0); c2 = c; c2.xb += 64 * 896; c2.wb += 32 * 3584; c2.sl += 8192;
        if (cw == 0) {
            l_dma<1>(c); l_dma<1>(c2); ST(1);
            WAITV(0); lds_add(lds0 + F_LAND + 4); ST(2);
            lds_wait_ge(lds0 + F_CONS, 256, lds_peek(lds0 + F_CONS)); l_dma<4>(c); l_dma<4>(c2); ST(19);
            WAITV(0); lds_add(lds0 + F_LAND + 16); ST(20);
            lds_wait_ge(lds0 + F_CONS + 8, 256, lds_peek(lds0 + F_CONS + 8)); l_dma<6>(c); l_dma<6>(c2); ST(3);
            ep = gld8c(0u, ws + WS_EPOCH);
            WAITV(1); lds_add(lds0 + F_LAND + 24); ST(21);
        } else {
            l_dma<2>(c); l_dma<2>(c2); ST(1);
            WAITV(0); lds_add(lds0 + F_LAND + 8); ST(2);
            lds_wait_ge(lds0 + F_CONS + 4, 256, lds_peek(lds0 + F_CONS + 4)); l_dma<5>(c); l_dma<5>(c2); ST(19);
            ep = gld8c(0u, ws + WS_EPOCH);
            WAITV(1); lds_add(lds0 + F_LAND + 20); ST(20);
        }
        lds_wait_ge(lds0 + F_CONS + 12, 256, lds_peek(lds0 + F_CONS + 12));
#pragma unroll
        for (int i = 0; i < 2; ++i) { G2Ctx g2; make_g2(g2, p, q, 2 * cw + i, lane, lds0); g2_issue_w<0>(g2); g2_issue_w<1>(g2); }
        asm volatile("s_waitcnt vmcnt(0)" : "+v"(ep) :: "memory");
        lds_set(lds0 + F_W + cw * 4);
    } else {
        const int kw = cw - 2;
        if (kw == 0 && lane < 8) dma16(lane * 16u, (const char*)p.b2 + q.D0 * 2, lds0 + L_B2);
        ep = gld8c(0u, ws + WS_EPOCH);
        { CL c; make_cl(c, p, q, 2 * kw, lane, lds0); l_dma<3>(c); }
        lds_wait_ge(lds0 + F_KVS, 256, lds_peek(lds0 + F_KVS));
        ST(2);
        {
            const int grp = lane >> 4, qq = (lane & 15) >> 2, pp = lane & 3;
            const u32 a = lds0 + L_KVS + (16 * grp + 8 * (pp >> 1) + 7 - qq) * 144 + kw * 64 + (pp & 1) * 8;
            const u32 vo = (u32)((q.D0 + 32 * kw + (lane & 7)) * TP + (2040 - (int)q.T0 - 8 * (lane >> 3)) * 2);
            u32x4 tr[4];
#pragma unroll
            for (int i = 0; i < 4; ++i) tr[i] = cat(ldtr(a + 16 * i), ldtr(a - 4 * 144 + 16 * i));
            SB();
#pragma unroll
            for (int i = 0; i < 4; ++i) gst16c<0>(vo, ws + WS_KVT + i * 8 * TP, tr[i]);
        }
        ST(20);
        u32x4 sk[5], uu[4];
        const u32x4 sksrd = mksrd((const char*)p.ssmk + (size_t)(2 * q.b + kw) * 4096, 4096);
#pragma unroll
        for (int r = 0; r < 5; ++r) sk[r] = bld16((u32)((lane - 1 + 63 * r) * 16), sksrd);
        ST(3);
        asm volatile("s_waitcnt vmcnt(9)" : "+v"(ep) :: "memory");
        lds_add(lds0 + F_LAND + 12);
        WAITV(5);
        ST(19);
        const u64 tg = (((u64)ep.y << 32) | ep.x) + TAG_SALT;
        if (lds_arrive_last(lds0 + F_KVCNT, 2)) {
            if (lane < 32) gst8c<0>(WS_KVF + (q.db * 32 + lane) * MB + q.tb64 * 8, ws, u32x2{(u32)tg, (u32)(tg >> 32)});
        }
        asm volatile("s_waitcnt vmcnt(0)" : "+v"(sk[0]), "+v"(sk[1]), "+v"(sk[2]), "+v"(sk[3]), "+v"(sk[4]) :: "memory");
        u32x4 tw[5][4];
#pragma unroll
        for (int r = 0; r < 5; ++r) {
            const u32x4 B = sk[r];
            u32x4 A;
#pragma unroll
            for (int j = 0; j < 4; ++j) A[j] = __builtin_amdgcn_update_dpp(0u, B[j], 0x138  , 0xf, 0xf, false);
            const u32 e0 = __builtin_amdgcn_alignbit(A.y, A.x, 16), e1 = __builtin_amdgcn_alignbit(A.z, A.y, 16), e2 = __builtin_amdgcn_alignbit(A.w, A.z, 16),
                      e3 = __builtin_amdgcn_alignbit(B.x, A.w, 16), e4 = __builtin_amdgcn_alignbit(B.y, B.x, 16);
            tw[r][0] = u32x4{e0, e1, e2, e3}; tw[r][1] = u32x4{A.y, A.z, A.w, B.x}; tw[r][2] = u32x4{e1, e2, e3, e4}; tw[r][3] = u32x4{A.z, A.w, B.x, B.y};
            PIN(tw[r][0]); PIN(tw[r][1]); PIN(tw[r][2]); PIN(tw[r][3]);
        }
        if (kw == 0) { poll8(WS_KVF + q.b * MB + s31 * 8, ws, true, (u32)tg, (u32)(tg >> 32)); lds_set(lds0 + F_KVRDY); } else lds_wait(lds0 + F_KVRDY);
        ST(7);
        const char* kb2 = ws + WS_KVT + (size_t)(2 * q.b + kw) * TP;
        uu[0] = gld16c<0>(lane * 16u, kb2); uu[1] = gld16c<1024>(lane * 16u, kb2); uu[2] = gld16c<2048>(lane * 16u, kb2); uu[3] = gld16c<3072>(lane * 16u, kb2);
        lds_wait_ge(lds0 + F_CONS + 20, 256, lds_peek(lds0 + F_CONS + 20)); lds_wait_ge(lds0 + F_CONS + 24, 256, lds_peek(lds0 + F_CONS + 24));
        ST(5);
        const u32 tq = lds0 + L_CONV + kw * CONV_CH + lane * 16;
#pragma unroll
        for (int r = 0; r < 5; ++r) {
            if ((lane > 0 || r == 0) && (r < 4 || lane < 6)) {
#pragma unroll
                for (int c = 0; c < 4; ++c) sts16(tq + r * 1008 + c * TOEP_CP, tw[r][c]);
            }
        }
        if (lane < 32) sts16(tq + TOEP_CH + 4096, u32x4{0u, 0u, 0u, 0u});
        asm volatile("s_waitcnt vmcnt(0)" : "+v"(uu[0]), "+v"(uu[1]), "+v"(uu[2]), "+v"(uu[3]) :: "memory");
        ST(14);
        sts16(tq + TOEP_CH, uu[0]); sts16(tq + TOEP_CH + 1024, uu[1]); sts16(tq + TOEP_CH + 2048, uu[2]); sts16(tq + TOEP_CH + 3072, uu[3]);
        asm volatile("s_waitcnt lgkmcnt(0)" ::: "memory");
        lds_set(lds0 + F_TOEP + kw * 4);
    }
    u32x4 xa = gld16((u32)(lane * 896), (const char*)p.x + (size_t)q.T0 * 896 + (q.D0 + 16 * cw) * 2);
    u32x4 xb_ = gld16((u32)(lane * 896 + 16), (const char*)p.x + (size_t)q.T0 * 896 + (q.D0 + 16 * cw) * 2);
    u32 dv = gld2(0u, (const char*)p.ssmd + chan * 2);
    const u64 epoch = ((u64)(u32)__builtin_amdgcn_readfirstlane(ep.y) << 32) | (u32)__builtin_amdgcn_readfirstlane(ep.x);
    const u64 tag = epoch + TAG_SALT;
    const u32 tlo = (u32)tag, thi = (u32)(tag >> 32);
    const u32 tbase = lds0 + L_CONV + chl * CONV_CH;
    const u32 ub = tbase + TOEP_CH;
    ST(6);
    lds_wait(lds0 + F_TOEP + chl * 4);
    ST(15);
    const int bb = lane & 15, gg = lane >> 4;
    const u32 aA = tbase + (bb & 3) * TOEP_CP + ((bb >> 2) + 2 * gg) * 8;
    const u32 aB = ub + (30 - 2 * bb + gg) * 16;
    f32x4 acc[4] = {};
    if (half == 0) conv_body<0>(aA, aB, acc); else conv_body<1>(aA, aB, acc);
    ST(16);
    asm volatile("s_waitcnt vmcnt(0)" : "+v"(xa), "+v"(xb_), "+v"(dv) :: "memory");
    ST(8);
    {
        const float dd = bflo(dv);
        char* yb = ws + WS_YT + (size_t)chan * TP;
        u32x2 uq[4];
#pragma unroll
        for (int i = 0; i < 4; ++i) uq[i] = lds8(ub + (2044 - (256 * (half ? (i + 2) : (i < 2 ? i : i + 4)) + 16 * bb + 4 * gg)) * 2);
        SB();
#pragma unroll
        for (int i = 0; i < 4; ++i) {
            const int qt = half ? (i + 2) : (i < 2 ? i : i + 4);
            const int t = 256 * qt + 16 * bb + 4 * gg;
            const u32x2 uu = uq[i];
            const u32 c01 = pk2(acc[i][0], acc[i][1]), c23 = pk2(acc[i][2], acc[i][3]);
            float y0 = fma1(bfhi(uu.y), dd, bflo(c01)), y1 = fma1(bflo(uu.y), dd, bfhi(c01));
            float y2 = fma1(bfhi(uu.x), dd, bflo(c23)), y3 = fma1(bflo(uu.x), dd, bfhi(c23));
            gst8c<0>((u32)(t * 2), yb, u32x2{pk2(y0, y1), pk2(y2, y3)});
        }
        WAITV(0);
        if (lds_arrive_last(lds0 + F_YCNT, 4)) { if (lane < 32) gst8c<0>(WS_YF + (lane * 7 + (q.b >> 5)) * MB + (q.b & 31) * 8, ws, u32x2{tlo, thi}); }
    }
    _Pragma("clang loop unroll(disable)") for (int spin = 0; spin < (1 << 22); ++spin) { if (__builtin_amdgcn_readfirstlane(*(volatile LAS u32*)(lds0 + F_YCNT)) >= 4) break; __builtin_amdgcn_s_sleep(1); }
    asm volatile("" ::: "memory");
    const u32 slab = slab_base(lds0, cw) + lane * 64;
    f32x4 xn[4];
    {
        _Pragma("clang loop unroll(disable)") for (int spin = 0; spin < (1 << 22); ++spin) { if (__builtin_amdgcn_readfirstlane(*(volatile LAS u32*)(lds0 + F_HCNT)) >= 4) break; __builtin_amdgcn_s_sleep(1); }
        asm volatile("" ::: "memory");
        const u32 rf = *(const LAS u32*)(lds0 + L_ROWF + lane * 4);
        const float m = bflo(rf), ri = __builtin_amdgcn_rcpf(bfhi(rf)), nmri = -m * ri;
        const u32 xx[8] = {xa.x, xa.y, xa.z, xa.w, xb_.x, xb_.y, xb_.z, xb_.w};
#pragma unroll
        for (int j = 0; j < 4; ++j) {
            f32x4 v = {bflo(xx[2 * j]), bfhi(xx[2 * j]), bflo(xx[2 * j + 1]), bfhi(xx[2 * j + 1])};
            sts16(slab + 4096 + j * 16, __builtin_bit_cast(u32x4, v));
            xn[j] = f32x4{fma1(v[0], ri, nmri), fma1(v[1], ri, nmri), fma1(v[2], ri, nmri), fma1(v[3], ri, nmri)};
        }
        if (cw == 0) *(LAS float*)(lds0 + L_R2 + lane * 4) = ri * ri;
        if (cw == 1) *(LAS float*)(lds0 + L_B2F + lane * 4) = bflo(*(const LAS u16*)(lds0 + L_B2 + lane * 2));
    }
    if (q.b == 0 && cw == 3) {
        _Pragma("clang loop unroll(disable)") for (int spin = 0; spin < SPIN_CAP; ++spin) {
            u32x4 f0 = gld16c<0>(lane * 16u, ws + WS_ALL), f1 = gld16c<1024>((lane < 48 ? lane : 0) * 16u, ws + WS_ALL);
            asm volatile("s_waitcnt vmcnt(0)" : "+v"(f0), "+v"(f1) :: "memory");
            bool ok = f0.x == tlo && f0.y == thi && f0.z == tlo && f0.w == thi && f1.x == tlo && f1.y == thi && f1.z == tlo && f1.w == thi;
            if (__all(ok)) break;
            __builtin_amdgcn_s_sleep(SLP);
        }
        if (lane == 0) { const u64 e1 = epoch + 1; gst8c<0>(0u, ws + WS_EPOCH, u32x2{(u32)e1, (u32)(e1 >> 32)}); }
    }
    if (cw == 0) { poll8(WS_YF + (q.tb64 * 7 + q.db) * MB + s31 * 8, ws, true, tlo, thi); lds_set(lds0 + F_YRDY); }
    else lds_wait(lds0 + F_YRDY);
    ST(9);
    {
        const char* yb = ws + WS_YT + (size_t)(q.D0 + 16 * cw) * TP + q.T0 * 2;
        const u32 vo = (u32)((lane >> 3) * TP + (lane & 7) * 16);
        u32x4 y0 = gld16c<0>(vo, yb), y1 = gld16c<0>(vo, yb + 8 * TP);
        asm volatile("s_waitcnt vmcnt(0)" : "+v"(y0), "+v"(y1) :: "memory");
        const u32 ys = tbase + TOEP_CH + 2 * U_SZ + half * YS_SZ;
        sts16(ys + (lane >> 3) * 144 + (lane & 7) * 16, y0);
        sts16(ys + (8 + (lane >> 3)) * 144 + (lane & 7) * 16, y1);
        const int qq = (lane & 15) >> 2, pp = lane & 3;
        const u32 a = ys + qq * 144 + gg * 32 + pp * 8;
        u32x2 t0 = ldtr(a), t1 = ldtr(a + 4 * 144), t2 = ldtr(a + 8 * 144), t3 = ldtr(a + 12 * 144);
        const u32 yy[8] = {t0.x, t0.y, t1.x, t1.y, t2.x, t2.y, t3.x, t3.y};
#pragma unroll
        for (int j = 0; j < 4; ++j) {
            f32x4 v = {bflo(yy[2 * j]), bfhi(yy[2 * j]), bflo(yy[2 * j + 1]), bfhi(yy[2 * j + 1])};
            sts16(slab + ((j ^ (lane & 3)) * 0 + j) * 16, __builtin_bit_cast(u32x4, f32x4{mul1(v[0], xn[j][0]), mul1(v[1], xn[j][1]), mul1(v[2], xn[j][2]), mul1(v[3], xn[j][3])}));
        }
    }
    BARL(); ST(10);
    final_epilogue(p, q, lds0);
    STAMP_OUT(cw + 4);
}

DI float dpp_xor_sum8(float v) {
    v += __uint_as_float(__builtin_amdgcn_mov_dpp(__float_as_uint(v), 0xB1, 0xf, 0xf, true));
    v += __uint_as_float(__builtin_amdgcn_mov_dpp(__float_as_uint(v), 0x4E, 0xf, 0xf, true));
    v += __uint_as_float(__builtin_amdgcn_mov_dpp(__float_as_uint(v), 0x141, 0xf, 0xf, true));
    return v;
}
DI void body_help(const Params& p, const int wave) {
    const u32 lds0 = (u32)(size_t)(LAS char*)smem;
    const int lane = threadIdx.x & 63, r8 = lane >> 3, c8 = lane & 7;
    const int xcd = blockIdx.x & 7, j = blockIdx.x >> 3;
    char* ws = p.ws;
    u32x2 ep = gld8c(0u, ws + WS_EPOCH);
    u32x4 d[14];
    const int ctile = (xcd >> 1) * 4 + j;
    const bool cols = wave >= 4;
    if (!cols) {
        const char* xb = (const char*)p.x + (size_t)(256 * xcd + 64 * j + 16 * wave) * 896;
#pragma unroll
        for (int i = 0; i < 14; ++i) d[i] = gld16((u32)((r8 + 8 * (i / 7)) * 896 + (i % 7) * 128 + c8 * 16), xb);
    } else {
        const char* wb = (const char*)p.w1 + (size_t)(112 * (wave - 4)) * 3584 + ((xcd & 1) * 14 + (ctile < 14 ? ctile : 0)) * 128;
#pragma unroll
        for (int i = 0; i < 14; ++i) d[i] = gld16((u32)((r8 + 8 * i) * 3584 + c8 * 16), wb);
    }
    asm volatile("s_waitcnt vmcnt(0)" : "+v"(ep), "+v"(d[0]), "+v"(d[1]), "+v"(d[2]), "+v"(d[3]), "+v"(d[4]), "+v"(d[5]), "+v"(d[6]), "+v"(d[7]), "+v"(d[8]), "+v"(d[9]),
                 "+v"(d[10]), "+v"(d[11]), "+v"(d[12]), "+v"(d[13]) :: "memory");
    const u32 tlo = (u32)((((u64)ep.y << 32) | ep.x) + TAG_SALT);
    if (!cols) {
#pragma unroll
        for (int h = 0; h < 2; ++h) {
            float s0 = 0.f, s1 = 0.f, q0 = 0.f, q1 = 0.f;
#pragma unroll
            for (int i = 0; i < 7; ++i)
#pragma unroll
                for (int e = 0; e < 4; e += 2) {
                    s0 = dot2(d[7 * h + i][e], 0x3f803f80u, s0); q0 = dot2(d[7 * h + i][e], d[7 * h + i][e], q0);
                    s1 = dot2(d[7 * h + i][e + 1], 0x3f803f80u, s1); q1 = dot2(d[7 * h + i][e + 1], d[7 * h + i][e + 1], q1);
                }
            const float mean = dpp_xor_sum8(s0 + s1) * (1.f / 448.f);
            const float var = fmaxf(dpp_xor_sum8(q0 + q1) * (1.f / 448.f) - mean * mean, 0.f);
            const float sd = __builtin_amdgcn_sqrtf(rbf(rbf(var) + 1e-5f));
            if (c8 == 0) gst8c<0>((u32)(WS_RG + (256 * xcd + 64 * j + 16 * wave + 8 * h + r8) * 8), ws, u32x2{pk2(mean, sd), tlo});
        }
    } else {
        float a[8] = {};
        u32 klo = 0x00003f80u, khi = 0x3f800000u; PIN(klo); PIN(khi);
#pragma unroll
        for (int i = 0; i < 14; ++i)
#pragma unroll
            for (int e = 0; e < 4; ++e) { a[2 * e] = dot2(d[i][e], klo, a[2 * e]); a[2 * e + 1] = dot2(d[i][e], khi, a[2 * e + 1]); }
        const u32 o = lds0 + ((wave - 4) * 8 + r8) * 272 + c8 * 32;
        sts16(o, __builtin_bit_cast(u32x4, f32x4{a[0], a[1], a[2], a[3]})); sts16(o + 16, __builtin_bit_cast(u32x4, f32x4{a[4], a[5], a[6], a[7]}));
    }
    BARL();
    if (wave == 4 && ctile < 14) {
        float t0 = 0.f, t1 = 0.f;
#pragma unroll
        for (int i = 0; i < 32; i += 2) { t0 += *(const LAS float*)(lds0 + i * 272 + lane * 4); t1 += *(const LAS float*)(lds0 + (i + 1) * 272 + lane * 4); }
        gst8c<0>((u32)(WS_CG + (((xcd & 1) * 14 + ctile) * 64 + lane) * 8), ws, u32x2{pk2(t0 + t1, (t0 + t1) - rbf(t0 + t1)), tlo});
    }
}

__global__ __launch_bounds__(512, 2) void fused_kernel(Params p) {
    const int wave = __builtin_amdgcn_readfirstlane(threadIdx.x >> 6);
    if (blockIdx.x < NHELP) { body_help(p, wave); return; }
    if (threadIdx.x < 32) *(LAS u32*)((u32)(size_t)(LAS char*)smem + L_FLAG + threadIdx.x * 4) = 0u;
    BARL();
    if (wave < 4) body_g(p, wave); else body_c(p, wave - 4);
}

extern "C" void kernel_launch(void* const* d_in, const int* in_sizes, int n_in,
                              void* d_out, int out_size, void* d_ws, size_t ws_size,
                              hipStream_t stream) {
    Params p;
    p.x = (const u16*)d_in[0]; p.k = (const u16*)d_in[1]; p.v = (const u16*)d_in[2]; p.shk = (const u16*)d_in[3];
    p.ssmk = (const u16*)d_in[4]; p.ssmd = (const u16*)d_in[5]; p.w1 = (const u16*)d_in[7]; p.b1 = (const u16*)d_in[8];
    p.w2 = (const u16*)d_in[9]; p.b2 = (const u16*)d_in[10];
    p.out = (u16*)d_out; p.ws = (char*)d_ws;
    (void)hipFuncSetAttribute((const void*)fused_kernel, hipFuncAttributeMaxDynamicSharedMemorySize, LDS_BYTES);
    fused_kernel<<<NWG + NHELP, 512, LDS_BYTES, stream>>>(p);
}
```

```cpp
#define DIAG 0
#define DIAGK 2
#include <hip/hip_runtime.h>
#include <stdint.h>
#include <type_traits>

typedef unsigned short u16;
typedef unsigned int u32;
typedef unsigned long long u64;
typedef u32 u32x4 __attribute__((ext_vector_type(4)));
typedef u32 u32x2 __attribute__((ext_vector_type(2)));
typedef float f32x4 __attribute__((ext_vector_type(4)));
typedef float f32x2 __attribute__((ext_vector_type(2)));
typedef float f32x16 __attribute__((ext_vector_type(16)));
typedef __bf16 bf16x8 __attribute__((ext_vector_type(8)));
typedef __bf16 bf16x2 __attribute__((ext_vector_type(2)));
typedef short s16x4 __attribute__((ext_vector_type(4)));
#define LAS __attribute__((address_space(3)))
#define GAS __attribute__((address_space(1)))
#define DI __device__ __forceinline__

constexpr int S_ = 2048, D_ = 448, F_ = 1792;
constexpr int NHELP = 32, NWG = 224;
constexpr u32 MB = 256;
constexpr u32 WS_EPOCH = 0, WS_ALL = 2048, WS_KVF = 4096, WS_HF = WS_KVF + 65536, WS_YF = WS_HF + 65536, WS_RG = WS_YF + 65536, WS_CG = WS_RG + 16384;
constexpr u32 TP = 4096 + 128;
constexpr u32 WS_H = 1u << 20, WS_KVT = 9u << 20, WS_YT = 11u << 20;
constexpr u32 STAMP_OFF = 16u << 20;
constexpr int NSTAMP = 22;
constexpr u32 L_PIECE = 16384, L_W = 65536, L_LOC0 = 131072, L_MISC = 147456, LDS_BYTES = 163840;
constexpr u32 KVRAW_SZ = 5632, L_KVS = L_LOC0 + 4 * KVRAW_SZ, L_B2 = L_KVS + 9216, L_FLAG = L_B2 + 128, L_ROWF = L_FLAG + 128, L_B2F = L_ROWF + 256, L_R2 = L_B2F + 256;
constexpr u32 L_SCR = L_LOC0 + 2 * KVRAW_SZ + 9728 + 64;
static_assert(L_R2 + 256 <= LDS_BYTES, "lds");
constexpr u32 F_TOEP = L_FLAG, F_HCNT = L_FLAG + 16, F_HRDY = L_FLAG + 20, F_KVRDY = L_FLAG + 24, F_YCNT = L_FLAG + 28, F_YRDY = L_FLAG + 32, F_S0 = L_FLAG + 36, F_KVCNT = L_FLAG + 40, F_W = L_FLAG + 48, F_LAND = L_FLAG + 64, F_CONS = L_FLAG + 96, F_KVS = L_FLAG + 124;
constexpr u32 L_CONV = 16384, CONV_CH = 65536, TOEP_CP = 4160, TOEP_CH = 4 * TOEP_CP, U_SZ = 4608, YS_SZ = 2304;
constexpr u64 TAG_SALT = 0x9E3779B97F4A7C15ull;
constexpr int SPIN_CAP = 1 << 15;
#define SLP 2

#define ST(i) do {} while (0)
#define STP nullptr

DI void dma16(u32 voff, const void* sbase, u32 lds) {
    asm volatile("s_mov_b32 m0, %2\n\ts_nop 0\n\tglobal_load_lds_dwordx4 %0, %1" :: "v"(voff), "s"(sbase), "s"(lds) : "memory");
}
DI void setm0(u32 lds) { asm volatile("s_mov_b32 m0, %0\n\ts_nop 0" :: "s"(lds) : "memory"); }
template <int OFF> DI void dmai(u32 voff, const void* sbase) { asm volatile("global_load_lds_dwordx4 %0, %1 offset:%2" :: "v"(voff), "s"(sbase), "i"(OFF) : "memory"); }
DI void bdma16(u32 voff, u32x4 srd, u32 lds) {
    asm volatile("s_mov_b32 m0, %2\n\ts_nop 0\n\tbuffer_load_dwordx4 %0, %1, 0 offen lds" :: "v"(voff), "s"(srd), "s"(lds) : "memory");
}
#define SB() __builtin_amdgcn_sched_barrier(0)
template <int OFF> DI u32x2 alds8(u32 a) { u32x2 r; asm volatile("ds_read_b64 %0, %1 offset:%2" : "=v"(r) : "v"(a), "i"(OFF)); return r; }
template <int OFF> DI u32x4 alds16(u32 a) { u32x4 r; asm volatile("ds_read_b128 %0, %1 offset:%2" : "=v"(r) : "v"(a), "i"(OFF)); return r; }
DI u32x4 gld16(u32 voff, const void* sbase) { u32x4 r; asm volatile("global_load_dwordx4 %0, %1, %2" : "=v"(r) : "v"(voff), "s"(sbase) : "memory"); return r; }
template <int OFF> DI u32x4 gld16c(u32 voff, const void* sbase) { u32x4 r; asm volatile("global_load_dwordx4 %0, %1, %2 offset:%3 sc1" : "=v"(r) : "v"(voff), "s"(sbase), "i"(OFF) : "memory"); return r; }
DI u32x2 gld8c(u32 voff, const void* sbase) { u32x2 r; asm volatile("global_load_dwordx2 %0, %1, %2 sc1" : "=v"(r) : "v"(voff), "s"(sbase) : "memory"); return r; }
DI void touch(const void* addr, u32 lds) { asm volatile("s_mov_b32 m0, %1\n\ts_nop 0\n\tglobal_load_lds_dword %0, off" :: "v"(addr), "s"(lds) : "memory"); }
DI u32 gld2(u32 voff, const void* sbase) { u32 r; asm volatile("global_load_ushort %0, %1, %2" : "=v"(r) : "v"(voff), "s"(sbase) : "memory"); return r; }
DI u32x4 bld16(u32 voff, u32x4 srd) { u32x4 r; asm volatile("buffer_load_dwordx4 %0, %1, %2, 0 offen" : "=v"(r) : "v"(voff), "s"(srd) : "memory"); return r; }
template <int OFF> DI u32x4 gld16o(u32 voff, const void* sbase) { u32x4 r; asm volatile("global_load_dwordx4 %0, %1, %2 offset:%3" : "=v"(r) : "v"(voff), "s"(sbase), "i"(OFF) : "memory"); return r; }
template <int OFF> DI void gst16c(u32 voff, void* sbase, u32x4 v) { asm volatile("global_store_dwordx4 %0, %1, %2 offset:%3 sc1\n\ts_nop 1" :: "v"(voff), "v"(v), "s"(sbase), "i"(OFF) : "memory"); }
template <int OFF> DI void gst8c(u32 voff, void* sbase, u32x2 v) { asm volatile("global_store_dwordx2 %0, %1, %2 offset:%3 sc1" :: "v"(voff), "v"(v), "s"(sbase), "i"(OFF) : "memory"); }
#define WAITV(n) asm volatile("s_waitcnt vmcnt(%0)" :: "i"(n) : "memory")
#define WAITBAR(n) asm volatile("s_waitcnt vmcnt(%0) lgkmcnt(0)\n\ts_barrier" :: "i"(n) : "memory")
#define BARL() asm volatile("s_waitcnt lgkmcnt(0)\n\ts_barrier" ::: "memory")
#define PIN(x) asm volatile("" : "+v"(x))

DI u32x4 mksrd(const void* p, u32 bytes) { u64 a = (u64)p; return u32x4{(u32)a, (u32)(a >> 32) & 0xffffu, bytes, 0x00020000u}; }
DI float bflo(u32 x) { return __uint_as_float(x << 16); }
DI float bfhi(u32 x) { return __uint_as_float(x & 0xffff0000u); }
DI u32 pk2(float a, float b) { bf16x2 v = {(__bf16)a, (__bf16)b}; return __builtin_bit_cast(u32, v); }
DI float rbf(float a) { return bflo(pk2(a, a)); }
DI float relu(float a) { int i = __float_as_int(a); return __int_as_float(i > 0 ? i : 0); }
DI float fma1(float a, float b, float c) { float r; asm("v_fma_f32 %0, %1, %2, %3" : "=v"(r) : "v"(a), "v"(b), "v"(c)); return r; }
DI float mul1(float a, float b) { float r; asm("v_mul_f32 %0, %1, %2" : "=v"(r) : "v"(a), "v"(b)); return r; }
DI float add1(float a, float b) { float r; asm("v_add_f32 %0, %1, %2" : "=v"(r) : "v"(a), "v"(b)); return r; }
DI float dot2(u32 a, u32 b, float c) { return __builtin_amdgcn_fdot2_f32_bf16(__builtin_bit_cast(bf16x2, a), __builtin_bit_cast(bf16x2, b), c, false); }
DI u32x4 lds16(u32 a) { return *(const LAS u32x4*)a; }
DI u32x2 lds8(u32 a) { return *(const LAS u32x2*)a; }
DI void sts16(u32 a, u32x4 v) { *(LAS u32x4*)a = v; }
DI void sts8(u32 a, u32x2 v) { *(LAS u32x2*)a = v; }
DI u32x2 ldtr(u32 a) { return __builtin_bit_cast(u32x2, __builtin_amdgcn_ds_read_tr16_b64_v4i16((LAS s16x4*)a)); }
DI u32x4 cat(u32x2 a, u32x2 b) { return u32x4{a.x, a.y, b.x, b.y}; }
DI f32x16 mfma32(u32x4 a, u32x4 b, f32x16 c) { return __builtin_amdgcn_mfma_f32_32x32x16_bf16(__builtin_bit_cast(bf16x8, a), __builtin_bit_cast(bf16x8, b), c, 0, 0, 0); }
DI f32x4 mfma16(u32x4 a, u32x4 b, f32x4 c) { return __builtin_amdgcn_mfma_f32_16x16x32_bf16(__builtin_bit_cast(bf16x8, a), __builtin_bit_cast(bf16x8, b), c, 0, 0, 0); }
DI float xhalf_sum(float v) { auto r = __builtin_amdgcn_permlane32_swap(__float_as_uint(v), __float_as_uint(v), false, false); return __uint_as_float(r[0]) + __uint_as_float(r[1]); }

struct Params {
    const u16 *x, *k, *v, *shk, *ssmk, *ssmd, *w1, *b1, *w2, *b2;
    u16* out; char* ws;
};

DI void poll8(u32 voff, const void* sbase, bool need, u32 tlo, u32 thi) {
    _Pragma("clang loop unroll(disable)") for (int spin = 0; spin < SPIN_CAP; ++spin) {
        u32x2 f = gld8c(voff, sbase);
        asm volatile("s_waitcnt vmcnt(0)" : "+v"(f) :: "memory");
        bool ok = !need || (f.x == tlo && f.y == thi);
        if (__all(ok)) break;
        __builtin_amdgcn_s_sleep(SLP);
    }
}
DI void poll16(u32 voff, const void* sbase, u32 tlo, u32 thi) {
    _Pragma("clang loop unroll(disable)") for (int spin = 0; spin < SPIN_CAP; ++spin) {
        u32x4 f = gld16c<0>(voff, sbase);
        asm volatile("s_waitcnt vmcnt(0)" : "+v"(f) :: "memory");
        bool ok = f.x == tlo && f.y == thi && f.z == tlo && f.w == thi;
        if (__all(ok)) break;
        __builtin_amdgcn_s_sleep(SLP);
    }
}

DI bool lds_arrive_last(u32 addr, u32 n) {
    u32 old = 0;
    if ((threadIdx.x & 63) == 0) old = __hip_atomic_fetch_add((LAS u32*)addr, 1u, __ATOMIC_RELAXED, __HIP_MEMORY_SCOPE_WORKGROUP);
    return (u32)__builtin_amdgcn_readfirstlane(old) == n - 1;
}
DI void lds_wait(u32 addr) {
    _Pragma("clang loop unroll(disable)") for (int spin = 0; spin < (1 << 22); ++spin) {
        if (__builtin_amdgcn_readfirstlane(*(volatile LAS u32*)addr)) break;
        __builtin_amdgcn_s_sleep(1);
    }
    asm volatile("" ::: "memory");
}
DI void lds_add(u32 addr) { asm volatile("" ::: "memory"); __hip_atomic_fetch_add((LAS u32*)addr, 1u, __ATOMIC_RELAXED, __HIP_MEMORY_SCOPE_WORKGROUP); asm volatile("" ::: "memory"); }
DI u32 lds_peek(u32 addr) { return *(volatile LAS u32*)addr; }
DI void lds_wait_ge(u32 addr, u32 n, u32 first) {
    u32 v = first;
    _Pragma("clang loop unroll(disable)") for (int spin = 0; spin < (1 << 22) && (u32)__builtin_amdgcn_readfirstlane(v) < n; ++spin) { __builtin_amdgcn_s_sleep(1); v = lds_peek(addr); }
    asm volatile("" ::: "memory");
}
DI void lds_set(u32 addr) { asm volatile("" ::: "memory"); if ((threadIdx.x & 63) == 0) *(volatile LAS u32*)addr = 1u; }
constexpr u32 loc_of(int c) { return (c % 5) == 0 ? L_LOC0 : (c % 5) == 1 ? 3 * L_PIECE : (c % 5) == 2 ? L_W + 3 * L_PIECE : (c % 5) == 3 ? 0u : L_W; }

#define HDV 3
constexpr int HD = HDV, HS = HD + 1;
constexpr int RD = 4, WS_ = RD + 1;
constexpr int g2_younger(int c) {
    const int wneed = c + RD;
    bool sh = false, sw = false; int ah = 0, aw = 0;
    const bool wdone = wneed < 5 + RD || wneed >= 14;
    for (int i = 0; i < HD; ++i) { if (sh) ah += 4; if (i == c) sh = true; }
    for (int i = 0; i < c; ++i) {
        if (i + HD < 14) { if (sh) ah += 4; if (sw) aw += 4; if (i + HD == c) sh = true; }
        if (i >= 1 && i + RD + 4 < 14) { if (sh) ah += 4; if (sw) aw += 4; if (i + RD + 4 == wneed) sw = true; }
    }
    if (wdone) return ah;
    return ah < aw ? ah : aw;
}

struct G2Ctx { const char* w2b; const char* hb; int crot; u32 voffw[4], ldsg, vlane, ta[3][2]; };
template <int C> DI const char* g2_wsrc(const G2Ctx& x) { int c = C + x.crot; if (c >= 14) c -= 14; return x.w2b + (size_t)c * (128 * 896); }
template <int C> DI u32 g2_loc(const G2Ctx& x) { return loc_of(C) + x.ldsg; }
template <int C, int N> DI void g2_w1(const G2Ctx& x, const char* sb) { if constexpr (N == 0) setm0(g2_loc<C>(x)); dmai<N * 1024>(x.voffw[N], sb); }
template <int C> DI void g2_issue_w(const G2Ctx& x) { const char* sb = g2_wsrc<C>(x); g2_w1<C, 0>(x, sb); g2_w1<C, 1>(x, sb); g2_w1<C, 2>(x, sb); g2_w1<C, 3>(x, sb); }
template <int C> DI const char* g2_hsrc(const G2Ctx& x) { int c = C + x.crot; if (c >= 14) c -= 14; return x.hb + c * 8192; }
template <int N> DI u32x4 g2_h1(const G2Ctx& x, const char* sb) { return gld16c<(N & 1) * 1024>(x.vlane + (N >> 1) * 112 * 1024, sb); }
template <int C> DI void g2_issue_h(const G2Ctx& x, u32x4 (&hf)[HS][4]) {
    const char* sb = g2_hsrc<C>(x);
    hf[C % HS][0] = g2_h1<0>(x, sb); hf[C % HS][1] = g2_h1<1>(x, sb); hf[C % HS][2] = g2_h1<2>(x, sb); hf[C % HS][3] = g2_h1<3>(x, sb);
}
template <int C> DI void g2_read(const G2Ctx& x, u32x4 (&wf)[WS_][4]) {
    const u32 a0 = x.ta[loc_of(C) >> 16][0], a1 = x.ta[loc_of(C) >> 16][1];
    constexpr u32 lo = loc_of(C) & 0xffffu;
#pragma unroll
    for (int ks = 0; ks < 2; ++ks) {
        wf[C % WS_][ks * 2] = cat(ldtr(a0 + lo + ks * 2048), ldtr(a0 + lo + ks * 2048 + 1024));
        wf[C % WS_][ks * 2 + 1] = cat(ldtr(a1 + lo + ks * 2048), ldtr(a1 + lo + ks * 2048 + 1024));
    }
}
template <int C, int I> DI void g2_rd(const G2Ctx& x, u32x4 (&wf)[WS_][4]) {
    if constexpr (C < 14) {
        const u32 a = x.ta[loc_of(C) >> 16][I & 1]; constexpr u32 lo = (loc_of(C) & 0xffffu) + (I >> 1) * 2048;
        wf[C % WS_][I] = cat(ldtr(a + lo), ldtr(a + lo + 1024));
    }
}
template <int C> DI void g2_step(const G2Ctx& x, u32x4 (&hf)[HS][4], f32x16 (&acc)[2][2], u32x4 (&wf)[WS_][4], u64* stamps) {
    asm volatile("s_waitcnt vmcnt(%4) lgkmcnt(0)" : "+v"(hf[C % HS][0]), "+v"(hf[C % HS][1]), "+v"(hf[C % HS][2]), "+v"(hf[C % HS][3]) : "i"(g2_younger(C)) : "memory");
    if constexpr (C % 3 == 0) ST(14 + C / 3);
    SB();
    constexpr bool DH = C + HD < 14, DW = C >= 1 && C + RD + 4 < 14;
    const char* hs = g2_hsrc<(DH ? C + HD : 0)>(x);
    const char* wsrc = g2_wsrc<(DW ? C + RD + 4 : 0)>(x);
#define G2M(i) acc[((i) >> 1) & 1][(i) & 1] = mfma32(wf[C % WS_][((i) >> 2) * 2 + (((i) >> 1) & 1)], hf[C % HS][((i) & 1) * 2 + ((i) >> 2)], acc[((i) >> 1) & 1][(i) & 1])
    G2M(0); g2_rd<C + RD, 0>(x, wf); if constexpr (DH) hf[(C + HD) % HS][0] = g2_h1<0>(x, hs); SB();
    G2M(1); g2_rd<C + RD, 1>(x, wf); if constexpr (DH) hf[(C + HD) % HS][1] = g2_h1<1>(x, hs); SB();
    G2M(2); g2_rd<C + RD, 2>(x, wf); if constexpr (DH) hf[(C + HD) % HS][2] = g2_h1<2>(x, hs); SB();
    G2M(3); g2_rd<C + RD, 3>(x, wf); if constexpr (DH) hf[(C + HD) % HS][3] = g2_h1<3>(x, hs); SB();
    G2M(4); if constexpr (DW) g2_w1<C + RD + 4, 0>(x, wsrc); SB();
    G2M(5); if constexpr (DW) g2_w1<C + RD + 4, 1>(x, wsrc); SB();
    G2M(6); if constexpr (DW) g2_w1<C + RD + 4, 2>(x, wsrc); SB();
    G2M(7); if constexpr (DW) g2_w1<C + RD + 4, 3>(x, wsrc); SB();
#undef G2M
    if constexpr (C + 1 < 14) g2_step<C + 1>(x, hf, acc, wf, stamps);
}

template <int NR> struct CF { u32x2 a0[NR], a1[NR]; u32x4 b[NR]; };
template <int S, int R, int NR> DI void cv_ld(CF<NR>& f, u32 aA, u32 aB) {
    if constexpr (R < NR && S < 8) {
        f.a0[R] = alds8<R * 512 + S * 64>(aA); f.a1[R] = alds8<R * 512 + S * 64 + 8>(aA);
        f.b[R] = alds16<(224 - 32 * R + 4 * S) * 16>(aB);
    }
}
template <int NR> DI void cv_wait(CF<NR>& f) {
    if constexpr (NR == 8)
        asm volatile("s_waitcnt lgkmcnt(0)" : "+v"(f.a0[0]), "+v"(f.a0[1]), "+v"(f.a0[2]), "+v"(f.a0[3]), "+v"(f.a0[4]), "+v"(f.a0[5]), "+v"(f.a0[6]), "+v"(f.a0[7]),
                     "+v"(f.a1[0]), "+v"(f.a1[1]), "+v"(f.a1[2]), "+v"(f.a1[3]), "+v"(f.a1[4]), "+v"(f.a1[5]), "+v"(f.a1[6]), "+v"(f.a1[7]),
                     "+v"(f.b[0]), "+v"(f.b[1]), "+v"(f.b[2]), "+v"(f.b[3]), "+v"(f.b[4]), "+v"(f.b[5]), "+v"(f.b[6]), "+v"(f.b[7]));
    else
        asm volatile("s_waitcnt lgkmcnt(0)" : "+v"(f.a0[0]), "+v"(f.a0[1]), "+v"(f.a0[2]), "+v"(f.a0[3]), "+v"(f.a0[4]), "+v"(f.a0[5]),
                     "+v"(f.a1[0]), "+v"(f.a1[1]), "+v"(f.a1[2]), "+v"(f.a1[3]), "+v"(f.a1[4]), "+v"(f.a1[5]),
                     "+v"(f.b[0]), "+v"(f.b[1]), "+v"(f.b[2]), "+v"(f.b[3]), "+v"(f.b[4]), "+v"(f.b[5]));
}
template <int HALF, int M> struct CvIdx {
    static constexpr int q(int i) { return HALF ? (i + 2) : (i < 2 ? i : i + 4); }
    static constexpr int tile() { int m = M; for (int r = 0; r < 8; ++r) for (int i = 3; i >= 0; --i) if (r <= q(i)) { if (m == 0) return i; --m; } return 0; }
    static constexpr int row() { int m = M; for (int r = 0; r < 8; ++r) for (int i = 3; i >= 0; --i) if (r <= q(i)) { if (m == 0) return r; --m; } return 0; }
};
template <int HALF, int S, int M, int NR> DI void cv_mm(CF<NR>& f, CF<NR>& n, u32 aA, u32 aB, f32x4 (&acc)[4]) {
    constexpr int i = CvIdx<HALF, M>::tile(), r = CvIdx<HALF, M>::row(), q = CvIdx<HALF, M>::q(i);
    acc[i] = mfma16(cat(f.a0[r], f.a1[r]), f.b[q - r], acc[i]);
    if constexpr ((M & 1) == 0) cv_ld<S + 1, M / 2, NR>(n, aA, aB);
    SB();
    if constexpr (M + 1 < 18) cv_mm<HALF, S, M + 1, NR>(f, n, aA, aB, acc);
}
template <int HALF, int S, int NR> DI void cv_step(CF<NR>& f, u32 aA, u32 aB, f32x4 (&acc)[4]) {
    cv_wait<NR>(f); SB();
    CF<NR> n;
    cv_mm<HALF, S, 0, NR>(f, n, aA, aB, acc);
    if constexpr (S + 1 < 8) cv_step<HALF, S + 1, NR>(n, aA, aB, acc);
}
template <int HALF> DI void conv_body(u32 aA, u32 aB, f32x4 (&acc)[4]) {
    constexpr int NR = HALF ? 6 : 8;
    CF<NR> f;
    asm volatile("s_waitcnt lgkmcnt(0)" ::: "memory");
    cv_ld<0, 0, NR>(f, aA, aB); cv_ld<0, 1, NR>(f, aA, aB); cv_ld<0, 2, NR>(f, aA, aB); cv_ld<0, 3, NR>(f, aA, aB);
    cv_ld<0, 4, NR>(f, aA, aB); cv_ld<0, 5, NR>(f, aA, aB); cv_ld<0, 6, NR>(f, aA, aB); cv_ld<0, 7, NR>(f, aA, aB);
    cv_step<HALF, 0, NR>(f, aA, aB, acc);
    asm volatile("" ::: "memory");
}

struct Geo { int bi, b, xcd, fh2, RB, CT, tb64, db, rot, crot; u32 R0, F0, T0, D0; };
DI Geo make_geo() {
    Geo g; g.b = blockIdx.x - NHELP; g.xcd = g.b & 7; const int bi = g.b >> 3; g.bi = bi;
    const int rq = g.xcd >> 1, rb = bi / 7, ct = bi - rb * 7;
    g.fh2 = g.xcd & 1; g.RB = 4 * rq + rb; g.CT = 7 * g.fh2 + ct; g.R0 = 128 * g.RB; g.F0 = 128 * g.CT;
    g.tb64 = 2 * g.RB + g.fh2; g.db = ct; g.T0 = 64 * g.tb64; g.D0 = 64 * g.db;
    g.rot = (ct + 2 * rb + rq * 3 + g.fh2) % 7; g.crot = (2 * ct + 7 * (rb & 1) + 3 * (rb >> 1) + rq + 5 * g.fh2) % 14;
    return g;
}
DI void make_g2(G2Ctx& g2, const Params& p, const Geo& q, int g, int lane, u32 lds0) {
    g2.w2b = (const char*)p.w2 + (size_t)(32 * g) * 896 + q.D0 * 2;
    g2.hb = p.ws + WS_H + (size_t)(q.tb64 * 2 * 112 + 2 * g) * 1024;
    g2.crot = q.crot;
#pragma unroll
    for (int n = 0; n < 4; ++n) g2.voffw[n] = (u32)((lane >> 3) * 896 + 16 * ((lane & 7) ^ (((lane >> 4) & 1) << 2)) + n * (8 * 896 - 1024));
    g2.ldsg = lds0 + g * 4096; g2.vlane = lane * 16u;
}
DI u32 slab_base(u32 lds0, int cw) { return lds0 + L_CONV + (cw >> 1) * CONV_CH + (cw & 1) * 8192; }
DI void final_epilogue(const Params& p, const Geo& q, u32 lds0) {
    int tz = threadIdx.x; PIN(tz);
    const int t = tz >> 3, c = tz & 7;
    const u32 pb = lds0 + ((t >> 5) ? 0u : 3 * L_PIECE) + ((t >> 4) & 1) * L_W + (t & 15) * 256;
    const u32 o0 = pb + 16 * ((2 * c) ^ (t & 7)), o1 = o0 ^ 16;
    f32x4 pa[4], pb_[4];
#pragma unroll
    for (int g = 0; g < 4; ++g) { pa[g] = __builtin_bit_cast(f32x4, lds16(o0 + g * 4096)); pb_[g] = __builtin_bit_cast(f32x4, lds16(o1 + g * 4096)); }
    const float r2 = *(const LAS float*)(lds0 + L_R2 + t * 4);
    const f32x4 b0 = __builtin_bit_cast(f32x4, lds16(lds0 + L_B2F + c * 32)), b1 = __builtin_bit_cast(f32x4, lds16(lds0 + L_B2F + c * 32 + 16));
    const u32 sb = slab_base(lds0, c >> 1) + t * 64 + (c & 1) * 32;
    const f32x4 sv0 = __builtin_bit_cast(f32x4, lds16(sb)), sv1 = __builtin_bit_cast(f32x4, lds16(sb + 16));
    const f32x4 xv0 = __builtin_bit_cast(f32x4, lds16(sb + 4096)), xv1 = __builtin_bit_cast(f32x4, lds16(sb + 4096 + 16));
    SB();
    const f32x4 s0 = (pa[0] + pa[1]) + (pa[2] + pa[3]), s1 = (pb_[0] + pb_[1]) + (pb_[2] + pb_[3]);
    f32x4 z0 = s0 * r2 + b0, z1 = s1 * r2 + b1;
    f32x4 e0 = z0 * -1.4426950408889634f, e1 = z1 * -1.4426950408889634f;
#pragma unroll
    for (int j = 0; j < 4; ++j) { e0[j] = __builtin_amdgcn_exp2f(e0[j]); e1[j] = __builtin_amdgcn_exp2f(e1[j]); }
    e0 += 1.f; e1 += 1.f;
#pragma unroll
    for (int j = 0; j < 4; ++j) { e0[j] = __builtin_amdgcn_rcpf(e0[j]); e1[j] = __builtin_amdgcn_rcpf(e1[j]); }
    z0 = z0 * e0; z1 = z1 * e1;
    z0 = z0 * sv0 + xv0; z1 = z1 * sv1 + xv1;
    const u32x4 res = {pk2(z0[0], z0[1]), pk2(z0[2], z0[3]), pk2(z1[0], z1[1]), pk2(z1[2], z1[3])};
    *(GAS u32x4*)((GAS char*)p.out + (size_t)(q.T0 + t) * 896 + q.D0 * 2 + c * 16) = res;
}
#define STAMP_DECL
#define STAMP_OUT(wave) do {} while (0)

extern __shared__ __attribute__((aligned(16))) char smem[];

struct CL { const char *xb, *wb; u32 vx[4], vw[4], sl; int rot; };
struct CSrc { const char *xs, *wsrc; };
template <int ST_> DI CSrc c_src(const CL& c) { int kc = ST_ + c.rot; if (kc >= 7) kc -= 7; return CSrc{c.xb + kc * 128, c.wb + (size_t)kc * 64 * 3584}; }
template <int ST_, int N> DI void c_dma1(const CL& c, const CSrc& s) {
    if constexpr (N == 0) setm0(c.sl + (ST_ & 3) * L_PIECE);
    if constexpr (N == 4) setm0(c.sl + (ST_ & 3) * L_PIECE + L_W);
    if constexpr (N < 4) dmai<N * 1024>(c.vx[N], s.xs); else dmai<(N - 4) * 1024>(c.vw[N - 4], s.wsrc);
}
template <int ST_> DI void c_dma(const CL& c) {
    const CSrc s = c_src<ST_>(c);
    c_dma1<ST_, 0>(c, s); c_dma1<ST_, 1>(c, s); c_dma1<ST_, 2>(c, s); c_dma1<ST_, 3>(c, s);
    c_dma1<ST_, 4>(c, s); c_dma1<ST_, 5>(c, s); c_dma1<ST_, 6>(c, s); c_dma1<ST_, 7>(c, s);
}
DI void make_cl(CL& c, const Params& p, const Geo& q, int cw, int lane, u32 lds0) {
    const int r8 = lane >> 3, c8 = lane & 7, r4 = lane >> 4, c16 = lane & 15;
#pragma unroll
    for (int n = 0; n < 4; ++n) {
        c.vx[n] = (u32)((32 * cw + r8) * 896 + 16 * (c8 ^ (((n & 1) << 2) | (r8 >> 1))) + n * (8 * 896 - 1024));
        c.vw[n] = (u32)((16 * cw + r4) * 3584 + 16 * (c16 ^ (r4 << 2)) + n * (4 * 3584 - 1024));
    }
    c.xb = (const char*)p.x + (size_t)q.R0 * 896; c.wb = (const char*)p.w1 + q.F0 * 2;
    c.sl = lds0 + cw * 4096; c.rot = q.rot;
}

template <int ST_> DI void l_dma(const CL& c) {
    const CSrc s = c_src<ST_>(c);
    const u32 sl = c.sl + (ST_ & 3) * L_PIECE;
    setm0(sl); dmai<0>(c.vx[0], s.xs); dmai<1024>(c.vx[1], s.xs); dmai<2048>(c.vx[2], s.xs); dmai<3072>(c.vx[3], s.xs);
    setm0(sl + L_W); dmai<0>(c.vw[0], s.wsrc); dmai<1024>(c.vw[1], s.wsrc); dmai<2048>(c.vw[2], s.wsrc); dmai<3072>(c.vw[3], s.wsrc);
    const char* x2 = s.xs + 32 * 896; const char* w2 = s.wsrc + 16 * 3584;
    setm0(sl + 4096); dmai<0>(c.vx[0], x2); dmai<1024>(c.vx[1], x2); dmai<2048>(c.vx[2], x2); dmai<3072>(c.vx[3], x2);
    setm0(sl + L_W + 4096); dmai<0>(c.vw[0], w2); dmai<1024>(c.vw[1], w2); dmai<2048>(c.vw[2], w2); dmai<3072>(c.vw[3], w2);
}

struct Fr { u32x4 x[4][2], w[4][2]; };
struct GA { u32 ax[4], aw[2]; };
template <int ST_> DI void g1_read(const GA& a, Fr& f) {
    constexpr u32 so = (ST_ & 3) * L_PIECE;
#pragma unroll
    for (int k = 0; k < 4; ++k) {
        f.w[k][0] = cat(ldtr(a.aw[0] + so + k * 4096), ldtr(a.aw[0] + so + k * 4096 + 1024));
        f.x[k][0] = lds16(a.ax[k] + so);
        f.w[k][1] = cat(ldtr(a.aw[1] + so + k * 4096), ldtr(a.aw[1] + so + k * 4096 + 1024));
        f.x[k][1] = lds16(a.ax[k] + so + 4096);
    }
}
template <int K0> DI void g1_mma(const Fr& f, f32x16 (&acc)[2][2]) {
#pragma unroll
    for (int k = K0; k < K0 + 2; ++k) {
        acc[0][0] = mfma32(f.w[k][0], f.x[k][0], acc[0][0]); acc[1][0] = mfma32(f.w[k][1], f.x[k][0], acc[1][0]);
        acc[0][1] = mfma32(f.w[k][0], f.x[k][1], acc[0][1]); acc[1][1] = mfma32(f.w[k][1], f.x[k][1], acc[1][1]);
    }
    SB();
}

template <int ST_, int I> DI void g1_rd(const GA& a, Fr& f) {
    constexpr u32 so = (ST_ & 3) * L_PIECE; constexpr int k = I >> 1, h = I & 1;
    f.w[k][h] = cat(ldtr(a.aw[h] + so + k * 4096), ldtr(a.aw[h] + so + k * 4096 + 1024));
    f.x[k][h] = lds16(a.ax[k] + so + h * 4096);
}
template <int K0, int I> DI void g1_m1(const Fr& f, f32x16 (&acc)[2][2]) {
    constexpr int k = K0 + (I >> 2), mt = I & 1, st = (I >> 1) & 1;
    acc[mt][st] = mfma32(f.w[k][mt], f.x[k][st], acc[mt][st]);
}
template <int ST_, int I = 0> DI void g1_carry(const GA& a, const Fr& fp, Fr& fn, f32x16 (&acc)[2][2]) {
    g1_m1<2, I>(fp, acc); g1_rd<ST_, I>(a, fn); SB();
    if constexpr (I + 1 < 8) g1_carry<ST_, I + 1>(a, fp, fn, acc);
}

DI void body_g(const Params& p, const int g) {
    STAMP_DECL
    const u64 dg0 = __builtin_amdgcn_s_memrealtime(); u64 dg1 = dg0;
    const u32 lds0 = (u32)(size_t)(LAS char*)smem;
    const int lane = threadIdx.x & 63;
    const Geo q = make_geo();
    char* ws = p.ws;
    const int hi = lane >> 5, s31 = lane & 31;
    const int r8 = lane >> 3, c8 = lane & 7;
    const u32 raw = lds0 + L_LOC0 + g * KVRAW_SZ;
    {
        const u32 vkv = (u32)(r8 * 896 + c8 * 16);
        bdma16((u32)(((int)q.T0 + 16 * g - 8) * 896 + (int)q.D0 * 2) + vkv, mksrd(p.k, S_ * D_ * 2), raw);
        const char* kb = (const char*)p.k + (size_t)(q.T0 + 16 * g) * 896 + q.D0 * 2;
        const char* vb = (const char*)p.v + (size_t)(q.T0 + 16 * g) * 896 + q.D0 * 2;
        dmai<1024>(vkv, kb - 1024); dmai<2048>(vkv, kb + 8 * 896 - 2048); dmai<3072>(vkv, vb - 3072);
        setm0(raw + 4096);
        dmai<0>(vkv, vb + 8 * 896);
        if (lane < 32) dmai<1024>(lane * 16u, (const char*)p.shk + q.D0 * 8 - 1024);
        CL c; make_cl(c, p, q, g, lane, lds0);
        c_dma<0>(c);
    }
    u32x2 ep = gld8c(0u, ws + WS_EPOCH);
    const int fh = g & 1, sh = g >> 1;
    GA ga;
    {
        const int blk = (lane >> 4) & 1, qq = (lane & 15) >> 2, pp = lane & 3;
        const u32 Z = (u32)(hi ^ ((s31 >> 1) & 7));
#pragma unroll
        for (int k = 0; k < 4; ++k) ga.ax[k] = lds0 + (64 * sh + s31) * 128 + 16 * ((2 * k) ^ Z);
        ga.aw[0] = lds0 + L_W + (8 * hi + qq) * 256 + 16 * ((8 * fh + 2 * blk + (pp >> 1)) ^ (qq << 2)) + 8 * (pp & 1);
        ga.aw[1] = ga.aw[0] ^ 64;
    }
    WAITV(9);
    ST(1);
    {
        u32x4 wr[4], kr[5], vr[2];
#pragma unroll
        for (int e = 0; e < 4; ++e) wr[e] = lds16(raw + 5120 + c8 * 64 + e * 16);
#pragma unroll
        for (int j = 0; j < 5; ++j) kr[j] = lds16(raw + (5 + 2 * r8 + j) * 128 + c8 * 16);
        vr[0] = lds16(raw + 3072 + 2 * r8 * 128 + c8 * 16); vr[1] = lds16(raw + 3072 + (2 * r8 + 1) * 128 + c8 * 16);
        SB();
        u32x4 o0, o1;
#pragma unroll
        for (int e = 0; e < 4; ++e) {
            const u32x4 w = wr[e];
            const f32x2 t0 = {bflo(w.x), bflo(w.z)}, t1 = {bfhi(w.x), bfhi(w.z)}, t2 = {bflo(w.y), bflo(w.w)}, t3 = {bfhi(w.y), bfhi(w.w)};
            f32x2 k[5];
#pragma unroll
            for (int j = 0; j < 5; ++j) k[j] = f32x2{bflo(kr[j][e]), bfhi(kr[j][e])};
            f32x2 a = k[0] * t3, b = k[1] * t3;
            a = __builtin_elementwise_fma(k[1], t2, a); b = __builtin_elementwise_fma(k[2], t2, b);
            a = __builtin_elementwise_fma(k[2], t1, a); b = __builtin_elementwise_fma(k[3], t1, b);
            a = __builtin_elementwise_fma(k[3], t0, a); b = __builtin_elementwise_fma(k[4], t0, b);
            a *= f32x2{bflo(vr[0][e]), bfhi(vr[0][e])}; b *= f32x2{bflo(vr[1][e]), bfhi(vr[1][e])};
            o0[e] = pk2(a.x, a.y); o1[e] = pk2(b.x, b.y);
        }
        sts16(lds0 + L_KVS + (16 * g + 2 * r8) * 144 + c8 * 16, o0);
        sts16(lds0 + L_KVS + (16 * g + 2 * r8 + 1) * 144 + c8 * 16, o1);
        asm volatile("s_waitcnt lgkmcnt(0)" ::: "memory");
        lds_add(lds0 + F_KVS);
    }
    f32x16 acc[2][2] = {};
    Fr fa, fb;
    WAITV(1);
    lds_add(lds0 + F_LAND);
    lds_wait_ge(lds0 + F_LAND, 256, lds_peek(lds0 + F_LAND));
    ST(2);
    if (DIAG == 1) dg1 = __builtin_amdgcn_s_memrealtime();
    u32 pk;
#define G1_STAGE(T, FP, FN) do { lds_wait_ge(lds0 + F_LAND + 4 * (T), (T) == 3 ? 128 : 64, pk); g1_carry<T>(ga, FP, FN, acc); pk = lds_peek(lds0 + F_LAND + 4 * (T) + 4); SB(); \
        g1_mma<0>(FN, acc); asm volatile("s_waitcnt lgkmcnt(0)" ::: "memory"); lds_add(lds0 + F_CONS + 4 * (T)); SB(); } while (0)
    g1_read<0>(ga, fa); pk = lds_peek(lds0 + F_LAND + 4); SB();
    g1_mma<0>(fa, acc); asm volatile("s_waitcnt lgkmcnt(0)" ::: "memory"); lds_add(lds0 + F_CONS); SB();
    ST(19);
    G1_STAGE(1, fa, fb);
    ST(20);
    G1_STAGE(2, fb, fa);
    asm volatile("s_waitcnt vmcnt(0)" : "+v"(ep) :: "memory");
    ST(3);
    const u64 epoch = ((u64)(u32)__builtin_amdgcn_readfirstlane(ep.y) << 32) | (u32)__builtin_amdgcn_readfirstlane(ep.x);
    const u64 tag = epoch + TAG_SALT;
    const u32 tlo = (u32)tag, thi = (u32)(tag >> 32);
    G1_STAGE(3, fa, fb);
    G1_STAGE(4, fb, fa);
    ST(21);
    const u32 rgo = WS_RG + (q.R0 + 64 * sh + lane) * 8, cgo = WS_CG + (q.F0 + 64 * fh + lane) * 8;
    u32x2 rg = gld8c(rgo, ws), cg = gld8c(cgo, ws);
    u32 b1v = gld2((u32)((64 * fh + lane) * 2), (const char*)p.b1 + q.F0 * 2);
    G1_STAGE(5, fa, fb);
    ST(4);
    G1_STAGE(6, fb, fa);
    ST(5);
    asm volatile("s_waitcnt vmcnt(0)" : "+v"(rg), "+v"(cg), "+v"(b1v) :: "memory");
    _Pragma("clang loop unroll(disable)") for (int spin = 0; spin < SPIN_CAP && !__all(rg.y == tlo && cg.y == tlo); ++spin) {
        rg = gld8c(rgo, ws); cg = gld8c(cgo, ws);
        asm volatile("s_waitcnt vmcnt(0)" : "+v"(rg), "+v"(cg) :: "memory");
    }
    if (fh == 0 && sh == q.fh2) *(LAS u32*)(lds0 + L_ROWF + lane * 4) = rg.x;
    g1_mma<2>(fa, acc);
    {
        u32x4 ax[2], bx[2];
        const auto rr = __builtin_amdgcn_permlane32_swap(rg.x, rg.x, false, false);
        const auto cc = __builtin_amdgcn_permlane32_swap(cg.x, cg.x, false, false);
        const auto bb = __builtin_amdgcn_permlane32_swap(b1v, b1v, false, false);
#pragma unroll
        for (int i = 0; i < 2; ++i) {
            const u32 nm = __builtin_amdgcn_perm(rr[i], rr[i], 0x01000100u) ^ 0x80008000u;
            ax[i] = u32x4{cc[i], bb[i], 0u, 0u};
            bx[i] = u32x4{hi ? 0u : nm, hi ? 0u : (rr[i] >> 16), 0u, 0u};
        }
#pragma unroll
        for (int st = 0; st < 2; ++st)
#pragma unroll
            for (int mt = 0; mt < 2; ++mt) acc[mt][st] = mfma32(ax[mt], bx[st], acc[mt][st]);
#pragma unroll
        for (int st = 0; st < 2; ++st) {
            char* hb = ws + WS_H + (size_t)((4 * q.RB + 2 * sh + st) * 112 + q.CT * 8 + fh * 4) * 1024;
#pragma unroll
            for (int mt = 0; mt < 2; ++mt)
#pragma unroll
                for (int t = 0; t < 2; ++t) {
                    u32x4 hv;
#pragma unroll
                    for (int j = 0; j < 4; ++j) {
                        f32x2 a = {acc[mt][st][8 * t + 2 * j], acc[mt][st][8 * t + 2 * j + 1]};
                        f32x2 u = {relu(a.x), relu(a.y)};
                        u = u * u;
                        hv[j] = pk2(u.x, u.y);
                    }
                    if (mt == 0 && t == 0) gst16c<0>(lane * 16u, hb, hv);
                    if (mt == 0 && t == 1) gst16c<1024>(lane * 16u, hb, hv);
                    if (mt == 1 && t == 0) gst16c<2048>(lane * 16u, hb, hv);
                    if (mt == 1 && t == 1) gst16c<3072>(lane * 16u, hb, hv);
                    SB();
                }
        }
    }
    ST(6);
    if (DIAG == 2) dg1 = __builtin_amdgcn_s_memrealtime();
    G2Ctx g2; make_g2(g2, p, q, g, lane, lds0);
    {
        const int blk = (lane >> 4) & 1, qq = (lane & 15) >> 2, pp = lane & 3;
        const u32 t0 = g2.ldsg + (4 * hi + qq) * 128 + 16 * ((2 * blk + (pp >> 1)) ^ ((qq >> 1) << 2)) + 8 * (pp & 1);
#pragma unroll
        for (int i = 0; i < 3; ++i) { g2.ta[i][0] = t0 + i * 65536; g2.ta[i][1] = (t0 ^ 64) + i * 65536; PIN(g2.ta[i][0]); PIN(g2.ta[i][1]); }
    }
    u32x4 wf[WS_][4];
    lds_wait_ge(lds0 + F_CONS + 16, 256, lds_peek(lds0 + F_CONS + 16));
    lds_wait(lds0 + F_W + (g >> 1) * 4);
    g2_read<0>(g2, wf); g2_read<1>(g2, wf);
    asm volatile("s_waitcnt vmcnt(0) lgkmcnt(0)" ::: "memory"); ST(9);
    if (lds_arrive_last(lds0 + F_HCNT, 4)) { if (lane < 15) gst8c<0>(lane < 14 ? WS_HF + (q.RB * 14 + lane) * MB + q.CT * 8 : WS_ALL + q.b * 8, ws, u32x2{tlo, thi}); }
    {
        const u32 po = WS_HF + (q.RB * 14 + q.CT) * MB + (lane < 14 ? lane : 0) * 8;
        g2_issue_w<2>(g2); g2_issue_w<3>(g2);
        u32x2 f = {0u, 0u};
        if (g == 0) f = gld8c(po, ws);
        g2_issue_w<4>(g2); g2_issue_w<5>(g2); g2_issue_w<6>(g2);
        asm volatile("s_waitcnt vmcnt(12)" : "+v"(f) :: "memory");
        bool rdy = __all(f.x == tlo && f.y == thi);
        if (g == 0 && rdy) lds_set(lds0 + F_HRDY);
        g2_read<2>(g2, wf); g2_read<3>(g2, wf);
        asm volatile("s_waitcnt lgkmcnt(0)" ::: "memory");
        if (g == 0 && !rdy) f = gld8c(po, ws);
        g2_issue_w<7>(g2); g2_issue_w<8>(g2);
        if (g == 0) {
            if (!rdy) {
                asm volatile("s_waitcnt vmcnt(8)" : "+v"(f) :: "memory");
                if (!__all(f.x == tlo && f.y == thi)) poll8(po, ws, true, tlo, thi);
                lds_set(lds0 + F_HRDY);
            }
        } else lds_wait(lds0 + F_HRDY);
    }
    ST(7);
    {
        u32x4 hf[HS][4];
        g2_issue_h<0>(g2, hf); g2_issue_h<1>(g2, hf); if constexpr (HD > 2) g2_issue_h<2 % HS>(g2, hf); if constexpr (HD > 3) g2_issue_h<3 % HS>(g2, hf); if constexpr (HD > 4) g2_issue_h<4 % HS>(g2, hf);
        f32x16 a2[2][2] = {};
        g2_step<0>(g2, hf, a2, wf, STP);
        ST(8);
        const u32 pw = g2.ldsg + (s31 >> 4) * L_W + (s31 & 15) * 256;
#pragma unroll
        for (int s2 = 0; s2 < 2; ++s2)
#pragma unroll
            for (int dt = 0; dt < 2; ++dt)
#pragma unroll
                for (int gr = 0; gr < 4; ++gr) {
                    const u32 a = pw + (s2 ? 0u : 3 * L_PIECE) + 16 * ((8 * dt + 2 * gr + hi) ^ (s31 & 7));
                    sts16(a, __builtin_bit_cast(u32x4, f32x4{a2[dt][s2][4 * gr], a2[dt][s2][4 * gr + 1], a2[dt][s2][4 * gr + 2], a2[dt][s2][4 * gr + 3]}));
                }
    }
    BARL(); ST(10);
    final_epilogue(p, q, lds0);
    if (DIAG == 3) dg1 = __builtin_amdgcn_s_memrealtime();
    if (q.b == 100 && g == 0) { const u64 e = __builtin_amdgcn_s_memrealtime() + DIAGK * (dg1 - dg0); while (__builtin_amdgcn_s_memrealtime() < e) __builtin_amdgcn_s_sleep(1); }
    STAMP_OUT(g);
}

DI void body_c(const Params& p, const int cw) {
    STAMP_DECL
    const u32 lds0 = (u32)(size_t)(LAS char*)smem;
    const int lane = threadIdx.x & 63;
    const Geo q = make_geo();
    char* ws = p.ws;
    const int hi = lane >> 5, s31 = lane & 31;
    const int half = cw & 1, chl = cw >> 1, chan = 2 * q.b + chl;
    u32x2 ep;
    if (cw < 2) {
        CL c, c2; make_cl(c, p, q, 0, lane, lds0); c2 = c; c2.xb += 64 * 896; c2.wb += 32 * 3584; c2.sl += 8192;
        if (cw == 0) {
            l_dma<1>(c); l_dma<1>(c2); ST(1);
            WAITV(0); lds_add(lds0 + F_LAND + 4); ST(2);
            lds_wait_ge(lds0 + F_CONS, 256, lds_peek(lds0 + F_CONS)); l_dma<4>(c); l_dma<4>(c2); ST(19);
            WAITV(0); lds_add(lds0 + F_LAND + 16); ST(20);
            lds_wait_ge(lds0 + F_CONS + 8, 256, lds_peek(lds0 + F_CONS + 8)); l_dma<6>(c); l_dma<6>(c2); ST(3);
            ep = gld8c(0u, ws + WS_EPOCH);
            WAITV(1); lds_add(lds0 + F_LAND + 24); ST(21);
        } else {
            l_dma<2>(c); l_dma<2>(c2); ST(1);
            WAITV(0); lds_add(lds0 + F_LAND + 8); ST(2);
            lds_wait_ge(lds0 + F_CONS + 4, 256, lds_peek(lds0 + F_CONS + 4)); l_dma<5>(c); l_dma<5>(c2); ST(19);
            ep = gld8c(0u, ws + WS_EPOCH);
            WAITV(1); lds_add(lds0 + F_LAND + 20); ST(20);
        }
        lds_wait_ge(lds0 + F_CONS + 12, 256, lds_peek(lds0 + F_CONS + 12));
#pragma unroll
        for (int i = 0; i < 2; ++i) { G2Ctx g2; make_g2(g2, p, q, 2 * cw + i, lane, lds0); g2_issue_w<0>(g2); g2_issue_w<1>(g2); }
        asm volatile("s_waitcnt vmcnt(0)" : "+v"(ep) :: "memory");
        lds_set(lds0 + F_W + cw * 4);
    } else {
        const int kw = cw - 2;
        if (kw == 0 && lane < 8) dma16(lane * 16u, (const char*)p.b2 + q.D0 * 2, lds0 + L_B2);
        ep = gld8c(0u, ws + WS_EPOCH);
        { CL c; make_cl(c, p, q, 2 * kw, lane, lds0); l_dma<3>(c); }
        lds_wait_ge(lds0 + F_KVS, 256, lds_peek(lds0 + F_KVS));
        ST(2);
        {
            const int grp = lane >> 4, qq = (lane & 15) >> 2, pp = lane & 3;
            const u32 a = lds0 + L_KVS + (16 * grp + 8 * (pp >> 1) + 7 - qq) * 144 + kw * 64 + (pp & 1) * 8;
            const u32 vo = (u32)((q.D0 + 32 * kw + (lane & 7)) * TP + (2040 - (int)q.T0 - 8 * (lane >> 3)) * 2);
            u32x4 tr[4];
#pragma unroll
            for (int i = 0; i < 4; ++i) tr[i] = cat(ldtr(a + 16 * i), ldtr(a - 4 * 144 + 16 * i));
            SB();
#pragma unroll
            for (int i = 0; i < 4; ++i) gst16c<0>(vo, ws + WS_KVT + i * 8 * TP, tr[i]);
        }
        ST(20);
        u32x4 sk[5], uu[4];
        const u32x4 sksrd = mksrd((const char*)p.ssmk + (size_t)(2 * q.b + kw) * 4096, 4096);
#pragma unroll
        for (int r = 0; r < 5; ++r) sk[r] = bld16((u32)((lane - 1 + 63 * r) * 16), sksrd);
        ST(3);
        asm volatile("s_waitcnt vmcnt(9)" : "+v"(ep) :: "memory");
        lds_add(lds0 + F_LAND + 12);
        WAITV(5);
        ST(19);
        const u64 tg = (((u64)ep.y << 32) | ep.x) + TAG_SALT;
        if (lds_arrive_last(lds0 + F_KVCNT, 2)) {
            if (lane < 32) gst8c<0>(WS_KVF + (q.db * 32 + lane) * MB + q.tb64 * 8, ws, u32x2{(u32)tg, (u32)(tg >> 32)});
        }
        asm volatile("s_waitcnt vmcnt(0)" : "+v"(sk[0]), "+v"(sk[1]), "+v"(sk[2]), "+v"(sk[3]), "+v"(sk[4]) :: "memory");
        u32x4 tw[5][4];
#pragma unroll
        for (int r = 0; r < 5; ++r) {
            const u32x4 B = sk[r];
            u32x4 A;
#pragma unroll
            for (int j = 0; j < 4; ++j) A[j] = __builtin_amdgcn_update_dpp(0u, B[j], 0x138  , 0xf, 0xf, false);
            const u32 e0 = __builtin_amdgcn_alignbit(A.y, A.x, 16), e1 = __builtin_amdgcn_alignbit(A.z, A.y, 16), e2 = __builtin_amdgcn_alignbit(A.w, A.z, 16),
                      e3 = __builtin_amdgcn_alignbit(B.x, A.w, 16), e4 = __builtin_amdgcn_alignbit(B.y, B.x, 16);
            tw[r][0] = u32x4{e0, e1, e2, e3}; tw[r][1] = u32x4{A.y, A.z, A.w, B.x}; tw[r][2] = u32x4{e1, e2, e3, e4}; tw[r][3] = u32x4{A.z, A.w, B.x, B.y};
            PIN(tw[r][0]); PIN(tw[r][1]); PIN(tw[r][2]); PIN(tw[r][3]);
        }
        if (kw == 0) { poll8(WS_KVF + q.b * MB + s31 * 8, ws, true, (u32)tg, (u32)(tg >> 32)); lds_set(lds0 + F_KVRDY); } else lds_wait(lds0 + F_KVRDY);
        ST(7);
        const char* kb2 = ws + WS_KVT + (size_t)(2 * q.b + kw) * TP;
        uu[0] = gld16c<0>(lane * 16u, kb2); uu[1] = gld16c<1024>(lane * 16u, kb2); uu[2] = gld16c<2048>(lane * 16u, kb2); uu[3] = gld16c<3072>(lane * 16u, kb2);
        lds_wait_ge(lds0 + F_CONS + 20, 256, lds_peek(lds0 + F_CONS + 20)); lds_wait_ge(lds0 + F_CONS + 24, 256, lds_peek(lds0 + F_CONS + 24));
        ST(5);
        const u32 tq = lds0 + L_CONV + kw * CONV_CH + lane * 16;
#pragma unroll
        for (int r = 0; r < 5; ++r) {
            if ((lane > 0 || r == 0) && (r < 4 || lane < 6)) {
#pragma unroll
                for (int c = 0; c < 4; ++c) sts16(tq + r * 1008 + c * TOEP_CP, tw[r][c]);
            }
        }
        if (lane < 32) sts16(tq + TOEP_CH + 4096, u32x4{0u, 0u, 0u, 0u});
        asm volatile("s_waitcnt vmcnt(0)" : "+v"(uu[0]), "+v"(uu[1]), "+v"(uu[2]), "+v"(uu[3]) :: "memory");
        ST(14);
        sts16(tq + TOEP_CH, uu[0]); sts16(tq + TOEP_CH + 1024, uu[1]); sts16(tq + TOEP_CH + 2048, uu[2]); sts16(tq + TOEP_CH + 3072, uu[3]);
        asm volatile("s_waitcnt lgkmcnt(0)" ::: "memory");
        lds_set(lds0 + F_TOEP + kw * 4);
    }
    u32x4 xa = gld16((u32)(lane * 896), (const char*)p.x + (size_t)q.T0 * 896 + (q.D0 + 16 * cw) * 2);
    u32x4 xb_ = gld16((u32)(lane * 896 + 16), (const char*)p.x + (size_t)q.T0 * 896 + (q.D0 + 16 * cw) * 2);
    u32 dv = gld2(0u, (const char*)p.ssmd + chan * 2);
    const u64 epoch = ((u64)(u32)__builtin_amdgcn_readfirstlane(ep.y) << 32) | (u32)__builtin_amdgcn_readfirstlane(ep.x);
    const u64 tag = epoch + TAG_SALT;
    const u32 tlo = (u32)tag, thi = (u32)(tag >> 32);
    const u32 tbase = lds0 + L_CONV + chl * CONV_CH;
    const u32 ub = tbase + TOEP_CH;
    ST(6);
    lds_wait(lds0 + F_TOEP + chl * 4);
    ST(15);
    const int bb = lane & 15, gg = lane >> 4;
    const u32 aA = tbase + (bb & 3) * TOEP_CP + ((bb >> 2) + 2 * gg) * 8;
    const u32 aB = ub + (30 - 2 * bb + gg) * 16;
    f32x4 acc[4] = {};
    if (half == 0) conv_body<0>(aA, aB, acc); else conv_body<1>(aA, aB, acc);
    ST(16);
    asm volatile("s_waitcnt vmcnt(0)" : "+v"(xa), "+v"(xb_), "+v"(dv) :: "memory");
    ST(8);
    {
        const float dd = bflo(dv);
        char* yb = ws + WS_YT + (size_t)chan * TP;
        u32x2 uq[4];
#pragma unroll
        for (int i = 0; i < 4; ++i) uq[i] = lds8(ub + (2044 - (256 * (half ? (i + 2) : (i < 2 ? i : i + 4)) + 16 * bb + 4 * gg)) * 2);
        SB();
#pragma unroll
        for (int i = 0; i < 4; ++i) {
            const int qt = half ? (i + 2) : (i < 2 ? i : i + 4);
            const int t = 256 * qt + 16 * bb + 4 * gg;
            const u32x2 uu = uq[i];
            const u32 c01 = pk2(acc[i][0], acc[i][1]), c23 = pk2(acc[i][2], acc[i][3]);
            float y0 = fma1(bfhi(uu.y), dd, bflo(c01)), y1 = fma1(bflo(uu.y), dd, bfhi(c01));
            float y2 = fma1(bfhi(uu.x), dd, bflo(c23)), y3 = fma1(bflo(uu.x), dd, bfhi(c23));
            gst8c<0>((u32)(t * 2), yb, u32x2{pk2(y0, y1), pk2(y2, y3)});
        }
        WAITV(0);
        if (lds_arrive_last(lds0 + F_YCNT, 4)) { if (lane < 32) gst8c<0>(WS_YF + (lane * 7 + (q.b >> 5)) * MB + (q.b & 31) * 8, ws, u32x2{tlo, thi}); }
    }
    _Pragma("clang loop unroll(disable)") for (int spin = 0; spin < (1 << 22); ++spin) { if (__builtin_amdgcn_readfirstlane(*(volatile LAS u32*)(lds0 + F_YCNT)) >= 4) break; __builtin_amdgcn_s_sleep(1); }
    asm volatile("" ::: "memory");
    const u32 slab = slab_base(lds0, cw) + lane * 64;
    f32x4 xn[4];
    {
        _Pragma("clang loop unroll(disable)") for (int spin = 0; spin < (1 << 22); ++spin) { if (__builtin_amdgcn_readfirstlane(*(volatile LAS u32*)(lds0 + F_HCNT)) >= 4) break; __builtin_amdgcn_s_sleep(1); }
        asm volatile("" ::: "memory");
        const u32 rf = *(const LAS u32*)(lds0 + L_ROWF + lane * 4);
        const float m = bflo(rf), ri = __builtin_amdgcn_rcpf(bfhi(rf)), nmri = -m * ri;
        const u32 xx[8] = {xa.x, xa.y, xa.z, xa.w, xb_.x, xb_.y, xb_.z, xb_.w};
#pragma unroll
        for (int j = 0; j < 4; ++j) {
            f32x4 v = {bflo(xx[2 * j]), bfhi(xx[2 * j]), bflo(xx[2 * j + 1]), bfhi(xx[2 * j + 1])};
            sts16(slab + 4096 + j * 16, __builtin_bit_cast(u32x4, v));
            xn[j] = f32x4{fma1(v[0], ri, nmri), fma1(v[1], ri, nmri), fma1(v[2], ri, nmri), fma1(v[3], ri, nmri)};
        }
        if (cw == 0) *(LAS float*)(lds0 + L_R2 + lane * 4) = ri * ri;
        if (cw == 1) *(LAS float*)(lds0 + L_B2F + lane * 4) = bflo(*(const LAS u16*)(lds0 + L_B2 + lane * 2));
    }
    if (q.b == 0 && cw == 3) {
        _Pragma("clang loop unroll(disable)") for (int spin = 0; spin < SPIN_CAP; ++spin) {
            u32x4 f0 = gld16c<0>(lane * 16u, ws + WS_ALL), f1 = gld16c<1024>((lane < 48 ? lane : 0) * 16u, ws + WS_ALL);
            asm volatile("s_waitcnt vmcnt(0)" : "+v"(f0), "+v"(f1) :: "memory");
            bool ok = f0.x == tlo && f0.y == thi && f0.z == tlo && f0.w == thi && f1.x == tlo && f1.y == thi && f1.z == tlo && f1.w == thi;
            if (__all(ok)) break;
            __builtin_amdgcn_s_sleep(SLP);
        }
        if (lane == 0) { const u64 e1 = epoch + 1; gst8c<0>(0u, ws + WS_EPOCH, u32x2{(u32)e1, (u32)(e1 >> 32)}); }
    }
    if (cw == 0) { poll8(WS_YF + (q.tb64 * 7 + q.db) * MB + s31 * 8, ws, true, tlo, thi); lds_set(lds0 + F_YRDY); }
    else lds_wait(lds0 + F_YRDY);
    ST(9);
    {
        const char* yb = ws + WS_YT + (size_t)(q.D0 + 16 * cw) * TP + q.T0 * 2;
        const u32 vo = (u32)((lane >> 3) * TP + (lane & 7) * 16);
        u32x4 y0 = gld16c<0>(vo, yb), y1 = gld16c<0>(vo, yb + 8 * TP);
        asm volatile("s_waitcnt vmcnt(0)" : "+v"(y0), "+v"(y1) :: "memory");
        const u32 ys = tbase + TOEP_CH + 2 * U_SZ + half * YS_SZ;
        sts16(ys + (lane >> 3) * 144 + (lane & 7) * 16, y0);
        sts16(ys + (8 + (lane >> 3)) * 144 + (lane & 7) * 16, y1);
        const int qq = (lane & 15) >> 2, pp = lane & 3;
        const u32 a = ys + qq * 144 + gg * 32 + pp * 8;
        u32x2 t0 = ldtr(a), t1 = ldtr(a + 4 * 144), t2 = ldtr(a + 8 * 144), t3 = ldtr(a + 12 * 144);
        const u32 yy[8] = {t0.x, t0.y, t1.x, t1.y, t2.x, t2.y, t3.x, t3.y};
#pragma unroll
        for (int j = 0; j < 4; ++j) {
            f32x4 v = {bflo(yy[2 * j]), bfhi(yy[2 * j]), bflo(yy[2 * j + 1]), bfhi(yy[2 * j + 1])};
            sts16(slab + ((j ^ (lane & 3)) * 0 + j) * 16, __builtin_bit_cast(u32x4, f32x4{mul1(v[0], xn[j][0]), mul1(v[1], xn[j][1]), mul1(v[2], xn[j][2]), mul1(v[3], xn[j][3])}));
        }
    }
    BARL(); ST(10);
    final_epilogue(p, q, lds0);
    STAMP_OUT(cw + 4);
}

DI float dpp_xor_sum8(float v) {
    v += __uint_as_float(__builtin_amdgcn_mov_dpp(__float_as_uint(v), 0xB1, 0xf, 0xf, true));
    v += __uint_as_float(__builtin_amdgcn_mov_dpp(__float_as_uint(v), 0x4E, 0xf, 0xf, true));
    v += __uint_as_float(__builtin_amdgcn_mov_dpp(__float_as_uint(v), 0x141, 0xf, 0xf, true));
    return v;
}
DI void body_help(const Params& p, const int wave) {
    const u32 lds0 = (u32)(size_t)(LAS char*)smem;
    const int lane = threadIdx.x & 63, r8 = lane >> 3, c8 = lane & 7;
    const int xcd = blockIdx.x & 7, j = blockIdx.x >> 3;
    char* ws = p.ws;
    u32x2 ep = gld8c(0u, ws + WS_EPOCH);
    u32x4 d[14];
    const int ctile = (xcd >> 1) * 4 + j;
    const bool cols = wave >= 4;
    if (!cols) {
        const char* xb = (const char*)p.x + (size_t)(256 * xcd + 64 * j + 16 * wave) * 896;
#pragma unroll
        for (int i = 0; i < 14; ++i) d[i] = gld16((u32)((r8 + 8 * (i / 7)) * 896 + (i % 7) * 128 + c8 * 16), xb);
    } else {
        const char* wb = (const char*)p.w1 + (size_t)(112 * (wave - 4)) * 3584 + ((xcd & 1) * 14 + (ctile < 14 ? ctile : 0)) * 128;
#pragma unroll
        for (int i = 0; i < 14; ++i) d[i] = gld16((u32)((r8 + 8 * i) * 3584 + c8 * 16), wb);
    }
    asm volatile("s_waitcnt vmcnt(0)" : "+v"(ep), "+v"(d[0]), "+v"(d[1]), "+v"(d[2]), "+v"(d[3]), "+v"(d[4]), "+v"(d[5]), "+v"(d[6]), "+v"(d[7]), "+v"(d[8]), "+v"(d[9]),
                 "+v"(d[10]), "+v"(d[11]), "+v"(d[12]), "+v"(d[13]) :: "memory");
    const u32 tlo = (u32)((((u64)ep.y << 32) | ep.x) + TAG_SALT);
    if (!cols) {
#pragma unroll
        for (int h = 0; h < 2; ++h) {
            float s0 = 0.f, s1 = 0.f, q0 = 0.f, q1 = 0.f;
#pragma unroll
            for (int i = 0; i < 7; ++i)
#pragma unroll
                for (int e = 0; e < 4; e += 2) {
                    s0 = dot2(d[7 * h + i][e], 0x3f803f80u, s0); q0 = dot2(d[7 * h + i][e], d[7 * h + i][e], q0);
                    s1 = dot2(d[7 * h + i][e + 1], 0x3f803f80u, s1); q1 = dot2(d[7 * h + i][e + 1], d[7 * h + i][e + 1], q1);
                }
            const float mean = dpp_xor_sum8(s0 + s1) * (1.f / 448.f);
            const float var = fmaxf(dpp_xor_sum8(q0 + q1) * (1.f / 448.f) - mean * mean, 0.f);
            const float sd = __builtin_amdgcn_sqrtf(rbf(rbf(var) + 1e-5f));
            if (c8 == 0) gst8c<0>((u32)(WS_RG + (256 * xcd + 64 * j + 16 * wave + 8 * h + r8) * 8), ws, u32x2{pk2(mean, sd), tlo});
        }
    } else {
        float a[8] = {};
        u32 klo = 0x00003f80u, khi = 0x3f800000u; PIN(klo); PIN(khi);
#pragma unroll
        for (int i = 0; i < 14; ++i)
#pragma unroll
            for (int e = 0; e < 4; ++e) { a[2 * e] = dot2(d[i][e], klo, a[2 * e]); a[2 * e + 1] = dot2(d[i][e], khi, a[2 * e + 1]); }
        const u32 o = lds0 + ((wave - 4) * 8 + r8) * 272 + c8 * 32;
        sts16(o, __builtin_bit_cast(u32x4, f32x4{a[0], a[1], a[2], a[3]})); sts16(o + 16, __builtin_bit_cast(u32x4, f32x4{a[4], a[5], a[6], a[7]}));
    }
    BARL();
    if (wave == 4 && ctile < 14) {
        float t0 = 0.f, t1 = 0.f;
#pragma unroll
        for (int i = 0; i < 32; i += 2) { t0 += *(const LAS float*)(lds0 + i * 272 + lane * 4); t1 += *(const LAS float*)(lds0 + (i + 1) * 272 + lane * 4); }
        gst8c<0>((u32)(WS_CG + (((xcd & 1) * 14 + ctile) * 64 + lane) * 8), ws, u32x2{pk2(t0 + t1, (t0 + t1) - rbf(t0 + t1)), tlo});
    }
    {
        const int crot = (2 * xcd) % 14;
        setm0(lds0 + 16384 + wave * 4096);
        for (int c = 1; c < 14; ++c) {
            int cc = c + crot; if (cc >= 14) cc -= 14;
            const char* sb = (const char*)p.w2 + (size_t)cc * 114688 + (j * 8 + wave) * 3584;
            if (lane < 32) dmai<3072>(lane * 16u, sb);
            dmai<0>(lane * 16u, sb); dmai<1024>(lane * 16u, sb); dmai<2048>(lane * 16u, sb);
            WAITV(8);
        }
        WAITV(0);
    }
}

__global__ __launch_bounds__(512, 2) void fused_kernel(Params p) {
    const int wave = __builtin_amdgcn_readfirstlane(threadIdx.x >> 6);
    if (blockIdx.x < NHELP) { body_help(p, wave); return; }
    if (threadIdx.x < 32) *(LAS u32*)((u32)(size_t)(LAS char*)smem + L_FLAG + threadIdx.x * 4) = 0u;
    BARL();
    if (wave < 4) body_g(p, wave); else body_c(p, wave - 4);
}

extern "C" void kernel_launch(void* const* d_in, const int* in_sizes, int n_in,
                              void* d_out, int out_size, void* d_ws, size_t ws_size,
                              hipStream_t stream) {
    Params p;
    p.x = (const u16*)d_in[0]; p.k = (const u16*)d_in[1]; p.v = (const u16*)d_in[2]; p.shk = (const u16*)d_in[3];
    p.ssmk = (const u16*)d_in[4]; p.ssmd = (const u16*)d_in[5]; p.w1 = (const u16*)d_in[7]; p.b1 = (const u16*)d_in[8];
    p.w2 = (const u16*)d_in[9]; p.b2 = (const u16*)d_in[10];
    p.out = (u16*)d_out; p.ws = (char*)d_ws;
    (void)hipFuncSetAttribute((const void*)fused_kernel, hipFuncAttributeMaxDynamicSharedMemorySize, LDS_BYTES);
    fused_kernel<<<NWG + NHELP, 512, LDS_BYTES, stream>>>(p);
}
```

```cpp
#include <hip/hip_runtime.h>
#include <stdint.h>
#include <type_traits>

typedef unsigned short u16;
typedef unsigned int u32;
typedef unsigned long long u64;
typedef u32 u32x4 __attribute__((ext_vector_type(4)));
typedef u32 u32x2 __attribute__((ext_vector_type(2)));
typedef float f32x4 __attribute__((ext_vector_type(4)));
typedef float f32x2 __attribute__((ext_vector_type(2)));
typedef float f32x16 __attribute__((ext_vector_type(16)));
typedef __bf16 bf16x8 __attribute__((ext_vector_type(8)));
typedef __bf16 bf16x2 __attribute__((ext_vector_type(2)));
typedef short s16x4 __attribute__((ext_vector_type(4)));
#define LAS __attribute__((address_space(3)))
#define GAS __attribute__((address_space(1)))
#define DI __device__ __forceinline__

constexpr int S_ = 2048, D_ = 448, F_ = 1792;
constexpr int NHELP = 32, NWG = 224;
constexpr u32 MB = 256;
constexpr u32 WS_EPOCH = 0, WS_ALL = 2048, WS_KVF = 4096, WS_HF = WS_KVF + 65536, WS_YF = WS_HF + 65536, WS_RG = WS_YF + 65536, WS_CG = WS_RG + 16384;
constexpr u32 TP = 4096 + 128;
constexpr u32 WS_H = 1u << 20, WS_KVT = 9u << 20, WS_YT = 11u << 20;
constexpr u32 STAMP_OFF = 16u << 20;
constexpr int NSTAMP = 22;
constexpr u32 L_PIECE = 16384, L_W = 65536, L_LOC0 = 131072, L_MISC = 147456, LDS_BYTES = 163840;
constexpr u32 KVRAW_SZ = 5632, L_KVS = L_LOC0 + 4 * KVRAW_SZ, L_B2 = L_KVS + 9216, L_FLAG = L_B2 + 128, L_ROWF = L_FLAG + 128, L_B2F = L_ROWF + 256, L_R2 = L_B2F + 256;
constexpr u32 L_SCR = L_LOC0 + 2 * KVRAW_SZ + 9728 + 64;
static_assert(L_R2 + 256 <= LDS_BYTES, "lds");
constexpr u32 F_TOEP = L_FLAG, F_HCNT = L_FLAG + 16, F_HRDY = L_FLAG + 20, F_KVRDY = L_FLAG + 24, F_YCNT = L_FLAG + 28, F_YRDY = L_FLAG + 32, F_S0 = L_FLAG + 36, F_KVCNT = L_FLAG + 40, F_W = L_FLAG + 48, F_LAND = L_FLAG + 64, F_CONS = L_FLAG + 96, F_KVS = L_FLAG + 124;
constexpr u32 L_CONV = 16384, CONV_CH = 65536, TOEP_CP = 4160, TOEP_CH = 4 * TOEP_CP, U_SZ = 4608, YS_SZ = 2304;
constexpr u64 TAG_SALT = 0x9E3779B97F4A7C15ull;
constexpr int SPIN_CAP = 1 << 15;
#define SLP 2

#define ST(i) do {} while (0)
#define STP nullptr

DI void dma16(u32 voff, const void* sbase, u32 lds) {
    asm volatile("s_mov_b32 m0, %2\n\ts_nop 0\n\tglobal_load_lds_dwordx4 %0, %1" :: "v"(voff), "s"(sbase), "s"(lds) : "memory");
}
DI void setm0(u32 lds) { asm volatile("s_mov_b32 m0, %0\n\ts_nop 0" :: "s"(lds) : "memory"); }
template <int OFF> DI void dmai(u32 voff, const void* sbase) { asm volatile("global_load_lds_dwordx4 %0, %1 offset:%2" :: "v"(voff), "s"(sbase), "i"(OFF) : "memory"); }
DI void bdma16(u32 voff, u32x4 srd, u32 lds) {
    asm volatile("s_mov_b32 m0, %2\n\ts_nop 0\n\tbuffer_load_dwordx4 %0, %1, 0 offen lds" :: "v"(voff), "s"(srd), "s"(lds) : "memory");
}
#define SB() __builtin_amdgcn_sched_barrier(0)
template <int OFF> DI u32x2 alds8(u32 a) { u32x2 r; asm volatile("ds_read_b64 %0, %1 offset:%2" : "=v"(r) : "v"(a), "i"(OFF)); return r; }
template <int OFF> DI u32x4 alds16(u32 a) { u32x4 r; asm volatile("ds_read_b128 %0, %1 offset:%2" : "=v"(r) : "v"(a), "i"(OFF)); return r; }
DI u32x4 gld16(u32 voff, const void* sbase) { u32x4 r; asm volatile("global_load_dwordx4 %0, %1, %2" : "=v"(r) : "v"(voff), "s"(sbase) : "memory"); return r; }
template <int OFF> DI u32x4 gld16c(u32 voff, const void* sbase) { u32x4 r; asm volatile("global_load_dwordx4 %0, %1, %2 offset:%3 sc1" : "=v"(r) : "v"(voff), "s"(sbase), "i"(OFF) : "memory"); return r; }
DI u32x2 gld8c(u32 voff, const void* sbase) { u32x2 r; asm volatile("global_load_dwordx2 %0, %1, %2 sc1" : "=v"(r) : "v"(voff), "s"(sbase) : "memory"); return r; }
DI void touch(const void* addr, u32 lds) { asm volatile("s_mov_b32 m0, %1\n\ts_nop 0\n\tglobal_load_lds_dword %0, off" :: "v"(addr), "s"(lds) : "memory"); }
DI u32 gld2(u32 voff, const void* sbase) { u32 r; asm volatile("global_load_ushort %0, %1, %2" : "=v"(r) : "v"(voff), "s"(sbase) : "memory"); return r; }
DI u32x4 bld16(u32 voff, u32x4 srd) { u32x4 r; asm volatile("buffer_load_dwordx4 %0, %1, %2, 0 offen" : "=v"(r) : "v"(voff), "s"(srd) : "memory"); return r; }
template <int OFF> DI u32x4 gld16o(u32 voff, const void* sbase) { u32x4 r; asm volatile("global_load_dwordx4 %0, %1, %2 offset:%3" : "=v"(r) : "v"(voff), "s"(sbase), "i"(OFF) : "memory"); return r; }
template <int OFF> DI void gst16c(u32 voff, void* sbase, u32x4 v) { asm volatile("global_store_dwordx4 %0, %1, %2 offset:%3 sc1\n\ts_nop 1" :: "v"(voff), "v"(v), "s"(sbase), "i"(OFF) : "memory"); }
template <int OFF> DI void gst8c(u32 voff, void* sbase, u32x2 v) { asm volatile("global_store_dwordx2 %0, %1, %2 offset:%3 sc1" :: "v"(voff), "v"(v), "s"(sbase), "i"(OFF) : "memory"); }
#define WAITV(n) asm volatile("s_waitcnt vmcnt(%0)" :: "i"(n) : "memory")
#define WAITBAR(n) asm volatile("s_waitcnt vmcnt(%0) lgkmcnt(0)\n\ts_barrier" :: "i"(n) : "memory")
#define BARL() asm volatile("s_waitcnt lgkmcnt(0)\n\ts_barrier" ::: "memory")
#define PIN(x) asm volatile("" : "+v"(x))

DI u32x4 mksrd(const void* p, u32 bytes) { u64 a = (u64)p; return u32x4{(u32)a, (u32)(a >> 32) & 0xffffu, bytes, 0x00020000u}; }
DI float bflo(u32 x) { return __uint_as_float(x << 16); }
DI float bfhi(u32 x) { return __uint_as_float(x & 0xffff0000u); }
DI u32 pk2(float a, float b) { bf16x2 v = {(__bf16)a, (__bf16)b}; return __builtin_bit_cast(u32, v); }
DI float rbf(float a) { return bflo(pk2(a, a)); }
DI float relu(float a) { int i = __float_as_int(a); return __int_as_float(i > 0 ? i : 0); }
DI float fma1(float a, float b, float c) { float r; asm("v_fma_f32 %0, %1, %2, %3" : "=v"(r) : "v"(a), "v"(b), "v"(c)); return r; }
DI float mul1(float a, float b) { float r; asm("v_mul_f32 %0, %1, %2" : "=v"(r) : "v"(a), "v"(b)); return r; }
DI float add1(float a, float b) { float r; asm("v_add_f32 %0, %1, %2" : "=v"(r) : "v"(a), "v"(b)); return r; }
DI float dot2(u32 a, u32 b, float c) { return __builtin_amdgcn_fdot2_f32_bf16(__builtin_bit_cast(bf16x2, a), __builtin_bit_cast(bf16x2, b), c, false); }
DI u32x4 lds16(u32 a) { return *(const LAS u32x4*)a; }
DI u32x2 lds8(u32 a) { return *(const LAS u32x2*)a; }
DI void sts16(u32 a, u32x4 v) { *(LAS u32x4*)a = v; }
DI void sts8(u32 a, u32x2 v) { *(LAS u32x2*)a = v; }
DI u32x2 ldtr(u32 a) { return __builtin_bit_cast(u32x2, __builtin_amdgcn_ds_read_tr16_b64_v4i16((LAS s16x4*)a)); }
DI u32x4 cat(u32x2 a, u32x2 b) { return u32x4{a.x, a.y, b.x, b.y}; }
DI f32x16 mfma32(u32x4 a, u32x4 b, f32x16 c) { return __builtin_amdgcn_mfma_f32_32x32x16_bf16(__builtin_bit_cast(bf16x8, a), __builtin_bit_cast(bf16x8, b), c, 0, 0, 0); }
DI f32x4 mfma16(u32x4 a, u32x4 b, f32x4 c) { return __builtin_amdgcn_mfma_f32_16x16x32_bf16(__builtin_bit_cast(bf16x8, a), __builtin_bit_cast(bf16x8, b), c, 0, 0, 0); }
DI float xhalf_sum(float v) { auto r = __builtin_amdgcn_permlane32_swap(__float_as_uint(v), __float_as_uint(v), false, false); return __uint_as_float(r[0]) + __uint_as_float(r[1]); }

struct Params {
    const u16 *x, *k, *v, *shk, *ssmk, *ssmd, *w1, *b1, *w2, *b2;
    u16* out; char* ws;
};

DI void poll8(u32 voff, const void* sbase, bool need, u32 tlo, u32 thi) {
    _Pragma("clang loop unroll(disable)") for (int spin = 0; spin < SPIN_CAP; ++spin) {
        u32x2 f = gld8c(voff, sbase);
        asm volatile("s_waitcnt vmcnt(0)" : "+v"(f) :: "memory");
        bool ok = !need || (f.x == tlo && f.y == thi);
        if (__all(ok)) break;
        __builtin_amdgcn_s_sleep(SLP);
    }
}
DI void poll16(u32 voff, const void* sbase, u32 tlo, u32 thi) {
    _Pragma("clang loop unroll(disable)") for (int spin = 0; spin < SPIN_CAP; ++spin) {
        u32x4 f = gld16c<0>(voff, sbase);
        asm volatile("s_waitcnt vmcnt(0)" : "+v"(f) :: "memory");
        bool ok = f.x == tlo && f.y == thi && f.z == tlo && f.w == thi;
        if (__all(ok)) break;
        __builtin_amdgcn_s_sleep(SLP);
    }
}

DI bool lds_arrive_last(u32 addr, u32 n) {
    u32 old = 0;
    if ((threadIdx.x & 63) == 0) old = __hip_atomic_fetch_add((LAS u32*)addr, 1u, __ATOMIC_RELAXED, __HIP_MEMORY_SCOPE_WORKGROUP);
    return (u32)__builtin_amdgcn_readfirstlane(old) == n - 1;
}
DI void lds_wait(u32 addr) {
    _Pragma("clang loop unroll(disable)") for (int spin = 0; spin < (1 << 22); ++spin) {
        if (__builtin_amdgcn_readfirstlane(*(volatile LAS u32*)addr)) break;
        __builtin_amdgcn_s_sleep(1);
    }
    asm volatile("" ::: "memory");
}
DI void lds_add(u32 addr) { asm volatile("" ::: "memory"); __hip_atomic_fetch_add((LAS u32*)addr, 1u, __ATOMIC_RELAXED, __HIP_MEMORY_SCOPE_WORKGROUP); asm volatile("" ::: "memory"); }
DI u32 lds_peek(u32 addr) { return *(volatile LAS u32*)addr; }
DI void lds_wait_ge(u32 addr, u32 n, u32 first) {
    u32 v = first;
    _Pragma("clang loop unroll(disable)") for (int spin = 0; spin < (1 << 22) && (u32)__builtin_amdgcn_readfirstlane(v) < n; ++spin) { __builtin_amdgcn_s_sleep(1); v = lds_peek(addr); }
    asm volatile("" ::: "memory");
}
DI void lds_set(u32 addr) { asm volatile("" ::: "memory"); if ((threadIdx.x & 63) == 0) *(volatile LAS u32*)addr = 1u; }
constexpr u32 loc_of(int c) { return (c % 5) == 0 ? L_LOC0 : (c % 5) == 1 ? 3 * L_PIECE : (c % 5) == 2 ? L_W + 3 * L_PIECE : (c % 5) == 3 ? 0u : L_W; }

#define HDV 3
constexpr int HD = HDV, HS = HD + 1;
constexpr int RD = 4, WS_ = RD + 1;
constexpr int g2_younger(int c) {
    const int wneed = c + RD;
    bool sh = false, sw = false; int ah = 0, aw = 0;
    const bool wdone = wneed < 5 + RD || wneed >= 14;
    for (int i = 0; i < HD; ++i) { if (sh) ah += 4; if (i == c) sh = true; }
    for (int i = 0; i < c; ++i) {
        if (i + HD < 14) { if (sh) ah += 4; if (sw) aw += 4; if (i + HD == c) sh = true; }
        if (i >= 1 && i + RD + 4 < 14) { if (sh) ah += 4; if (sw) aw += 4; if (i + RD + 4 == wneed) sw = true; }
    }
    if (wdone) return ah;
    return ah < aw ? ah : aw;
}

struct G2Ctx { const char* w2b; const char* hb; int crot; u32 voffw[4], ldsg, vlane, ta[3][2]; };
template <int C> DI const char* g2_wsrc(const G2Ctx& x) { int c = C + x.crot; if (c >= 14) c -= 14; return x.w2b + (size_t)c * (128 * 896); }
template <int C> DI u32 g2_loc(const G2Ctx& x) { return loc_of(C) + x.ldsg; }
template <int C, int N> DI void g2_w1(const G2Ctx& x, const char* sb) { if constexpr (N == 0) setm0(g2_loc<C>(x)); dmai<N * 1024>(x.voffw[N], sb); }
template <int C> DI void g2_issue_w(const G2Ctx& x) { const char* sb = g2_wsrc<C>(x); g2_w1<C, 0>(x, sb); g2_w1<C, 1>(x, sb); g2_w1<C, 2>(x, sb); g2_w1<C, 3>(x, sb); }
template <int C> DI const char* g2_hsrc(const G2Ctx& x) { int c = C + x.crot; if (c >= 14) c -= 14; return x.hb + c * 8192; }
template <int N> DI u32x4 g2_h1(const G2Ctx& x, const char* sb) { return gld16c<(N & 1) * 1024>(x.vlane + (N >> 1) * 112 * 1024, sb); }
template <int C> DI void g2_issue_h(const G2Ctx& x, u32x4 (&hf)[HS][4]) {
    const char* sb = g2_hsrc<C>(x);
    hf[C % HS][0] = g2_h1<0>(x, sb); hf[C % HS][1] = g2_h1<1>(x, sb); hf[C % HS][2] = g2_h1<2>(x, sb); hf[C % HS][3] = g2_h1<3>(x, sb);
}
template <int C> DI void g2_read(const G2Ctx& x, u32x4 (&wf)[WS_][4]) {
    const u32 a0 = x.ta[loc_of(C) >> 16][0], a1 = x.ta[loc_of(C) >> 16][1];
    constexpr u32 lo = loc_of(C) & 0xffffu;
#pragma unroll
    for (int ks = 0; ks < 2; ++ks) {
        wf[C % WS_][ks * 2] = cat(ldtr(a0 + lo + ks * 2048), ldtr(a0 + lo + ks * 2048 + 1024));
        wf[C % WS_][ks * 2 + 1] = cat(ldtr(a1 + lo + ks * 2048), ldtr(a1 + lo + ks * 2048 + 1024));
    }
}
template <int C, int I> DI void g2_rd(const G2Ctx& x, u32x4 (&wf)[WS_][4]) {
    if constexpr (C < 14) {
        const u32 a = x.ta[loc_of(C) >> 16][I & 1]; constexpr u32 lo = (loc_of(C) & 0xffffu) + (I >> 1) * 2048;
        wf[C % WS_][I] = cat(ldtr(a + lo), ldtr(a + lo + 1024));
    }
}
template <int C> DI void g2_step(const G2Ctx& x, u32x4 (&hf)[HS][4], f32x16 (&acc)[2][2], u32x4 (&wf)[WS_][4], u64* stamps) {
    asm volatile("s_waitcnt vmcnt(%4) lgkmcnt(0)" : "+v"(hf[C % HS][0]), "+v"(hf[C % HS][1]), "+v"(hf[C % HS][2]), "+v"(hf[C % HS][3]) : "i"(g2_younger(C)) : "memory");
    if constexpr (C % 3 == 0) ST(14 + C / 3);
    SB();
    constexpr bool DH = C + HD < 14, DW = C >= 1 && C + RD + 4 < 14;
    const char* hs = g2_hsrc<(DH ? C + HD : 0)>(x);
    const char* wsrc = g2_wsrc<(DW ? C + RD + 4 : 0)>(x);
#define G2M(i) acc[((i) >> 1) & 1][(i) & 1] = mfma32(wf[C % WS_][((i) >> 2) * 2 + (((i) >> 1) & 1)], hf[C % HS][((i) & 1) * 2 + ((i) >> 2)], acc[((i) >> 1) & 1][(i) & 1])
    G2M(0); g2_rd<C + RD, 0>(x, wf); if constexpr (DH) hf[(C + HD) % HS][0] = g2_h1<0>(x, hs); SB();
    G2M(1); g2_rd<C + RD, 1>(x, wf); if constexpr (DH) hf[(C + HD) % HS][1] = g2_h1<1>(x, hs); SB();
    G2M(2); g2_rd<C + RD, 2>(x, wf); if constexpr (DH) hf[(C + HD) % HS][2] = g2_h1<2>(x, hs); SB();
    G2M(3); g2_rd<C + RD, 3>(x, wf); if constexpr (DH) hf[(C + HD) % HS][3] = g2_h1<3>(x, hs); SB();
    G2M(4); if constexpr (DW) g2_w1<C + RD + 4, 0>(x, wsrc); SB();
    G2M(5); if constexpr (DW) g2_w1<C + RD + 4, 1>(x, wsrc); SB();
    G2M(6); if constexpr (DW) g2_w1<C + RD + 4, 2>(x, wsrc); SB();
    G2M(7); if constexpr (DW) g2_w1<C + RD + 4, 3>(x, wsrc); SB();
#undef G2M
    if constexpr (C + 1 < 14) g2_step<C + 1>(x, hf, acc, wf, stamps);
}

template <int NR> struct CF { u32x2 a0[NR], a1[NR]; u32x4 b[NR]; };
template <int S, int R, int NR> DI void cv_ld(CF<NR>& f, u32 aA, u32 aB) {
    if constexpr (R < NR && S < 8) {
        f.a0[R] = alds8<R * 512 + S * 64>(aA); f.a1[R] = alds8<R * 512 + S * 64 + 8>(aA);
        f.b[R] = alds16<(224 - 32 * R + 4 * S) * 16>(aB);
    }
}
template <int NR> DI void cv_wait(CF<NR>& f) {
    if constexpr (NR == 8)
        asm volatile("s_waitcnt lgkmcnt(0)" : "+v"(f.a0[0]), "+v"(f.a0[1]), "+v"(f.a0[2]), "+v"(f.a0[3]), "+v"(f.a0[4]), "+v"(f.a0[5]), "+v"(f.a0[6]), "+v"(f.a0[7]),
                     "+v"(f.a1[0]), "+v"(f.a1[1]), "+v"(f.a1[2]), "+v"(f.a1[3]), "+v"(f.a1[4]), "+v"(f.a1[5]), "+v"(f.a1[6]), "+v"(f.a1[7]),
                     "+v"(f.b[0]), "+v"(f.b[1]), "+v"(f.b[2]), "+v"(f.b[3]), "+v"(f.b[4]), "+v"(f.b[5]), "+v"(f.b[6]), "+v"(f.b[7]));
    else
        asm volatile("s_waitcnt lgkmcnt(0)" : "+v"(f.a0[0]), "+v"(f.a0[1]), "+v"(f.a0[2]), "+v"(f.a0[3]), "+v"(f.a0[4]), "+v"(f.a0[5]),
                     "+v"(f.a1[0]), "+v"(f.a1[1]), "+v"(f.a1[2]), "+v"(f.a1[3]), "+v"(f.a1[4]), "+v"(f.a1[5]),
                     "+v"(f.b[0]), "+v"(f.b[1]), "+v"(f.b[2]), "+v"(f.b[3]), "+v"(f.b[4]), "+v"(f.b[5]));
}
template <int HALF, int M> struct CvIdx {
    static constexpr int q(int i) { return HALF ? (i + 2) : (i < 2 ? i : i + 4); }
    static constexpr int tile() { int m = M; for (int r = 0; r < 8; ++r) for (int i = 3; i >= 0; --i) if (r <= q(i)) { if (m == 0) return i; --m; } return 0; }
    static constexpr int row() { int m = M; for (int r = 0; r < 8; ++r) for (int i = 3; i >= 0; --i) if (r <= q(i)) { if (m == 0) return r; --m; } return 0; }
};
template <int HALF, int S, int M, int NR> DI void cv_mm(CF<NR>& f, CF<NR>& n, u32 aA, u32 aB, f32x4 (&acc)[4]) {
    constexpr int i = CvIdx<HALF, M>::tile(), r = CvIdx<HALF, M>::row(), q = CvIdx<HALF, M>::q(i);
    acc[i] = mfma16(cat(f.a0[r], f.a1[r]), f.b[q - r], acc[i]);
    if constexpr ((M & 1) == 0) cv_ld<S + 1, M / 2, NR>(n, aA, aB);
    SB();
    if constexpr (M + 1 < 18) cv_mm<HALF, S, M + 1, NR>(f, n, aA, aB, acc);
}
template <int HALF, int S, int NR> DI void cv_step(CF<NR>& f, u32 aA, u32 aB, f32x4 (&acc)[4]) {
    cv_wait<NR>(f); SB();
    CF<NR> n;
    cv_mm<HALF, S, 0, NR>(f, n, aA, aB, acc);
    if constexpr (S + 1 < 8) cv_step<HALF, S + 1, NR>(n, aA, aB, acc);
}
template <int HALF> DI void conv_body(u32 aA, u32 aB, f32x4 (&acc)[4]) {
    constexpr int NR = HALF ? 6 : 8;
    CF<NR> f;
    asm volatile("s_waitcnt lgkmcnt(0)" ::: "memory");
    cv_ld<0, 0, NR>(f, aA, aB); cv_ld<0, 1, NR>(f, aA, aB); cv_ld<0, 2, NR>(f, aA, aB); cv_ld<0, 3, NR>(f, aA, aB);
    cv_ld<0, 4, NR>(f, aA, aB); cv_ld<0, 5, NR>(f, aA, aB); cv_ld<0, 6, NR>(f, aA, aB); cv_ld<0, 7, NR>(f, aA, aB);
    cv_step<HALF, 0, NR>(f, aA, aB, acc);
    asm volatile("" ::: "memory");
}

struct Geo { int bi, b, xcd, fh2, RB, CT, tb64, db, rot, crot; u32 R0, F0, T0, D0; };
DI Geo make_geo() {
    Geo g; g.b = blockIdx.x - NHELP; g.xcd = g.b & 7; const int bi = g.b >> 3; g.bi = bi;
    const int rq = g.xcd >> 1, rb = bi / 7, ct = bi - rb * 7;
    g.fh2 = g.xcd & 1; g.RB = 4 * rq + rb; g.CT = 7 * g.fh2 + ct; g.R0 = 128 * g.RB; g.F0 = 128 * g.CT;
    g.tb64 = 2 * g.RB + g.fh2; g.db = ct; g.T0 = 64 * g.tb64; g.D0 = 64 * g.db;
    g.rot = (ct + 2 * rb + rq * 3 + g.fh2) % 7; g.crot = (2 * ct + 7 * (rb & 1) + 3 * (rb >> 1) + rq + 5 * g.fh2) % 14;
    return g;
}
DI void make_g2(G2Ctx& g2, const Params& p, const Geo& q, int g, int lane, u32 lds0) {
    g2.w2b = (const char*)p.w2 + (size_t)(32 * g) * 896 + q.D0 * 2;
    g2.hb = p.ws + WS_H + (size_t)(q.tb64 * 2 * 112 + 2 * g) * 1024;
    g2.crot = q.crot;
#pragma unroll
    for (int n = 0; n < 4; ++n) g2.voffw[n] = (u32)((lane >> 3) * 896 + 16 * ((lane & 7) ^ (((lane >> 4) & 1) << 2)) + n * (8 * 896 - 1024));
    g2.ldsg = lds0 + g * 4096; g2.vlane = lane * 16u;
}
DI u32 slab_base(u32 lds0, int cw) { return lds0 + L_CONV + (cw >> 1) * CONV_CH + (cw & 1) * 8192; }
DI void final_epilogue(const Params& p, const Geo& q, u32 lds0) {
    int tz = threadIdx.x; PIN(tz);
    const int t = tz >> 3, c = tz & 7;
    const u32 pb = lds0 + ((t >> 5) ? 0u : 3 * L_PIECE) + ((t >> 4) & 1) * L_W + (t & 15) * 256;
    const u32 o0 = pb + 16 * ((2 * c) ^ (t & 7)), o1 = o0 ^ 16;
    f32x4 pa[4], pb_[4];
#pragma unroll
    for (int g = 0; g < 4; ++g) { pa[g] = __builtin_bit_cast(f32x4, lds16(o0 + g * 4096)); pb_[g] = __builtin_bit_cast(f32x4, lds16(o1 + g * 4096)); }
    const float r2 = *(const LAS float*)(lds0 + L_R2 + t * 4);
    const f32x4 b0 = __builtin_bit_cast(f32x4, lds16(lds0 + L_B2F + c * 32)), b1 = __builtin_bit_cast(f32x4, lds16(lds0 + L_B2F + c * 32 + 16));
    const u32 sb = slab_base(lds0, c >> 1) + t * 64 + (c & 1) * 32;
    const f32x4 sv0 = __builtin_bit_cast(f32x4, lds16(sb)), sv1 = __builtin_bit_cast(f32x4, lds16(sb + 16));
    const f32x4 xv0 = __builtin_bit_cast(f32x4, lds16(sb + 4096)), xv1 = __builtin_bit_cast(f32x4, lds16(sb + 4096 + 16));
    SB();
    const f32x4 s0 = (pa[0] + pa[1]) + (pa[2] + pa[3]), s1 = (pb_[0] + pb_[1]) + (pb_[2] + pb_[3]);
    f32x4 z0 = s0 * r2 + b0, z1 = s1 * r2 + b1;
    f32x4 e0 = z0 * -1.4426950408889634f, e1 = z1 * -1.4426950408889634f;
#pragma unroll
    for (int j = 0; j < 4; ++j) { e0[j] = __builtin_amdgcn_exp2f(e0[j]); e1[j] = __builtin_amdgcn_exp2f(e1[j]); }
    e0 += 1.f; e1 += 1.f;
#pragma unroll
    for (int j = 0; j < 4; ++j) { e0[j] = __builtin_amdgcn_rcpf(e0[j]); e1[j] = __builtin_amdgcn_rcpf(e1[j]); }
    z0 = z0 * e0; z1 = z1 * e1;
    z0 = z0 * sv0 + xv0; z1 = z1 * sv1 + xv1;
    const u32x4 res = {pk2(z0[0], z0[1]), pk2(z0[2], z0[3]), pk2(z1[0], z1[1]), pk2(z1[2], z1[3])};
    *(GAS u32x4*)((GAS char*)p.out + (size_t)(q.T0 + t) * 896 + q.D0 * 2 + c * 16) = res;
}
#define STAMP_DECL
#define STAMP_OUT(wave) do {} while (0)

extern __shared__ __attribute__((aligned(16))) char smem[];

struct CL { const char *xb, *wb; u32 vx[4], vw[4], sl; int rot; };
struct CSrc { const char *xs, *wsrc; };
template <int ST_> DI CSrc c_src(const CL& c) { int kc = ST_ + c.rot; if (kc >= 7) kc -= 7; return CSrc{c.xb + kc * 128, c.wb + (size_t)kc * 64 * 3584}; }
template <int ST_, int N> DI void c_dma1(const CL& c, const CSrc& s) {
    if constexpr (N == 0) setm0(c.sl + (ST_ & 3) * L_PIECE);
    if constexpr (N == 4) setm0(c.sl + (ST_ & 3) * L_PIECE + L_W);
    if constexpr (N < 4) dmai<N * 1024>(c.vx[N], s.xs); else dmai<(N - 4) * 1024>(c.vw[N - 4], s.wsrc);
}
template <int ST_> DI void c_dma(const CL& c) {
    const CSrc s = c_src<ST_>(c);
    c_dma1<ST_, 0>(c, s); c_dma1<ST_, 1>(c, s); c_dma1<ST_, 2>(c, s); c_dma1<ST_, 3>(c, s);
    c_dma1<ST_, 4>(c, s); c_dma1<ST_, 5>(c, s); c_dma1<ST_, 6>(c, s); c_dma1<ST_, 7>(c, s);
}
DI void make_cl(CL& c, const Params& p, const Geo& q, int cw, int lane, u32 lds0) {
    const int r8 = lane >> 3, c8 = lane & 7, r4 = lane >> 4, c16 = lane & 15;
#pragma unroll
    for (int n = 0; n < 4; ++n) {
        c.vx[n] = (u32)((32 * cw + r8) * 896 + 16 * (c8 ^ (((n & 1) << 2) | (r8 >> 1))) + n * (8 * 896 - 1024));
        c.vw[n] = (u32)((16 * cw + r4) * 3584 + 16 * (c16 ^ (r4 << 2)) + n * (4 * 3584 - 1024));
    }
    c.xb = (const char*)p.x + (size_t)q.R0 * 896; c.wb = (const char*)p.w1 + q.F0 * 2;
    c.sl = lds0 + cw * 4096; c.rot = q.rot;
}

template <int ST_> DI void l_dma(const CL& c) {
    const CSrc s = c_src<ST_>(c);
    const u32 sl = c.sl + (ST_ & 3) * L_PIECE;
    setm0(sl); dmai<0>(c.vx[0], s.xs); dmai<1024>(c.vx[1], s.xs); dmai<2048>(c.vx[2], s.xs); dmai<3072>(c.vx[3], s.xs);
    setm0(sl + L_W); dmai<0>(c.vw[0], s.wsrc); dmai<1024>(c.vw[1], s.wsrc); dmai<2048>(c.vw[2], s.wsrc); dmai<3072>(c.vw[3], s.wsrc);
    const char* x2 = s.xs + 32 * 896; const char* w2 = s.wsrc + 16 * 3584;
    setm0(sl + 4096); dmai<0>(c.vx[0], x2); dmai<1024>(c.vx[1], x2); dmai<2048>(c.vx[2], x2); dmai<3072>(c.vx[3], x2);
    setm0(sl + L_W + 4096); dmai<0>(c.vw[0], w2); dmai<1024>(c.vw[1], w2); dmai<2048>(c.vw[2], w2); dmai<3072>(c.vw[3], w2);
}

struct Fr { u32x4 x[4][2], w[4][2]; };
struct GA { u32 ax[4], aw[2]; };
template <int ST_> DI void g1_read(const GA& a, Fr& f) {
    constexpr u32 so = (ST_ & 3) * L_PIECE;
#pragma unroll
    for (int k = 0; k < 4; ++k) {
        f.w[k][0] = cat(ldtr(a.aw[0] + so + k * 4096), ldtr(a.aw[0] + so + k * 4096 + 1024));
        f.x[k][0] = lds16(a.ax[k] + so);
        f.w[k][1] = cat(ldtr(a.aw[1] + so + k * 4096), ldtr(a.aw[1] + so + k * 4096 + 1024));
        f.x[k][1] = lds16(a.ax[k] + so + 4096);
    }
}
template <int K0> DI void g1_mma(const Fr& f, f32x16 (&acc)[2][2]) {
#pragma unroll
    for (int k = K0; k < K0 + 2; ++k) {
        acc[0][0] = mfma32(f.w[k][0], f.x[k][0], acc[0][0]); acc[1][0] = mfma32(f.w[k][1], f.x[k][0], acc[1][0]);
        acc[0][1] = mfma32(f.w[k][0], f.x[k][1], acc[0][1]); acc[1][1] = mfma32(f.w[k][1], f.x[k][1], acc[1][1]);
    }
    SB();
}

template <int ST_, int I> DI void g1_rd(const GA& a, Fr& f) {
    constexpr u32 so = (ST_ & 3) * L_PIECE; constexpr int k = I >> 1, h = I & 1;
    f.w[k][h] = cat(ldtr(a.aw[h] + so + k * 4096), ldtr(a.aw[h] + so + k * 4096 + 1024));
    f.x[k][h] = lds16(a.ax[k] + so + h * 4096);
}
template <int K0, int I> DI void g1_m1(const Fr& f, f32x16 (&acc)[2][2]) {
    constexpr int k = K0 + (I >> 2), mt = I & 1, st = (I >> 1) & 1;
    acc[mt][st] = mfma32(f.w[k][mt], f.x[k][st], acc[mt][st]);
}
template <int ST_, int I = 0> DI void g1_carry(const GA& a, const Fr& fp, Fr& fn, f32x16 (&acc)[2][2]) {
    g1_m1<2, I>(fp, acc); g1_rd<ST_, I>(a, fn); SB();
    if constexpr (I + 1 < 8) g1_carry<ST_, I + 1>(a, fp, fn, acc);
}

DI void body_g(const Params& p, const int g) {
    STAMP_DECL
    const u32 lds0 = (u32)(size_t)(LAS char*)smem;
    const int lane = threadIdx.x & 63;
    const Geo q = make_geo();
    char* ws = p.ws;
    const int hi = lane >> 5, s31 = lane & 31;
    const int r8 = lane >> 3, c8 = lane & 7;
    const u32 raw = lds0 + L_LOC0 + g * KVRAW_SZ;
    {
        const u32 vkv = (u32)(r8 * 896 + c8 * 16);
        bdma16((u32)(((int)q.T0 + 16 * g - 8) * 896 + (int)q.D0 * 2) + vkv, mksrd(p.k, S_ * D_ * 2), raw);
        const char* kb = (const char*)p.k + (size_t)(q.T0 + 16 * g) * 896 + q.D0 * 2;
        const char* vb = (const char*)p.v + (size_t)(q.T0 + 16 * g) * 896 + q.D0 * 2;
        dmai<1024>(vkv, kb - 1024); dmai<2048>(vkv, kb + 8 * 896 - 2048); dmai<3072>(vkv, vb - 3072);
        setm0(raw + 4096);
        dmai<0>(vkv, vb + 8 * 896);
        if (lane < 32) dmai<1024>(lane * 16u, (const char*)p.shk + q.D0 * 8 - 1024);
        CL c; make_cl(c, p, q, g, lane, lds0);
        c_dma<0>(c);
    }
    u32x2 ep = gld8c(0u, ws + WS_EPOCH);
    const int fh = g & 1, sh = g >> 1;
    GA ga;
    {
        const int blk = (lane >> 4) & 1, qq = (lane & 15) >> 2, pp = lane & 3;
        const u32 Z = (u32)(hi ^ ((s31 >> 1) & 7));
#pragma unroll
        for (int k = 0; k < 4; ++k) ga.ax[k] = lds0 + (64 * sh + s31) * 128 + 16 * ((2 * k) ^ Z);
        ga.aw[0] = lds0 + L_W + (8 * hi + qq) * 256 + 16 * ((8 * fh + 2 * blk + (pp >> 1)) ^ (qq << 2)) + 8 * (pp & 1);
        ga.aw[1] = ga.aw[0] ^ 64;
    }
    WAITV(9);
    ST(1);
    {
        u32x4 wr[4], kr[5], vr[2];
#pragma unroll
        for (int e = 0; e < 4; ++e) wr[e] = lds16(raw + 5120 + c8 * 64 + e * 16);
#pragma unroll
        for (int j = 0; j < 5; ++j) kr[j] = lds16(raw + (5 + 2 * r8 + j) * 128 + c8 * 16);
        vr[0] = lds16(raw + 3072 + 2 * r8 * 128 + c8 * 16); vr[1] = lds16(raw + 3072 + (2 * r8 + 1) * 128 + c8 * 16);
        SB();
        u32x4 o0, o1;
#pragma unroll
        for (int e = 0; e < 4; ++e) {
            const u32x4 w = wr[e];
            const f32x2 t0 = {bflo(w.x), bflo(w.z)}, t1 = {bfhi(w.x), bfhi(w.z)}, t2 = {bflo(w.y), bflo(w.w)}, t3 = {bfhi(w.y), bfhi(w.w)};
            f32x2 k[5];
#pragma unroll
            for (int j = 0; j < 5; ++j) k[j] = f32x2{bflo(kr[j][e]), bfhi(kr[j][e])};
            f32x2 a = k[0] * t3, b = k[1] * t3;
            a = __builtin_elementwise_fma(k[1], t2, a); b = __builtin_elementwise_fma(k[2], t2, b);
            a = __builtin_elementwise_fma(k[2], t1, a); b = __builtin_elementwise_fma(k[3], t1, b);
            a = __builtin_elementwise_fma(k[3], t0, a); b = __builtin_elementwise_fma(k[4], t0, b);
            a *= f32x2{bflo(vr[0][e]), bfhi(vr[0][e])}; b *= f32x2{bflo(vr[1][e]), bfhi(vr[1][e])};
            o0[e] = pk2(a.x, a.y); o1[e] = pk2(b.x, b.y);
        }
        sts16(lds0 + L_KVS + (16 * g + 2 * r8) * 144 + c8 * 16, o0);
        sts16(lds0 + L_KVS + (16 * g + 2 * r8 + 1) * 144 + c8 * 16, o1);
        asm volatile("s_waitcnt lgkmcnt(0)" ::: "memory");
        lds_add(lds0 + F_KVS);
    }
    f32x16 acc[2][2] = {};
    Fr fa, fb;
    WAITV(1);
    lds_add(lds0 + F_LAND);
    lds_wait_ge(lds0 + F_LAND, 256, lds_peek(lds0 + F_LAND));
    ST(2);
    u32 pk;
#define G1_STAGE(T, FP, FN) do { lds_wait_ge(lds0 + F_LAND + 4 * (T), (T) == 3 ? 128 : 64, pk); g1_carry<T>(ga, FP, FN, acc); pk = lds_peek(lds0 + F_LAND + 4 * (T) + 4); SB(); \
        g1_mma<0>(FN, acc); asm volatile("s_waitcnt lgkmcnt(0)" ::: "memory"); lds_add(lds0 + F_CONS + 4 * (T)); SB(); } while (0)
    g1_read<0>(ga, fa); pk = lds_peek(lds0 + F_LAND + 4); SB();
    g1_mma<0>(fa, acc); asm volatile("s_waitcnt lgkmcnt(0)" ::: "memory"); lds_add(lds0 + F_CONS); SB();
    ST(19);
    G1_STAGE(1, fa, fb);
    ST(20);
    G1_STAGE(2, fb, fa);
    asm volatile("s_waitcnt vmcnt(0)" : "+v"(ep) :: "memory");
    ST(3);
    const u64 epoch = ((u64)(u32)__builtin_amdgcn_readfirstlane(ep.y) << 32) | (u32)__builtin_amdgcn_readfirstlane(ep.x);
    const u64 tag = epoch + TAG_SALT;
    const u32 tlo = (u32)tag, thi = (u32)(tag >> 32);
    G1_STAGE(3, fa, fb);
    G1_STAGE(4, fb, fa);
    ST(21);
    const u32 rgo = WS_RG + (q.R0 + 64 * sh + lane) * 8, cgo = WS_CG + (q.F0 + 64 * fh + lane) * 8;
    u32x2 rg = gld8c(rgo, ws), cg = gld8c(cgo, ws);
    u32 b1v = gld2((u32)((64 * fh + lane) * 2), (const char*)p.b1 + q.F0 * 2);
    G1_STAGE(5, fa, fb);
    ST(4);
    G1_STAGE(6, fb, fa);
    ST(5);
    asm volatile("s_waitcnt vmcnt(0)" : "+v"(rg), "+v"(cg), "+v"(b1v) :: "memory");
    _Pragma("clang loop unroll(disable)") for (int spin = 0; spin < SPIN_CAP && !__all(rg.y == tlo && cg.y == tlo); ++spin) {
        rg = gld8c(rgo, ws); cg = gld8c(cgo, ws);
        asm volatile("s_waitcnt vmcnt(0)" : "+v"(rg), "+v"(cg) :: "memory");
    }
    if (fh == 0 && sh == q.fh2) *(LAS u32*)(lds0 + L_ROWF + lane * 4) = rg.x;
    g1_mma<2>(fa, acc);
    {
        u32x4 ax[2], bx[2];
        const auto rr = __builtin_amdgcn_permlane32_swap(rg.x, rg.x, false, false);
        const auto cc = __builtin_amdgcn_permlane32_swap(cg.x, cg.x, false, false);
        const auto bb = __builtin_amdgcn_permlane32_swap(b1v, b1v, false, false);
#pragma unroll
        for (int i = 0; i < 2; ++i) {
            const u32 nm = __builtin_amdgcn_perm(rr[i], rr[i], 0x01000100u) ^ 0x80008000u;
            ax[i] = u32x4{cc[i], bb[i], 0u, 0u};
            bx[i] = u32x4{hi ? 0u : nm, hi ? 0u : (rr[i] >> 16), 0u, 0u};
        }
#pragma unroll
        for (int st = 0; st < 2; ++st)
#pragma unroll
            for (int mt = 0; mt < 2; ++mt) acc[mt][st] = mfma32(ax[mt], bx[st], acc[mt][st]);
#pragma unroll
        for (int st = 0; st < 2; ++st) {
            char* hb = ws + WS_H + (size_t)((4 * q.RB + 2 * sh + st) * 112 + q.CT * 8 + fh * 4) * 1024;
#pragma unroll
            for (int mt = 0; mt < 2; ++mt)
#pragma unroll
                for (int t = 0; t < 2; ++t) {
                    u32x4 hv;
#pragma unroll
                    for (int j = 0; j < 4; ++j) {
                        f32x2 a = {acc[mt][st][8 * t + 2 * j], acc[mt][st][8 * t + 2 * j + 1]};
                        f32x2 u = {relu(a.x), relu(a.y)};
                        u = u * u;
                        hv[j] = pk2(u.x, u.y);
                    }
                    if (mt == 0 && t == 0) gst16c<0>(lane * 16u, hb, hv);
                    if (mt == 0 && t == 1) gst16c<1024>(lane * 16u, hb, hv);
                    if (mt == 1 && t == 0) gst16c<2048>(lane * 16u, hb, hv);
                    if (mt == 1 && t == 1) gst16c<3072>(lane * 16u, hb, hv);
                    SB();
                }
        }
    }
    ST(6);
    G2Ctx g2; make_g2(g2, p, q, g, lane, lds0);
    {
        const int blk = (lane >> 4) & 1, qq = (lane & 15) >> 2, pp = lane & 3;
        const u32 t0 = g2.ldsg + (4 * hi + qq) * 128 + 16 * ((2 * blk + (pp >> 1)) ^ ((qq >> 1) << 2)) + 8 * (pp & 1);
#pragma unroll
        for (int i = 0; i < 3; ++i) { g2.ta[i][0] = t0 + i * 65536; g2.ta[i][1] = (t0 ^ 64) + i * 65536; PIN(g2.ta[i][0]); PIN(g2.ta[i][1]); }
    }
    u32x4 wf[WS_][4];
    lds_wait_ge(lds0 + F_CONS + 16, 256, lds_peek(lds0 + F_CONS + 16));
    lds_wait(lds0 + F_W + (g >> 1) * 4);
    g2_read<0>(g2, wf); g2_read<1>(g2, wf);
    asm volatile("s_waitcnt vmcnt(0) lgkmcnt(0)" ::: "memory"); ST(9);
    if (lds_arrive_last(lds0 + F_HCNT, 4)) { if (lane < 15) gst8c<0>(lane < 14 ? WS_HF + (q.RB * 14 + lane) * MB + q.CT * 8 : WS_ALL + q.b * 8, ws, u32x2{tlo, thi}); }
    {
        const u32 po = WS_HF + (q.RB * 14 + q.CT) * MB + (lane < 14 ? lane : 0) * 8;
        g2_issue_w<2>(g2); g2_issue_w<3>(g2);
        u32x2 f = {0u, 0u};
        if (g == 0) f = gld8c(po, ws);
        g2_issue_w<4>(g2); g2_issue_w<5>(g2); g2_issue_w<6>(g2);
        asm volatile("s_waitcnt vmcnt(12)" : "+v"(f) :: "memory");
        bool rdy = __all(f.x == tlo && f.y == thi);
        if (g == 0 && rdy) lds_set(lds0 + F_HRDY);
        g2_read<2>(g2, wf); g2_read<3>(g2, wf);
        asm volatile("s_waitcnt lgkmcnt(0)" ::: "memory");
        if (g == 0 && !rdy) f = gld8c(po, ws);
        g2_issue_w<7>(g2); g2_issue_w<8>(g2);
        if (g == 0) {
            if (!rdy) {
                asm volatile("s_waitcnt vmcnt(8)" : "+v"(f) :: "memory");
                if (!__all(f.x == tlo && f.y == thi)) poll8(po, ws, true, tlo, thi);
                lds_set(lds0 + F_HRDY);
            }
        } else lds_wait(lds0 + F_HRDY);
    }
    ST(7);
    {
        u32x4 hf[HS][4];
        g2_issue_h<0>(g2, hf); g2_issue_h<1>(g2, hf); if constexpr (HD > 2) g2_issue_h<2 % HS>(g2, hf); if constexpr (HD > 3) g2_issue_h<3 % HS>(g2, hf); if constexpr (HD > 4) g2_issue_h<4 % HS>(g2, hf);
        f32x16 a2[2][2] = {};
        g2_step<0>(g2, hf, a2, wf, STP);
        ST(8);
        const u32 pw = g2.ldsg + (s31 >> 4) * L_W + (s31 & 15) * 256;
#pragma unroll
        for (int s2 = 0; s2 < 2; ++s2)
#pragma unroll
            for (int dt = 0; dt < 2; ++dt)
#pragma unroll
                for (int gr = 0; gr < 4; ++gr) {
                    const u32 a = pw + (s2 ? 0u : 3 * L_PIECE) + 16 * ((8 * dt + 2 * gr + hi) ^ (s31 & 7));
                    sts16(a, __builtin_bit_cast(u32x4, f32x4{a2[dt][s2][4 * gr], a2[dt][s2][4 * gr + 1], a2[dt][s2][4 * gr + 2], a2[dt][s2][4 * gr + 3]}));
                }
    }
    BARL(); ST(10);
    final_epilogue(p, q, lds0);
    STAMP_OUT(g);
}

DI void body_c(const Params& p, const int cw) {
    STAMP_DECL
    const u32 lds0 = (u32)(size_t)(LAS char*)smem;
    const int lane = threadIdx.x & 63;
    const Geo q = make_geo();
    char* ws = p.ws;
    const int hi = lane >> 5, s31 = lane & 31;
    const int half = cw & 1, chl = cw >> 1, chan = 2 * q.b + chl;
    u32x2 ep;
    if (cw < 2) {
        CL c, c2; make_cl(c, p, q, 0, lane, lds0); c2 = c; c2.xb += 64 * 896; c2.wb += 32 * 3584; c2.sl += 8192;
        if (cw == 0) {
            l_dma<1>(c); l_dma<1>(c2); ST(1);
            WAITV(0); lds_add(lds0 + F_LAND + 4); ST(2);
            lds_wait_ge(lds0 + F_CONS, 256, lds_peek(lds0 + F_CONS)); l_dma<4>(c); l_dma<4>(c2); ST(19);
            WAITV(0); lds_add(lds0 + F_LAND + 16); ST(20);
            lds_wait_ge(lds0 + F_CONS + 8, 256, lds_peek(lds0 + F_CONS + 8)); l_dma<6>(c); l_dma<6>(c2); ST(3);
            ep = gld8c(0u, ws + WS_EPOCH);
            WAITV(1); lds_add(lds0 + F_LAND + 24); ST(21);
        } else {
            l_dma<2>(c); l_dma<2>(c2); ST(1);
            WAITV(0); lds_add(lds0 + F_LAND + 8); ST(2);
            lds_wait_ge(lds0 + F_CONS + 4, 256, lds_peek(lds0 + F_CONS + 4)); l_dma<5>(c); l_dma<5>(c2); ST(19);
            ep = gld8c(0u, ws + WS_EPOCH);
            WAITV(1); lds_add(lds0 + F_LAND + 20); ST(20);
        }
        lds_wait_ge(lds0 + F_CONS + 12, 256, lds_peek(lds0 + F_CONS + 12));
#pragma unroll
        for (int i = 0; i < 2; ++i) { G2Ctx g2; make_g2(g2, p, q, 2 * cw + i, lane, lds0); g2_issue_w<0>(g2); g2_issue_w<1>(g2); }
        asm volatile("s_waitcnt vmcnt(0)" : "+v"(ep) :: "memory");
        lds_set(lds0 + F_W + cw * 4);
    } else {
        const int kw = cw - 2;
        if (kw == 0 && lane < 8) dma16(lane * 16u, (const char*)p.b2 + q.D0 * 2, lds0 + L_B2);
        ep = gld8c(0u, ws + WS_EPOCH);
        { CL c; make_cl(c, p, q, 2 * kw, lane, lds0); l_dma<3>(c); }
        lds_wait_ge(lds0 + F_KVS, 256, lds_peek(lds0 + F_KVS));
        ST(2);
        {
            const int grp = lane >> 4, qq = (lane & 15) >> 2, pp = lane & 3;
            const u32 a = lds0 + L_KVS + (16 * grp + 8 * (pp >> 1) + 7 - qq) * 144 + kw * 64 + (pp & 1) * 8;
            const u32 vo = (u32)((q.D0 + 32 * kw + (lane & 7)) * TP + (2040 - (int)q.T0 - 8 * (lane >> 3)) * 2);
            u32x4 tr[4];
#pragma unroll
            for (int i = 0; i < 4; ++i) tr[i] = cat(ldtr(a + 16 * i), ldtr(a - 4 * 144 + 16 * i));
            SB();
#pragma unroll
            for (int i = 0; i < 4; ++i) gst16c<0>(vo, ws + WS_KVT + i * 8 * TP, tr[i]);
        }
        ST(20);
        u32x4 sk[5], uu[4];
        const u32x4 sksrd = mksrd((const char*)p.ssmk + (size_t)(2 * q.b + kw) * 4096, 4096);
#pragma unroll
        for (int r = 0; r < 5; ++r) sk[r] = bld16((u32)((lane - 1 + 63 * r) * 16), sksrd);
        ST(3);
        asm volatile("s_waitcnt vmcnt(9)" : "+v"(ep) :: "memory");
        lds_add(lds0 + F_LAND + 12);
        WAITV(5);
        ST(19);
        const u64 tg = (((u64)ep.y << 32) | ep.x) + TAG_SALT;
        if (lds_arrive_last(lds0 + F_KVCNT, 2)) {
            if (lane < 32) gst8c<0>(WS_KVF + (q.db * 32 + lane) * MB + q.tb64 * 8, ws, u32x2{(u32)tg, (u32)(tg >> 32)});
        }
        asm volatile("s_waitcnt vmcnt(0)" : "+v"(sk[0]), "+v"(sk[1]), "+v"(sk[2]), "+v"(sk[3]), "+v"(sk[4]) :: "memory");
        u32x4 tw[5][4];
#pragma unroll
        for (int r = 0; r < 5; ++r) {
            const u32x4 B = sk[r];
            u32x4 A;
#pragma unroll
            for (int j = 0; j < 4; ++j) A[j] = __builtin_amdgcn_update_dpp(0u, B[j], 0x138  , 0xf, 0xf, false);
            const u32 e0 = __builtin_amdgcn_alignbit(A.y, A.x, 16), e1 = __builtin_amdgcn_alignbit(A.z, A.y, 16), e2 = __builtin_amdgcn_alignbit(A.w, A.z, 16),
                      e3 = __builtin_amdgcn_alignbit(B.x, A.w, 16), e4 = __builtin_amdgcn_alignbit(B.y, B.x, 16);
            tw[r][0] = u32x4{e0, e1, e2, e3}; tw[r][1] = u32x4{A.y, A.z, A.w, B.x}; tw[r][2] = u32x4{e1, e2, e3, e4}; tw[r][3] = u32x4{A.z, A.w, B.x, B.y};
            PIN(tw[r][0]); PIN(tw[r][1]); PIN(tw[r][2]); PIN(tw[r][3]);
        }
        if (kw == 0) { poll8(WS_KVF + q.b * MB + s31 * 8, ws, true, (u32)tg, (u32)(tg >> 32)); lds_set(lds0 + F_KVRDY); } else lds_wait(lds0 + F_KVRDY);
        ST(7);
        const char* kb2 = ws + WS_KVT + (size_t)(2 * q.b + kw) * TP;
        uu[0] = gld16c<0>(lane * 16u, kb2); uu[1] = gld16c<1024>(lane * 16u, kb2); uu[2] = gld16c<2048>(lane * 16u, kb2); uu[3] = gld16c<3072>(lane * 16u, kb2);
        lds_wait_ge(lds0 + F_CONS + 20, 256, lds_peek(lds0 + F_CONS + 20)); lds_wait_ge(lds0 + F_CONS + 24, 256, lds_peek(lds0 + F_CONS + 24));
        ST(5);
        const u32 tq = lds0 + L_CONV + kw * CONV_CH + lane * 16;
#pragma unroll
        for (int r = 0; r < 5; ++r) {
            if ((lane > 0 || r == 0) && (r < 4 || lane < 6)) {
#pragma unroll
                for (int c = 0; c < 4; ++c) sts16(tq + r * 1008 + c * TOEP_CP, tw[r][c]);
            }
        }
        if (lane < 32) sts16(tq + TOEP_CH + 4096, u32x4{0u, 0u, 0u, 0u});
        asm volatile("s_waitcnt vmcnt(0)" : "+v"(uu[0]), "+v"(uu[1]), "+v"(uu[2]), "+v"(uu[3]) :: "memory");
        ST(14);
        sts16(tq + TOEP_CH, uu[0]); sts16(tq + TOEP_CH + 1024, uu[1]); sts16(tq + TOEP_CH + 2048, uu[2]); sts16(tq + TOEP_CH + 3072, uu[3]);
        asm volatile("s_waitcnt lgkmcnt(0)" ::: "memory");
        lds_set(lds0 + F_TOEP + kw * 4);
    }
    u32x4 xa = gld16((u32)(lane * 896), (const char*)p.x + (size_t)q.T0 * 896 + (q.D0 + 16 * cw) * 2);
    u32x4 xb_ = gld16((u32)(lane * 896 + 16), (const char*)p.x + (size_t)q.T0 * 896 + (q.D0 + 16 * cw) * 2);
    u32 dv = gld2(0u, (const char*)p.ssmd + chan * 2);
    const u64 epoch = ((u64)(u32)__builtin_amdgcn_readfirstlane(ep.y) << 32) | (u32)__builtin_amdgcn_readfirstlane(ep.x);
    const u64 tag = epoch + TAG_SALT;
    const u32 tlo = (u32)tag, thi = (u32)(tag >> 32);
    const u32 tbase = lds0 + L_CONV + chl * CONV_CH;
    const u32 ub = tbase + TOEP_CH;
    ST(6);
    lds_wait(lds0 + F_TOEP + chl * 4);
    ST(15);
    const int bb = lane & 15, gg = lane >> 4;
    const u32 aA = tbase + (bb & 3) * TOEP_CP + ((bb >> 2) + 2 * gg) * 8;
    const u32 aB = ub + (30 - 2 * bb + gg) * 16;
    f32x4 acc[4] = {};
    if (half == 0) conv_body<0>(aA, aB, acc); else conv_body<1>(aA, aB, acc);
    ST(16);
    asm volatile("s_waitcnt vmcnt(0)" : "+v"(xa), "+v"(xb_), "+v"(dv) :: "memory");
    ST(8);
    {
        const float dd = bflo(dv);
        char* yb = ws + WS_YT + (size_t)chan * TP;
        u32x2 uq[4];
#pragma unroll
        for (int i = 0; i < 4; ++i) uq[i] = lds8(ub + (2044 - (256 * (half ? (i + 2) : (i < 2 ? i : i + 4)) + 16 * bb + 4 * gg)) * 2);
        SB();
#pragma unroll
        for (int i = 0; i < 4; ++i) {
            const int qt = half ? (i + 2) : (i < 2 ? i : i + 4);
            const int t = 256 * qt + 16 * bb + 4 * gg;
            const u32x2 uu = uq[i];
            const u32 c01 = pk2(acc[i][0], acc[i][1]), c23 = pk2(acc[i][2], acc[i][3]);
            float y0 = fma1(bfhi(uu.y), dd, bflo(c01)), y1 = fma1(bflo(uu.y), dd, bfhi(c01));
            float y2 = fma1(bfhi(uu.x), dd, bflo(c23)), y3 = fma1(bflo(uu.x), dd, bfhi(c23));
            gst8c<0>((u32)(t * 2), yb, u32x2{pk2(y0, y1), pk2(y2, y3)});
        }
        WAITV(0);
        if (lds_arrive_last(lds0 + F_YCNT, 4)) { if (lane < 32) gst8c<0>(WS_YF + (lane * 7 + (q.b >> 5)) * MB + (q.b & 31) * 8, ws, u32x2{tlo, thi}); }
    }
    _Pragma("clang loop unroll(disable)") for (int spin = 0; spin < (1 << 22); ++spin) { if (__builtin_amdgcn_readfirstlane(*(volatile LAS u32*)(lds0 + F_YCNT)) >= 4) break; __builtin_amdgcn_s_sleep(1); }
    asm volatile("" ::: "memory");
    const u32 slab = slab_base(lds0, cw) + lane * 64;
    f32x4 xn[4];
    {
        _Pragma("clang loop unroll(disable)") for (int spin = 0; spin < (1 << 22); ++spin) { if (__builtin_amdgcn_readfirstlane(*(volatile LAS u32*)(lds0 + F_HCNT)) >= 4) break; __builtin_amdgcn_s_sleep(1); }
        asm volatile("" ::: "memory");
        const u32 rf = *(const LAS u32*)(lds0 + L_ROWF + lane * 4);
        const float m = bflo(rf), ri = __builtin_amdgcn_rcpf(bfhi(rf)), nmri = -m * ri;
        const u32 xx[8] = {xa.x, xa.y, xa.z, xa.w, xb_.x, xb_.y, xb_.z, xb_.w};
#pragma unroll
        for (int j = 0; j < 4; ++j) {
            f32x4 v = {bflo(xx[2 * j]), bfhi(xx[2 * j]), bflo(xx[2 * j + 1]), bfhi(xx[2 * j + 1])};
            sts16(slab + 4096 + j * 16, __builtin_bit_cast(u32x4, v));
            xn[j] = f32x4{fma1(v[0], ri, nmri), fma1(v[1], ri, nmri), fma1(v[2], ri, nmri), fma1(v[3], ri, nmri)};
        }
        if (cw == 0) *(LAS float*)(lds0 + L_R2 + lane * 4) = ri * ri;
        if (cw == 1) *(LAS float*)(lds0 + L_B2F + lane * 4) = bflo(*(const LAS u16*)(lds0 + L_B2 + lane * 2));
    }
    if (q.b == 0 && cw == 3) {
        _Pragma("clang loop unroll(disable)") for (int spin = 0; spin < SPIN_CAP; ++spin) {
            u32x4 f0 = gld16c<0>(lane * 16u, ws + WS_ALL), f1 = gld16c<1024>((lane < 48 ? lane : 0) * 16u, ws + WS_ALL);
            asm volatile("s_waitcnt vmcnt(0)" : "+v"(f0), "+v"(f1) :: "memory");
            bool ok = f0.x == tlo && f0.y == thi && f0.z == tlo && f0.w == thi && f1.x == tlo && f1.y == thi && f1.z == tlo && f1.w == thi;
            if (__all(ok)) break;
            __builtin_amdgcn_s_sleep(SLP);
        }
        if (lane == 0) { const u64 e1 = epoch + 1; gst8c<0>(0u, ws + WS_EPOCH, u32x2{(u32)e1, (u32)(e1 >> 32)}); }
    }
    if (cw == 0) { poll8(WS_YF + (q.tb64 * 7 + q.db) * MB + s31 * 8, ws, true, tlo, thi); lds_set(lds0 + F_YRDY); }
    else lds_wait(lds0 + F_YRDY);
    ST(9);
    {
        const char* yb = ws + WS_YT + (size_t)(q.D0 + 16 * cw) * TP + q.T0 * 2;
        const u32 vo = (u32)((lane >> 3) * TP + (lane & 7) * 16);
        u32x4 y0 = gld16c<0>(vo, yb), y1 = gld16c<0>(vo, yb + 8 * TP);
        asm volatile("s_waitcnt vmcnt(0)" : "+v"(y0), "+v"(y1) :: "memory");
        const u32 ys = tbase + TOEP_CH + 2 * U_SZ + half * YS_SZ;
        sts16(ys + (lane >> 3) * 144 + (lane & 7) * 16, y0);
        sts16(ys + (8 + (lane >> 3)) * 144 + (lane & 7) * 16, y1);
        const int qq = (lane & 15) >> 2, pp = lane & 3;
        const u32 a = ys + qq * 144 + gg * 32 + pp * 8;
        u32x2 t0 = ldtr(a), t1 = ldtr(a + 4 * 144), t2 = ldtr(a + 8 * 144), t3 = ldtr(a + 12 * 144);
        const u32 yy[8] = {t0.x, t0.y, t1.x, t1.y, t2.x, t2.y, t3.x, t3.y};
#pragma unroll
        for (int j = 0; j < 4; ++j) {
            f32x4 v = {bflo(yy[2 * j]), bfhi(yy[2 * j]), bflo(yy[2 * j + 1]), bfhi(yy[2 * j + 1])};
            sts16(slab + ((j ^ (lane & 3)) * 0 + j) * 16, __builtin_bit_cast(u32x4, f32x4{mul1(v[0], xn[j][0]), mul1(v[1], xn[j][1]), mul1(v[2], xn[j][2]), mul1(v[3], xn[j][3])}));
        }
    }
    BARL(); ST(10);
    final_epilogue(p, q, lds0);
    STAMP_OUT(cw + 4);
}

DI float dpp_xor_sum8(float v) {
    v += __uint_as_float(__builtin_amdgcn_mov_dpp(__float_as_uint(v), 0xB1, 0xf, 0xf, true));
    v += __uint_as_float(__builtin_amdgcn_mov_dpp(__float_as_uint(v), 0x4E, 0xf, 0xf, true));
    v += __uint_as_float(__builtin_amdgcn_mov_dpp(__float_as_uint(v), 0x141, 0xf, 0xf, true));
    return v;
}
DI void body_help(const Params& p, const int wave) {
    const u32 lds0 = (u32)(size_t)(LAS char*)smem;
    const int lane = threadIdx.x & 63, r8 = lane >> 3, c8 = lane & 7;
    const int xcd = blockIdx.x & 7, j = blockIdx.x >> 3;
    char* ws = p.ws;
    u32x2 ep = gld8c(0u, ws + WS_EPOCH);
    u32x4 d[14];
    const int ctile = (xcd >> 1) * 4 + j;
    const bool cols = wave >= 4;
    if (!cols) {
        const char* xb = (const char*)p.x + (size_t)(256 * xcd + 64 * j + 16 * wave) * 896;
#pragma unroll
        for (int i = 0; i < 14; ++i) d[i] = gld16((u32)((r8 + 8 * (i / 7)) * 896 + (i % 7) * 128 + c8 * 16), xb);
    } else {
        const char* wb = (const char*)p.w1 + (size_t)(112 * (wave - 4)) * 3584 + ((xcd & 1) * 14 + (ctile < 14 ? ctile : 0)) * 128;
#pragma unroll
        for (int i = 0; i < 14; ++i) d[i] = gld16((u32)((r8 + 8 * i) * 3584 + c8 * 16), wb);
    }
    asm volatile("s_waitcnt vmcnt(0)" : "+v"(ep), "+v"(d[0]), "+v"(d[1]), "+v"(d[2]), "+v"(d[3]), "+v"(d[4]), "+v"(d[5]), "+v"(d[6]), "+v"(d[7]), "+v"(d[8]), "+v"(d[9]),
                 "+v"(d[10]), "+v"(d[11]), "+v"(d[12]), "+v"(d[13]) :: "memory");
    const u32 tlo = (u32)((((u64)ep.y << 32) | ep.x) + TAG_SALT);
    if (!cols) {
#pragma unroll
        for (int h = 0; h < 2; ++h) {
            float s0 = 0.f, s1 = 0.f, q0 = 0.f, q1 = 0.f;
#pragma unroll
            for (int i = 0; i < 7; ++i)
#pragma unroll
                for (int e = 0; e < 4; e += 2) {
                    s0 = dot2(d[7 * h + i][e], 0x3f803f80u, s0); q0 = dot2(d[7 * h + i][e], d[7 * h + i][e], q0);
                    s1 = dot2(d[7 * h + i][e + 1], 0x3f803f80u, s1); q1 = dot2(d[7 * h + i][e + 1], d[7 * h + i][e + 1], q1);
                }
            const float mean = dpp_xor_sum8(s0 + s1) * (1.f / 448.f);
            const float var = fmaxf(dpp_xor_sum8(q0 + q1) * (1.f / 448.f) - mean * mean, 0.f);
            const float sd = __builtin_amdgcn_sqrtf(rbf(rbf(var) + 1e-5f));
            if (c8 == 0) gst8c<0>((u32)(WS_RG + (256 * xcd + 64 * j + 16 * wave + 8 * h + r8) * 8), ws, u32x2{pk2(mean, sd), tlo});
        }
    } else {
        float a[8] = {};
        u32 klo = 0x00003f80u, khi = 0x3f800000u; PIN(klo); PIN(khi);
#pragma unroll
        for (int i = 0; i < 14; ++i)
#pragma unroll
            for (int e = 0; e < 4; ++e) { a[2 * e] = dot2(d[i][e], klo, a[2 * e]); a[2 * e + 1] = dot2(d[i][e], khi, a[2 * e + 1]); }
        const u32 o = lds0 + ((wave - 4) * 8 + r8) * 272 + c8 * 32;
        sts16(o, __builtin_bit_cast(u32x4, f32x4{a[0], a[1], a[2], a[3]})); sts16(o + 16, __builtin_bit_cast(u32x4, f32x4{a[4], a[5], a[6], a[7]}));
    }
    BARL();
    if (wave == 4 && ctile < 14) {
        float t0 = 0.f, t1 = 0.f;
#pragma unroll
        for (int i = 0; i < 32; i += 2) { t0 += *(const LAS float*)(lds0 + i * 272 + lane * 4); t1 += *(const LAS float*)(lds0 + (i + 1) * 272 + lane * 4); }
        gst8c<0>((u32)(WS_CG + (((xcd & 1) * 14 + ctile) * 64 + lane) * 8), ws, u32x2{pk2(t0 + t1, (t0 + t1) - rbf(t0 + t1)), tlo});
    }
    {
        const int crot = (2 * xcd) % 14;
        setm0(lds0 + 16384 + wave * 4096);
        for (int c = 1; c < 14; ++c) {
            int cc = c + crot; if (cc >= 14) cc -= 14;
            const char* sb = (const char*)p.w2 + (size_t)cc * 114688 + (j * 8 + wave) * 3584;
            if (lane < 32) dmai<3072>(lane * 16u, sb);
            dmai<0>(lane * 16u, sb); dmai<1024>(lane * 16u, sb); dmai<2048>(lane * 16u, sb);
            WAITV(8);
        }
        WAITV(0);
    }
}

__global__ __launch_bounds__(512, 2) void fused_kernel(Params p) {
    const int wave = __builtin_amdgcn_readfirstlane(threadIdx.x >> 6);
    if (blockIdx.x < NHELP) { body_help(p, wave); return; }
    if (threadIdx.x < 32) *(LAS u32*)((u32)(size_t)(LAS char*)smem + L_FLAG + threadIdx.x * 4) = 0u;
    BARL();
    if (wave < 4) body_g(p, wave); else body_c(p, wave - 4);
}

__global__ __launch_bounds__(512) void probe_kernel(char* ws) { if (ws == nullptr) smem[0] = 1; }
extern "C" void kernel_launch(void* const* d_in, const int* in_sizes, int n_in,
                              void* d_out, int out_size, void* d_ws, size_t ws_size,
                              hipStream_t stream) {
    Params p;
    p.x = (const u16*)d_in[0]; p.k = (const u16*)d_in[1]; p.v = (const u16*)d_in[2]; p.shk = (const u16*)d_in[3];
    p.ssmk = (const u16*)d_in[4]; p.ssmd = (const u16*)d_in[5]; p.w1 = (const u16*)d_in[7]; p.b1 = (const u16*)d_in[8];
    p.w2 = (const u16*)d_in[9]; p.b2 = (const u16*)d_in[10];
    p.out = (u16*)d_out; p.ws = (char*)d_ws;
    (void)hipFuncSetAttribute((const void*)fused_kernel, hipFuncAttributeMaxDynamicSharedMemorySize, LDS_BYTES);
    fused_kernel<<<NWG + NHELP, 512, LDS_BYTES, stream>>>(p);
}
```

```cpp
#include <hip/hip_runtime.h>
#include <stdint.h>
#include <type_traits>

typedef unsigned short u16;
typedef unsigned int u32;
typedef unsigned long long u64;
typedef u32 u32x4 __attribute__((ext_vector_type(4)));
typedef u32 u32x2 __attribute__((ext_vector_type(2)));
typedef float f32x4 __attribute__((ext_vector_type(4)));
typedef float f32x2 __attribute__((ext_vector_type(2)));
typedef float f32x16 __attribute__((ext_vector_type(16)));
typedef __bf16 bf16x8 __attribute__((ext_vector_type(8)));
typedef __bf16 bf16x2 __attribute__((ext_vector_type(2)));
typedef short s16x4 __attribute__((ext_vector_type(4)));
#define LAS __attribute__((address_space(3)))
#define GAS __attribute__((address_space(1)))
#define DI __device__ __forceinline__

constexpr int S_ = 2048, D_ = 448, F_ = 1792;
constexpr int NHELP = 32, NWG = 224;
constexpr u32 MB = 256;
constexpr u32 WS_EPOCH = 0, WS_ALL = 2048, WS_KVF = 4096, WS_HF = WS_KVF + 65536, WS_YF = WS_HF + 65536, WS_RG = WS_YF + 65536, WS_CG = WS_RG + 16384;
constexpr u32 TP = 8192 + 128;
constexpr u32 WS_H = 1u << 20, WS_KVT = 9u << 20, WS_YT = 13u << 20;
constexpr u32 STAMP_OFF = 24u << 20;
constexpr int NSTAMP = 22;
constexpr u32 L_PIECE = 16384, L_W = 65536, L_LOC0 = 131072, L_MISC = 147456, LDS_BYTES = 163840;
constexpr u32 KVRAW_SZ = 5632, L_KVS = L_LOC0 + 4 * KVRAW_SZ, L_B2 = L_KVS + 9216, L_FLAG = L_B2 + 128, L_ROWF = L_FLAG + 128, L_B2F = L_ROWF + 256, L_R2 = L_B2F + 256;
constexpr u32 L_SCR = L_LOC0 + 2 * KVRAW_SZ + 9728 + 64;
static_assert(L_R2 + 256 <= LDS_BYTES, "lds");
constexpr u32 F_TOEP = L_FLAG, F_HCNT = L_FLAG + 16, F_HRDY = L_FLAG + 20, F_KVRDY = L_FLAG + 24, F_YCNT = L_FLAG + 28, F_YRDY = L_FLAG + 32, F_S0 = L_FLAG + 36, F_KVCNT = L_FLAG + 40, F_W = L_FLAG + 48, F_LAND = L_FLAG + 64, F_XCH = L_FLAG + 96, F_KVS = L_FLAG + 124, F_CONS = L_KVS + 128;
constexpr u32 L_CONV = 16384, CONV_CH = 65536, TOEP_CP = 4160, TOEP_CH = 4 * TOEP_CP, U_SZ = 4608, YS_SZ = 2304;
constexpr u64 TAG_SALT = 0x9E3779B97F4A7C15ull;
constexpr int SPIN_CAP = 1 << 15;
#define SLP 2

#define ST(i) do {} while (0)
#define STP nullptr

DI void dma16(u32 voff, const void* sbase, u32 lds) {
    asm volatile("s_mov_b32 m0, %2\n\ts_nop 0\n\tglobal_load_lds_dwordx4 %0, %1" :: "v"(voff), "s"(sbase), "s"(lds) : "memory");
}
DI void setm0(u32 lds) { asm volatile("s_mov_b32 m0, %0\n\ts_nop 0" :: "s"(lds) : "memory"); }
template <int OFF> DI void dmai(u32 voff, const void* sbase) { asm volatile("global_load_lds_dwordx4 %0, %1 offset:%2" :: "v"(voff), "s"(sbase), "i"(OFF) : "memory"); }
DI void bdma16(u32 voff, u32x4 srd, u32 lds) {
    asm volatile("s_mov_b32 m0, %2\n\ts_nop 0\n\tbuffer_load_dwordx4 %0, %1, 0 offen lds" :: "v"(voff), "s"(srd), "s"(lds) : "memory");
}
#define SB() __builtin_amdgcn_sched_barrier(0)
template <int OFF> DI u32x2 alds8(u32 a) { u32x2 r; asm volatile("ds_read_b64 %0, %1 offset:%2" : "=v"(r) : "v"(a), "i"(OFF)); return r; }
template <int OFF> DI u32x4 alds16(u32 a) { u32x4 r; asm volatile("ds_read_b128 %0, %1 offset:%2" : "=v"(r) : "v"(a), "i"(OFF)); return r; }
DI u32x4 gld16(u32 voff, const void* sbase) { u32x4 r; asm volatile("global_load_dwordx4 %0, %1, %2" : "=v"(r) : "v"(voff), "s"(sbase) : "memory"); return r; }
template <int OFF> DI u32x4 gld16c(u32 voff, const void* sbase) { u32x4 r; asm volatile("global_load_dwordx4 %0, %1, %2 offset:%3 sc1" : "=v"(r) : "v"(voff), "s"(sbase), "i"(OFF) : "memory"); return r; }
DI u32x2 gld8c(u32 voff, const void* sbase) { u32x2 r; asm volatile("global_load_dwordx2 %0, %1, %2 sc1" : "=v"(r) : "v"(voff), "s"(sbase) : "memory"); return r; }
DI void touch(const void* addr, u32 lds) { asm volatile("s_mov_b32 m0, %1\n\ts_nop 0\n\tglobal_load_lds_dword %0, off" :: "v"(addr), "s"(lds) : "memory"); }
DI u32 gld2(u32 voff, const void* sbase) { u32 r; asm volatile("global_load_ushort %0, %1, %2" : "=v"(r) : "v"(voff), "s"(sbase) : "memory"); return r; }
DI u32x4 bld16(u32 voff, u32x4 srd) { u32x4 r; asm volatile("buffer_load_dwordx4 %0, %1, %2, 0 offen" : "=v"(r) : "v"(voff), "s"(srd) : "memory"); return r; }
template <int OFF> DI u32x4 gld16o(u32 voff, const void* sbase) { u32x4 r; asm volatile("global_load_dwordx4 %0, %1, %2 offset:%3" : "=v"(r) : "v"(voff), "s"(sbase), "i"(OFF) : "memory"); return r; }
template <int OFF> DI void gst16c(u32 voff, void* sbase, u32x4 v) { asm volatile("global_store_dwordx4 %0, %1, %2 offset:%3 sc1\n\ts_nop 1" :: "v"(voff), "v"(v), "s"(sbase), "i"(OFF) : "memory"); }
template <int OFF> DI void gst8c(u32 voff, void* sbase, u32x2 v) { asm volatile("global_store_dwordx2 %0, %1, %2 offset:%3 sc1" :: "v"(voff), "v"(v), "s"(sbase), "i"(OFF) : "memory"); }
#define WAITV(n) asm volatile("s_waitcnt vmcnt(%0)" :: "i"(n) : "memory")
#define WAITBAR(n) asm volatile("s_waitcnt vmcnt(%0) lgkmcnt(0)\n\ts_barrier" :: "i"(n) : "memory")
#define BARL() asm volatile("s_waitcnt lgkmcnt(0)\n\ts_barrier" ::: "memory")
#define PIN(x) asm volatile("" : "+v"(x))

DI u32x4 mksrd(const void* p, u32 bytes) { u64 a = (u64)p; return u32x4{(u32)a, (u32)(a >> 32) & 0xffffu, bytes, 0x00020000u}; }
DI float bflo(u32 x) { return __uint_as_float(x << 16); }
DI float bfhi(u32 x) { return __uint_as_float(x & 0xffff0000u); }
DI u32 pk2(float a, float b) { bf16x2 v = {(__bf16)a, (__bf16)b}; return __builtin_bit_cast(u32, v); }
DI float rbf(float a) { return bflo(pk2(a, a)); }
DI float relu(float a) { int i = __float_as_int(a); return __int_as_float(i > 0 ? i : 0); }
DI float fma1(float a, float b, float c) { float r; asm("v_fma_f32 %0, %1, %2, %3" : "=v"(r) : "v"(a), "v"(b), "v"(c)); return r; }
DI float mul1(float a, float b) { float r; asm("v_mul_f32 %0, %1, %2" : "=v"(r) : "v"(a), "v"(b)); return r; }
DI float add1(float a, float b) { float r; asm("v_add_f32 %0, %1, %2" : "=v"(r) : "v"(a), "v"(b)); return r; }
DI float dot2(u32 a, u32 b, float c) { return __builtin_amdgcn_fdot2_f32_bf16(__builtin_bit_cast(bf16x2, a), __builtin_bit_cast(bf16x2, b), c, false); }
DI u32x4 lds16(u32 a) { return *(const LAS u32x4*)a; }
DI u32x2 lds8(u32 a) { return *(const LAS u32x2*)a; }
DI void sts16(u32 a, u32x4 v) { *(LAS u32x4*)a = v; }
DI void sts8(u32 a, u32x2 v) { *(LAS u32x2*)a = v; }
DI u32x2 ldtr(u32 a) { return __builtin_bit_cast(u32x2, __builtin_amdgcn_ds_read_tr16_b64_v4i16((LAS s16x4*)a)); }
DI u32x4 cat(u32x2 a, u32x2 b) { return u32x4{a.x, a.y, b.x, b.y}; }
DI f32x16 mfma32(u32x4 a, u32x4 b, f32x16 c) { return __builtin_amdgcn_mfma_f32_32x32x16_bf16(__builtin_bit_cast(bf16x8, a), __builtin_bit_cast(bf16x8, b), c, 0, 0, 0); }
DI f32x4 mfma16(u32x4 a, u32x4 b, f32x4 c) { return __builtin_amdgcn_mfma_f32_16x16x32_bf16(__builtin_bit_cast(bf16x8, a), __builtin_bit_cast(bf16x8, b), c, 0, 0, 0); }
DI float xhalf_sum(float v) { auto r = __builtin_amdgcn_permlane32_swap(__float_as_uint(v), __float_as_uint(v), false, false); return __uint_as_float(r[0]) + __uint_as_float(r[1]); }

struct Params {
    const u16 *x, *k, *v, *shk, *ssmk, *ssmd, *w1, *b1, *w2, *b2;
    u16* out; char* ws;
};

DI void poll8(u32 voff, const void* sbase, bool need, u32 tlo, u32 thi) {
    _Pragma("clang loop unroll(disable)") for (int spin = 0; spin < SPIN_CAP; ++spin) {
        u32x2 f = gld8c(voff, sbase);
        asm volatile("s_waitcnt vmcnt(0)" : "+v"(f) :: "memory");
        bool ok = !need || (f.x == tlo && f.y == thi);
        if (__all(ok)) break;
        __builtin_amdgcn_s_sleep(SLP);
    }
}
DI void poll16(u32 voff, const void* sbase, u32 tlo, u32 thi) {
    _Pragma("clang loop unroll(disable)") for (int spin = 0; spin < SPIN_CAP; ++spin) {
        u32x4 f = gld16c<0>(voff, sbase);
        asm volatile("s_waitcnt vmcnt(0)" : "+v"(f) :: "memory");
        bool ok = f.x == tlo && f.y == thi && f.z == tlo && f.w == thi;
        if (__all(ok)) break;
        __builtin_amdgcn_s_sleep(SLP);
    }
}

DI bool lds_arrive_last(u32 addr, u32 n) {
    u32 old = 0;
    if ((threadIdx.x & 63) == 0) old = __hip_atomic_fetch_add((LAS u32*)addr, 1u, __ATOMIC_RELAXED, __HIP_MEMORY_SCOPE_WORKGROUP);
    return (u32)__builtin_amdgcn_readfirstlane(old) == n - 1;
}
DI void lds_wait(u32 addr) {
    _Pragma("clang loop unroll(disable)") for (int spin = 0; spin < (1 << 22); ++spin) {
        if (__builtin_amdgcn_readfirstlane(*(volatile LAS u32*)addr)) break;
        __builtin_amdgcn_s_sleep(1);
    }
    asm volatile("" ::: "memory");
}
DI void lds_add(u32 addr) { asm volatile("" ::: "memory"); __hip_atomic_fetch_add((LAS u32*)addr, 1u, __ATOMIC_RELAXED, __HIP_MEMORY_SCOPE_WORKGROUP); asm volatile("" ::: "memory"); }
DI u32 lds_peek(u32 addr) { return *(volatile LAS u32*)addr; }
DI void lds_wait_ge(u32 addr, u32 n, u32 first) {
    u32 v = first;
    _Pragma("clang loop unroll(disable)") for (int spin = 0; spin < (1 << 22) && (u32)__builtin_amdgcn_readfirstlane(v) < n; ++spin) { __builtin_amdgcn_s_sleep(1); v = lds_peek(addr); }
    asm volatile("" ::: "memory");
}
template <int t> DI void cons_set(u32 lds0, int g) { asm volatile("s_waitcnt lgkmcnt(0)\n\tds_write_b32 %0, %1 offset:%2" :: "v"(lds0 + 131072 + g * 4), "v"(1u), "i"(F_CONS + t * 144 - 131072) : "memory"); }
DI void cons_wait(u32 lds0, int t) {
    _Pragma("clang loop unroll(disable)") for (int spin = 0; spin < (1 << 22); ++spin) {
        const u32x4 v = *(volatile LAS u32x4*)(lds0 + F_CONS + t * 144);
        if (__builtin_amdgcn_readfirstlane(v.x & v.y & v.z & v.w)) break;
        __builtin_amdgcn_s_sleep(1);
    }
    asm volatile("" ::: "memory");
}
DI void lds_set(u32 addr) { asm volatile("" ::: "memory"); if ((threadIdx.x & 63) == 0) *(volatile LAS u32*)addr = 1u; }
constexpr u32 loc_of(int c) { return (c % 5) == 0 ? L_LOC0 : (c % 5) == 1 ? 3 * L_PIECE : (c % 5) == 2 ? L_W + 3 * L_PIECE : (c % 5) == 3 ? 0u : L_W; }

#define CPRIO 2
#define YSLP 16
#define YFIRST 0
#define PRIOALL 0
#define RSLP 8
#define HWAIT 2
#define KW 0
#define CROTM 0
#define ROTM 1
#define LDELAY 0
#define KDELAY 0
#define HDV 4
constexpr int HD = HDV, HS = HD + 1;
constexpr int WS_ = 2;
constexpr int g2_younger(int c) {
    const int n = c + 1;
    if (n >= 14) return 63;
    bool sh = false, sw = n < (KW ? 5 : 2); int ah = 0, aw = sw ? 1000 : 0;
    for (int i = 2; i < 5; ++i) { if (sw) aw += 4; if (i == n) sw = true; }
    for (int i = 0; i < HD; ++i) { if (sh) ah += 4; if (sw) aw += 4; if (i == n) sh = true; }
    for (int i = 0; i < c; ++i) {
        if (i + HD < 14) { if (sh) ah += 4; if (sw) aw += 4; if (i + HD == n) sh = true; }
        if (i + 5 < 14) { if (sh) ah += 4; if (sw) aw += 4; if (i + 5 == n) sw = true; }
    }
    return ah < aw ? ah : aw;
}

struct G2Ctx { const char* w2b; const char* hb; int crot, fh2; u32 voffw[4], ldsg, vlane, ta[3][2], smask; };
template <int C> DI int g2_chunk(const G2Ctx& x) {
    int c = C + x.crot; if (c >= 14) c -= 14;
    return c;
}
template <int C> DI const char* g2_wsrc(const G2Ctx& x) { return x.w2b + (size_t)g2_chunk<C>(x) * (128 * 896); }
template <int C> DI u32 g2_loc(const G2Ctx& x) { return loc_of(C) + x.ldsg; }
template <int C, int N> DI void g2_w1(const G2Ctx& x, const char* sb) { if constexpr (N == 0) setm0(g2_loc<C>(x)); dmai<N * 1024>(x.voffw[N], sb); }
template <int C> DI void g2_issue_w(const G2Ctx& x) { const char* sb = g2_wsrc<C>(x); g2_w1<C, 0>(x, sb); g2_w1<C, 1>(x, sb); g2_w1<C, 2>(x, sb); g2_w1<C, 3>(x, sb); }
template <int C> DI const char* g2_hsrc(const G2Ctx& x) { return x.hb + g2_chunk<C>(x) * 8192; }
template <int N> DI u32x4 g2_h1(const G2Ctx& x, const char* sb) { return gld16c<(N & 1) * 1024>(x.vlane + (N >> 1) * 112 * 1024, sb); }
template <int C> DI void g2_issue_h(const G2Ctx& x, u32x4 (&hf)[HS][4]) {
    const char* sb = g2_hsrc<C>(x);
    hf[C % HS][0] = g2_h1<0>(x, sb); hf[C % HS][1] = g2_h1<1>(x, sb); hf[C % HS][2] = g2_h1<2>(x, sb); hf[C % HS][3] = g2_h1<3>(x, sb);
}
template <int C> DI void g2_read(const G2Ctx& x, u32x4 (&wf)[WS_][4]) {
    const u32 a0 = x.ta[loc_of(C) >> 16][0], a1 = x.ta[loc_of(C) >> 16][1];
    constexpr u32 lo = loc_of(C) & 0xffffu;
#pragma unroll
    for (int ks = 0; ks < 2; ++ks) {
        wf[C % WS_][ks * 2] = cat(ldtr(a0 + lo + ks * 2048), ldtr(a0 + lo + ks * 2048 + 1024));
        wf[C % WS_][ks * 2 + 1] = cat(ldtr(a1 + lo + ks * 2048), ldtr(a1 + lo + ks * 2048 + 1024));
    }
}
template <int C, int I> DI void g2_rd(const G2Ctx& x, u32x4 (&wf)[WS_][4]) {
    if constexpr (C < 14) {
        const u32 a = x.ta[loc_of(C) >> 16][I & 1]; constexpr u32 lo = (loc_of(C) & 0xffffu) + (I >> 1) * 2048;
        wf[C % WS_][I] = cat(ldtr(a + lo), ldtr(a + lo + 1024));
    }
}
DI u32 chk(u32 d, u32 m, u32 bad) { return __builtin_amdgcn_bitop3_b32(d, m, bad, 0xBE); }
template <int C> DI void g2_retry(const G2Ctx& x, u32x4 (&hf)[HS][4], u32 bad, u64* stamps) {
    if (__builtin_expect(!__any((bad & 0x80008000u) != 0u), 1)) return;
    _Pragma("clang loop unroll(disable)") for (int spin = 0; spin < SPIN_CAP; ++spin) {
        __builtin_amdgcn_s_sleep(RSLP);
        g2_issue_h<C>(x, hf);
        asm volatile("s_waitcnt vmcnt(0)" : "+v"(hf[C % HS][0]), "+v"(hf[C % HS][1]), "+v"(hf[C % HS][2]), "+v"(hf[C % HS][3]) :: "memory");
        bad = 0u;
#pragma unroll
        for (int i = 0; i < 16; ++i) bad = chk(hf[C % HS][i >> 2][i & 3], x.smask, bad);
        if (!__any((bad & 0x80008000u) != 0u)) break;
    }
}
template <int C> DI void g2_step(const G2Ctx& x, u32x4 (&hf)[HS][4], f32x16 (&acc)[2][2], u32x4 (&wf)[WS_][4], u64* stamps) {
    constexpr int N = (C + 1 < 14 ? C + 1 : C) % HS;
    asm volatile("s_waitcnt vmcnt(%4) lgkmcnt(0)" : "+v"(hf[N][0]), "+v"(hf[N][1]), "+v"(hf[N][2]), "+v"(hf[N][3]) : "i"(g2_younger(C)) : "memory");
    if constexpr (C % 3 == 0) ST(14 + C / 3);
    SB();
    constexpr bool DH = C + HD < 14, DW = C + 5 < 14, DC = C + 1 < 14;
    const char* hs = g2_hsrc<(DH ? C + HD : 0)>(x);
    const char* wsrc = g2_wsrc<(DW ? C + 5 : 0)>(x);
    u32 bad = 0u;
#define G2M(i) acc[((i) >> 1) & 1][(i) & 1] = mfma32(wf[C % WS_][((i) >> 2) * 2 + (((i) >> 1) & 1)], hf[C % HS][((i) & 1) * 2 + ((i) >> 2)], acc[((i) >> 1) & 1][(i) & 1]); \
               if constexpr (DC) { bad = chk(hf[N][(i) >> 1][((i) & 1) * 2], x.smask, bad); bad = chk(hf[N][(i) >> 1][((i) & 1) * 2 + 1], x.smask, bad); }
    G2M(0); g2_rd<C + 1, 0>(x, wf); if constexpr (DH) hf[(C + HD) % HS][0] = g2_h1<0>(x, hs); SB();
    G2M(1); g2_rd<C + 1, 1>(x, wf); if constexpr (DH) hf[(C + HD) % HS][1] = g2_h1<1>(x, hs); SB();
    G2M(2); g2_rd<C + 1, 2>(x, wf); if constexpr (DH) hf[(C + HD) % HS][2] = g2_h1<2>(x, hs); SB();
    G2M(3); g2_rd<C + 1, 3>(x, wf); if constexpr (DH) hf[(C + HD) % HS][3] = g2_h1<3>(x, hs); SB();
    G2M(4); if constexpr (DW) g2_w1<C + 5, 0>(x, wsrc); SB();
    G2M(5); if constexpr (DW) g2_w1<C + 5, 1>(x, wsrc); SB();
    G2M(6); if constexpr (DW) g2_w1<C + 5, 2>(x, wsrc); SB();
    G2M(7); if constexpr (DW) g2_w1<C + 5, 3>(x, wsrc); SB();
#undef G2M
    if constexpr (DC) { g2_retry<C + 1>(x, hf, bad, stamps); g2_step<C + 1>(x, hf, acc, wf, stamps); }
}

struct CF { u32x2 a0[8], a1[8]; u32x4 b[8]; };
template <int S, int R> DI void cv_ld(CF& f, u32 aA, u32 aB) {
    f.a0[R] = alds8<R * 512 + S * 64>(aA); f.a1[R] = alds8<R * 512 + S * 64 + 8>(aA);
    f.b[R] = alds16<(224 - 32 * R + 4 * S) * 16>(aB);
}
DI void cv_wait(CF& f) {
    asm volatile("s_waitcnt lgkmcnt(0)" : "+v"(f.a0[0]), "+v"(f.a0[1]), "+v"(f.a0[2]), "+v"(f.a0[3]), "+v"(f.a0[4]), "+v"(f.a0[5]), "+v"(f.a0[6]), "+v"(f.a0[7]),
                 "+v"(f.a1[0]), "+v"(f.a1[1]), "+v"(f.a1[2]), "+v"(f.a1[3]), "+v"(f.a1[4]), "+v"(f.a1[5]), "+v"(f.a1[6]), "+v"(f.a1[7]),
                 "+v"(f.b[0]), "+v"(f.b[1]), "+v"(f.b[2]), "+v"(f.b[3]), "+v"(f.b[4]), "+v"(f.b[5]), "+v"(f.b[6]), "+v"(f.b[7]));
}
constexpr int cv_r(int m) { int r = 0; while (m >= 8 - r) { m -= 8 - r; ++r; } return r; }
constexpr int cv_c(int m) { int r = 0; while (m >= 8 - r) { m -= 8 - r; ++r; } return m; }
template <int S, int M> DI void cv_mm(CF& f, CF& n, u32 aA, u32 aB, f32x4 (&acc)[8]) {
    constexpr int r = cv_r(M), c = cv_c(M);
    acc[r + c] = mfma16(cat(f.a0[r], f.a1[r]), f.b[c], acc[r + c]);
    if constexpr ((M & 3) == 0 && M / 4 < 8) cv_ld<S + 1, M / 4>(n, aA, aB);
    SB();
    if constexpr (M + 1 < 36) cv_mm<S, M + 1>(f, n, aA, aB, acc);
}
DI void conv_body(u32 aA, u32 aB, f32x4 (&acc)[8]) {
    CF f, n;
    asm volatile("s_waitcnt lgkmcnt(0)" ::: "memory");
    cv_ld<0, 0>(f, aA, aB); cv_ld<0, 1>(f, aA, aB); cv_ld<0, 2>(f, aA, aB); cv_ld<0, 3>(f, aA, aB);
    cv_ld<0, 4>(f, aA, aB); cv_ld<0, 5>(f, aA, aB); cv_ld<0, 6>(f, aA, aB); cv_ld<0, 7>(f, aA, aB);
    _Pragma("clang loop unroll(disable)") for (int it = 0; it < 2; ++it) {
        cv_wait(f); SB();
        cv_mm<0, 0>(f, n, aA, aB, acc);
        cv_wait(n); SB();
        cv_mm<1, 0>(n, f, aA, aB, acc);
        aA += 128; aB += 128;
    }
    cv_wait(f);
    asm volatile("" ::: "memory");
}

struct Geo { int bi, b, xcd, fh2, RB, CT, tb64, db, rot, crot; u32 R0, F0, T0, D0; };
DI Geo make_geo() {
    Geo g; g.b = blockIdx.x - NHELP; g.xcd = g.b & 7; const int bi = g.b >> 3; g.bi = bi;
    const int rq = g.xcd >> 1, rb = bi / 7, ct = bi - rb * 7;
    g.fh2 = g.xcd & 1; g.RB = 4 * rq + rb; g.CT = 7 * g.fh2 + ct; g.R0 = 128 * g.RB; g.F0 = 128 * g.CT;
    g.tb64 = 2 * g.RB + g.fh2; g.db = ct; g.T0 = 64 * g.tb64; g.D0 = 64 * g.db;

    g.rot = g.xcd >= 7 ? 0 : g.xcd;
     g.crot = CROTM == 0 ? g.CT : CROTM == 1 ? (g.CT + 3 * rb + (rb >> 1)) % 14 : CROTM == 2 ? 2 * ct : (2 * ct + 4 * rb - (rb == 3)) % 14;
    return g;
}
DI void make_g2(G2Ctx& g2, const Params& p, const Geo& q, int g, int lane, u32 lds0) {
    g2.w2b = (const char*)p.w2 + (size_t)(32 * g) * 896 + q.D0 * 2;
    g2.hb = p.ws + WS_H + (size_t)(q.tb64 * 2 * 112 + 2 * g) * 1024;
    g2.crot = q.crot; g2.fh2 = q.fh2;
#pragma unroll
    for (int n = 0; n < 4; ++n) g2.voffw[n] = (u32)((lane >> 3) * 896 + 16 * ((lane & 7) ^ (((lane >> 4) & 1) << 2)) + n * (8 * 896 - 1024));
    g2.ldsg = lds0 + g * 4096; g2.vlane = lane * 16u;
}
DI u32 slab_base(u32 lds0, int cw) { return lds0 + L_CONV + (cw >> 1) * CONV_CH + (cw & 1) * 8192; }
DI void final_epilogue(const Params& p, const Geo& q, u32 lds0) {
    int tz = threadIdx.x; PIN(tz);
    const int t = tz >> 3, c = tz & 7;
    const u32 pb = lds0 + ((t >> 5) ? 0u : 3 * L_PIECE) + ((t >> 4) & 1) * L_W + (t & 15) * 256;
    const u32 o0 = pb + 16 * ((2 * c) ^ (t & 7)), o1 = o0 ^ 16;
    f32x4 pa[4], pb_[4];
#pragma unroll
    for (int g = 0; g < 4; ++g) { pa[g] = __builtin_bit_cast(f32x4, lds16(o0 + g * 4096)); pb_[g] = __builtin_bit_cast(f32x4, lds16(o1 + g * 4096)); }
    const float r2 = *(const LAS float*)(lds0 + L_R2 + t * 4);
    const f32x4 b0 = __builtin_bit_cast(f32x4, lds16(lds0 + L_B2F + c * 32)), b1 = __builtin_bit_cast(f32x4, lds16(lds0 + L_B2F + c * 32 + 16));
    const u32 sb = slab_base(lds0, c >> 1) + t * 64 + (c & 1) * 32;
    const f32x4 sv0 = __builtin_bit_cast(f32x4, lds16(sb)), sv1 = __builtin_bit_cast(f32x4, lds16(sb + 16));
    const f32x4 xv0 = __builtin_bit_cast(f32x4, lds16(sb + 4096)), xv1 = __builtin_bit_cast(f32x4, lds16(sb + 4096 + 16));
    SB();
    const f32x4 s0 = (pa[0] + pa[1]) + (pa[2] + pa[3]), s1 = (pb_[0] + pb_[1]) + (pb_[2] + pb_[3]);
    f32x4 z0 = s0 * r2 + b0, z1 = s1 * r2 + b1;
    f32x4 e0 = z0 * -1.4426950408889634f, e1 = z1 * -1.4426950408889634f;
#pragma unroll
    for (int j = 0; j < 4; ++j) { e0[j] = __builtin_amdgcn_exp2f(e0[j]); e1[j] = __builtin_amdgcn_exp2f(e1[j]); }
    e0 += 1.f; e1 += 1.f;
#pragma unroll
    for (int j = 0; j < 4; ++j) { e0[j] = __builtin_amdgcn_rcpf(e0[j]); e1[j] = __builtin_amdgcn_rcpf(e1[j]); }
    z0 = z0 * e0; z1 = z1 * e1;
    z0 = z0 * sv0 + xv0; z1 = z1 * sv1 + xv1;
    const u32x4 res = {pk2(z0[0], z0[1]), pk2(z0[2], z0[3]), pk2(z1[0], z1[1]), pk2(z1[2], z1[3])};
    *(GAS u32x4*)((GAS char*)p.out + (size_t)(q.T0 + t) * 896 + q.D0 * 2 + c * 16) = res;
}
#define STAMP_DECL
#define STAMP_OUT(wave) do {} while (0)

extern __shared__ __attribute__((aligned(16))) char smem[];

struct CL { const char *xb, *wb; u32 vx[4], vw[4], sl; int rot; };
struct CSrc { const char *xs, *wsrc; };
template <int ST_> DI CSrc c_src(const CL& c) { int kc = ST_ + c.rot; if (kc >= 7) kc -= 7; return CSrc{c.xb + kc * 128, c.wb + (size_t)kc * 64 * 3584}; }
template <int ST_, int N> DI void c_dma1(const CL& c, const CSrc& s) {
    if constexpr (N == 0) setm0(c.sl + (ST_ & 3) * L_PIECE);
    if constexpr (N == 4) setm0(c.sl + (ST_ & 3) * L_PIECE + L_W);
    if constexpr (N < 4) dmai<N * 1024>(c.vx[N], s.xs); else dmai<(N - 4) * 1024>(c.vw[N - 4], s.wsrc);
}
template <int ST_> DI void c_dma(const CL& c) {
    const CSrc s = c_src<ST_>(c);
    c_dma1<ST_, 0>(c, s); c_dma1<ST_, 1>(c, s); c_dma1<ST_, 2>(c, s); c_dma1<ST_, 3>(c, s);
    c_dma1<ST_, 4>(c, s); c_dma1<ST_, 5>(c, s); c_dma1<ST_, 6>(c, s); c_dma1<ST_, 7>(c, s);
}
DI void make_cl(CL& c, const Params& p, const Geo& q, int cw, int lane, u32 lds0) {
    const int r8 = lane >> 3, c8 = lane & 7, r4 = lane >> 4, c16 = lane & 15;
#pragma unroll
    for (int n = 0; n < 4; ++n) {
        c.vx[n] = (u32)((32 * cw + r8) * 896 + 16 * (c8 ^ (((n & 1) << 2) | (r8 >> 1))) + n * (8 * 896 - 1024));
        c.vw[n] = (u32)((16 * cw + r4) * 3584 + 16 * (c16 ^ (r4 << 2)) + n * (4 * 3584 - 1024));
    }
    c.xb = (const char*)p.x + (size_t)q.R0 * 896; c.wb = (const char*)p.w1 + q.F0 * 2;
    c.sl = lds0 + cw * 4096; c.rot = q.rot;
}

template <int ST_> DI void l_dma(const CL& c) {
    const CSrc s = c_src<ST_>(c);
    const u32 sl = c.sl + (ST_ & 3) * L_PIECE;
    setm0(sl); dmai<0>(c.vx[0], s.xs); dmai<1024>(c.vx[1], s.xs); dmai<2048>(c.vx[2], s.xs); dmai<3072>(c.vx[3], s.xs);
    setm0(sl + L_W); dmai<0>(c.vw[0], s.wsrc); dmai<1024>(c.vw[1], s.wsrc); dmai<2048>(c.vw[2], s.wsrc); dmai<3072>(c.vw[3], s.wsrc);
    const char* x2 = s.xs + 32 * 896; const char* w2 = s.wsrc + 16 * 3584;
    setm0(sl + 4096); dmai<0>(c.vx[0], x2); dmai<1024>(c.vx[1], x2); dmai<2048>(c.vx[2], x2); dmai<3072>(c.vx[3], x2);
    setm0(sl + L_W + 4096); dmai<0>(c.vw[0], w2); dmai<1024>(c.vw[1], w2); dmai<2048>(c.vw[2], w2); dmai<3072>(c.vw[3], w2);
}

struct Fr { u32x4 x[4][2], w[4][2]; };
struct GA { u32 ax[4], aw[2]; };
template <int ST_> DI void g1_read(const GA& a, Fr& f) {
    constexpr u32 so = (ST_ & 3) * L_PIECE;
#pragma unroll
    for (int k = 0; k < 4; ++k) {
        f.w[k][0] = cat(ldtr(a.aw[0] + so + k * 4096), ldtr(a.aw[0] + so + k * 4096 + 1024));
        f.x[k][0] = lds16(a.ax[k] + so);
        f.w[k][1] = cat(ldtr(a.aw[1] + so + k * 4096), ldtr(a.aw[1] + so + k * 4096 + 1024));
        f.x[k][1] = lds16(a.ax[k] + so + 4096);
    }
}
template <int K0> DI void g1_mma(const Fr& f, f32x16 (&acc)[2][2]) {
#pragma unroll
    for (int k = K0; k < K0 + 2; ++k) {
        acc[0][0] = mfma32(f.w[k][0], f.x[k][0], acc[0][0]); acc[1][0] = mfma32(f.w[k][1], f.x[k][0], acc[1][0]);
        acc[0][1] = mfma32(f.w[k][0], f.x[k][1], acc[0][1]); acc[1][1] = mfma32(f.w[k][1], f.x[k][1], acc[1][1]);
    }
    SB();
}

template <int ST_, int I> DI void g1_rd(const GA& a, Fr& f) {
    constexpr u32 so = (ST_ & 3) * L_PIECE; constexpr int k = I >> 1, h = I & 1;
    f.w[k][h] = cat(ldtr(a.aw[h] + so + k * 4096), ldtr(a.aw[h] + so + k * 4096 + 1024));
    f.x[k][h] = lds16(a.ax[k] + so + h * 4096);
}
template <int K0, int I> DI void g1_m1(const Fr& f, f32x16 (&acc)[2][2]) {
    constexpr int k = K0 + (I >> 2), mt = I & 1, st = (I >> 1) & 1;
    acc[mt][st] = mfma32(f.w[k][mt], f.x[k][st], acc[mt][st]);
}
template <int ST_, bool CARRY> DI void g1_stage(const GA& a, const Fr& fp, Fr& fn, f32x16 (&acc)[2][2], u32 lds0, int g, u32& pk) {
    if constexpr (CARRY) { g1_m1<2, 0>(fp, acc); SB(); cons_set<ST_ - 1>(lds0, g); SB(); }
    lds_wait_ge(lds0 + F_LAND + 4 * ST_, ST_ == 0 ? 256 : (ST_ == 3 || ST_ == 6) ? 128 : 64, pk);
    if constexpr (CARRY) {
        g1_rd<ST_, 0>(a, fn); SB();
        g1_m1<2, 1>(fp, acc); SB(); g1_m1<2, 2>(fp, acc); g1_rd<ST_, 1>(a, fn); SB();
        g1_m1<2, 3>(fp, acc); SB(); g1_m1<2, 4>(fp, acc); g1_rd<ST_, 2>(a, fn); SB();
        g1_m1<2, 5>(fp, acc); g1_rd<ST_, 3>(a, fn); SB(); g1_m1<2, 6>(fp, acc); SB();
        g1_m1<2, 7>(fp, acc); SB();
    } else { g1_rd<ST_, 0>(a, fn); g1_rd<ST_, 1>(a, fn); g1_rd<ST_, 2>(a, fn); g1_rd<ST_, 3>(a, fn); SB(); }
    g1_m1<0, 0>(fn, acc); g1_rd<ST_, 4>(a, fn); SB(); g1_m1<0, 1>(fn, acc); SB();
    g1_m1<0, 2>(fn, acc); g1_rd<ST_, 5>(a, fn); SB(); g1_m1<0, 3>(fn, acc); g1_rd<ST_, 6>(a, fn); SB();
    g1_m1<0, 4>(fn, acc); g1_rd<ST_, 7>(a, fn); SB(); g1_m1<0, 5>(fn, acc); pk = lds_peek(lds0 + F_LAND + 4 * ST_ + 4); SB();
    g1_m1<0, 6>(fn, acc); SB(); g1_m1<0, 7>(fn, acc); SB();
}

DI void body_g(const Params& p, const int g) {
    STAMP_DECL
    const u32 lds0 = (u32)(size_t)(LAS char*)smem;
    const int lane = threadIdx.x & 63;
    const Geo q = make_geo();
    char* ws = p.ws;
    const int hi = lane >> 5, s31 = lane & 31;
    const int r8 = lane >> 3, c8 = lane & 7;
    const u32 raw = lds0 + L_LOC0 + g * KVRAW_SZ;
    {
        const u32 vkv = (u32)(r8 * 896 + c8 * 16);
        bdma16((u32)(((int)q.T0 + 16 * g - 8) * 896 + (int)q.D0 * 2) + vkv, mksrd(p.k, S_ * D_ * 2), raw);
        const char* kb = (const char*)p.k + (size_t)(q.T0 + 16 * g) * 896 + q.D0 * 2;
        const char* vb = (const char*)p.v + (size_t)(q.T0 + 16 * g) * 896 + q.D0 * 2;
        dmai<1024>(vkv, kb - 1024); dmai<2048>(vkv, kb + 8 * 896 - 2048); dmai<3072>(vkv, vb - 3072);
        setm0(raw + 4096);
        dmai<0>(vkv, vb + 8 * 896);
        if (lane < 32) dmai<1024>(lane * 16u, (const char*)p.shk + q.D0 * 8 - 1024);
        CL c; make_cl(c, p, q, g, lane, lds0);
        c_dma<0>(c);
    }
    u32x2 ep = gld8c(0u, ws + WS_EPOCH);
    const int fh = g & 1, sh = g >> 1;
    GA ga;
    {
        const int blk = (lane >> 4) & 1, qq = (lane & 15) >> 2, pp = lane & 3;
        const u32 Z = (u32)(hi ^ ((s31 >> 1) & 7));
#pragma unroll
        for (int k = 0; k < 4; ++k) ga.ax[k] = lds0 + (64 * sh + s31) * 128 + 16 * ((2 * k) ^ Z);
        ga.aw[0] = lds0 + L_W + (8 * hi + qq) * 256 + 16 * ((8 * fh + 2 * blk + (pp >> 1)) ^ (qq << 2)) + 8 * (pp & 1);
        ga.aw[1] = ga.aw[0] ^ 64;
    }
    WAITV(9);
    ST(1);
    {
        u32x4 wr[4], kr[5], vr[2];
#pragma unroll
        for (int e = 0; e < 4; ++e) wr[e] = lds16(raw + 5120 + c8 * 64 + e * 16);
#pragma unroll
        for (int j = 0; j < 5; ++j) kr[j] = lds16(raw + (5 + 2 * r8 + j) * 128 + c8 * 16);
        vr[0] = lds16(raw + 3072 + 2 * r8 * 128 + c8 * 16); vr[1] = lds16(raw + 3072 + (2 * r8 + 1) * 128 + c8 * 16);
        SB();
        u32x4 o0, o1;
#pragma unroll
        for (int e = 0; e < 4; ++e) {
            u32 pl[4], ph[4];
#pragma unroll
            for (int j = 0; j < 4; ++j) { pl[j] = __builtin_amdgcn_perm(kr[j][e], kr[j + 1][e], 0x05040100u); ph[j] = __builtin_amdgcn_perm(kr[j][e], kr[j + 1][e], 0x07060302u); }
            f32x2 a = {dot2(pl[0], wr[e].y, dot2(pl[2], wr[e].x, 0.f)), dot2(ph[0], wr[e].w, dot2(ph[2], wr[e].z, 0.f))};
            f32x2 b = {dot2(pl[1], wr[e].y, dot2(pl[3], wr[e].x, 0.f)), dot2(ph[1], wr[e].w, dot2(ph[3], wr[e].z, 0.f))};
            a *= f32x2{bflo(vr[0][e]), bfhi(vr[0][e])}; b *= f32x2{bflo(vr[1][e]), bfhi(vr[1][e])};
            o0[e] = pk2(a.x, a.y); o1[e] = pk2(b.x, b.y);
        }
        sts16(lds0 + L_KVS + (16 * g + 2 * r8) * 144 + c8 * 16, o0);
        sts16(lds0 + L_KVS + (16 * g + 2 * r8 + 1) * 144 + c8 * 16, o1);
        asm volatile("s_waitcnt lgkmcnt(0)" ::: "memory");
        lds_add(lds0 + F_KVS);
    }
    f32x16 acc[2][2] = {};
    Fr fa, fb;
    WAITV(1);
    lds_add(lds0 + F_LAND);
    u32 pk = lds_peek(lds0 + F_LAND);
    ST(2);
    g1_stage<0, false>(ga, fb, fa, acc, lds0, g, pk);
    g1_stage<1, true>(ga, fa, fb, acc, lds0, g, pk);
    g1_stage<2, true>(ga, fb, fa, acc, lds0, g, pk);
    asm volatile("s_waitcnt vmcnt(0)" : "+v"(ep) :: "memory");
    const u64 epoch = ((u64)(u32)__builtin_amdgcn_readfirstlane(ep.y) << 32) | (u32)__builtin_amdgcn_readfirstlane(ep.x);
    const u64 tag = epoch + TAG_SALT;
    const u32 tlo = (u32)tag, thi = (u32)(tag >> 32);
    const u32 smask = ((lane ^ (u32)epoch) & 1u) ? 0x80008000u : 0u;
    g1_stage<3, true>(ga, fa, fb, acc, lds0, g, pk);
    ST(3);
    g1_stage<4, true>(ga, fb, fa, acc, lds0, g, pk);
    const u32 rgo = WS_RG + (q.R0 + 64 * sh + lane) * 8, cgo = WS_CG + (q.F0 + 64 * fh + lane) * 8;
    u32x2 rg = gld8c(rgo, ws), cg = gld8c(cgo, ws);
    u32 b1v = gld2((u32)((64 * fh + lane) * 2), (const char*)p.b1 + q.F0 * 2);
    g1_stage<5, true>(ga, fa, fb, acc, lds0, g, pk);
    g1_stage<6, true>(ga, fb, fa, acc, lds0, g, pk);
    cons_set<6>(lds0, g);
    ST(5);
    asm volatile("s_waitcnt vmcnt(0)" : "+v"(rg), "+v"(cg), "+v"(b1v) :: "memory");
    _Pragma("clang loop unroll(disable)") for (int spin = 0; spin < SPIN_CAP && !__all(rg.y == tlo && cg.y == tlo); ++spin) {
        rg = gld8c(rgo, ws); cg = gld8c(cgo, ws);
        asm volatile("s_waitcnt vmcnt(0)" : "+v"(rg), "+v"(cg) :: "memory");
    }
    if (fh == 0 && sh == q.fh2) *(LAS u32*)(lds0 + L_ROWF + lane * 4) = rg.x;
    g1_mma<2>(fa, acc);
    {
        u32x4 ax[2], bx[2];
        const auto rr = __builtin_amdgcn_permlane32_swap(rg.x, rg.x, false, false);
        const auto cc = __builtin_amdgcn_permlane32_swap(cg.x, cg.x, false, false);
        const auto bb = __builtin_amdgcn_permlane32_swap(b1v, b1v, false, false);
#pragma unroll
        for (int i = 0; i < 2; ++i) {
            const u32 nm = __builtin_amdgcn_perm(rr[i], rr[i], 0x01000100u) ^ 0x80008000u;
            ax[i] = u32x4{cc[i], bb[i], 0u, 0u};
            bx[i] = u32x4{hi ? 0u : nm, hi ? 0u : (rr[i] >> 16), 0u, 0u};
        }
#pragma unroll
        for (int st = 0; st < 2; ++st)
#pragma unroll
            for (int mt = 0; mt < 2; ++mt) acc[mt][st] = mfma32(ax[mt], bx[st], acc[mt][st]);
#pragma unroll
        for (int st = 0; st < 2; ++st) {
            char* hb = ws + WS_H + (size_t)((4 * q.RB + 2 * sh + st) * 112 + q.CT * 8 + fh * 4) * 1024;
#pragma unroll
            for (int mt = 0; mt < 2; ++mt)
#pragma unroll
                for (int t = 0; t < 2; ++t) {
                    u32x4 hv;
#pragma unroll
                    for (int j = 0; j < 4; ++j) {
                        f32x2 a = {acc[mt][st][8 * t + 2 * j], acc[mt][st][8 * t + 2 * j + 1]};
                        f32x2 u = {relu(a.x), relu(a.y)};
                        u = u * u;
                        hv[j] = pk2(u.x, u.y) ^ smask;
                    }
                    if (mt == 0 && t == 0) gst16c<0>(lane * 16u, hb, hv);
                    if (mt == 0 && t == 1) gst16c<1024>(lane * 16u, hb, hv);
                    if (mt == 1 && t == 0) gst16c<2048>(lane * 16u, hb, hv);
                    if (mt == 1 && t == 1) gst16c<3072>(lane * 16u, hb, hv);
                    SB();
                }
        }
    }
    ST(6);
    G2Ctx g2; make_g2(g2, p, q, g, lane, lds0);
    {
        const int blk = (lane >> 4) & 1, qq = (lane & 15) >> 2, pp = lane & 3;
        const u32 t0 = g2.ldsg + (4 * hi + qq) * 128 + 16 * ((2 * blk + (pp >> 1)) ^ ((qq >> 1) << 2)) + 8 * (pp & 1);
#pragma unroll
        for (int i = 0; i < 3; ++i) { g2.ta[i][0] = t0 + i * 65536; g2.ta[i][1] = (t0 ^ 64) + i * 65536; PIN(g2.ta[i][0]); PIN(g2.ta[i][1]); }
    }
    g2.smask = smask;
    u32x4 wf[WS_][4];
    cons_wait(lds0, 4);
    asm volatile("s_waitcnt lgkmcnt(0)" ::: "memory");
    {
        u32x4 hf[HS][4];
        if (lds_arrive_last(lds0 + F_HCNT, 4)) { if (lane == 0) gst8c<0>(WS_ALL + q.b * 8, ws, u32x2{tlo, thi}); }
        if constexpr (!KW) { g2_issue_w<2>(g2); g2_issue_w<3>(g2); g2_issue_w<4>(g2); }
        if constexpr (HWAIT == 1) WAITV(12);
        if constexpr (HWAIT == 2) WAITV(0);
        g2_issue_h<0>(g2, hf); g2_issue_h<1>(g2, hf); if constexpr (HD > 2) g2_issue_h<2 % HS>(g2, hf); if constexpr (HD > 3) g2_issue_h<3 % HS>(g2, hf); if constexpr (HD > 4) g2_issue_h<4 % HS>(g2, hf);
        if constexpr (HD > 5) g2_issue_h<5 % HS>(g2, hf);
        ST(9);
        lds_wait(lds0 + F_W);
        if constexpr (KW) lds_wait(lds0 + F_W + 4 + (g >> 1) * 4);
        g2_read<0>(g2, wf);
        asm volatile("s_waitcnt vmcnt(%4)" : "+v"(hf[0][0]), "+v"(hf[0][1]), "+v"(hf[0][2]), "+v"(hf[0][3]) : "i"(4 * (HD - 1)) : "memory");
        {
            u32 bad = 0u;
#pragma unroll
            for (int i = 0; i < 16; ++i) bad = chk(hf[0][i >> 2][i & 3], smask, bad);
            g2_retry<0>(g2, hf, bad, STP);
        }
        ST(7);
        f32x16 a2[2][2] = {};
        g2_step<0>(g2, hf, a2, wf, STP);
        ST(8);
        const u32 pw = g2.ldsg + (s31 >> 4) * L_W + (s31 & 15) * 256;
#pragma unroll
        for (int s2 = 0; s2 < 2; ++s2)
#pragma unroll
            for (int dt = 0; dt < 2; ++dt)
#pragma unroll
                for (int gr = 0; gr < 4; ++gr) {
                    const u32 a = pw + (s2 ? 0u : 3 * L_PIECE) + 16 * ((8 * dt + 2 * gr + hi) ^ (s31 & 7));
                    sts16(a, __builtin_bit_cast(u32x4, f32x4{a2[dt][s2][4 * gr], a2[dt][s2][4 * gr + 1], a2[dt][s2][4 * gr + 2], a2[dt][s2][4 * gr + 3]}));
                }
    }
    BARL(); ST(10);
    final_epilogue(p, q, lds0);
    STAMP_OUT(g);
}

DI void body_c(const Params& p, const int cw) {
    STAMP_DECL
    const u32 lds0 = (u32)(size_t)(LAS char*)smem;
    const int lane = threadIdx.x & 63;
    const Geo q = make_geo();
    char* ws = p.ws;
    const int hi = lane >> 5, s31 = lane & 31;
    const int half = cw & 1, chl = cw >> 1, chan = 2 * q.b + chl;
    if (PRIOALL) __builtin_amdgcn_s_setprio(CPRIO);
    u32x2 ep;
    u32x4 xa, xb_; u32 dv;
    const char* xsl = (const char*)p.x + (size_t)q.T0 * 896 + (q.D0 + 16 * cw) * 2;
    if (cw < 2) {
        CL c, c2; make_cl(c, p, q, 0, lane, lds0); c2 = c; c2.xb += 64 * 896; c2.wb += 32 * 3584; c2.sl += 8192;
        ep = gld8c(0u, ws + WS_EPOCH);
        if (cw == 0) {
            l_dma<1>(c); l_dma<1>(c2); ST(1);
            WAITV(0); lds_add(lds0 + F_LAND + 4); ST(2);
            cons_wait(lds0, 0); l_dma<4>(c); l_dma<4>(c2); ST(19);
            WAITV(0); lds_add(lds0 + F_LAND + 16); ST(20);
        } else {
            l_dma<2>(c); l_dma<2>(c2); ST(1);
            WAITV(0); lds_add(lds0 + F_LAND + 8); ST(2);
            cons_wait(lds0, 1); l_dma<5>(c); l_dma<5>(c2); ST(19);
            WAITV(0); lds_add(lds0 + F_LAND + 20); ST(20);
        }
        asm volatile("s_waitcnt vmcnt(0)" : "+v"(ep) :: "memory");
        const u32 tg32 = (u32)((((u64)ep.y << 32) | ep.x) + TAG_SALT);
        if (cw == 0) {
            cons_wait(lds0, 3);
#pragma unroll
            for (int i = 0; i < 4; ++i) { G2Ctx g2; make_g2(g2, p, q, i, lane, lds0); g2_issue_w<0>(g2); g2_issue_w<1>(g2); }
        }
        xa = gld16((u32)(lane * 896), xsl); xb_ = gld16((u32)(lane * 896 + 16), xsl); dv = gld2(0u, (const char*)p.ssmd + chan * 2);
        const char* kb2 = ws + WS_KVT + (size_t)(2 * q.b + cw) * TP;
        u32x4 u[8];
        _Pragma("clang loop unroll(disable)") for (int spin = 0; spin < SPIN_CAP; ++spin) {
            u[0] = gld16c<0>(lane * 16u, kb2); u[1] = gld16c<1024>(lane * 16u, kb2); u[2] = gld16c<2048>(lane * 16u, kb2); u[3] = gld16c<3072>(lane * 16u, kb2);
            u[4] = gld16c<0>(lane * 16u, kb2 + 4096); u[5] = gld16c<1024>(lane * 16u, kb2 + 4096); u[6] = gld16c<2048>(lane * 16u, kb2 + 4096); u[7] = gld16c<3072>(lane * 16u, kb2 + 4096);
            asm volatile("s_waitcnt vmcnt(0)" : "+v"(u[0]), "+v"(u[1]), "+v"(u[2]), "+v"(u[3]), "+v"(u[4]), "+v"(u[5]), "+v"(u[6]), "+v"(u[7]), "+v"(xa), "+v"(xb_), "+v"(dv) :: "memory");
            if (cw == 0 && spin == 0) lds_set(lds0 + F_W);
            u32 bad = 0u;
#pragma unroll
            for (int i = 0; i < 8; ++i) bad = chk(u[i].w, tg32, chk(u[i].y, tg32, bad));
            if (!__any(bad != 0u)) break;
            __builtin_amdgcn_s_sleep(4);
        }
        ST(7);
        cons_wait(lds0, 5); cons_wait(lds0, 6);
        ST(14);
        const u32 uq_ = lds0 + L_CONV + cw * CONV_CH + TOEP_CH + lane * 8;
#pragma unroll
        for (int i = 0; i < 8; ++i) sts8(uq_ + i * 512, u32x2{u[i].x, u[i].z});
        sts8(uq_ + 4096, u32x2{0u, 0u});
        asm volatile("s_waitcnt lgkmcnt(0)" ::: "memory");
        lds_add(lds0 + F_TOEP + cw * 4);
    } else {
        const int kw = cw - 2;
        if (kw == 0 && lane < 8) dma16(lane * 16u, (const char*)p.b2 + q.D0 * 2, lds0 + L_B2);
        ep = gld8c(0u, ws + WS_EPOCH);
        CL c; make_cl(c, p, q, 2 * kw, lane, lds0);
        l_dma<3>(c);
        lds_wait_ge(lds0 + F_KVS, 256, lds_peek(lds0 + F_KVS));
        ST(2);
        asm volatile("s_waitcnt vmcnt(16)" : "+v"(ep) :: "memory");
        const u32 tg32 = (u32)((((u64)ep.y << 32) | ep.x) + TAG_SALT);
        {
            const int grp = lane >> 4, qq = (lane & 15) >> 2, pp = lane & 3;
            const u32 a = lds0 + L_KVS + (16 * grp + 4 * pp + 3 - qq) * 144 + kw * 64;
            const u32 vo = (u32)((q.D0 + 32 * kw + (lane & 3)) * TP + (2044 - (int)q.T0 - 4 * (lane >> 2)) * 4);
            u32x2 tr[8];
#pragma unroll
            for (int i = 0; i < 8; ++i) tr[i] = ldtr(a + 8 * i);
            SB();
#pragma unroll
            for (int i = 0; i < 8; ++i) gst16c<0>(vo, ws + WS_KVT + i * 4 * TP, u32x4{tr[i].x, tg32, tr[i].y, tg32});
        }
        ST(20);
        WAITV(8);
        lds_add(lds0 + F_LAND + 12);
        ST(19);
        cons_wait(lds0, 2);
        l_dma<6>(c);
        ST(3);
        u32x4 sk[5];
        const u32x4 sksrd = mksrd((const char*)p.ssmk + (size_t)(2 * q.b + kw) * 4096, 4096);
#pragma unroll
        for (int r = 0; r < 5; ++r) sk[r] = bld16((u32)((lane - 1 + 63 * r) * 16), sksrd);
        xa = gld16((u32)(lane * 896), xsl); xb_ = gld16((u32)(lane * 896 + 16), xsl); dv = gld2(0u, (const char*)p.ssmd + chan * 2);
        WAITV(8);
        lds_add(lds0 + F_LAND + 24);
        ST(21);
        if constexpr (KW) {
            G2Ctx ga_, gb_; make_g2(ga_, p, q, 2 * kw, lane, lds0); gb_ = ga_; gb_.w2b += 32 * 896; gb_.ldsg += 4096;
            cons_wait(lds0, 3); g2_issue_w<2>(ga_); g2_issue_w<2>(gb_);
            cons_wait(lds0, 4); g2_issue_w<3>(ga_); g2_issue_w<3>(gb_); g2_issue_w<4>(ga_); g2_issue_w<4>(gb_);
        }
        asm volatile("s_waitcnt vmcnt(%8)" : "+v"(sk[0]), "+v"(sk[1]), "+v"(sk[2]), "+v"(sk[3]), "+v"(sk[4]), "+v"(xa), "+v"(xb_), "+v"(dv) : "i"(KW ? 24 : 0) : "memory");
        u32x4 tw[5][4];
#pragma unroll
        for (int r = 0; r < 5; ++r) {
            const u32x4 B = sk[r];
            u32x4 A;
#pragma unroll
            for (int j = 0; j < 4; ++j) A[j] = __builtin_amdgcn_update_dpp(0u, B[j], 0x138  , 0xf, 0xf, false);
            const u32 e0 = __builtin_amdgcn_alignbit(A.y, A.x, 16), e1 = __builtin_amdgcn_alignbit(A.z, A.y, 16), e2 = __builtin_amdgcn_alignbit(A.w, A.z, 16),
                      e3 = __builtin_amdgcn_alignbit(B.x, A.w, 16), e4 = __builtin_amdgcn_alignbit(B.y, B.x, 16);
            tw[r][0] = u32x4{e0, e1, e2, e3}; tw[r][1] = u32x4{A.y, A.z, A.w, B.x}; tw[r][2] = u32x4{e1, e2, e3, e4}; tw[r][3] = u32x4{A.z, A.w, B.x, B.y};
            PIN(tw[r][0]); PIN(tw[r][1]); PIN(tw[r][2]); PIN(tw[r][3]);
        }
        cons_wait(lds0, 5); cons_wait(lds0, 6);
        ST(5);
        const u32 tq = lds0 + L_CONV + kw * CONV_CH + lane * 16;
#pragma unroll
        for (int r = 0; r < 5; ++r) {
            if ((lane > 0 || r == 0) && (r < 4 || lane < 6)) {
#pragma unroll
                for (int c = 0; c < 4; ++c) sts16(tq + r * 1008 + c * TOEP_CP, tw[r][c]);
            }
        }
        asm volatile("s_waitcnt lgkmcnt(0)" ::: "memory");
        lds_add(lds0 + F_TOEP + kw * 4);
        WAITV(0);
        lds_set(lds0 + F_W + 4 + kw * 4);
    }
    const u64 epoch = ((u64)(u32)__builtin_amdgcn_readfirstlane(ep.y) << 32) | (u32)__builtin_amdgcn_readfirstlane(ep.x);
    const u64 tag = epoch + TAG_SALT;
    const u32 tlo = (u32)tag, thi = (u32)(tag >> 32);
    const u32 tbase = lds0 + L_CONV + chl * CONV_CH;
    const u32 ub = tbase + TOEP_CH;
    ST(6);
    lds_wait_ge(lds0 + F_TOEP + chl * 4, 128, lds_peek(lds0 + F_TOEP + chl * 4));
    ST(15);
    const int bb = lane & 15, gg = lane >> 4;
    const u32 aA = tbase + (bb & 3) * TOEP_CP + ((bb >> 2) + 2 * gg) * 8 + half * 256;
    const u32 aB = ub + (30 - 2 * bb + gg) * 16 + half * 256;
    f32x4 ac8[8] = {};
    if (!PRIOALL) __builtin_amdgcn_s_setprio(CPRIO);
    conv_body(aA, aB, ac8);
    ST(16);
    f32x4 acc[4];
    {
        const u32 xo = ub + U_SZ + lane * 16;
#pragma unroll
        for (int i = 0; i < 4; ++i) { const f32x4 give = half ? ac8[i] : ac8[4 + i]; acc[i] = half ? ac8[4 + i] : ac8[i]; sts16(xo + half * 4096 + i * 1024, __builtin_bit_cast(u32x4, give)); }
        asm volatile("s_waitcnt lgkmcnt(0)" ::: "memory");
        lds_set(lds0 + F_XCH + cw * 4);
        lds_wait(lds0 + F_XCH + (cw ^ 1) * 4);
        f32x4 got[4];
#pragma unroll
        for (int i = 0; i < 4; ++i) got[i] = __builtin_bit_cast(f32x4, lds16(xo + (half ^ 1) * 4096 + i * 1024));
        SB();
#pragma unroll
        for (int i = 0; i < 4; ++i) acc[i] = f32x4{add1(acc[i][0], got[i][0]), add1(acc[i][1], got[i][1]), add1(acc[i][2], got[i][2]), add1(acc[i][3], got[i][3])};
    }
    ST(8);
    {
        const float dd = bflo(dv);
        char* yb = ws + WS_YT + (size_t)chan * TP;
        u32x2 uq[4];
#pragma unroll
        for (int i = 0; i < 4; ++i) uq[i] = lds8(ub + (2044 - (256 * (4 * half + i) + 16 * bb + 4 * gg)) * 2);
        SB();
#pragma unroll
        for (int i = 0; i < 4; ++i) {
            const int qt = 4 * half + i;
            const int t = 256 * qt + 16 * bb + 4 * gg;
            const u32x2 uu = uq[i];
            const u32 c01 = pk2(acc[i][0], acc[i][1]), c23 = pk2(acc[i][2], acc[i][3]);
            float y0 = fma1(bfhi(uu.y), dd, bflo(c01)), y1 = fma1(bflo(uu.y), dd, bfhi(c01));
            float y2 = fma1(bfhi(uu.x), dd, bflo(c23)), y3 = fma1(bflo(uu.x), dd, bfhi(c23));
            gst16c<0>((u32)(t * 4), yb, u32x4{pk2(y0, y1), tlo, pk2(y2, y3), tlo});
        }
        lds_arrive_last(lds0 + F_YCNT, 4);
    }
    _Pragma("clang loop unroll(disable)") for (int spin = 0; spin < (1 << 22); ++spin) { if (__builtin_amdgcn_readfirstlane(*(volatile LAS u32*)(lds0 + F_YCNT)) >= 4) break; __builtin_amdgcn_s_sleep(1); }
    asm volatile("" ::: "memory");
    const u32 slab = slab_base(lds0, cw) + lane * 64;
    f32x4 xn[4];
    {
        _Pragma("clang loop unroll(disable)") for (int spin = 0; spin < (1 << 22); ++spin) { if (__builtin_amdgcn_readfirstlane(*(volatile LAS u32*)(lds0 + F_HCNT)) >= 4) break; __builtin_amdgcn_s_sleep(1); }
        asm volatile("" ::: "memory");
        const u32 rf = *(const LAS u32*)(lds0 + L_ROWF + lane * 4);
        const float m = bflo(rf), ri = __builtin_amdgcn_rcpf(bfhi(rf)), nmri = -m * ri;
        const u32 xx[8] = {xa.x, xa.y, xa.z, xa.w, xb_.x, xb_.y, xb_.z, xb_.w};
#pragma unroll
        for (int j = 0; j < 4; ++j) {
            f32x4 v = {bflo(xx[2 * j]), bfhi(xx[2 * j]), bflo(xx[2 * j + 1]), bfhi(xx[2 * j + 1])};
            sts16(slab + 4096 + j * 16, __builtin_bit_cast(u32x4, v));
            xn[j] = f32x4{fma1(v[0], ri, nmri), fma1(v[1], ri, nmri), fma1(v[2], ri, nmri), fma1(v[3], ri, nmri)};
        }
        if (cw == 0) *(LAS float*)(lds0 + L_R2 + lane * 4) = ((lane ^ (u32)epoch) & 1u) ? -(ri * ri) : ri * ri;
        if (cw == 1) *(LAS float*)(lds0 + L_B2F + lane * 4) = bflo(*(const LAS u16*)(lds0 + L_B2 + lane * 2));
    }
    {
        const char* yb = ws + WS_YT + (size_t)(q.D0 + 16 * cw) * TP + q.T0 * 4;
        const u32 vo = (u32)(gg * TP + bb * 16);
        u32x4 y[4];
        __builtin_amdgcn_s_sleep(YFIRST);
        _Pragma("clang loop unroll(disable)") for (int spin = 0; spin < SPIN_CAP; ++spin) {
#pragma unroll
            for (int i = 0; i < 4; ++i) y[i] = gld16c<0>(vo, yb + i * 4 * TP);
            asm volatile("s_waitcnt vmcnt(0)" : "+v"(y[0]), "+v"(y[1]), "+v"(y[2]), "+v"(y[3]) :: "memory");
            u32 bad = 0u;
#pragma unroll
            for (int i = 0; i < 4; ++i) bad = chk(y[i].w, tlo, chk(y[i].y, tlo, bad));
            if (!__any(bad != 0u)) break;
            __builtin_amdgcn_s_sleep(YSLP);
        }
        ST(9);
        const u32 ys = tbase + TOEP_CH + U_SZ + half * 4096;
#pragma unroll
        for (int i = 0; i < 4; ++i) sts8(ys + (4 * i + gg) * 144 + bb * 8, u32x2{y[i].x, y[i].z});
        const int qq = (lane & 15) >> 2, pp = lane & 3;
        const u32 a = ys + qq * 144 + gg * 32 + pp * 8;
        u32x2 t0 = ldtr(a), t1 = ldtr(a + 4 * 144), t2 = ldtr(a + 8 * 144), t3 = ldtr(a + 12 * 144);
        const u32 yy[8] = {t0.x, t0.y, t1.x, t1.y, t2.x, t2.y, t3.x, t3.y};
#pragma unroll
        for (int j = 0; j < 4; ++j) {
            f32x4 v = {bflo(yy[2 * j]), bfhi(yy[2 * j]), bflo(yy[2 * j + 1]), bfhi(yy[2 * j + 1])};
            sts16(slab + ((j ^ (lane & 3)) * 0 + j) * 16, __builtin_bit_cast(u32x4, f32x4{mul1(v[0], xn[j][0]), mul1(v[1], xn[j][1]), mul1(v[2], xn[j][2]), mul1(v[3], xn[j][3])}));
        }
    }
    if (q.b == 0 && cw == 3) {
        _Pragma("clang loop unroll(disable)") for (int spin = 0; spin < SPIN_CAP; ++spin) {
            u32x4 f0 = gld16c<0>(lane * 16u, ws + WS_ALL), f1 = gld16c<1024>((lane < 48 ? lane : 0) * 16u, ws + WS_ALL);
            asm volatile("s_waitcnt vmcnt(0)" : "+v"(f0), "+v"(f1) :: "memory");
            bool ok = f0.x == tlo && f0.y == thi && f0.z == tlo && f0.w == thi && f1.x == tlo && f1.y == thi && f1.z == tlo && f1.w == thi;
            if (__all(ok)) break;
            __builtin_amdgcn_s_sleep(SLP);
        }
        if (lane == 0) { const u64 e1 = epoch + 1; gst8c<0>(0u, ws + WS_EPOCH, u32x2{(u32)e1, (u32)(e1 >> 32)}); }
    }
    __builtin_amdgcn_s_setprio(0);
    BARL(); ST(10);
    final_epilogue(p, q, lds0);
    STAMP_OUT(cw + 4);
}

DI float dpp_xor_sum8(float v) {
    v += __uint_as_float(__builtin_amdgcn_mov_dpp(__float_as_uint(v), 0xB1, 0xf, 0xf, true));
    v += __uint_as_float(__builtin_amdgcn_mov_dpp(__float_as_uint(v), 0x4E, 0xf, 0xf, true));
    v += __uint_as_float(__builtin_amdgcn_mov_dpp(__float_as_uint(v), 0x141, 0xf, 0xf, true));
    return v;
}
DI void body_help(const Params& p, const int wave) {
    const u32 lds0 = (u32)(size_t)(LAS char*)smem;
    const int lane = threadIdx.x & 63, r8 = lane >> 3, c8 = lane & 7;
    const int xcd = blockIdx.x & 7, j = blockIdx.x >> 3;
    char* ws = p.ws;
    u32x2 ep = gld8c(0u, ws + WS_EPOCH);
    u32x4 d[14];
    const int ctile = (xcd >> 1) * 4 + j;
    const bool cols = wave >= 4;
    if (!cols) {
        const char* xb = (const char*)p.x + (size_t)(256 * xcd + 64 * j + 16 * wave) * 896;
#pragma unroll
        for (int i = 0; i < 14; ++i) d[i] = gld16((u32)((r8 + 8 * (i / 7)) * 896 + (i % 7) * 128 + c8 * 16), xb);
    } else {
        const char* wb = (const char*)p.w1 + (size_t)(112 * (wave - 4)) * 3584 + ((xcd & 1) * 14 + (ctile < 14 ? ctile : 0)) * 128;
#pragma unroll
        for (int i = 0; i < 14; ++i) d[i] = gld16((u32)((r8 + 8 * i) * 3584 + c8 * 16), wb);
    }
    asm volatile("s_waitcnt vmcnt(0)" : "+v"(ep), "+v"(d[0]), "+v"(d[1]), "+v"(d[2]), "+v"(d[3]), "+v"(d[4]), "+v"(d[5]), "+v"(d[6]), "+v"(d[7]), "+v"(d[8]), "+v"(d[9]),
                 "+v"(d[10]), "+v"(d[11]), "+v"(d[12]), "+v"(d[13]) :: "memory");
    const u32 tlo = (u32)((((u64)ep.y << 32) | ep.x) + TAG_SALT);
    if (!cols) {
#pragma unroll
        for (int h = 0; h < 2; ++h) {
            float s0 = 0.f, s1 = 0.f, q0 = 0.f, q1 = 0.f;
#pragma unroll
            for (int i = 0; i < 7; ++i)
#pragma unroll
                for (int e = 0; e < 4; e += 2) {
                    s0 = dot2(d[7 * h + i][e], 0x3f803f80u, s0); q0 = dot2(d[7 * h + i][e], d[7 * h + i][e], q0);
                    s1 = dot2(d[7 * h + i][e + 1], 0x3f803f80u, s1); q1 = dot2(d[7 * h + i][e + 1], d[7 * h + i][e + 1], q1);
                }
            const float mean = dpp_xor_sum8(s0 + s1) * (1.f / 448.f);
            const float var = fmaxf(dpp_xor_sum8(q0 + q1) * (1.f / 448.f) - mean * mean, 0.f);
            const float sd = __builtin_amdgcn_sqrtf(rbf(rbf(var) + 1e-5f));
            if (c8 == 0) gst8c<0>((u32)(WS_RG + (256 * xcd + 64 * j + 16 * wave + 8 * h + r8) * 8), ws, u32x2{pk2(mean, sd), tlo});
        }
    } else {
        float a[8] = {};
        u32 klo = 0x00003f80u, khi = 0x3f800000u; PIN(klo); PIN(khi);
#pragma unroll
        for (int i = 0; i < 14; ++i)
#pragma unroll
            for (int e = 0; e < 4; ++e) { a[2 * e] = dot2(d[i][e], klo, a[2 * e]); a[2 * e + 1] = dot2(d[i][e], khi, a[2 * e + 1]); }
        const u32 o = lds0 + ((wave - 4) * 8 + r8) * 272 + c8 * 32;
        sts16(o, __builtin_bit_cast(u32x4, f32x4{a[0], a[1], a[2], a[3]})); sts16(o + 16, __builtin_bit_cast(u32x4, f32x4{a[4], a[5], a[6], a[7]}));
    }
    BARL();
    if (wave == 4 && ctile < 14) {
        float t0 = 0.f, t1 = 0.f;
#pragma unroll
        for (int i = 0; i < 32; i += 2) { t0 += *(const LAS float*)(lds0 + i * 272 + lane * 4); t1 += *(const LAS float*)(lds0 + (i + 1) * 272 + lane * 4); }
        gst8c<0>((u32)(WS_CG + (((xcd & 1) * 14 + ctile) * 64 + lane) * 8), ws, u32x2{pk2(t0 + t1, (t0 + t1) - rbf(t0 + t1)), tlo});
    }
    {
        const int crot = (2 * xcd) % 14;
        setm0(lds0 + 16384 + wave * 4096);
        for (int c = 1; c < 14; ++c) {
            int cc = c + crot; if (cc >= 14) cc -= 14;
            const char* sb = (const char*)p.w2 + (size_t)cc * 114688 + (j * 8 + wave) * 3584;
            if (lane < 32) dmai<3072>(lane * 16u, sb);
            dmai<0>(lane * 16u, sb); dmai<1024>(lane * 16u, sb); dmai<2048>(lane * 16u, sb);
            WAITV(8);
        }
        WAITV(0);
    }
}

__global__ __launch_bounds__(512, 2) void fused_kernel(Params p) {
    const int wave = __builtin_amdgcn_readfirstlane(threadIdx.x >> 6);
    if (blockIdx.x < NHELP) { body_help(p, wave); return; }
    if (threadIdx.x < 32) *(LAS u32*)((u32)(size_t)(LAS char*)smem + L_FLAG + threadIdx.x * 4) = 0u;
    else if (threadIdx.x < 64) *(LAS u32*)((u32)(size_t)(LAS char*)smem + F_CONS + ((threadIdx.x - 32) >> 2) * 144 + (threadIdx.x & 3) * 4) = 0u;
    BARL();
    if (wave < 4) body_g(p, wave); else body_c(p, wave - 4);
}

extern "C" void kernel_launch(void* const* d_in, const int* in_sizes, int n_in,
                              void* d_out, int out_size, void* d_ws, size_t ws_size,
                              hipStream_t stream) {
    Params p;
    p.x = (const u16*)d_in[0]; p.k = (const u16*)d_in[1]; p.v = (const u16*)d_in[2]; p.shk = (const u16*)d_in[3];
    p.ssmk = (const u16*)d_in[4]; p.ssmd = (const u16*)d_in[5]; p.w1 = (const u16*)d_in[7]; p.b1 = (const u16*)d_in[8];
    p.w2 = (const u16*)d_in[9]; p.b2 = (const u16*)d_in[10];
    p.out = (u16*)d_out; p.ws = (char*)d_ws;
    (void)hipFuncSetAttribute((const void*)fused_kernel, hipFuncAttributeMaxDynamicSharedMemorySize, LDS_BYTES);
    fused_kernel<<<NWG + NHELP, 512, LDS_BYTES, stream>>>(p);
}
```

```cpp
#include <hip/hip_runtime.h>
#include <stdint.h>
#include <type_traits>

typedef unsigned short u16;
typedef unsigned int u32;
typedef unsigned long long u64;
typedef u32 u32x4 __attribute__((ext_vector_type(4)));
typedef u32 u32x2 __attribute__((ext_vector_type(2)));
typedef float f32x4 __attribute__((ext_vector_type(4)));
typedef float f32x2 __attribute__((ext_vector_type(2)));
typedef float f32x16 __attribute__((ext_vector_type(16)));
typedef __bf16 bf16x8 __attribute__((ext_vector_type(8)));
typedef __bf16 bf16x2 __attribute__((ext_vector_type(2)));
typedef short s16x4 __attribute__((ext_vector_type(4)));
#define LAS __attribute__((address_space(3)))
#define GAS __attribute__((address_space(1)))
#define DI __device__ __forceinline__

constexpr int S_ = 2048, D_ = 448, F_ = 1792;
constexpr int NHELP = 32, NWG = 224;
constexpr u32 MB = 256;
constexpr u32 WS_EPOCH = 0, WS_ALL = 2048, WS_KVF = 4096, WS_HF = WS_KVF + 65536, WS_YF = WS_HF + 65536, WS_RG = WS_YF + 65536, WS_CG = WS_RG + 16384;
constexpr u32 TP = 4096 + 128;
constexpr u32 WS_H = 1u << 20, WS_KVT = 9u << 20, WS_YT = 11u << 20;
constexpr u32 STAMP_OFF = 16u << 20;
constexpr int NSTAMP = 22;
constexpr u32 L_PIECE = 16384, L_W = 65536, L_LOC0 = 131072, L_MISC = 147456, LDS_BYTES = 163840;
constexpr u32 KVRAW_SZ = 5632, L_KVS = L_LOC0 + 4 * KVRAW_SZ, L_B2 = L_KVS + 9216, L_FLAG = L_B2 + 128, L_ROWF = L_FLAG + 128, L_B2F = L_ROWF + 256, L_R2 = L_B2F + 256;
constexpr u32 L_SCR = L_LOC0 + 2 * KVRAW_SZ + 9728 + 64;
static_assert(L_R2 + 256 <= LDS_BYTES, "lds");
constexpr u32 F_TOEP = L_FLAG, F_HCNT = L_FLAG + 16, F_HRDY = L_FLAG + 20, F_KVRDY = L_FLAG + 24, F_YCNT = L_FLAG + 28, F_YRDY = L_FLAG + 32, F_S0 = L_FLAG + 36, F_KVCNT = L_FLAG + 40, F_W = L_FLAG + 48, F_LAND = L_FLAG + 64, F_XCH = L_FLAG + 96, F_KVS = L_FLAG + 124, F_CONS = L_KVS + 128;
constexpr u32 L_CONV = 16384, CONV_CH = 65536, TOEP_CP = 4160, TOEP_CH = 4 * TOEP_CP, U_SZ = 4608, YS_SZ = 2304;
constexpr u64 TAG_SALT = 0x9E3779B97F4A7C15ull;
constexpr int SPIN_CAP = 1 << 15;
#define SLP 2

#define ST(i) do {} while (0)
#define STP nullptr

DI void dma16(u32 voff, const void* sbase, u32 lds) {
    asm volatile("s_mov_b32 m0, %2\n\ts_nop 0\n\tglobal_load_lds_dwordx4 %0, %1" :: "v"(voff), "s"(sbase), "s"(lds) : "memory");
}
DI void setm0(u32 lds) { asm volatile("s_mov_b32 m0, %0\n\ts_nop 0" :: "s"(lds) : "memory"); }
template <int OFF> DI void dmai(u32 voff, const void* sbase) { asm volatile("global_load_lds_dwordx4 %0, %1 offset:%2" :: "v"(voff), "s"(sbase), "i"(OFF) : "memory"); }
DI void bdma16(u32 voff, u32x4 srd, u32 lds) {
    asm volatile("s_mov_b32 m0, %2\n\ts_nop 0\n\tbuffer_load_dwordx4 %0, %1, 0 offen lds" :: "v"(voff), "s"(srd), "s"(lds) : "memory");
}
#define SB() __builtin_amdgcn_sched_barrier(0)
template <int OFF> DI u32x2 alds8(u32 a) { u32x2 r; asm volatile("ds_read_b64 %0, %1 offset:%2" : "=v"(r) : "v"(a), "i"(OFF)); return r; }
template <int OFF> DI u32x4 alds16(u32 a) { u32x4 r; asm volatile("ds_read_b128 %0, %1 offset:%2" : "=v"(r) : "v"(a), "i"(OFF)); return r; }
DI u32x4 gld16(u32 voff, const void* sbase) { u32x4 r; asm volatile("global_load_dwordx4 %0, %1, %2" : "=v"(r) : "v"(voff), "s"(sbase) : "memory"); return r; }
template <int OFF> DI u32x4 gld16c(u32 voff, const void* sbase) { u32x4 r; asm volatile("global_load_dwordx4 %0, %1, %2 offset:%3 sc1" : "=v"(r) : "v"(voff), "s"(sbase), "i"(OFF) : "memory"); return r; }
DI u32x2 gld8c(u32 voff, const void* sbase) { u32x2 r; asm volatile("global_load_dwordx2 %0, %1, %2 sc1" : "=v"(r) : "v"(voff), "s"(sbase) : "memory"); return r; }
DI void touch(const void* addr, u32 lds) { asm volatile("s_mov_b32 m0, %1\n\ts_nop 0\n\tglobal_load_lds_dword %0, off" :: "v"(addr), "s"(lds) : "memory"); }
DI u32 gld2(u32 voff, const void* sbase) { u32 r; asm volatile("global_load_ushort %0, %1, %2" : "=v"(r) : "v"(voff), "s"(sbase) : "memory"); return r; }
DI u32x4 bld16(u32 voff, u32x4 srd) { u32x4 r; asm volatile("buffer_load_dwordx4 %0, %1, %2, 0 offen" : "=v"(r) : "v"(voff), "s"(srd) : "memory"); return r; }
template <int OFF> DI u32x4 gld16o(u32 voff, const void* sbase) { u32x4 r; asm volatile("global_load_dwordx4 %0, %1, %2 offset:%3" : "=v"(r) : "v"(voff), "s"(sbase), "i"(OFF) : "memory"); return r; }
template <int OFF> DI void gst16c(u32 voff, void* sbase, u32x4 v) { asm volatile("global_store_dwordx4 %0, %1, %2 offset:%3 sc1\n\ts_nop 1" :: "v"(voff), "v"(v), "s"(sbase), "i"(OFF) : "memory"); }
template <int OFF> DI void gst8c(u32 voff, void* sbase, u32x2 v) { asm volatile("global_store_dwordx2 %0, %1, %2 offset:%3 sc1" :: "v"(voff), "v"(v), "s"(sbase), "i"(OFF) : "memory"); }
#define WAITV(n) asm volatile("s_waitcnt vmcnt(%0)" :: "i"(n) : "memory")
#define WAITBAR(n) asm volatile("s_waitcnt vmcnt(%0) lgkmcnt(0)\n\ts_barrier" :: "i"(n) : "memory")
#define BARL() asm volatile("s_waitcnt lgkmcnt(0)\n\ts_barrier" ::: "memory")
#define PIN(x) asm volatile("" : "+v"(x))

DI u32x4 mksrd(const void* p, u32 bytes) { u64 a = (u64)p; return u32x4{(u32)a, (u32)(a >> 32) & 0xffffu, bytes, 0x00020000u}; }
DI float bflo(u32 x) { return __uint_as_float(x << 16); }
DI float bfhi(u32 x) { return __uint_as_float(x & 0xffff0000u); }
DI u32 pk2(float a, float b) { bf16x2 v = {(__bf16)a, (__bf16)b}; return __builtin_bit_cast(u32, v); }
DI float rbf(float a) { return bflo(pk2(a, a)); }
DI float relu(float a) { int i = __float_as_int(a); return __int_as_float(i > 0 ? i : 0); }
DI float fma1(float a, float b, float c) { float r; asm("v_fma_f32 %0, %1, %2, %3" : "=v"(r) : "v"(a), "v"(b), "v"(c)); return r; }
DI float mul1(float a, float b) { float r; asm("v_mul_f32 %0, %1, %2" : "=v"(r) : "v"(a), "v"(b)); return r; }
DI float add1(float a, float b) { float r; asm("v_add_f32 %0, %1, %2" : "=v"(r) : "v"(a), "v"(b)); return r; }
DI float dot2(u32 a, u32 b, float c) { return __builtin_amdgcn_fdot2_f32_bf16(__builtin_bit_cast(bf16x2, a), __builtin_bit_cast(bf16x2, b), c, false); }
DI u32x4 lds16(u32 a) { return *(const LAS u32x4*)a; }
DI u32x2 lds8(u32 a) { return *(const LAS u32x2*)a; }
DI void sts16(u32 a, u32x4 v) { *(LAS u32x4*)a = v; }
DI void sts8(u32 a, u32x2 v) { *(LAS u32x2*)a = v; }
DI u32x2 ldtr(u32 a) { return __builtin_bit_cast(u32x2, __builtin_amdgcn_ds_read_tr16_b64_v4i16((LAS s16x4*)a)); }
DI u32x4 cat(u32x2 a, u32x2 b) { return u32x4{a.x, a.y, b.x, b.y}; }
DI f32x16 mfma32(u32x4 a, u32x4 b, f32x16 c) { return __builtin_amdgcn_mfma_f32_32x32x16_bf16(__builtin_bit_cast(bf16x8, a), __builtin_bit_cast(bf16x8, b), c, 0, 0, 0); }
DI f32x4 mfma16(u32x4 a, u32x4 b, f32x4 c) { return __builtin_amdgcn_mfma_f32_16x16x32_bf16(__builtin_bit_cast(bf16x8, a), __builtin_bit_cast(bf16x8, b), c, 0, 0, 0); }
DI float xhalf_sum(float v) { auto r = __builtin_amdgcn_permlane32_swap(__float_as_uint(v), __float_as_uint(v), false, false); return __uint_as_float(r[0]) + __uint_as_float(r[1]); }

struct Params {
    const u16 *x, *k, *v, *shk, *ssmk, *ssmd, *w1, *b1, *w2, *b2;
    u16* out; char* ws;
};

DI void poll8(u32 voff, const void* sbase, bool need, u32 tlo, u32 thi) {
    _Pragma("clang loop unroll(disable)") for (int spin = 0; spin < SPIN_CAP; ++spin) {
        u32x2 f = gld8c(voff, sbase);
        asm volatile("s_waitcnt vmcnt(0)" : "+v"(f) :: "memory");
        bool ok = !need || (f.x == tlo && f.y == thi);
        if (__all(ok)) break;
        __builtin_amdgcn_s_sleep(SLP);
    }
}
DI void poll16(u32 voff, const void* sbase, u32 tlo, u32 thi) {
    _Pragma("clang loop unroll(disable)") for (int spin = 0; spin < SPIN_CAP; ++spin) {
        u32x4 f = gld16c<0>(voff, sbase);
        asm volatile("s_waitcnt vmcnt(0)" : "+v"(f) :: "memory");
        bool ok = f.x == tlo && f.y == thi && f.z == tlo && f.w == thi;
        if (__all(ok)) break;
        __builtin_amdgcn_s_sleep(SLP);
    }
}

DI bool lds_arrive_last(u32 addr, u32 n) {
    u32 old = 0;
    if ((threadIdx.x & 63) == 0) old = __hip_atomic_fetch_add((LAS u32*)addr, 1u, __ATOMIC_RELAXED, __HIP_MEMORY_SCOPE_WORKGROUP);
    return (u32)__builtin_amdgcn_readfirstlane(old) == n - 1;
}
DI void lds_wait(u32 addr) {
    _Pragma("clang loop unroll(disable)") for (int spin = 0; spin < (1 << 22); ++spin) {
        if (__builtin_amdgcn_readfirstlane(*(volatile LAS u32*)addr)) break;
        __builtin_amdgcn_s_sleep(1);
    }
    asm volatile("" ::: "memory");
}
DI void lds_add(u32 addr) { asm volatile("" ::: "memory"); __hip_atomic_fetch_add((LAS u32*)addr, 1u, __ATOMIC_RELAXED, __HIP_MEMORY_SCOPE_WORKGROUP); asm volatile("" ::: "memory"); }
DI u32 lds_peek(u32 addr) { return *(volatile LAS u32*)addr; }
DI void lds_wait_ge(u32 addr, u32 n, u32 first) {
    u32 v = first;
    _Pragma("clang loop unroll(disable)") for (int spin = 0; spin < (1 << 22) && (u32)__builtin_amdgcn_readfirstlane(v) < n; ++spin) { __builtin_amdgcn_s_sleep(1); v = lds_peek(addr); }
    asm volatile("" ::: "memory");
}
template <int t> DI void cons_set(u32 lds0, int g) { asm volatile("s_waitcnt lgkmcnt(0)\n\tds_write_b32 %0, %1 offset:%2" :: "v"(lds0 + 131072 + g * 4), "v"(1u), "i"(F_CONS + t * 144 - 131072) : "memory"); }
DI void cons_wait(u32 lds0, int t) {
    _Pragma("clang loop unroll(disable)") for (int spin = 0; spin < (1 << 22); ++spin) {
        const u32x4 v = *(volatile LAS u32x4*)(lds0 + F_CONS + t * 144);
        if (__builtin_amdgcn_readfirstlane(v.x & v.y & v.z & v.w)) break;
        __builtin_amdgcn_s_sleep(1);
    }
    asm volatile("" ::: "memory");
}
DI void lds_set(u32 addr) { asm volatile("" ::: "memory"); if ((threadIdx.x & 63) == 0) *(volatile LAS u32*)addr = 1u; }
constexpr u32 loc_of(int c) { return (c % 5) == 0 ? L_LOC0 : (c % 5) == 1 ? 3 * L_PIECE : (c % 5) == 2 ? L_W + 3 * L_PIECE : (c % 5) == 3 ? 0u : L_W; }

#define HDV 3
constexpr int HD = HDV, HS = HD + 1;
constexpr int RD = 4, WS_ = RD + 1;
constexpr int g2_younger(int c) {
    const int wneed = c + RD;
    bool sh = false, sw = false; int ah = 0, aw = 0;
    const bool wdone = wneed < 5 + RD || wneed >= 14;
    for (int i = 0; i < HD; ++i) { if (sh) ah += 4; if (i == c) sh = true; }
    for (int i = 0; i < c; ++i) {
        if (i + HD < 14) { if (sh) ah += 4; if (sw) aw += 4; if (i + HD == c) sh = true; }
        if (i >= 1 && i + RD + 4 < 14) { if (sh) ah += 4; if (sw) aw += 4; if (i + RD + 4 == wneed) sw = true; }
    }
    if (wdone) return ah;
    return ah < aw ? ah : aw;
}

struct G2Ctx { const char* w2b; const char* hb; int crot; u32 voffw[4], ldsg, vlane, ta[3][2]; };
template <int C> DI const char* g2_wsrc(const G2Ctx& x) { int c = C + x.crot; if (c >= 14) c -= 14; return x.w2b + (size_t)c * (128 * 896); }
template <int C> DI u32 g2_loc(const G2Ctx& x) { return loc_of(C) + x.ldsg; }
template <int C, int N> DI void g2_w1(const G2Ctx& x, const char* sb) { if constexpr (N == 0) setm0(g2_loc<C>(x)); dmai<N * 1024>(x.voffw[N], sb); }
template <int C> DI void g2_issue_w(const G2Ctx& x) { const char* sb = g2_wsrc<C>(x); g2_w1<C, 0>(x, sb); g2_w1<C, 1>(x, sb); g2_w1<C, 2>(x, sb); g2_w1<C, 3>(x, sb); }
template <int C> DI const char* g2_hsrc(const G2Ctx& x) { int c = C + x.crot; if (c >= 14) c -= 14; return x.hb + c * 8192; }
template <int N> DI u32x4 g2_h1(const G2Ctx& x, const char* sb) { return gld16c<(N & 1) * 1024>(x.vlane + (N >> 1) * 112 * 1024, sb); }
template <int C> DI void g2_issue_h(const G2Ctx& x, u32x4 (&hf)[HS][4]) {
    const char* sb = g2_hsrc<C>(x);
    hf[C % HS][0] = g2_h1<0>(x, sb); hf[C % HS][1] = g2_h1<1>(x, sb); hf[C % HS][2] = g2_h1<2>(x, sb); hf[C % HS][3] = g2_h1<3>(x, sb);
}
template <int C> DI void g2_read(const G2Ctx& x, u32x4 (&wf)[WS_][4]) {
    const u32 a0 = x.ta[loc_of(C) >> 16][0], a1 = x.ta[loc_of(C) >> 16][1];
    constexpr u32 lo = loc_of(C) & 0xffffu;
#pragma unroll
    for (int ks = 0; ks < 2; ++ks) {
        wf[C % WS_][ks * 2] = cat(ldtr(a0 + lo + ks * 2048), ldtr(a0 + lo + ks * 2048 + 1024));
        wf[C % WS_][ks * 2 + 1] = cat(ldtr(a1 + lo + ks * 2048), ldtr(a1 + lo + ks * 2048 + 1024));
    }
}
template <int C, int I> DI void g2_rd(const G2Ctx& x, u32x4 (&wf)[WS_][4]) {
    if constexpr (C < 14) {
        const u32 a = x.ta[loc_of(C) >> 16][I & 1]; constexpr u32 lo = (loc_of(C) & 0xffffu) + (I >> 1) * 2048;
        wf[C % WS_][I] = cat(ldtr(a + lo), ldtr(a + lo + 1024));
    }
}
template <int C> DI void g2_step(const G2Ctx& x, u32x4 (&hf)[HS][4], f32x16 (&acc)[2][2], u32x4 (&wf)[WS_][4], u64* stamps) {
    asm volatile("s_waitcnt vmcnt(%4) lgkmcnt(0)" : "+v"(hf[C % HS][0]), "+v"(hf[C % HS][1]), "+v"(hf[C % HS][2]), "+v"(hf[C % HS][3]) : "i"(g2_younger(C)) : "memory");
    if constexpr (C % 3 == 0) ST(14 + C / 3);
    SB();
    constexpr bool DH = C + HD < 14, DW = C >= 1 && C + RD + 4 < 14;
    const char* hs = g2_hsrc<(DH ? C + HD : 0)>(x);
    const char* wsrc = g2_wsrc<(DW ? C + RD + 4 : 0)>(x);
#define G2M(i) acc[((i) >> 1) & 1][(i) & 1] = mfma32(wf[C % WS_][((i) >> 2) * 2 + (((i) >> 1) & 1)], hf[C % HS][((i) & 1) * 2 + ((i) >> 2)], acc[((i) >> 1) & 1][(i) & 1])
    G2M(0); g2_rd<C + RD, 0>(x, wf); if constexpr (DH) hf[(C + HD) % HS][0] = g2_h1<0>(x, hs); SB();
    G2M(1); g2_rd<C + RD, 1>(x, wf); if constexpr (DH) hf[(C + HD) % HS][1] = g2_h1<1>(x, hs); SB();
    G2M(2); g2_rd<C + RD, 2>(x, wf); if constexpr (DH) hf[(C + HD) % HS][2] = g2_h1<2>(x, hs); SB();
    G2M(3); g2_rd<C + RD, 3>(x, wf); if constexpr (DH) hf[(C + HD) % HS][3] = g2_h1<3>(x, hs); SB();
    G2M(4); if constexpr (DW) g2_w1<C + RD + 4, 0>(x, wsrc); SB();
    G2M(5); if constexpr (DW) g2_w1<C + RD + 4, 1>(x, wsrc); SB();
    G2M(6); if constexpr (DW) g2_w1<C + RD + 4, 2>(x, wsrc); SB();
    G2M(7); if constexpr (DW) g2_w1<C + RD + 4, 3>(x, wsrc); SB();
#undef G2M
    if constexpr (C + 1 < 14) g2_step<C + 1>(x, hf, acc, wf, stamps);
}

struct CF { u32x2 a0[8], a1[8]; u32x4 b[8]; };
template <int S, int R> DI void cv_ld(CF& f, u32 aA, u32 aB) {
    f.a0[R] = alds8<R * 512 + S * 64>(aA); f.a1[R] = alds8<R * 512 + S * 64 + 8>(aA);
    f.b[R] = alds16<(224 - 32 * R + 4 * S) * 16>(aB);
}
DI void cv_wait(CF& f) {
    asm volatile("s_waitcnt lgkmcnt(0)" : "+v"(f.a0[0]), "+v"(f.a0[1]), "+v"(f.a0[2]), "+v"(f.a0[3]), "+v"(f.a0[4]), "+v"(f.a0[5]), "+v"(f.a0[6]), "+v"(f.a0[7]),
                 "+v"(f.a1[0]), "+v"(f.a1[1]), "+v"(f.a1[2]), "+v"(f.a1[3]), "+v"(f.a1[4]), "+v"(f.a1[5]), "+v"(f.a1[6]), "+v"(f.a1[7]),
                 "+v"(f.b[0]), "+v"(f.b[1]), "+v"(f.b[2]), "+v"(f.b[3]), "+v"(f.b[4]), "+v"(f.b[5]), "+v"(f.b[6]), "+v"(f.b[7]));
}
constexpr int cv_r(int m) { int r = 0; while (m >= 8 - r) { m -= 8 - r; ++r; } return r; }
constexpr int cv_c(int m) { int r = 0; while (m >= 8 - r) { m -= 8 - r; ++r; } return m; }
template <int S, int M> DI void cv_mm(CF& f, CF& n, u32 aA, u32 aB, f32x4 (&acc)[8]) {
    constexpr int r = cv_r(M), c = cv_c(M);
    acc[r + c] = mfma16(cat(f.a0[r], f.a1[r]), f.b[c], acc[r + c]);
    if constexpr ((M & 3) == 0 && M / 4 < 8) cv_ld<S + 1, M / 4>(n, aA, aB);
    SB();
    if constexpr (M + 1 < 36) cv_mm<S, M + 1>(f, n, aA, aB, acc);
}
DI void conv_body(u32 aA, u32 aB, f32x4 (&acc)[8]) {
    CF f, n;
    asm volatile("s_waitcnt lgkmcnt(0)" ::: "memory");
    cv_ld<0, 0>(f, aA, aB); cv_ld<0, 1>(f, aA, aB); cv_ld<0, 2>(f, aA, aB); cv_ld<0, 3>(f, aA, aB);
    cv_ld<0, 4>(f, aA, aB); cv_ld<0, 5>(f, aA, aB); cv_ld<0, 6>(f, aA, aB); cv_ld<0, 7>(f, aA, aB);
    _Pragma("clang loop unroll(disable)") for (int it = 0; it < 2; ++it) {
        cv_wait(f); SB();
        cv_mm<0, 0>(f, n, aA, aB, acc);
        cv_wait(n); SB();
        cv_mm<1, 0>(n, f, aA, aB, acc);
        aA += 128; aB += 128;
    }
    cv_wait(f);
    asm volatile("" ::: "memory");
}

#define ROTM 1
#define LDELAY 0
#define KDELAY 0
struct Geo { int bi, b, xcd, fh2, RB, CT, tb64, db, rot, crot; u32 R0, F0, T0, D0; };
DI Geo make_geo() {
    Geo g; g.b = blockIdx.x - NHELP; g.xcd = g.b & 7; const int bi = g.b >> 3; g.bi = bi;
    const int rq = g.xcd >> 1, rb = bi / 7, ct = bi - rb * 7;
    g.fh2 = g.xcd & 1; g.RB = 4 * rq + rb; g.CT = 7 * g.fh2 + ct; g.R0 = 128 * g.RB; g.F0 = 128 * g.CT;
    g.tb64 = 2 * g.RB + g.fh2; g.db = ct; g.T0 = 64 * g.tb64; g.D0 = 64 * g.db;

    g.rot = g.xcd >= 7 ? 0 : g.xcd;
     g.crot = (2 * ct + 7 * (rb & 1) + 3 * (rb >> 1) + rq + 5 * g.fh2) % 14;
    return g;
}
DI void make_g2(G2Ctx& g2, const Params& p, const Geo& q, int g, int lane, u32 lds0) {
    g2.w2b = (const char*)p.w2 + (size_t)(32 * g) * 896 + q.D0 * 2;
    g2.hb = p.ws + WS_H + (size_t)(q.tb64 * 2 * 112 + 2 * g) * 1024;
    g2.crot = q.crot;
#pragma unroll
    for (int n = 0; n < 4; ++n) g2.voffw[n] = (u32)((lane >> 3) * 896 + 16 * ((lane & 7) ^ (((lane >> 4) & 1) << 2)) + n * (8 * 896 - 1024));
    g2.ldsg = lds0 + g * 4096; g2.vlane = lane * 16u;
}
DI u32 slab_base(u32 lds0, int cw) { return lds0 + L_CONV + (cw >> 1) * CONV_CH + (cw & 1) * 8192; }
DI void final_epilogue(const Params& p, const Geo& q, u32 lds0) {
    int tz = threadIdx.x; PIN(tz);
    const int t = tz >> 3, c = tz & 7;
    const u32 pb = lds0 + ((t >> 5) ? 0u : 3 * L_PIECE) + ((t >> 4) & 1) * L_W + (t & 15) * 256;
    const u32 o0 = pb + 16 * ((2 * c) ^ (t & 7)), o1 = o0 ^ 16;
    f32x4 pa[4], pb_[4];
#pragma unroll
    for (int g = 0; g < 4; ++g) { pa[g] = __builtin_bit_cast(f32x4, lds16(o0 + g * 4096)); pb_[g] = __builtin_bit_cast(f32x4, lds16(o1 + g * 4096)); }
    const float r2 = *(const LAS float*)(lds0 + L_R2 + t * 4);
    const f32x4 b0 = __builtin_bit_cast(f32x4, lds16(lds0 + L_B2F + c * 32)), b1 = __builtin_bit_cast(f32x4, lds16(lds0 + L_B2F + c * 32 + 16));
    const u32 sb = slab_base(lds0, c >> 1) + t * 64 + (c & 1) * 32;
    const f32x4 sv0 = __builtin_bit_cast(f32x4, lds16(sb)), sv1 = __builtin_bit_cast(f32x4, lds16(sb + 16));
    const f32x4 xv0 = __builtin_bit_cast(f32x4, lds16(sb + 4096)), xv1 = __builtin_bit_cast(f32x4, lds16(sb + 4096 + 16));
    SB();
    const f32x4 s0 = (pa[0] + pa[1]) + (pa[2] + pa[3]), s1 = (pb_[0] + pb_[1]) + (pb_[2] + pb_[3]);
    f32x4 z0 = s0 * r2 + b0, z1 = s1 * r2 + b1;
    f32x4 e0 = z0 * -1.4426950408889634f, e1 = z1 * -1.4426950408889634f;
#pragma unroll
    for (int j = 0; j < 4; ++j) { e0[j] = __builtin_amdgcn_exp2f(e0[j]); e1[j] = __builtin_amdgcn_exp2f(e1[j]); }
    e0 += 1.f; e1 += 1.f;
#pragma unroll
    for (int j = 0; j < 4; ++j) { e0[j] = __builtin_amdgcn_rcpf(e0[j]); e1[j] = __builtin_amdgcn_rcpf(e1[j]); }
    z0 = z0 * e0; z1 = z1 * e1;
    z0 = z0 * sv0 + xv0; z1 = z1 * sv1 + xv1;
    const u32x4 res = {pk2(z0[0], z0[1]), pk2(z0[2], z0[3]), pk2(z1[0], z1[1]), pk2(z1[2], z1[3])};
    *(GAS u32x4*)((GAS char*)p.out + (size_t)(q.T0 + t) * 896 + q.D0 * 2 + c * 16) = res;
}
#define STAMP_DECL
#define STAMP_OUT(wave) do {} while (0)

extern __shared__ __attribute__((aligned(16))) char smem[];

struct CL { const char *xb, *wb; u32 vx[4], vw[4], sl; int rot; };
struct CSrc { const char *xs, *wsrc; };
template <int ST_> DI CSrc c_src(const CL& c) { int kc = ST_ + c.rot; if (kc >= 7) kc -= 7; return CSrc{c.xb + kc * 128, c.wb + (size_t)kc * 64 * 3584}; }
template <int ST_, int N> DI void c_dma1(const CL& c, const CSrc& s) {
    if constexpr (N == 0) setm0(c.sl + (ST_ & 3) * L_PIECE);
    if constexpr (N == 4) setm0(c.sl + (ST_ & 3) * L_PIECE + L_W);
    if constexpr (N < 4) dmai<N * 1024>(c.vx[N], s.xs); else dmai<(N - 4) * 1024>(c.vw[N - 4], s.wsrc);
}
template <int ST_> DI void c_dma(const CL& c) {
    const CSrc s = c_src<ST_>(c);
    c_dma1<ST_, 0>(c, s); c_dma1<ST_, 1>(c, s); c_dma1<ST_, 2>(c, s); c_dma1<ST_, 3>(c, s);
    c_dma1<ST_, 4>(c, s); c_dma1<ST_, 5>(c, s); c_dma1<ST_, 6>(c, s); c_dma1<ST_, 7>(c, s);
}
DI void make_cl(CL& c, const Params& p, const Geo& q, int cw, int lane, u32 lds0) {
    const int r8 = lane >> 3, c8 = lane & 7, r4 = lane >> 4, c16 = lane & 15;
#pragma unroll
    for (int n = 0; n < 4; ++n) {
        c.vx[n] = (u32)((32 * cw + r8) * 896 + 16 * (c8 ^ (((n & 1) << 2) | (r8 >> 1))) + n * (8 * 896 - 1024));
        c.vw[n] = (u32)((16 * cw + r4) * 3584 + 16 * (c16 ^ (r4 << 2)) + n * (4 * 3584 - 1024));
    }
    c.xb = (const char*)p.x + (size_t)q.R0 * 896; c.wb = (const char*)p.w1 + q.F0 * 2;
    c.sl = lds0 + cw * 4096; c.rot = q.rot;
}

template <int ST_> DI void l_dma(const CL& c) {
    const CSrc s = c_src<ST_>(c);
    const u32 sl = c.sl + (ST_ & 3) * L_PIECE;
    setm0(sl); dmai<0>(c.vx[0], s.xs); dmai<1024>(c.vx[1], s.xs); dmai<2048>(c.vx[2], s.xs); dmai<3072>(c.vx[3], s.xs);
    setm0(sl + L_W); dmai<0>(c.vw[0], s.wsrc); dmai<1024>(c.vw[1], s.wsrc); dmai<2048>(c.vw[2], s.wsrc); dmai<3072>(c.vw[3], s.wsrc);
    const char* x2 = s.xs + 32 * 896; const char* w2 = s.wsrc + 16 * 3584;
    setm0(sl + 4096); dmai<0>(c.vx[0], x2); dmai<1024>(c.vx[1], x2); dmai<2048>(c.vx[2], x2); dmai<3072>(c.vx[3], x2);
    setm0(sl + L_W + 4096); dmai<0>(c.vw[0], w2); dmai<1024>(c.vw[1], w2); dmai<2048>(c.vw[2], w2); dmai<3072>(c.vw[3], w2);
}

struct Fr { u32x4 x[4][2], w[4][2]; };
struct GA { u32 ax[4], aw[2]; };
template <int ST_> DI void g1_read(const GA& a, Fr& f) {
    constexpr u32 so = (ST_ & 3) * L_PIECE;
#pragma unroll
    for (int k = 0; k < 4; ++k) {
        f.w[k][0] = cat(ldtr(a.aw[0] + so + k * 4096), ldtr(a.aw[0] + so + k * 4096 + 1024));
        f.x[k][0] = lds16(a.ax[k] + so);
        f.w[k][1] = cat(ldtr(a.aw[1] + so + k * 4096), ldtr(a.aw[1] + so + k * 4096 + 1024));
        f.x[k][1] = lds16(a.ax[k] + so + 4096);
    }
}
template <int K0> DI void g1_mma(const Fr& f, f32x16 (&acc)[2][2]) {
#pragma unroll
    for (int k = K0; k < K0 + 2; ++k) {
        acc[0][0] = mfma32(f.w[k][0], f.x[k][0], acc[0][0]); acc[1][0] = mfma32(f.w[k][1], f.x[k][0], acc[1][0]);
        acc[0][1] = mfma32(f.w[k][0], f.x[k][1], acc[0][1]); acc[1][1] = mfma32(f.w[k][1], f.x[k][1], acc[1][1]);
    }
    SB();
}

template <int ST_, int I> DI void g1_rd(const GA& a, Fr& f) {
    constexpr u32 so = (ST_ & 3) * L_PIECE; constexpr int k = I >> 1, h = I & 1;
    f.w[k][h] = cat(ldtr(a.aw[h] + so + k * 4096), ldtr(a.aw[h] + so + k * 4096 + 1024));
    f.x[k][h] = lds16(a.ax[k] + so + h * 4096);
}
template <int K0, int I> DI void g1_m1(const Fr& f, f32x16 (&acc)[2][2]) {
    constexpr int k = K0 + (I >> 2), mt = I & 1, st = (I >> 1) & 1;
    acc[mt][st] = mfma32(f.w[k][mt], f.x[k][st], acc[mt][st]);
}
template <int ST_, bool CARRY> DI void g1_stage(const GA& a, const Fr& fp, Fr& fn, f32x16 (&acc)[2][2], u32 lds0, int g, u32& pk) {
    if constexpr (CARRY) { g1_m1<2, 0>(fp, acc); SB(); cons_set<ST_ - 1>(lds0, g); SB(); }
    lds_wait_ge(lds0 + F_LAND + 4 * ST_, ST_ == 0 ? 256 : (ST_ == 3 || ST_ == 6) ? 128 : 64, pk);
    if constexpr (CARRY) {
        g1_rd<ST_, 0>(a, fn); SB();
        g1_m1<2, 1>(fp, acc); SB(); g1_m1<2, 2>(fp, acc); g1_rd<ST_, 1>(a, fn); SB();
        g1_m1<2, 3>(fp, acc); SB(); g1_m1<2, 4>(fp, acc); g1_rd<ST_, 2>(a, fn); SB();
        g1_m1<2, 5>(fp, acc); g1_rd<ST_, 3>(a, fn); SB(); g1_m1<2, 6>(fp, acc); SB();
        g1_m1<2, 7>(fp, acc); SB();
    } else { g1_rd<ST_, 0>(a, fn); g1_rd<ST_, 1>(a, fn); g1_rd<ST_, 2>(a, fn); g1_rd<ST_, 3>(a, fn); SB(); }
    g1_m1<0, 0>(fn, acc); g1_rd<ST_, 4>(a, fn); SB(); g1_m1<0, 1>(fn, acc); SB();
    g1_m1<0, 2>(fn, acc); g1_rd<ST_, 5>(a, fn); SB(); g1_m1<0, 3>(fn, acc); g1_rd<ST_, 6>(a, fn); SB();
    g1_m1<0, 4>(fn, acc); g1_rd<ST_, 7>(a, fn); SB(); g1_m1<0, 5>(fn, acc); pk = lds_peek(lds0 + F_LAND + 4 * ST_ + 4); SB();
    g1_m1<0, 6>(fn, acc); SB(); g1_m1<0, 7>(fn, acc); SB();
}

DI void body_g(const Params& p, const int g) {
    STAMP_DECL
    const u32 lds0 = (u32)(size_t)(LAS char*)smem;
    const int lane = threadIdx.x & 63;
    const Geo q = make_geo();
    char* ws = p.ws;
    const int hi = lane >> 5, s31 = lane & 31;
    const int r8 = lane >> 3, c8 = lane & 7;
    const u32 raw = lds0 + L_LOC0 + g * KVRAW_SZ;
    {
        const u32 vkv = (u32)(r8 * 896 + c8 * 16);
        bdma16((u32)(((int)q.T0 + 16 * g - 8) * 896 + (int)q.D0 * 2) + vkv, mksrd(p.k, S_ * D_ * 2), raw);
        const char* kb = (const char*)p.k + (size_t)(q.T0 + 16 * g) * 896 + q.D0 * 2;
        const char* vb = (const char*)p.v + (size_t)(q.T0 + 16 * g) * 896 + q.D0 * 2;
        dmai<1024>(vkv, kb - 1024); dmai<2048>(vkv, kb + 8 * 896 - 2048); dmai<3072>(vkv, vb - 3072);
        setm0(raw + 4096);
        dmai<0>(vkv, vb + 8 * 896);
        if (lane < 32) dmai<1024>(lane * 16u, (const char*)p.shk + q.D0 * 8 - 1024);
        CL c; make_cl(c, p, q, g, lane, lds0);
        c_dma<0>(c);
    }
    u32x2 ep = gld8c(0u, ws + WS_EPOCH);
    const int fh = g & 1, sh = g >> 1;
    GA ga;
    {
        const int blk = (lane >> 4) & 1, qq = (lane & 15) >> 2, pp = lane & 3;
        const u32 Z = (u32)(hi ^ ((s31 >> 1) & 7));
#pragma unroll
        for (int k = 0; k < 4; ++k) ga.ax[k] = lds0 + (64 * sh + s31) * 128 + 16 * ((2 * k) ^ Z);
        ga.aw[0] = lds0 + L_W + (8 * hi + qq) * 256 + 16 * ((8 * fh + 2 * blk + (pp >> 1)) ^ (qq << 2)) + 8 * (pp & 1);
        ga.aw[1] = ga.aw[0] ^ 64;
    }
    WAITV(9);
    ST(1);
    {
        u32x4 wr[4], kr[5], vr[2];
#pragma unroll
        for (int e = 0; e < 4; ++e) wr[e] = lds16(raw + 5120 + c8 * 64 + e * 16);
#pragma unroll
        for (int j = 0; j < 5; ++j) kr[j] = lds16(raw + (5 + 2 * r8 + j) * 128 + c8 * 16);
        vr[0] = lds16(raw + 3072 + 2 * r8 * 128 + c8 * 16); vr[1] = lds16(raw + 3072 + (2 * r8 + 1) * 128 + c8 * 16);
        SB();
        u32x4 o0, o1;
#pragma unroll
        for (int e = 0; e < 4; ++e) {
            u32 pl[4], ph[4];
#pragma unroll
            for (int j = 0; j < 4; ++j) { pl[j] = __builtin_amdgcn_perm(kr[j][e], kr[j + 1][e], 0x05040100u); ph[j] = __builtin_amdgcn_perm(kr[j][e], kr[j + 1][e], 0x07060302u); }
            f32x2 a = {dot2(pl[0], wr[e].y, dot2(pl[2], wr[e].x, 0.f)), dot2(ph[0], wr[e].w, dot2(ph[2], wr[e].z, 0.f))};
            f32x2 b = {dot2(pl[1], wr[e].y, dot2(pl[3], wr[e].x, 0.f)), dot2(ph[1], wr[e].w, dot2(ph[3], wr[e].z, 0.f))};
            a *= f32x2{bflo(vr[0][e]), bfhi(vr[0][e])}; b *= f32x2{bflo(vr[1][e]), bfhi(vr[1][e])};
            o0[e] = pk2(a.x, a.y); o1[e] = pk2(b.x, b.y);
        }
        sts16(lds0 + L_KVS + (16 * g + 2 * r8) * 144 + c8 * 16, o0);
        sts16(lds0 + L_KVS + (16 * g + 2 * r8 + 1) * 144 + c8 * 16, o1);
        asm volatile("s_waitcnt lgkmcnt(0)" ::: "memory");
        lds_add(lds0 + F_KVS);
    }
    f32x16 acc[2][2] = {};
    Fr fa, fb;
    WAITV(1);
    lds_add(lds0 + F_LAND);
    u32 pk = lds_peek(lds0 + F_LAND);
    ST(2);
    g1_stage<0, false>(ga, fb, fa, acc, lds0, g, pk);
    g1_stage<1, true>(ga, fa, fb, acc, lds0, g, pk);
    g1_stage<2, true>(ga, fb, fa, acc, lds0, g, pk);
    asm volatile("s_waitcnt vmcnt(0)" : "+v"(ep) :: "memory");
    const u64 epoch = ((u64)(u32)__builtin_amdgcn_readfirstlane(ep.y) << 32) | (u32)__builtin_amdgcn_readfirstlane(ep.x);
    const u64 tag = epoch + TAG_SALT;
    const u32 tlo = (u32)tag, thi = (u32)(tag >> 32);
    g1_stage<3, true>(ga, fa, fb, acc, lds0, g, pk);
    ST(3);
    g1_stage<4, true>(ga, fb, fa, acc, lds0, g, pk);
    const u32 rgo = WS_RG + (q.R0 + 64 * sh + lane) * 8, cgo = WS_CG + (q.F0 + 64 * fh + lane) * 8;
    u32x2 rg = gld8c(rgo, ws), cg = gld8c(cgo, ws);
    u32 b1v = gld2((u32)((64 * fh + lane) * 2), (const char*)p.b1 + q.F0 * 2);
    g1_stage<5, true>(ga, fa, fb, acc, lds0, g, pk);
    g1_stage<6, true>(ga, fb, fa, acc, lds0, g, pk);
    cons_set<6>(lds0, g);
    ST(5);
    asm volatile("s_waitcnt vmcnt(0)" : "+v"(rg), "+v"(cg), "+v"(b1v) :: "memory");
    _Pragma("clang loop unroll(disable)") for (int spin = 0; spin < SPIN_CAP && !__all(rg.y == tlo && cg.y == tlo); ++spin) {
        rg = gld8c(rgo, ws); cg = gld8c(cgo, ws);
        asm volatile("s_waitcnt vmcnt(0)" : "+v"(rg), "+v"(cg) :: "memory");
    }
    if (fh == 0 && sh == q.fh2) *(LAS u32*)(lds0 + L_ROWF + lane * 4) = rg.x;
    g1_mma<2>(fa, acc);
    {
        u32x4 ax[2], bx[2];
        const auto rr = __builtin_amdgcn_permlane32_swap(rg.x, rg.x, false, false);
        const auto cc = __builtin_amdgcn_permlane32_swap(cg.x, cg.x, false, false);
        const auto bb = __builtin_amdgcn_permlane32_swap(b1v, b1v, false, false);
#pragma unroll
        for (int i = 0; i < 2; ++i) {
            const u32 nm = __builtin_amdgcn_perm(rr[i], rr[i], 0x01000100u) ^ 0x80008000u;
            ax[i] = u32x4{cc[i], bb[i], 0u, 0u};
            bx[i] = u32x4{hi ? 0u : nm, hi ? 0u : (rr[i] >> 16), 0u, 0u};
        }
#pragma unroll
        for (int st = 0; st < 2; ++st)
#pragma unroll
            for (int mt = 0; mt < 2; ++mt) acc[mt][st] = mfma32(ax[mt], bx[st], acc[mt][st]);
#pragma unroll
        for (int st = 0; st < 2; ++st) {
            char* hb = ws + WS_H + (size_t)((4 * q.RB + 2 * sh + st) * 112 + q.CT * 8 + fh * 4) * 1024;
#pragma unroll
            for (int mt = 0; mt < 2; ++mt)
#pragma unroll
                for (int t = 0; t < 2; ++t) {
                    u32x4 hv;
#pragma unroll
                    for (int j = 0; j < 4; ++j) {
                        f32x2 a = {acc[mt][st][8 * t + 2 * j], acc[mt][st][8 * t + 2 * j + 1]};
                        f32x2 u = {relu(a.x), relu(a.y)};
                        u = u * u;
                        hv[j] = pk2(u.x, u.y);
                    }
                    if (mt == 0 && t == 0) gst16c<0>(lane * 16u, hb, hv);
                    if (mt == 0 && t == 1) gst16c<1024>(lane * 16u, hb, hv);
                    if (mt == 1 && t == 0) gst16c<2048>(lane * 16u, hb, hv);
                    if (mt == 1 && t == 1) gst16c<3072>(lane * 16u, hb, hv);
                    SB();
                }
        }
    }
    ST(6);
    G2Ctx g2; make_g2(g2, p, q, g, lane, lds0);
    {
        const int blk = (lane >> 4) & 1, qq = (lane & 15) >> 2, pp = lane & 3;
        const u32 t0 = g2.ldsg + (4 * hi + qq) * 128 + 16 * ((2 * blk + (pp >> 1)) ^ ((qq >> 1) << 2)) + 8 * (pp & 1);
#pragma unroll
        for (int i = 0; i < 3; ++i) { g2.ta[i][0] = t0 + i * 65536; g2.ta[i][1] = (t0 ^ 64) + i * 65536; PIN(g2.ta[i][0]); PIN(g2.ta[i][1]); }
    }
    u32x4 wf[WS_][4];
    cons_wait(lds0, 4);
    asm volatile("s_waitcnt vmcnt(0) lgkmcnt(0)" ::: "memory"); ST(9);
    if (lds_arrive_last(lds0 + F_HCNT, 4)) { if (lane < 15) gst8c<0>(lane < 14 ? WS_HF + (q.RB * 14 + lane) * MB + q.CT * 8 : WS_ALL + q.b * 8, ws, u32x2{tlo, thi}); }
    {
        const u32 po = WS_HF + (q.RB * 14 + q.CT) * MB + (lane < 14 ? lane : 0) * 8;
        g2_issue_w<2>(g2); g2_issue_w<3>(g2);
        u32x2 f = {0u, 0u};
        if (g == 0) f = gld8c(po, ws);
        g2_issue_w<4>(g2);
        lds_wait(lds0 + F_W);
        g2_read<0>(g2, wf); g2_read<1>(g2, wf);
        asm volatile("s_waitcnt lgkmcnt(0)" ::: "memory");
        g2_issue_w<5>(g2); g2_issue_w<6>(g2);
        asm volatile("s_waitcnt vmcnt(12)" : "+v"(f) :: "memory");
        bool rdy = __all(f.x == tlo && f.y == thi);
        if (g == 0 && rdy) lds_set(lds0 + F_HRDY);
        g2_read<2>(g2, wf); g2_read<3>(g2, wf);
        asm volatile("s_waitcnt lgkmcnt(0)" ::: "memory");
        if (g == 0 && !rdy) f = gld8c(po, ws);
        g2_issue_w<7>(g2); g2_issue_w<8>(g2);
        if (g == 0) {
            if (!rdy) {
                asm volatile("s_waitcnt vmcnt(8)" : "+v"(f) :: "memory");
                if (!__all(f.x == tlo && f.y == thi)) poll8(po, ws, true, tlo, thi);
                lds_set(lds0 + F_HRDY);
            }
        } else lds_wait(lds0 + F_HRDY);
    }
    ST(7);
    {
        u32x4 hf[HS][4];
        g2_issue_h<0>(g2, hf); g2_issue_h<1>(g2, hf); if constexpr (HD > 2) g2_issue_h<2 % HS>(g2, hf); if constexpr (HD > 3) g2_issue_h<3 % HS>(g2, hf); if constexpr (HD > 4) g2_issue_h<4 % HS>(g2, hf);
        f32x16 a2[2][2] = {};
        g2_step<0>(g2, hf, a2, wf, STP);
        ST(8);
        const u32 pw = g2.ldsg + (s31 >> 4) * L_W + (s31 & 15) * 256;
#pragma unroll
        for (int s2 = 0; s2 < 2; ++s2)
#pragma unroll
            for (int dt = 0; dt < 2; ++dt)
#pragma unroll
                for (int gr = 0; gr < 4; ++gr) {
                    const u32 a = pw + (s2 ? 0u : 3 * L_PIECE) + 16 * ((8 * dt + 2 * gr + hi) ^ (s31 & 7));
                    sts16(a, __builtin_bit_cast(u32x4, f32x4{a2[dt][s2][4 * gr], a2[dt][s2][4 * gr + 1], a2[dt][s2][4 * gr + 2], a2[dt][s2][4 * gr + 3]}));
                }
    }
    BARL(); ST(10);
    final_epilogue(p, q, lds0);
    STAMP_OUT(g);
}

DI void body_c(const Params& p, const int cw) {
    STAMP_DECL
    const u32 lds0 = (u32)(size_t)(LAS char*)smem;
    const int lane = threadIdx.x & 63;
    const Geo q = make_geo();
    char* ws = p.ws;
    const int hi = lane >> 5, s31 = lane & 31;
    const int half = cw & 1, chl = cw >> 1, chan = 2 * q.b + chl;
    u32x2 ep;
    u32x4 xa, xb_; u32 dv;
    const char* xsl = (const char*)p.x + (size_t)q.T0 * 896 + (q.D0 + 16 * cw) * 2;
    if (cw < 2) {
        CL c, c2; make_cl(c, p, q, 0, lane, lds0); c2 = c; c2.xb += 64 * 896; c2.wb += 32 * 3584; c2.sl += 8192;
        ep = gld8c(0u, ws + WS_EPOCH);
        if (cw == 0) {
            l_dma<1>(c); l_dma<1>(c2); ST(1);
            WAITV(0); lds_add(lds0 + F_LAND + 4); ST(2);
            cons_wait(lds0, 0); l_dma<4>(c); l_dma<4>(c2); ST(19);
            WAITV(0); lds_add(lds0 + F_LAND + 16); ST(20);
        } else {
            l_dma<2>(c); l_dma<2>(c2); ST(1);
            WAITV(0); lds_add(lds0 + F_LAND + 8); ST(2);
            cons_wait(lds0, 1); l_dma<5>(c); l_dma<5>(c2); ST(19);
            WAITV(0); lds_add(lds0 + F_LAND + 20); ST(20);
        }
        asm volatile("s_waitcnt vmcnt(0)" : "+v"(ep) :: "memory");
        const u64 tg = (((u64)ep.y << 32) | ep.x) + TAG_SALT;
        if (cw == 1) { poll8(WS_KVF + q.b * MB + s31 * 8, ws, true, (u32)tg, (u32)(tg >> 32)); lds_set(lds0 + F_KVRDY); }
        else {
            cons_wait(lds0, 3);
#pragma unroll
            for (int i = 0; i < 4; ++i) { G2Ctx g2; make_g2(g2, p, q, i, lane, lds0); g2_issue_w<0>(g2); g2_issue_w<1>(g2); }
            lds_wait(lds0 + F_KVRDY);
        }
        ST(7);
        const char* kb2 = ws + WS_KVT + (size_t)(2 * q.b + cw) * TP;
        u32x4 u0 = gld16c<0>(lane * 16u, kb2), u1 = gld16c<1024>(lane * 16u, kb2), u2 = gld16c<2048>(lane * 16u, kb2), u3 = gld16c<3072>(lane * 16u, kb2);
        xa = gld16((u32)(lane * 896), xsl); xb_ = gld16((u32)(lane * 896 + 16), xsl); dv = gld2(0u, (const char*)p.ssmd + chan * 2);
        if (cw == 0) { WAITV(7); lds_set(lds0 + F_W); }
        cons_wait(lds0, 5); cons_wait(lds0, 6);
        const u32 uq_ = lds0 + L_CONV + cw * CONV_CH + TOEP_CH + lane * 16;
        if (lane < 32) sts16(uq_ + 4096, u32x4{0u, 0u, 0u, 0u});
        asm volatile("s_waitcnt vmcnt(0)" : "+v"(u0), "+v"(u1), "+v"(u2), "+v"(u3), "+v"(xa), "+v"(xb_), "+v"(dv) :: "memory");
        ST(14);
        sts16(uq_, u0); sts16(uq_ + 1024, u1); sts16(uq_ + 2048, u2); sts16(uq_ + 3072, u3);
        asm volatile("s_waitcnt lgkmcnt(0)" ::: "memory");
        lds_add(lds0 + F_TOEP + cw * 4);
    } else {
        const int kw = cw - 2;
        if (kw == 0 && lane < 8) dma16(lane * 16u, (const char*)p.b2 + q.D0 * 2, lds0 + L_B2);
        ep = gld8c(0u, ws + WS_EPOCH);
        CL c; make_cl(c, p, q, 2 * kw, lane, lds0);
        l_dma<3>(c);
        lds_wait_ge(lds0 + F_KVS, 256, lds_peek(lds0 + F_KVS));
        ST(2);
        {
            const int grp = lane >> 4, qq = (lane & 15) >> 2, pp = lane & 3;
            const u32 a = lds0 + L_KVS + (16 * grp + 8 * (pp >> 1) + 7 - qq) * 144 + kw * 64 + (pp & 1) * 8;
            const u32 vo = (u32)((q.D0 + 32 * kw + (lane & 7)) * TP + (2040 - (int)q.T0 - 8 * (lane >> 3)) * 2);
            u32x4 tr[4];
#pragma unroll
            for (int i = 0; i < 4; ++i) tr[i] = cat(ldtr(a + 16 * i), ldtr(a - 4 * 144 + 16 * i));
            SB();
#pragma unroll
            for (int i = 0; i < 4; ++i) gst16c<0>(vo, ws + WS_KVT + i * 8 * TP, tr[i]);
        }
        ST(20);
        asm volatile("s_waitcnt vmcnt(4)" : "+v"(ep) :: "memory");
        lds_add(lds0 + F_LAND + 12);
        WAITV(0);
        ST(19);
        const u64 tg = (((u64)ep.y << 32) | ep.x) + TAG_SALT;
        if (lds_arrive_last(lds0 + F_KVCNT, 2)) {
            if (lane < 32) gst8c<0>(WS_KVF + (q.db * 32 + lane) * MB + q.tb64 * 8, ws, u32x2{(u32)tg, (u32)(tg >> 32)});
        }
        cons_wait(lds0, 2);
        l_dma<6>(c);
        ST(3);
        u32x4 sk[5];
        const u32x4 sksrd = mksrd((const char*)p.ssmk + (size_t)(2 * q.b + kw) * 4096, 4096);
#pragma unroll
        for (int r = 0; r < 5; ++r) sk[r] = bld16((u32)((lane - 1 + 63 * r) * 16), sksrd);
        xa = gld16((u32)(lane * 896), xsl); xb_ = gld16((u32)(lane * 896 + 16), xsl); dv = gld2(0u, (const char*)p.ssmd + chan * 2);
        WAITV(8);
        lds_add(lds0 + F_LAND + 24);
        ST(21);
        asm volatile("s_waitcnt vmcnt(0)" : "+v"(sk[0]), "+v"(sk[1]), "+v"(sk[2]), "+v"(sk[3]), "+v"(sk[4]), "+v"(xa), "+v"(xb_), "+v"(dv) :: "memory");
        u32x4 tw[5][4];
#pragma unroll
        for (int r = 0; r < 5; ++r) {
            const u32x4 B = sk[r];
            u32x4 A;
#pragma unroll
            for (int j = 0; j < 4; ++j) A[j] = __builtin_amdgcn_update_dpp(0u, B[j], 0x138  , 0xf, 0xf, false);
            const u32 e0 = __builtin_amdgcn_alignbit(A.y, A.x, 16), e1 = __builtin_amdgcn_alignbit(A.z, A.y, 16), e2 = __builtin_amdgcn_alignbit(A.w, A.z, 16),
                      e3 = __builtin_amdgcn_alignbit(B.x, A.w, 16), e4 = __builtin_amdgcn_alignbit(B.y, B.x, 16);
            tw[r][0] = u32x4{e0, e1, e2, e3}; tw[r][1] = u32x4{A.y, A.z, A.w, B.x}; tw[r][2] = u32x4{e1, e2, e3, e4}; tw[r][3] = u32x4{A.z, A.w, B.x, B.y};
            PIN(tw[r][0]); PIN(tw[r][1]); PIN(tw[r][2]); PIN(tw[r][3]);
        }
        cons_wait(lds0, 5); cons_wait(lds0, 6);
        ST(5);
        const u32 tq = lds0 + L_CONV + kw * CONV_CH + lane * 16;
#pragma unroll
        for (int r = 0; r < 5; ++r) {
            if ((lane > 0 || r == 0) && (r < 4 || lane < 6)) {
#pragma unroll
                for (int c = 0; c < 4; ++c) sts16(tq + r * 1008 + c * TOEP_CP, tw[r][c]);
            }
        }
        asm volatile("s_waitcnt lgkmcnt(0)" ::: "memory");
        lds_add(lds0 + F_TOEP + kw * 4);
    }
    const u64 epoch = ((u64)(u32)__builtin_amdgcn_readfirstlane(ep.y) << 32) | (u32)__builtin_amdgcn_readfirstlane(ep.x);
    const u64 tag = epoch + TAG_SALT;
    const u32 tlo = (u32)tag, thi = (u32)(tag >> 32);
    const u32 tbase = lds0 + L_CONV + chl * CONV_CH;
    const u32 ub = tbase + TOEP_CH;
    ST(6);
    lds_wait_ge(lds0 + F_TOEP + chl * 4, 128, lds_peek(lds0 + F_TOEP + chl * 4));
    ST(15);
    const int bb = lane & 15, gg = lane >> 4;
    const u32 aA = tbase + (bb & 3) * TOEP_CP + ((bb >> 2) + 2 * gg) * 8 + half * 256;
    const u32 aB = ub + (30 - 2 * bb + gg) * 16 + half * 256;
    f32x4 ac8[8] = {};
    conv_body(aA, aB, ac8);
    ST(16);
    f32x4 acc[4];
    {
        const u32 xo = ub + U_SZ + lane * 16;
#pragma unroll
        for (int i = 0; i < 4; ++i) { const f32x4 give = half ? ac8[i] : ac8[4 + i]; acc[i] = half ? ac8[4 + i] : ac8[i]; sts16(xo + half * 4096 + i * 1024, __builtin_bit_cast(u32x4, give)); }
        asm volatile("s_waitcnt lgkmcnt(0)" ::: "memory");
        lds_set(lds0 + F_XCH + cw * 4);
        lds_wait(lds0 + F_XCH + (cw ^ 1) * 4);
        f32x4 got[4];
#pragma unroll
        for (int i = 0; i < 4; ++i) got[i] = __builtin_bit_cast(f32x4, lds16(xo + (half ^ 1) * 4096 + i * 1024));
        SB();
#pragma unroll
        for (int i = 0; i < 4; ++i) acc[i] = f32x4{add1(acc[i][0], got[i][0]), add1(acc[i][1], got[i][1]), add1(acc[i][2], got[i][2]), add1(acc[i][3], got[i][3])};
    }
    ST(8);
    {
        const float dd = bflo(dv);
        char* yb = ws + WS_YT + (size_t)chan * TP;
        u32x2 uq[4];
#pragma unroll
        for (int i = 0; i < 4; ++i) uq[i] = lds8(ub + (2044 - (256 * (4 * half + i) + 16 * bb + 4 * gg)) * 2);
        SB();
#pragma unroll
        for (int i = 0; i < 4; ++i) {
            const int qt = 4 * half + i;
            const int t = 256 * qt + 16 * bb + 4 * gg;
            const u32x2 uu = uq[i];
            const u32 c01 = pk2(acc[i][0], acc[i][1]), c23 = pk2(acc[i][2], acc[i][3]);
            float y0 = fma1(bfhi(uu.y), dd, bflo(c01)), y1 = fma1(bflo(uu.y), dd, bfhi(c01));
            float y2 = fma1(bfhi(uu.x), dd, bflo(c23)), y3 = fma1(bflo(uu.x), dd, bfhi(c23));
            gst8c<0>((u32)(t * 2), yb, u32x2{pk2(y0, y1), pk2(y2, y3)});
        }
        WAITV(0);
        if (lds_arrive_last(lds0 + F_YCNT, 4)) { if (lane < 32) gst8c<0>(WS_YF + (lane * 7 + (q.b >> 5)) * MB + (q.b & 31) * 8, ws, u32x2{tlo, thi}); }
    }
    _Pragma("clang loop unroll(disable)") for (int spin = 0; spin < (1 << 22); ++spin) { if (__builtin_amdgcn_readfirstlane(*(volatile LAS u32*)(lds0 + F_YCNT)) >= 4) break; __builtin_amdgcn_s_sleep(1); }
    asm volatile("" ::: "memory");
    const u32 slab = slab_base(lds0, cw) + lane * 64;
    f32x4 xn[4];
    {
        _Pragma("clang loop unroll(disable)") for (int spin = 0; spin < (1 << 22); ++spin) { if (__builtin_amdgcn_readfirstlane(*(volatile LAS u32*)(lds0 + F_HCNT)) >= 4) break; __builtin_amdgcn_s_sleep(1); }
        asm volatile("" ::: "memory");
        const u32 rf = *(const LAS u32*)(lds0 + L_ROWF + lane * 4);
        const float m = bflo(rf), ri = __builtin_amdgcn_rcpf(bfhi(rf)), nmri = -m * ri;
        const u32 xx[8] = {xa.x, xa.y, xa.z, xa.w, xb_.x, xb_.y, xb_.z, xb_.w};
#pragma unroll
        for (int j = 0; j < 4; ++j) {
            f32x4 v = {bflo(xx[2 * j]), bfhi(xx[2 * j]), bflo(xx[2 * j + 1]), bfhi(xx[2 * j + 1])};
            sts16(slab + 4096 + j * 16, __builtin_bit_cast(u32x4, v));
            xn[j] = f32x4{fma1(v[0], ri, nmri), fma1(v[1], ri, nmri), fma1(v[2], ri, nmri), fma1(v[3], ri, nmri)};
        }
        if (cw == 0) *(LAS float*)(lds0 + L_R2 + lane * 4) = ri * ri;
        if (cw == 1) *(LAS float*)(lds0 + L_B2F + lane * 4) = bflo(*(const LAS u16*)(lds0 + L_B2 + lane * 2));
    }
    if (q.b == 0 && cw == 3) {
        _Pragma("clang loop unroll(disable)") for (int spin = 0; spin < SPIN_CAP; ++spin) {
            u32x4 f0 = gld16c<0>(lane * 16u, ws + WS_ALL), f1 = gld16c<1024>((lane < 48 ? lane : 0) * 16u, ws + WS_ALL);
            asm volatile("s_waitcnt vmcnt(0)" : "+v"(f0), "+v"(f1) :: "memory");
            bool ok = f0.x == tlo && f0.y == thi && f0.z == tlo && f0.w == thi && f1.x == tlo && f1.y == thi && f1.z == tlo && f1.w == thi;
            if (__all(ok)) break;
            __builtin_amdgcn_s_sleep(SLP);
        }
        if (lane == 0) { const u64 e1 = epoch + 1; gst8c<0>(0u, ws + WS_EPOCH, u32x2{(u32)e1, (u32)(e1 >> 32)}); }
    }
    if (cw == 0) { poll8(WS_YF + (q.tb64 * 7 + q.db) * MB + s31 * 8, ws, true, tlo, thi); lds_set(lds0 + F_YRDY); }
    else lds_wait(lds0 + F_YRDY);
    ST(9);
    {
        const char* yb = ws + WS_YT + (size_t)(q.D0 + 16 * cw) * TP + q.T0 * 2;
        const u32 vo = (u32)((lane >> 3) * TP + (lane & 7) * 16);
        u32x4 y0 = gld16c<0>(vo, yb), y1 = gld16c<0>(vo, yb + 8 * TP);
        asm volatile("s_waitcnt vmcnt(0)" : "+v"(y0), "+v"(y1) :: "memory");
        const u32 ys = tbase + TOEP_CH + U_SZ + half * 4096;
        sts16(ys + (lane >> 3) * 144 + (lane & 7) * 16, y0);
        sts16(ys + (8 + (lane >> 3)) * 144 + (lane & 7) * 16, y1);
        const int qq = (lane & 15) >> 2, pp = lane & 3;
        const u32 a = ys + qq * 144 + gg * 32 + pp * 8;
        u32x2 t0 = ldtr(a), t1 = ldtr(a + 4 * 144), t2 = ldtr(a + 8 * 144), t3 = ldtr(a + 12 * 144);
        const u32 yy[8] = {t0.x, t0.y, t1.x, t1.y, t2.x, t2.y, t3.x, t3.y};
#pragma unroll
        for (int j = 0; j < 4; ++j) {
            f32x4 v = {bflo(yy[2 * j]), bfhi(yy[2 * j]), bflo(yy[2 * j + 1]), bfhi(yy[2 * j + 1])};
            sts16(slab + ((j ^ (lane & 3)) * 0 + j) * 16, __builtin_bit_cast(u32x4, f32x4{mul1(v[0], xn[j][0]), mul1(v[1], xn[j][1]), mul1(v[2], xn[j][2]), mul1(v[3], xn[j][3])}));
        }
    }
    BARL(); ST(10);
    final_epilogue(p, q, lds0);
    STAMP_OUT(cw + 4);
}

DI float dpp_xor_sum8(float v) {
    v += __uint_as_float(__builtin_amdgcn_mov_dpp(__float_as_uint(v), 0xB1, 0xf, 0xf, true));
    v += __uint_as_float(__builtin_amdgcn_mov_dpp(__float_as_uint(v), 0x4E, 0xf, 0xf, true));
    v += __uint_as_float(__builtin_amdgcn_mov_dpp(__float_as_uint(v), 0x141, 0xf, 0xf, true));
    return v;
}
DI void body_help(const Params& p, const int wave) {
    const u32 lds0 = (u32)(size_t)(LAS char*)smem;
    const int lane = threadIdx.x & 63, r8 = lane >> 3, c8 = lane & 7;
    const int xcd = blockIdx.x & 7, j = blockIdx.x >> 3;
    char* ws = p.ws;
    u32x2 ep = gld8c(0u, ws + WS_EPOCH);
    u32x4 d[14];
    const int ctile = (xcd >> 1) * 4 + j;
    const bool cols = wave >= 4;
    if (!cols) {
        const char* xb = (const char*)p.x + (size_t)(256 * xcd + 64 * j + 16 * wave) * 896;
#pragma unroll
        for (int i = 0; i < 14; ++i) d[i] = gld16((u32)((r8 + 8 * (i / 7)) * 896 + (i % 7) * 128 + c8 * 16), xb);
    } else {
        const char* wb = (const char*)p.w1 + (size_t)(112 * (wave - 4)) * 3584 + ((xcd & 1) * 14 + (ctile < 14 ? ctile : 0)) * 128;
#pragma unroll
        for (int i = 0; i < 14; ++i) d[i] = gld16((u32)((r8 + 8 * i) * 3584 + c8 * 16), wb);
    }
    asm volatile("s_waitcnt vmcnt(0)" : "+v"(ep), "+v"(d[0]), "+v"(d[1]), "+v"(d[2]), "+v"(d[3]), "+v"(d[4]), "+v"(d[5]), "+v"(d[6]), "+v"(d[7]), "+v"(d[8]), "+v"(d[9]),
                 "+v"(d[10]), "+v"(d[11]), "+v"(d[12]), "+v"(d[13]) :: "memory");
    const u32 tlo = (u32)((((u64)ep.y << 32) | ep.x) + TAG_SALT);
    if (!cols) {
#pragma unroll
        for (int h = 0; h < 2; ++h) {
            float s0 = 0.f, s1 = 0.f, q0 = 0.f, q1 = 0.f;
#pragma unroll
            for (int i = 0; i < 7; ++i)
#pragma unroll
                for (int e = 0; e < 4; e += 2) {
                    s0 = dot2(d[7 * h + i][e], 0x3f803f80u, s0); q0 = dot2(d[7 * h + i][e], d[7 * h + i][e], q0);
                    s1 = dot2(d[7 * h + i][e + 1], 0x3f803f80u, s1); q1 = dot2(d[7 * h + i][e + 1], d[7 * h + i][e + 1], q1);
                }
            const float mean = dpp_xor_sum8(s0 + s1) * (1.f / 448.f);
            const float var = fmaxf(dpp_xor_sum8(q0 + q1) * (1.f / 448.f) - mean * mean, 0.f);
            const float sd = __builtin_amdgcn_sqrtf(rbf(rbf(var) + 1e-5f));
            if (c8 == 0) gst8c<0>((u32)(WS_RG + (256 * xcd + 64 * j + 16 * wave + 8 * h + r8) * 8), ws, u32x2{pk2(mean, sd), tlo});
        }
    } else {
        float a[8] = {};
        u32 klo = 0x00003f80u, khi = 0x3f800000u; PIN(klo); PIN(khi);
#pragma unroll
        for (int i = 0; i < 14; ++i)
#pragma unroll
            for (int e = 0; e < 4; ++e) { a[2 * e] = dot2(d[i][e], klo, a[2 * e]); a[2 * e + 1] = dot2(d[i][e], khi, a[2 * e + 1]); }
        const u32 o = lds0 + ((wave - 4) * 8 + r8) * 272 + c8 * 32;
        sts16(o, __builtin_bit_cast(u32x4, f32x4{a[0], a[1], a[2], a[3]})); sts16(o + 16, __builtin_bit_cast(u32x4, f32x4{a[4], a[5], a[6], a[7]}));
    }
    BARL();
    if (wave == 4 && ctile < 14) {
        float t0 = 0.f, t1 = 0.f;
#pragma unroll
        for (int i = 0; i < 32; i += 2) { t0 += *(const LAS float*)(lds0 + i * 272 + lane * 4); t1 += *(const LAS float*)(lds0 + (i + 1) * 272 + lane * 4); }
        gst8c<0>((u32)(WS_CG + (((xcd & 1) * 14 + ctile) * 64 + lane) * 8), ws, u32x2{pk2(t0 + t1, (t0 + t1) - rbf(t0 + t1)), tlo});
    }
    {
        const int crot = (2 * xcd) % 14;
        setm0(lds0 + 16384 + wave * 4096);
        for (int c = 1; c < 14; ++c) {
            int cc = c + crot; if (cc >= 14) cc -= 14;
            const char* sb = (const char*)p.w2 + (size_t)cc * 114688 + (j * 8 + wave) * 3584;
            if (lane < 32) dmai<3072>(lane * 16u, sb);
            dmai<0>(lane * 16u, sb); dmai<1024>(lane * 16u, sb); dmai<2048>(lane * 16u, sb);
            WAITV(8);
        }
        WAITV(0);
    }
}

__global__ __launch_bounds__(512, 2) void fused_kernel(Params p) {
    const int wave = __builtin_amdgcn_readfirstlane(threadIdx.x >> 6);
    if (blockIdx.x < NHELP) { body_help(p, wave); return; }
    if (threadIdx.x < 32) *(LAS u32*)((u32)(size_t)(LAS char*)smem + L_FLAG + threadIdx.x * 4) = 0u;
    else if (threadIdx.x < 64) *(LAS u32*)((u32)(size_t)(LAS char*)smem + F_CONS + ((threadIdx.x - 32) >> 2) * 144 + (threadIdx.x & 3) * 4) = 0u;
    BARL();
    if (wave < 4) body_g(p, wave); else body_c(p, wave - 4);
}

extern "C" void kernel_launch(void* const* d_in, const int* in_sizes, int n_in,
                              void* d_out, int out_size, void* d_ws, size_t ws_size,
                              hipStream_t stream) {
    Params p;
    p.x = (const u16*)d_in[0]; p.k = (const u16*)d_in[1]; p.v = (const u16*)d_in[2]; p.shk = (const u16*)d_in[3];
    p.ssmk = (const u16*)d_in[4]; p.ssmd = (const u16*)d_in[5]; p.w1 = (const u16*)d_in[7]; p.b1 = (const u16*)d_in[8];
    p.w2 = (const u16*)d_in[9]; p.b2 = (const u16*)d_in[10];
    p.out = (u16*)d_out; p.ws = (char*)d_ws;
    (void)hipFuncSetAttribute((const void*)fused_kernel, hipFuncAttributeMaxDynamicSharedMemorySize, LDS_BYTES);
    fused_kernel<<<NWG + NHELP, 512, LDS_BYTES, stream>>>(p);
}
```

```cpp
#define DIAG 99
#include <hip/hip_runtime.h>
#include <stdint.h>
#include <type_traits>

typedef unsigned short u16;
typedef unsigned int u32;
typedef unsigned long long u64;
typedef u32 u32x4 __attribute__((ext_vector_type(4)));
typedef u32 u32x2 __attribute__((ext_vector_type(2)));
typedef float f32x4 __attribute__((ext_vector_type(4)));
typedef float f32x2 __attribute__((ext_vector_type(2)));
typedef float f32x16 __attribute__((ext_vector_type(16)));
typedef __bf16 bf16x8 __attribute__((ext_vector_type(8)));
typedef __bf16 bf16x2 __attribute__((ext_vector_type(2)));
typedef short s16x4 __attribute__((ext_vector_type(4)));
#define LAS __attribute__((address_space(3)))
#define GAS __attribute__((address_space(1)))
#define DI __device__ __forceinline__

constexpr int S_ = 2048, D_ = 448, F_ = 1792;
constexpr int NHELP = 32, NWG = 224;
constexpr u32 MB = 256;
constexpr u32 WS_EPOCH = 0, WS_ALL = 2048, WS_KVF = 4096, WS_HF = WS_KVF + 65536, WS_YF = WS_HF + 65536, WS_RG = WS_YF + 65536, WS_CG = WS_RG + 16384;
constexpr u32 TP = 8192 + 128;
constexpr u32 WS_H = 1u << 20, WS_KVT = 9u << 20, WS_YT = 13u << 20;
constexpr u32 STAMP_OFF = 24u << 20;
constexpr int NSTAMP = 22;
constexpr u32 L_PIECE = 16384, L_W = 65536, L_LOC0 = 131072, L_MISC = 147456, LDS_BYTES = 163840;
constexpr u32 KVRAW_SZ = 5632, L_KVS = L_LOC0 + 4 * KVRAW_SZ, L_B2 = L_KVS + 9216, L_FLAG = L_B2 + 128, L_ROWF = L_FLAG + 128, L_B2F = L_ROWF + 256, L_R2 = L_B2F + 256;
constexpr u32 L_SCR = L_B2F;
static_assert(L_R2 + 256 <= LDS_BYTES, "lds");
constexpr u32 F_TOEP = L_FLAG, F_HCNT = L_FLAG + 16, F_HRDY = L_FLAG + 20, F_KVRDY = L_FLAG + 24, F_YCNT = L_FLAG + 28, F_YRDY = L_FLAG + 32, F_S0 = L_FLAG + 36, F_KVCNT = L_FLAG + 40, F_W = L_FLAG + 48, F_LAND = L_FLAG + 64, F_XCH = L_FLAG + 96, F_KVS = L_FLAG + 124, F_CONS = L_KVS + 128;
constexpr u32 F_GP = F_CONS + 8 * 144, F_WF = F_CONS + 9 * 144;
constexpr u32 L_CONV = 16384, CONV_CH = 65536, TOEP_CP = 4160, TOEP_CH = 4 * TOEP_CP, U_SZ = 4608, YS_SZ = 2304;
constexpr u64 TAG_SALT = 0x9E3779B97F4A7C15ull;
constexpr int SPIN_CAP = 1 << 15;
#define SLP 2

#define ST(i) do { if (DIAG == (i)) dg1 = __builtin_amdgcn_s_memrealtime(); } while (0)
#define STP nullptr

DI void dma16(u32 voff, const void* sbase, u32 lds) {
    asm volatile("s_mov_b32 m0, %2\n\ts_nop 0\n\tglobal_load_lds_dwordx4 %0, %1" :: "v"(voff), "s"(sbase), "s"(lds) : "memory");
}
DI void setm0(u32 lds) { asm volatile("s_mov_b32 m0, %0\n\ts_nop 0" :: "s"(lds) : "memory"); }
template <int OFF> DI void dmai(u32 voff, const void* sbase) { asm volatile("global_load_lds_dwordx4 %0, %1 offset:%2" :: "v"(voff), "s"(sbase), "i"(OFF) : "memory"); }
DI void bdma16(u32 voff, u32x4 srd, u32 lds) {
    asm volatile("s_mov_b32 m0, %2\n\ts_nop 0\n\tbuffer_load_dwordx4 %0, %1, 0 offen lds" :: "v"(voff), "s"(srd), "s"(lds) : "memory");
}
#define SB() __builtin_amdgcn_sched_barrier(0)
template <int OFF> DI u32x2 alds8(u32 a) { u32x2 r; asm volatile("ds_read_b64 %0, %1 offset:%2" : "=v"(r) : "v"(a), "i"(OFF)); return r; }
template <int OFF> DI u32x4 alds16(u32 a) { u32x4 r; asm volatile("ds_read_b128 %0, %1 offset:%2" : "=v"(r) : "v"(a), "i"(OFF)); return r; }
DI u32x4 gld16(u32 voff, const void* sbase) { u32x4 r; asm volatile("global_load_dwordx4 %0, %1, %2" : "=v"(r) : "v"(voff), "s"(sbase) : "memory"); return r; }
template <int OFF> DI u32x4 gld16c(u32 voff, const void* sbase) { u32x4 r; asm volatile("global_load_dwordx4 %0, %1, %2 offset:%3 sc1" : "=v"(r) : "v"(voff), "s"(sbase), "i"(OFF) : "memory"); return r; }
DI u32x2 gld8c(u32 voff, const void* sbase) { u32x2 r; asm volatile("global_load_dwordx2 %0, %1, %2 sc1" : "=v"(r) : "v"(voff), "s"(sbase) : "memory"); return r; }
DI void touch(const void* addr, u32 lds) { asm volatile("s_mov_b32 m0, %1\n\ts_nop 0\n\tglobal_load_lds_dword %0, off" :: "v"(addr), "s"(lds) : "memory"); }
DI u32 gld2(u32 voff, const void* sbase) { u32 r; asm volatile("global_load_ushort %0, %1, %2" : "=v"(r) : "v"(voff), "s"(sbase) : "memory"); return r; }
DI u32x4 bld16(u32 voff, u32x4 srd) { u32x4 r; asm volatile("buffer_load_dwordx4 %0, %1, %2, 0 offen" : "=v"(r) : "v"(voff), "s"(srd) : "memory"); return r; }
template <int OFF> DI u32x4 gld16o(u32 voff, const void* sbase) { u32x4 r; asm volatile("global_load_dwordx4 %0, %1, %2 offset:%3" : "=v"(r) : "v"(voff), "s"(sbase), "i"(OFF) : "memory"); return r; }
template <int OFF> DI void gst16c(u32 voff, void* sbase, u32x4 v) { asm volatile("global_store_dwordx4 %0, %1, %2 offset:%3 sc1\n\ts_nop 1" :: "v"(voff), "v"(v), "s"(sbase), "i"(OFF) : "memory"); }
template <int OFF> DI void gst8c(u32 voff, void* sbase, u32x2 v) { asm volatile("global_store_dwordx2 %0, %1, %2 offset:%3 sc1" :: "v"(voff), "v"(v), "s"(sbase), "i"(OFF) : "memory"); }
#define WAITV(n) asm volatile("s_waitcnt vmcnt(%0)" :: "i"(n) : "memory")
#define WAITBAR(n) asm volatile("s_waitcnt vmcnt(%0) lgkmcnt(0)\n\ts_barrier" :: "i"(n) : "memory")
#define BARL() asm volatile("s_waitcnt lgkmcnt(0)\n\ts_barrier" ::: "memory")
#define PIN(x) asm volatile("" : "+v"(x))

DI u32x4 mksrd(const void* p, u32 bytes) { u64 a = (u64)p; return u32x4{(u32)a, (u32)(a >> 32) & 0xffffu, bytes, 0x00020000u}; }
DI float bflo(u32 x) { return __uint_as_float(x << 16); }
DI float bfhi(u32 x) { return __uint_as_float(x & 0xffff0000u); }
DI u32 pk2(float a, float b) { bf16x2 v = {(__bf16)a, (__bf16)b}; return __builtin_bit_cast(u32, v); }
DI float rbf(float a) { return bflo(pk2(a, a)); }
DI float relu(float a) { int i = __float_as_int(a); return __int_as_float(i > 0 ? i : 0); }
DI float fma1(float a, float b, float c) { float r; asm("v_fma_f32 %0, %1, %2, %3" : "=v"(r) : "v"(a), "v"(b), "v"(c)); return r; }
DI float mul1(float a, float b) { float r; asm("v_mul_f32 %0, %1, %2" : "=v"(r) : "v"(a), "v"(b)); return r; }
DI float add1(float a, float b) { float r; asm("v_add_f32 %0, %1, %2" : "=v"(r) : "v"(a), "v"(b)); return r; }
DI float dot2(u32 a, u32 b, float c) { return __builtin_amdgcn_fdot2_f32_bf16(__builtin_bit_cast(bf16x2, a), __builtin_bit_cast(bf16x2, b), c, false); }
DI u32x4 lds16(u32 a) { return *(const LAS u32x4*)a; }
DI u32x2 lds8(u32 a) { return *(const LAS u32x2*)a; }
DI void sts16(u32 a, u32x4 v) { *(LAS u32x4*)a = v; }
DI void sts8(u32 a, u32x2 v) { *(LAS u32x2*)a = v; }
DI u32x2 ldtr(u32 a) { return __builtin_bit_cast(u32x2, __builtin_amdgcn_ds_read_tr16_b64_v4i16((LAS s16x4*)a)); }
DI u32x4 cat(u32x2 a, u32x2 b) { return u32x4{a.x, a.y, b.x, b.y}; }
DI f32x16 mfma32(u32x4 a, u32x4 b, f32x16 c) { return __builtin_amdgcn_mfma_f32_32x32x16_bf16(__builtin_bit_cast(bf16x8, a), __builtin_bit_cast(bf16x8, b), c, 0, 0, 0); }
DI f32x4 mfma16(u32x4 a, u32x4 b, f32x4 c) { return __builtin_amdgcn_mfma_f32_16x16x32_bf16(__builtin_bit_cast(bf16x8, a), __builtin_bit_cast(bf16x8, b), c, 0, 0, 0); }
DI float xhalf_sum(float v) { auto r = __builtin_amdgcn_permlane32_swap(__float_as_uint(v), __float_as_uint(v), false, false); return __uint_as_float(r[0]) + __uint_as_float(r[1]); }

struct Params {
    const u16 *x, *k, *v, *shk, *ssmk, *ssmd, *w1, *b1, *w2, *b2;
    u16* out; char* ws;
};

DI void poll8(u32 voff, const void* sbase, bool need, u32 tlo, u32 thi) {
    _Pragma("clang loop unroll(disable)") for (int spin = 0; spin < SPIN_CAP; ++spin) {
        u32x2 f = gld8c(voff, sbase);
        asm volatile("s_waitcnt vmcnt(0)" : "+v"(f) :: "memory");
        bool ok = !need || (f.x == tlo && f.y == thi);
        if (__all(ok)) break;
        __builtin_amdgcn_s_sleep(SLP);
    }
}
DI void poll16(u32 voff, const void* sbase, u32 tlo, u32 thi) {
    _Pragma("clang loop unroll(disable)") for (int spin = 0; spin < SPIN_CAP; ++spin) {
        u32x4 f = gld16c<0>(voff, sbase);
        asm volatile("s_waitcnt vmcnt(0)" : "+v"(f) :: "memory");
        bool ok = f.x == tlo && f.y == thi && f.z == tlo && f.w == thi;
        if (__all(ok)) break;
        __builtin_amdgcn_s_sleep(SLP);
    }
}

template <int SL> DI void probe(u32 voff, const void* sbase, u32 tag) {
    _Pragma("clang loop unroll(disable)") for (int spin = 0; spin < SPIN_CAP; ++spin) {
        u32x2 f = gld8c(voff, sbase);
        asm volatile("s_waitcnt vmcnt(0)" : "+v"(f) :: "memory");
        if (__all(f.y == tag)) break;
        __builtin_amdgcn_s_sleep(SL);
    }
}
DI bool lds_arrive_last(u32 addr, u32 n) {
    u32 old = 0;
    if ((threadIdx.x & 63) == 0) old = __hip_atomic_fetch_add((LAS u32*)addr, 1u, __ATOMIC_RELAXED, __HIP_MEMORY_SCOPE_WORKGROUP);
    return (u32)__builtin_amdgcn_readfirstlane(old) == n - 1;
}
DI void lds_wait(u32 addr) {
    _Pragma("clang loop unroll(disable)") for (int spin = 0; spin < (1 << 22); ++spin) {
        if (__builtin_amdgcn_readfirstlane(*(volatile LAS u32*)addr)) break;
        __builtin_amdgcn_s_sleep(1);
    }
    asm volatile("" ::: "memory");
}
DI void lds_add(u32 addr) { asm volatile("" ::: "memory"); __hip_atomic_fetch_add((LAS u32*)addr, 1u, __ATOMIC_RELAXED, __HIP_MEMORY_SCOPE_WORKGROUP); asm volatile("" ::: "memory"); }
DI u32 lds_peek(u32 addr) { return *(volatile LAS u32*)addr; }
DI void lds_wait_ge(u32 addr, u32 n, u32 first) {
    u32 v = first;
    _Pragma("clang loop unroll(disable)") for (int spin = 0; spin < (1 << 22) && (u32)__builtin_amdgcn_readfirstlane(v) < n; ++spin) { __builtin_amdgcn_s_sleep(1); v = lds_peek(addr); }
    asm volatile("" ::: "memory");
}
template <int t> DI void cons_set(u32 lds0, int g) { asm volatile("s_waitcnt lgkmcnt(0)\n\tds_write_b32 %0, %1 offset:%2" :: "v"(lds0 + 131072 + g * 4), "v"(1u), "i"(F_CONS + t * 144 - 131072) : "memory"); }
DI void cons_wait(u32 lds0, int t) {
    _Pragma("clang loop unroll(disable)") for (int spin = 0; spin < (1 << 22); ++spin) {
        const u32x4 v = *(volatile LAS u32x4*)(lds0 + F_CONS + t * 144);
        if (__builtin_amdgcn_readfirstlane(v.x & v.y & v.z & v.w)) break;
        __builtin_amdgcn_s_sleep(1);
    }
    asm volatile("" ::: "memory");
}
DI void lds_set(u32 addr) { asm volatile("" ::: "memory"); if ((threadIdx.x & 63) == 0) *(volatile LAS u32*)addr = 1u; }
constexpr u32 loc_of(int c) { return (c % 5) == 0 ? L_LOC0 : (c % 5) == 1 ? 3 * L_PIECE : (c % 5) == 2 ? L_W + 3 * L_PIECE : (c % 5) == 3 ? 0u : L_W; }

#define CPRIO 2
#define YSLP 4
#define YFIRST 0
#define PRIOALL 0
#define LW 2
#define WEARLY 0
#define WDEPTH 8
#define TW 0
#define WC0 14
#define RSLP 8
#define HWAIT 2
#define KW 0
#define CROTM 0
#define ROTM 1
#define LDELAY 0
#define KDELAY 0
#define HDV 4
constexpr int HD = HDV, HS = HD + 1;
constexpr int WS_ = 2;
constexpr int g2_younger(int c) {
    const int n = c + 1;
    if (n >= 14) return 63;
    bool sh = false, sw = n < LW || n >= WC0; int ah = 0, aw = sw ? 1000 : 0;
    for (int i = LW; i < 5; ++i) { if (sw) aw += 4; if (i == n) sw = true; }
    for (int i = 0; i < HD; ++i) { if (sh) ah += 4; if (sw) aw += 4; if (i == n) sh = true; }
    for (int i = 0; i < c; ++i) {
        if (i + HD < 14) { if (sh) ah += 4; if (sw) aw += 4; if (i + HD == n) sh = true; }
        if (i + 5 < WC0) { if (sh) ah += 4; if (sw) aw += 4; if (i + 5 == n) sw = true; }
    }
    return ah < aw ? ah : aw;
}

struct G2Ctx { const char* w2b; const char* hb; int crot, fh2; u32 voffw[4], ldsg, vlane, ta[3][2], smask, fl; };
template <int C> DI int g2_chunk(const G2Ctx& x) {
    int c = C + x.crot; if (c >= 14) c -= 14;
    return c;
}
template <int C> DI const char* g2_wsrc(const G2Ctx& x) { return x.w2b + (size_t)g2_chunk<C>(x) * (128 * 896); }
template <int C> DI u32 g2_loc(const G2Ctx& x) { return loc_of(C) + x.ldsg; }
template <int C, int N> DI void g2_w1(const G2Ctx& x, const char* sb) { if constexpr (N == 0) setm0(g2_loc<C>(x)); dmai<N * 1024>(x.voffw[N], sb); }
template <int C> DI void g2_issue_w(const G2Ctx& x) { const char* sb = g2_wsrc<C>(x); g2_w1<C, 0>(x, sb); g2_w1<C, 1>(x, sb); g2_w1<C, 2>(x, sb); g2_w1<C, 3>(x, sb); }
template <int C> DI const char* g2_hsrc(const G2Ctx& x) { return x.hb + g2_chunk<C>(x) * 8192; }
template <int N> DI u32x4 g2_h1(const G2Ctx& x, const char* sb) { return gld16c<(N & 1) * 1024>(x.vlane + (N >> 1) * 112 * 1024, sb); }
template <int C> DI void g2_issue_h(const G2Ctx& x, u32x4 (&hf)[HS][4]) {
    const char* sb = g2_hsrc<C>(x);
    hf[C % HS][0] = g2_h1<0>(x, sb); hf[C % HS][1] = g2_h1<1>(x, sb); hf[C % HS][2] = g2_h1<2>(x, sb); hf[C % HS][3] = g2_h1<3>(x, sb);
}
template <int C> DI void g2_read(const G2Ctx& x, u32x4 (&wf)[WS_][4]) {
    const u32 a0 = x.ta[loc_of(C) >> 16][0], a1 = x.ta[loc_of(C) >> 16][1];
    constexpr u32 lo = loc_of(C) & 0xffffu;
#pragma unroll
    for (int ks = 0; ks < 2; ++ks) {
        wf[C % WS_][ks * 2] = cat(ldtr(a0 + lo + ks * 2048), ldtr(a0 + lo + ks * 2048 + 1024));
        wf[C % WS_][ks * 2 + 1] = cat(ldtr(a1 + lo + ks * 2048), ldtr(a1 + lo + ks * 2048 + 1024));
    }
}
template <int C, int I> DI void g2_rd(const G2Ctx& x, u32x4 (&wf)[WS_][4]) {
    if constexpr (C < 14) {
        const u32 a = x.ta[loc_of(C) >> 16][I & 1]; constexpr u32 lo = (loc_of(C) & 0xffffu) + (I >> 1) * 2048;
        wf[C % WS_][I] = cat(ldtr(a + lo), ldtr(a + lo + 1024));
    }
}
DI u32 chk(u32 d, u32 m, u32 bad) { return __builtin_amdgcn_bitop3_b32(d, m, bad, 0xBE); }
template <int C> DI void g2_retry(const G2Ctx& x, u32x4 (&hf)[HS][4], u32 bad, u64* stamps) {
    if (__builtin_expect(!__any((bad & 0x80008000u) != 0u), 1)) return;
    _Pragma("clang loop unroll(disable)") for (int spin = 0; spin < SPIN_CAP; ++spin) {
        __builtin_amdgcn_s_sleep(RSLP);
        g2_issue_h<C>(x, hf);
        asm volatile("s_waitcnt vmcnt(0)" : "+v"(hf[C % HS][0]), "+v"(hf[C % HS][1]), "+v"(hf[C % HS][2]), "+v"(hf[C % HS][3]) :: "memory");
        bad = 0u;
#pragma unroll
        for (int i = 0; i < 16; ++i) { hf[C % HS][i >> 2][i & 3] ^= x.smask; bad |= hf[C % HS][i >> 2][i & 3]; }
        if (!__any((bad & 0x80008000u) != 0u)) break;
    }
}
template <int C> DI void g2_step(const G2Ctx& x, u32x4 (&hf)[HS][4], f32x16 (&acc)[2][2], u32x4 (&wf)[WS_][4], u32 pk, u64* stamps) {
    constexpr int N = (C + 1 < 14 ? C + 1 : C) % HS;
    asm volatile("s_waitcnt vmcnt(%4) lgkmcnt(0)" : "+v"(hf[N][0]), "+v"(hf[N][1]), "+v"(hf[N][2]), "+v"(hf[N][3]) : "i"(g2_younger(C)) : "memory");
    if constexpr (C + 5 >= WC0 && C + 5 < 14) asm volatile("ds_write_b32 %0, %1 offset:%2" :: "v"(x.fl), "v"(C + 1), "i"(F_GP - 131072) : "memory");
    if constexpr (C + 1 >= WC0 && C + 1 < 14) lds_wait_ge(x.fl + (F_WF - 131072), C + 2, pk);
    SB();
    constexpr bool DH = C + HD < 14, DW = C + 5 < WC0, DC = C + 1 < 14;
    const char* hs = g2_hsrc<(DH ? C + HD : 0)>(x);
    const char* wsrc = g2_wsrc<(DW ? C + 5 : 0)>(x);
    u32 bad = 0u;
#define G2M(i) acc[((i) >> 1) & 1][(i) & 1] = mfma32(wf[C % WS_][((i) >> 2) * 2 + (((i) >> 1) & 1)], hf[C % HS][((i) & 1) * 2 + ((i) >> 2)], acc[((i) >> 1) & 1][(i) & 1]); \
               if constexpr (DC) { hf[N][(i) >> 1][((i) & 1) * 2] ^= x.smask; hf[N][(i) >> 1][((i) & 1) * 2 + 1] ^= x.smask; bad = bad | hf[N][(i) >> 1][((i) & 1) * 2] | hf[N][(i) >> 1][((i) & 1) * 2 + 1]; }
    G2M(0); g2_rd<C + 1, 0>(x, wf); if constexpr (DH) hf[(C + HD) % HS][0] = g2_h1<0>(x, hs); SB();
    G2M(1); g2_rd<C + 1, 1>(x, wf); if constexpr (DH) hf[(C + HD) % HS][1] = g2_h1<1>(x, hs); SB();
    G2M(2); g2_rd<C + 1, 2>(x, wf); if constexpr (DH) hf[(C + HD) % HS][2] = g2_h1<2>(x, hs); SB();
    G2M(3); g2_rd<C + 1, 3>(x, wf); if constexpr (DH) hf[(C + HD) % HS][3] = g2_h1<3>(x, hs); SB();
    G2M(4); if constexpr (DW) g2_w1<C + 5, 0>(x, wsrc); SB();
    G2M(5); if constexpr (DW) g2_w1<C + 5, 1>(x, wsrc); SB();
    G2M(6); if constexpr (DW) g2_w1<C + 5, 2>(x, wsrc); SB();
    G2M(7); if constexpr (DW) g2_w1<C + 5, 3>(x, wsrc); if constexpr (C + 2 >= WC0 && C + 2 < 14) pk = lds_peek(x.fl + (F_WF - 131072)); SB();
#undef G2M
    if constexpr (DC) { g2_retry<C + 1>(x, hf, bad, stamps); g2_step<C + 1>(x, hf, acc, wf, pk, stamps); }
}

struct CF { u32x2 a0[8], a1[8]; u32x4 b[8]; };
template <int S, int R> DI void cv_ld(CF& f, u32 aA, u32 aB) {
    f.a0[R] = alds8<R * 512 + S * 64>(aA); f.a1[R] = alds8<R * 512 + S * 64 + 8>(aA);
    f.b[R] = alds16<(224 - 32 * R + 4 * S) * 16>(aB);
}
DI void cv_wait(CF& f) {
    asm volatile("s_waitcnt lgkmcnt(0)" : "+v"(f.a0[0]), "+v"(f.a0[1]), "+v"(f.a0[2]), "+v"(f.a0[3]), "+v"(f.a0[4]), "+v"(f.a0[5]), "+v"(f.a0[6]), "+v"(f.a0[7]),
                 "+v"(f.a1[0]), "+v"(f.a1[1]), "+v"(f.a1[2]), "+v"(f.a1[3]), "+v"(f.a1[4]), "+v"(f.a1[5]), "+v"(f.a1[6]), "+v"(f.a1[7]),
                 "+v"(f.b[0]), "+v"(f.b[1]), "+v"(f.b[2]), "+v"(f.b[3]), "+v"(f.b[4]), "+v"(f.b[5]), "+v"(f.b[6]), "+v"(f.b[7]));
}
constexpr int cv_r(int m) { int r = 0; while (m >= 8 - r) { m -= 8 - r; ++r; } return r; }
constexpr int cv_c(int m) { int r = 0; while (m >= 8 - r) { m -= 8 - r; ++r; } return m; }
template <int S, int M> DI void cv_mm(CF& f, CF& n, u32 aA, u32 aB, f32x4 (&acc)[8]) {
    constexpr int r = cv_r(M), c = cv_c(M);
    acc[r + c] = mfma16(cat(f.a0[r], f.a1[r]), f.b[c], acc[r + c]);
    if constexpr ((M & 3) == 0 && M / 4 < 8) cv_ld<S + 1, M / 4>(n, aA, aB);
    SB();
    if constexpr (M + 1 < 36) cv_mm<S, M + 1>(f, n, aA, aB, acc);
}
DI void conv_body(u32 aA, u32 aB, f32x4 (&acc)[8]) {
    CF f, n;
    asm volatile("s_waitcnt lgkmcnt(0)" ::: "memory");
    cv_ld<0, 0>(f, aA, aB); cv_ld<0, 1>(f, aA, aB); cv_ld<0, 2>(f, aA, aB); cv_ld<0, 3>(f, aA, aB);
    cv_ld<0, 4>(f, aA, aB); cv_ld<0, 5>(f, aA, aB); cv_ld<0, 6>(f, aA, aB); cv_ld<0, 7>(f, aA, aB);
    _Pragma("clang loop unroll(disable)") for (int it = 0; it < 2; ++it) {
        cv_wait(f); SB();
        cv_mm<0, 0>(f, n, aA, aB, acc);
        cv_wait(n); SB();
        cv_mm<1, 0>(n, f, aA, aB, acc);
        aA += 128; aB += 128;
    }
    cv_wait(f);
    asm volatile("" ::: "memory");
}

struct Geo { int bi, b, xcd, fh2, RB, CT, tb64, db, rot, crot; u32 R0, F0, T0, D0; };
DI Geo make_geo() {
    Geo g; g.b = blockIdx.x - NHELP; g.xcd = g.b & 7; const int bi = g.b >> 3; g.bi = bi;
    const int rq = g.xcd >> 1, rb = bi / 7, ct = bi - rb * 7;
    g.fh2 = g.xcd & 1; g.RB = 4 * rq + rb; g.CT = 7 * g.fh2 + ct; g.R0 = 128 * g.RB; g.F0 = 128 * g.CT;
    g.tb64 = 2 * g.RB + g.fh2; g.db = ct; g.T0 = 64 * g.tb64; g.D0 = 64 * g.db;

    g.rot = g.xcd >= 7 ? 0 : g.xcd;
     g.crot = CROTM == 4 ? (2 * g.xcd) % 14 : CROTM == 0 ? g.CT : CROTM == 1 ? (g.CT + 3 * rb + (rb >> 1)) % 14 : CROTM == 2 ? 2 * ct : (2 * ct + 4 * rb - (rb == 3)) % 14;
    return g;
}
DI void make_g2(G2Ctx& g2, const Params& p, const Geo& q, int g, int lane, u32 lds0) {
    g2.w2b = (const char*)p.w2 + (size_t)(32 * g) * 896 + q.D0 * 2;
    g2.hb = p.ws + WS_H + (size_t)(q.tb64 * 2 * 112 + 2 * g) * 1024;
    g2.crot = q.crot; g2.fh2 = q.fh2;
#pragma unroll
    for (int n = 0; n < 4; ++n) g2.voffw[n] = (u32)((lane >> 3) * 896 + 16 * ((lane & 7) ^ (((lane >> 4) & 1) << 2)) + n * (8 * 896 - 1024));
    g2.ldsg = lds0 + g * 4096; g2.vlane = lane * 16u;
}
DI u32 slab_base(u32 lds0, int cw) { return lds0 + L_CONV + (cw >> 1) * CONV_CH + (cw & 1) * 8192; }
DI void final_epilogue(const Params& p, const Geo& q, u32 lds0) {
    int tz = threadIdx.x; PIN(tz);
    const int t = tz >> 3, c = tz & 7;
    const u32 pb = lds0 + ((t >> 5) ? 0u : 3 * L_PIECE) + ((t >> 4) & 1) * L_W + (t & 15) * 256;
    const u32 o0 = pb + 16 * ((2 * c) ^ (t & 7)), o1 = o0 ^ 16;
    f32x4 pa[4], pb_[4];
#pragma unroll
    for (int g = 0; g < 4; ++g) { pa[g] = __builtin_bit_cast(f32x4, lds16(o0 + g * 4096)); pb_[g] = __builtin_bit_cast(f32x4, lds16(o1 + g * 4096)); }
    const float r2 = *(const LAS float*)(lds0 + L_R2 + t * 4);
    const f32x4 b0 = __builtin_bit_cast(f32x4, lds16(lds0 + L_B2F + c * 32)), b1 = __builtin_bit_cast(f32x4, lds16(lds0 + L_B2F + c * 32 + 16));
    const u32 sb = slab_base(lds0, c >> 1) + t * 64 + (c & 1) * 32;
    const f32x4 sv0 = __builtin_bit_cast(f32x4, lds16(sb)), sv1 = __builtin_bit_cast(f32x4, lds16(sb + 16));
    const f32x4 xv0 = __builtin_bit_cast(f32x4, lds16(sb + 4096)), xv1 = __builtin_bit_cast(f32x4, lds16(sb + 4096 + 16));
    SB();
    const f32x4 s0 = (pa[0] + pa[1]) + (pa[2] + pa[3]), s1 = (pb_[0] + pb_[1]) + (pb_[2] + pb_[3]);
    f32x4 z0 = s0 * r2 + b0, z1 = s1 * r2 + b1;
    f32x4 e0 = z0 * -1.4426950408889634f, e1 = z1 * -1.4426950408889634f;
#pragma unroll
    for (int j = 0; j < 4; ++j) { e0[j] = __builtin_amdgcn_exp2f(e0[j]); e1[j] = __builtin_amdgcn_exp2f(e1[j]); }
    e0 += 1.f; e1 += 1.f;
#pragma unroll
    for (int j = 0; j < 4; ++j) { e0[j] = __builtin_amdgcn_rcpf(e0[j]); e1[j] = __builtin_amdgcn_rcpf(e1[j]); }
    z0 = z0 * e0; z1 = z1 * e1;
    z0 = z0 * sv0 + xv0; z1 = z1 * sv1 + xv1;
    const u32x4 res = {pk2(z0[0], z0[1]), pk2(z0[2], z0[3]), pk2(z1[0], z1[1]), pk2(z1[2], z1[3])};
    *(GAS u32x4*)((GAS char*)p.out + (size_t)(q.T0 + t) * 896 + q.D0 * 2 + c * 16) = res;
}
#define STAMP_DECL
#define STAMP_OUT(wave) do {} while (0)

extern __shared__ __attribute__((aligned(16))) char smem[];

struct CL { const char *xb, *wb; u32 vx[4], vw[4], sl; int rot; };
struct CSrc { const char *xs, *wsrc; };
template <int ST_> DI CSrc c_src(const CL& c) { int kc = ST_ + c.rot; if (kc >= 7) kc -= 7; return CSrc{c.xb + kc * 128, c.wb + (size_t)kc * 64 * 3584}; }
template <int ST_, int N> DI void c_dma1(const CL& c, const CSrc& s) {
    if constexpr (N == 0) setm0(c.sl + (ST_ & 3) * L_PIECE);
    if constexpr (N == 4) setm0(c.sl + (ST_ & 3) * L_PIECE + L_W);
    if constexpr (N < 4) dmai<N * 1024>(c.vx[N], s.xs); else dmai<(N - 4) * 1024>(c.vw[N - 4], s.wsrc);
}
template <int ST_> DI void c_dma(const CL& c) {
    const CSrc s = c_src<ST_>(c);
    c_dma1<ST_, 0>(c, s); c_dma1<ST_, 1>(c, s); c_dma1<ST_, 2>(c, s); c_dma1<ST_, 3>(c, s);
    c_dma1<ST_, 4>(c, s); c_dma1<ST_, 5>(c, s); c_dma1<ST_, 6>(c, s); c_dma1<ST_, 7>(c, s);
}
DI void make_cl(CL& c, const Params& p, const Geo& q, int cw, int lane, u32 lds0) {
    const int r8 = lane >> 3, c8 = lane & 7, r4 = lane >> 4, c16 = lane & 15;
#pragma unroll
    for (int n = 0; n < 4; ++n) {
        c.vx[n] = (u32)((32 * cw + r8) * 896 + 16 * (c8 ^ (((n & 1) << 2) | (r8 >> 1))) + n * (8 * 896 - 1024));
        c.vw[n] = (u32)((16 * cw + r4) * 3584 + 16 * (c16 ^ (r4 << 2)) + n * (4 * 3584 - 1024));
    }
    c.xb = (const char*)p.x + (size_t)q.R0 * 896; c.wb = (const char*)p.w1 + q.F0 * 2;
    c.sl = lds0 + cw * 4096; c.rot = q.rot;
}

template <int ST_> DI void l_dma(const CL& c) {
    const CSrc s = c_src<ST_>(c);
    const u32 sl = c.sl + (ST_ & 3) * L_PIECE;
    setm0(sl); dmai<0>(c.vx[0], s.xs); dmai<1024>(c.vx[1], s.xs); dmai<2048>(c.vx[2], s.xs); dmai<3072>(c.vx[3], s.xs);
    setm0(sl + L_W); dmai<0>(c.vw[0], s.wsrc); dmai<1024>(c.vw[1], s.wsrc); dmai<2048>(c.vw[2], s.wsrc); dmai<3072>(c.vw[3], s.wsrc);
    const char* x2 = s.xs + 32 * 896; const char* w2 = s.wsrc + 16 * 3584;
    setm0(sl + 4096); dmai<0>(c.vx[0], x2); dmai<1024>(c.vx[1], x2); dmai<2048>(c.vx[2], x2); dmai<3072>(c.vx[3], x2);
    setm0(sl + L_W + 4096); dmai<0>(c.vw[0], w2); dmai<1024>(c.vw[1], w2); dmai<2048>(c.vw[2], w2); dmai<3072>(c.vw[3], w2);
}

struct Fr { u32x4 x[4][2], w[4][2]; };
struct GA { u32 ax[4], aw[2]; };
template <int ST_> DI void g1_read(const GA& a, Fr& f) {
    constexpr u32 so = (ST_ & 3) * L_PIECE;
#pragma unroll
    for (int k = 0; k < 4; ++k) {
        f.w[k][0] = cat(ldtr(a.aw[0] + so + k * 4096), ldtr(a.aw[0] + so + k * 4096 + 1024));
        f.x[k][0] = lds16(a.ax[k] + so);
        f.w[k][1] = cat(ldtr(a.aw[1] + so + k * 4096), ldtr(a.aw[1] + so + k * 4096 + 1024));
        f.x[k][1] = lds16(a.ax[k] + so + 4096);
    }
}
template <int K0> DI void g1_mma(const Fr& f, f32x16 (&acc)[2][2]) {
#pragma unroll
    for (int k = K0; k < K0 + 2; ++k) {
        acc[0][0] = mfma32(f.w[k][0], f.x[k][0], acc[0][0]); acc[1][0] = mfma32(f.w[k][1], f.x[k][0], acc[1][0]);
        acc[0][1] = mfma32(f.w[k][0], f.x[k][1], acc[0][1]); acc[1][1] = mfma32(f.w[k][1], f.x[k][1], acc[1][1]);
    }
    SB();
}

template <int ST_, int I> DI void g1_rd(const GA& a, Fr& f) {
    constexpr u32 so = (ST_ & 3) * L_PIECE; constexpr int k = I >> 1, h = I & 1;
    f.w[k][h] = cat(ldtr(a.aw[h] + so + k * 4096), ldtr(a.aw[h] + so + k * 4096 + 1024));
    f.x[k][h] = lds16(a.ax[k] + so + h * 4096);
}
template <int K0, int I> DI void g1_m1(const Fr& f, f32x16 (&acc)[2][2]) {
    constexpr int k = K0 + (I >> 2), mt = I & 1, st = (I >> 1) & 1;
    acc[mt][st] = mfma32(f.w[k][mt], f.x[k][st], acc[mt][st]);
}
template <int ST_, bool CARRY> DI void g1_stage(const GA& a, const Fr& fp, Fr& fn, f32x16 (&acc)[2][2], u32 lds0, int g, u32& pk) {
    if constexpr (CARRY) { g1_m1<2, 0>(fp, acc); SB(); cons_set<ST_ - 1>(lds0, g); SB(); }
    lds_wait_ge(lds0 + F_LAND + 4 * ST_, ST_ == 0 ? 256 : (ST_ == 3 || ST_ == 6) ? 128 : 64, pk);
    if constexpr (CARRY) {
        g1_rd<ST_, 0>(a, fn); SB();
        g1_m1<2, 1>(fp, acc); SB(); g1_m1<2, 2>(fp, acc); g1_rd<ST_, 1>(a, fn); SB();
        g1_m1<2, 3>(fp, acc); SB(); g1_m1<2, 4>(fp, acc); g1_rd<ST_, 2>(a, fn); SB();
        g1_m1<2, 5>(fp, acc); g1_rd<ST_, 3>(a, fn); SB(); g1_m1<2, 6>(fp, acc); SB();
        g1_m1<2, 7>(fp, acc); SB();
    } else { g1_rd<ST_, 0>(a, fn); g1_rd<ST_, 1>(a, fn); g1_rd<ST_, 2>(a, fn); g1_rd<ST_, 3>(a, fn); SB(); }
    g1_m1<0, 0>(fn, acc); g1_rd<ST_, 4>(a, fn); SB(); g1_m1<0, 1>(fn, acc); SB();
    g1_m1<0, 2>(fn, acc); g1_rd<ST_, 5>(a, fn); SB(); g1_m1<0, 3>(fn, acc); g1_rd<ST_, 6>(a, fn); SB();
    g1_m1<0, 4>(fn, acc); g1_rd<ST_, 7>(a, fn); SB(); g1_m1<0, 5>(fn, acc); pk = lds_peek(lds0 + F_LAND + 4 * ST_ + 4); SB();
    g1_m1<0, 6>(fn, acc); SB(); g1_m1<0, 7>(fn, acc); SB();
}

DI void body_g(const Params& p, const int g) {
    STAMP_DECL
    const u64 dg0 = __builtin_amdgcn_s_memrealtime(); u64 dg1 = dg0;
    const u32 lds0 = (u32)(size_t)(LAS char*)smem;
    const int lane = threadIdx.x & 63;
    const Geo q = make_geo();
    char* ws = p.ws;
    const int hi = lane >> 5, s31 = lane & 31;
    const int r8 = lane >> 3, c8 = lane & 7;
    const u32 raw = lds0 + L_LOC0 + g * KVRAW_SZ;
    {
        const u32 vkv = (u32)(r8 * 896 + c8 * 16);
        bdma16((u32)(((int)q.T0 + 16 * g - 8) * 896 + (int)q.D0 * 2) + vkv, mksrd(p.k, S_ * D_ * 2), raw);
        const char* kb = (const char*)p.k + (size_t)(q.T0 + 16 * g) * 896 + q.D0 * 2;
        const char* vb = (const char*)p.v + (size_t)(q.T0 + 16 * g) * 896 + q.D0 * 2;
        dmai<1024>(vkv, kb - 1024); dmai<2048>(vkv, kb + 8 * 896 - 2048); dmai<3072>(vkv, vb - 3072);
        setm0(raw + 4096);
        dmai<0>(vkv, vb + 8 * 896);
        if (lane < 32) dmai<1024>(lane * 16u, (const char*)p.shk + q.D0 * 8 - 1024);
        CL c; make_cl(c, p, q, g, lane, lds0);
        c_dma<0>(c);
    }
    u32x2 ep = gld8c(0u, ws + WS_EPOCH);
    const int fh = g & 1, sh = g >> 1;
    GA ga;
    {
        const int blk = (lane >> 4) & 1, qq = (lane & 15) >> 2, pp = lane & 3;
        const u32 Z = (u32)(hi ^ ((s31 >> 1) & 7));
#pragma unroll
        for (int k = 0; k < 4; ++k) ga.ax[k] = lds0 + (64 * sh + s31) * 128 + 16 * ((2 * k) ^ Z);
        ga.aw[0] = lds0 + L_W + (8 * hi + qq) * 256 + 16 * ((8 * fh + 2 * blk + (pp >> 1)) ^ (qq << 2)) + 8 * (pp & 1);
        ga.aw[1] = ga.aw[0] ^ 64;
    }
    WAITV(9);
    {
        u32x4 wr[4], kr[5], vr[2];
#pragma unroll
        for (int e = 0; e < 4; ++e) wr[e] = lds16(raw + 5120 + c8 * 64 + e * 16);
#pragma unroll
        for (int j = 0; j < 5; ++j) kr[j] = lds16(raw + (5 + 2 * r8 + j) * 128 + c8 * 16);
        vr[0] = lds16(raw + 3072 + 2 * r8 * 128 + c8 * 16); vr[1] = lds16(raw + 3072 + (2 * r8 + 1) * 128 + c8 * 16);
        SB();
        u32x4 o0, o1;
#pragma unroll
        for (int e = 0; e < 4; ++e) {
            u32 pl[4], ph[4];
#pragma unroll
            for (int j = 0; j < 4; ++j) { pl[j] = __builtin_amdgcn_perm(kr[j][e], kr[j + 1][e], 0x05040100u); ph[j] = __builtin_amdgcn_perm(kr[j][e], kr[j + 1][e], 0x07060302u); }
            f32x2 a = {dot2(pl[0], wr[e].y, dot2(pl[2], wr[e].x, 0.f)), dot2(ph[0], wr[e].w, dot2(ph[2], wr[e].z, 0.f))};
            f32x2 b = {dot2(pl[1], wr[e].y, dot2(pl[3], wr[e].x, 0.f)), dot2(ph[1], wr[e].w, dot2(ph[3], wr[e].z, 0.f))};
            const u32 ar = pk2(a.x, a.y), br = pk2(b.x, b.y);
            a = f32x2{bflo(ar), bfhi(ar)} * f32x2{bflo(vr[0][e]), bfhi(vr[0][e])}; b = f32x2{bflo(br), bfhi(br)} * f32x2{bflo(vr[1][e]), bfhi(vr[1][e])};
            o0[e] = pk2(a.x, a.y); o1[e] = pk2(b.x, b.y);
        }
        sts16(lds0 + L_KVS + (16 * g + 2 * r8) * 144 + c8 * 16, o0);
        sts16(lds0 + L_KVS + (16 * g + 2 * r8 + 1) * 144 + c8 * 16, o1);
        asm volatile("s_waitcnt lgkmcnt(0)" ::: "memory");
        lds_add(lds0 + F_KVS);
    }
    f32x16 acc[2][2] = {};
    Fr fa, fb;
    WAITV(1);
    lds_add(lds0 + F_LAND);
    u32 pk = lds_peek(lds0 + F_LAND);
    ST(2);
    g1_stage<0, false>(ga, fb, fa, acc, lds0, g, pk);
    g1_stage<1, true>(ga, fa, fb, acc, lds0, g, pk);
    g1_stage<2, true>(ga, fb, fa, acc, lds0, g, pk);
    asm volatile("s_waitcnt vmcnt(0)" : "+v"(ep) :: "memory");
    const u64 epoch = ((u64)(u32)__builtin_amdgcn_readfirstlane(ep.y) << 32) | (u32)__builtin_amdgcn_readfirstlane(ep.x);
    const u64 tag = epoch + TAG_SALT;
    const u32 tlo = (u32)tag, thi = (u32)(tag >> 32);
    const u32 smask = ((lane ^ (u32)epoch) & 1u) ? 0x80008000u : 0u;
    g1_stage<3, true>(ga, fa, fb, acc, lds0, g, pk);
    ST(3);
    const u32 rgo = WS_RG + (q.R0 + 64 * sh + lane) * 8, cgo = WS_CG + (q.F0 + 64 * fh + lane) * 8;
    u32x2 rg = gld8c(rgo, ws), cg = gld8c(cgo, ws);
    u32 b1v = gld2((u32)((64 * fh + lane) * 2), (const char*)p.b1 + q.F0 * 2);
    g1_stage<4, true>(ga, fb, fa, acc, lds0, g, pk);
    asm volatile("s_waitcnt vmcnt(0)" : "+v"(rg), "+v"(cg), "+v"(b1v) :: "memory");
    if (!__all(rg.y == tlo && cg.y == tlo)) {
        rg = gld8c(rgo, ws); cg = gld8c(cgo, ws);
    }
    g1_stage<5, true>(ga, fa, fb, acc, lds0, g, pk);
    asm volatile("s_waitcnt vmcnt(0)" : "+v"(rg), "+v"(cg) :: "memory");
    if (!__all(rg.y == tlo && cg.y == tlo)) {
        rg = gld8c(rgo, ws); cg = gld8c(cgo, ws);
    }
    g1_stage<6, true>(ga, fb, fa, acc, lds0, g, pk);
    cons_set<6>(lds0, g);
    ST(5);
    asm volatile("s_waitcnt vmcnt(0)" : "+v"(rg), "+v"(cg), "+v"(b1v) :: "memory");
    _Pragma("clang loop unroll(disable)") for (int spin = 0; spin < SPIN_CAP && !__all(rg.y == tlo && cg.y == tlo); ++spin) {
        rg = gld8c(rgo, ws); cg = gld8c(cgo, ws);
        asm volatile("s_waitcnt vmcnt(0)" : "+v"(rg), "+v"(cg) :: "memory");
    }
    if (fh == 0 && sh == q.fh2) *(LAS u32*)(lds0 + L_ROWF + lane * 4) = rg.x;
    g1_mma<2>(fa, acc);
    {
        u32x4 ax[2], bx[2];
        const auto rr = __builtin_amdgcn_permlane32_swap(rg.x, rg.x, false, false);
        const auto cc = __builtin_amdgcn_permlane32_swap(cg.x, cg.x, false, false);
        const auto bb = __builtin_amdgcn_permlane32_swap(b1v, b1v, false, false);
#pragma unroll
        for (int i = 0; i < 2; ++i) {
            const u32 nm = __builtin_amdgcn_perm(rr[i], rr[i], 0x01000100u) ^ 0x80008000u;
            ax[i] = u32x4{cc[i], bb[i], 0u, 0u};
            bx[i] = u32x4{hi ? 0u : nm, hi ? 0u : (rr[i] >> 16), 0u, 0u};
        }
#pragma unroll
        for (int st = 0; st < 2; ++st)
#pragma unroll
            for (int mt = 0; mt < 2; ++mt) acc[mt][st] = mfma32(ax[mt], bx[st], acc[mt][st]);
#pragma unroll
        for (int st = 0; st < 2; ++st) {
            char* hb = ws + WS_H + (size_t)((4 * q.RB + 2 * sh + st) * 112 + q.CT * 8 + fh * 4) * 1024;
#pragma unroll
            for (int mt = 0; mt < 2; ++mt)
#pragma unroll
                for (int t = 0; t < 2; ++t) {
                    u32x4 hv;
#pragma unroll
                    for (int j = 0; j < 4; ++j) {
                        f32x2 a = {acc[mt][st][8 * t + 2 * j], acc[mt][st][8 * t + 2 * j + 1]};
                        f32x2 u = {relu(a.x), relu(a.y)};
                        u = u * u;
                        hv[j] = pk2(u.x, u.y) ^ smask;
                    }
                    if (mt == 0 && t == 0) gst16c<0>(lane * 16u, hb, hv);
                    if (mt == 0 && t == 1) gst16c<1024>(lane * 16u, hb, hv);
                    if (mt == 1 && t == 0) gst16c<2048>(lane * 16u, hb, hv);
                    if (mt == 1 && t == 1) gst16c<3072>(lane * 16u, hb, hv);
                    SB();
                }
        }
    }
    ST(6);
    G2Ctx g2; make_g2(g2, p, q, g, lane, lds0);
    {
        const int blk = (lane >> 4) & 1, qq = (lane & 15) >> 2, pp = lane & 3;
        const u32 t0 = g2.ldsg + (4 * hi + qq) * 128 + 16 * ((2 * blk + (pp >> 1)) ^ ((qq >> 1) << 2)) + 8 * (pp & 1);
#pragma unroll
        for (int i = 0; i < 3; ++i) { g2.ta[i][0] = t0 + i * 65536; g2.ta[i][1] = (t0 ^ 64) + i * 65536; PIN(g2.ta[i][0]); PIN(g2.ta[i][1]); }
    }
    g2.smask = smask; g2.fl = lds0 + 131072 + g * 4;
    u32x4 wf[WS_][4];
    cons_wait(lds0, 4);
    asm volatile("s_waitcnt lgkmcnt(0)" ::: "memory");
    {
        u32x4 hf[HS][4];
        if (lds_arrive_last(lds0 + F_HCNT, 4)) { if (lane == 0) gst8c<0>(WS_ALL + q.b * 8, ws, u32x2{tlo, thi}); }
        if constexpr (LW <= 2) g2_issue_w<2>(g2);
        if constexpr (LW <= 3) g2_issue_w<3>(g2);
        if constexpr (LW <= 4) g2_issue_w<4>(g2);
        if constexpr (HWAIT == 1) WAITV(12);
        if constexpr (HWAIT == 2) WAITV(0);
        g2_issue_h<0>(g2, hf); g2_issue_h<1>(g2, hf); if constexpr (HD > 2) g2_issue_h<2 % HS>(g2, hf); if constexpr (HD > 3) g2_issue_h<3 % HS>(g2, hf); if constexpr (HD > 4) g2_issue_h<4 % HS>(g2, hf);
        if constexpr (HD > 5) g2_issue_h<5 % HS>(g2, hf);
        ST(9);
        lds_wait(lds0 + F_W);
        if constexpr (KW) lds_wait(lds0 + F_W + 4 + (g >> 1) * 4);
        g2_read<0>(g2, wf);
        asm volatile("s_waitcnt vmcnt(%4)" : "+v"(hf[0][0]), "+v"(hf[0][1]), "+v"(hf[0][2]), "+v"(hf[0][3]) : "i"(4 * (HD - 1)) : "memory");
        {
            u32 bad = 0u;
#pragma unroll
            for (int i = 0; i < 16; ++i) { hf[0][i >> 2][i & 3] ^= smask; bad |= hf[0][i >> 2][i & 3]; }
            g2_retry<0>(g2, hf, bad, STP);
        }
        ST(7);
        f32x16 a2[2][2] = {};
        g2_step<0>(g2, hf, a2, wf, 0u, STP);
        ST(8);
        const u32 pw = g2.ldsg + (s31 >> 4) * L_W + (s31 & 15) * 256;
#pragma unroll
        for (int s2 = 0; s2 < 2; ++s2)
#pragma unroll
            for (int dt = 0; dt < 2; ++dt)
#pragma unroll
                for (int gr = 0; gr < 4; ++gr) {
                    const u32 a = pw + (s2 ? 0u : 3 * L_PIECE) + 16 * ((8 * dt + 2 * gr + hi) ^ (s31 & 7));
                    sts16(a, __builtin_bit_cast(u32x4, f32x4{a2[dt][s2][4 * gr], a2[dt][s2][4 * gr + 1], a2[dt][s2][4 * gr + 2], a2[dt][s2][4 * gr + 3]}));
                }
    }
    BARL(); ST(10);
    final_epilogue(p, q, lds0);
    ST(11);
    if (q.b == 100 && g == 0) { const u64 e = __builtin_amdgcn_s_memrealtime() + 3 * (dg1 - dg0); while (__builtin_amdgcn_s_memrealtime() < e) __builtin_amdgcn_s_sleep(1); }
    STAMP_OUT(g);
}

DI void body_c(const Params& p, const int cw) {
    STAMP_DECL
    u64 dg1 = 0; (void)dg1;
    const u32 lds0 = (u32)(size_t)(LAS char*)smem;
    const int lane = threadIdx.x & 63;
    const Geo q = make_geo();
    char* ws = p.ws;
    const int hi = lane >> 5, s31 = lane & 31;
    const int half = cw & 1, chl = cw >> 1, chan = 2 * q.b + chl;
    if (PRIOALL) __builtin_amdgcn_s_setprio(CPRIO);
    u32x2 ep;
    u32x4 xs[4]; u32 dv;
    if (cw < 2) {
        CL c, c2; make_cl(c, p, q, 0, lane, lds0); c2 = c; c2.xb += 64 * 896; c2.wb += 32 * 3584; c2.sl += 8192;
        ep = gld8c(0u, ws + WS_EPOCH);
        if (cw == 0) {
            l_dma<1>(c); l_dma<1>(c2); ST(1);
            WAITV(0); lds_add(lds0 + F_LAND + 4); ST(2);
            cons_wait(lds0, 0); l_dma<4>(c); l_dma<4>(c2); ST(19);
            WAITV(0); lds_add(lds0 + F_LAND + 16); ST(20);
        } else {
            l_dma<2>(c); l_dma<2>(c2); ST(1);
            WAITV(0); lds_add(lds0 + F_LAND + 8); ST(2);
            cons_wait(lds0, 1); l_dma<5>(c); l_dma<5>(c2); ST(19);
            WAITV(0); lds_add(lds0 + F_LAND + 20); ST(20);
        }
        asm volatile("s_waitcnt vmcnt(0)" : "+v"(ep) :: "memory");
        const u32 tg32 = (u32)((((u64)ep.y << 32) | ep.x) + TAG_SALT);
        if (cw == 1) {
            G2Ctx g2; make_g2(g2, p, q, 0, lane, lds0);
            cons_wait(lds0, 3);
#define W4(C) do { const char* sb = g2_wsrc<C>(g2); _Pragma("unroll") for (int i = 0; i < 4; ++i) { setm0(g2_loc<C>(g2) + i * 4096); \
                dmai<0>(g2.voffw[0], sb + i * 32 * 896); dmai<1024>(g2.voffw[1], sb + i * 32 * 896); dmai<2048>(g2.voffw[2], sb + i * 32 * 896); dmai<3072>(g2.voffw[3], sb + i * 32 * 896); } } while (0)
            W4(0); W4(1);
            if constexpr (LW > 2) { WAITV(16); W4(2); }
            if constexpr (LW > 3) { cons_wait(lds0, 4); WAITV(16); W4(3); }
            if constexpr (LW > 4) { WAITV(16); W4(4); }
#undef W4
            dv = gld2(0u, (const char*)p.ssmd + chan * 2);
            WAITV(1);
            lds_set(lds0 + F_W);
            asm volatile("s_waitcnt vmcnt(0)" : "+v"(dv) :: "memory");
            ST(7);
        } else {
            const char* kb2 = ws + WS_KVT + (size_t)(2 * q.b) * TP;
            u32x4 u[16];
            probe<2>((u32)(hi * TP + s31 * 256), kb2, tg32);
            _Pragma("clang loop unroll(disable)") for (int spin = 0; spin < SPIN_CAP; ++spin) {
#pragma unroll
                for (int c = 0; c < 4; ++c) {
                    const char* sb = kb2 + (c >> 1) * TP + (c & 1) * 4096;
                    u[4 * c] = gld16c<0>(lane * 16u, sb); u[4 * c + 1] = gld16c<1024>(lane * 16u, sb); u[4 * c + 2] = gld16c<2048>(lane * 16u, sb); u[4 * c + 3] = gld16c<3072>(lane * 16u, sb);
                }
                asm volatile("s_waitcnt vmcnt(0)" : "+v"(u[0]), "+v"(u[1]), "+v"(u[2]), "+v"(u[3]), "+v"(u[4]), "+v"(u[5]), "+v"(u[6]), "+v"(u[7]), "+v"(u[8]), "+v"(u[9]), "+v"(u[10]),
                             "+v"(u[11]), "+v"(u[12]), "+v"(u[13]), "+v"(u[14]), "+v"(u[15]) :: "memory");
                u32 bad = 0u;
#pragma unroll
                for (int i = 0; i < 16; ++i) bad = chk(u[i].w, tg32, chk(u[i].y, tg32, bad));
                if (!__any(bad != 0u)) break;
                __builtin_amdgcn_s_sleep(4);
            }
            ST(7);
            dv = gld2(0u, (const char*)p.ssmd + chan * 2);
            cons_wait(lds0, 5); cons_wait(lds0, 6);
            ST(14);
            const u32 uq_ = lds0 + L_CONV + TOEP_CH + lane * 8;
#pragma unroll
            for (int i = 0; i < 16; ++i) sts8(uq_ + (i >> 3) * CONV_CH + (i & 7) * 512, u32x2{u[i].x, u[i].z});
            sts8(uq_ + 4096, u32x2{0u, 0u}); sts8(uq_ + CONV_CH + 4096, u32x2{0u, 0u});
            asm volatile("s_waitcnt lgkmcnt(0)" ::: "memory");
            lds_add(lds0 + F_TOEP); lds_add(lds0 + F_TOEP + 4);
            asm volatile("s_waitcnt vmcnt(0)" : "+v"(dv) :: "memory");
        }
    } else {
        const int kw = cw - 2;
        if (kw == 0 && lane < 8) dma16(lane * 16u, (const char*)p.b2 + q.D0 * 2, lds0 + L_B2);
        ep = gld8c(0u, ws + WS_EPOCH);
        CL c; make_cl(c, p, q, 2 * kw, lane, lds0);
        l_dma<3>(c);
        lds_wait_ge(lds0 + F_KVS, 256, lds_peek(lds0 + F_KVS));
        ST(2);
        asm volatile("s_waitcnt vmcnt(16)" : "+v"(ep) :: "memory");
        const u32 tg32 = (u32)((((u64)ep.y << 32) | ep.x) + TAG_SALT);
        {
            const int grp = lane >> 4, qq = (lane & 15) >> 2, pp = lane & 3;
            const u32 a = lds0 + L_KVS + (16 * grp + 4 * pp + 3 - qq) * 144 + kw * 64;
            const u32 vo = (u32)((q.D0 + 32 * kw + (lane & 3)) * TP + (2044 - (int)q.T0 - 4 * (lane >> 2)) * 4);
            u32x2 tr[8];
#pragma unroll
            for (int i = 0; i < 8; ++i) tr[i] = ldtr(a + 8 * i);
            SB();
#pragma unroll
            for (int i = 0; i < 8; ++i) gst16c<0>(vo, ws + WS_KVT + i * 4 * TP, u32x4{tr[i].x, tg32, tr[i].y, tg32});
        }
        ST(20);
        WAITV(8);
        lds_add(lds0 + F_LAND + 12);
        ST(19);
#pragma unroll
        for (int i = 0; i < TW; ++i) { const int L = ((2 * i + kw) * 28 + q.bi) * 64 + lane; if (2 * i + kw < 7) touch((const char*)p.w2 + (u32)(12543 - L) * 128u, lds0 + L_SCR); }
        cons_wait(lds0, 2);
        l_dma<6>(c);
        ST(3);
        u32x4 sk[5];
        const u32x4 sksrd = mksrd((const char*)p.ssmk + (size_t)(2 * q.b + kw) * 4096, 4096);
#pragma unroll
        for (int r = 0; r < 5; ++r) sk[r] = bld16((u32)((lane - 1 + 63 * r) * 16), sksrd);
        { const char* xsl = (const char*)p.x + (size_t)q.T0 * 896 + (q.D0 + 32 * kw) * 2;
          xs[0] = gld16o<0>((u32)(lane * 896), xsl); xs[1] = gld16o<16>((u32)(lane * 896), xsl); xs[2] = gld16o<32>((u32)(lane * 896), xsl); xs[3] = gld16o<48>((u32)(lane * 896), xsl); }
        dv = gld2(0u, (const char*)p.ssmd + chan * 2);
        WAITV(10);
        lds_add(lds0 + F_LAND + 24);
        ST(21);
        if constexpr (KW) {
            G2Ctx ga_, gb_; make_g2(ga_, p, q, 2 * kw, lane, lds0); gb_ = ga_; gb_.w2b += 32 * 896; gb_.ldsg += 4096;
            cons_wait(lds0, 3); g2_issue_w<2>(ga_); g2_issue_w<2>(gb_);
            cons_wait(lds0, 4); g2_issue_w<3>(ga_); g2_issue_w<3>(gb_); g2_issue_w<4>(ga_); g2_issue_w<4>(gb_);
        }
        asm volatile("s_waitcnt vmcnt(0)" : "+v"(sk[0]), "+v"(sk[1]), "+v"(sk[2]), "+v"(sk[3]), "+v"(sk[4]), "+v"(xs[0]), "+v"(xs[1]), "+v"(xs[2]), "+v"(xs[3]), "+v"(dv) :: "memory");
        u32x4 tw[5][4];
#pragma unroll
        for (int r = 0; r < 5; ++r) {
            const u32x4 B = sk[r];
            u32x4 A;
#pragma unroll
            for (int j = 0; j < 4; ++j) A[j] = __builtin_amdgcn_update_dpp(0u, B[j], 0x138  , 0xf, 0xf, false);
            const u32 e0 = __builtin_amdgcn_alignbit(A.y, A.x, 16), e1 = __builtin_amdgcn_alignbit(A.z, A.y, 16), e2 = __builtin_amdgcn_alignbit(A.w, A.z, 16),
                      e3 = __builtin_amdgcn_alignbit(B.x, A.w, 16), e4 = __builtin_amdgcn_alignbit(B.y, B.x, 16);
            tw[r][0] = u32x4{e0, e1, e2, e3}; tw[r][1] = u32x4{A.y, A.z, A.w, B.x}; tw[r][2] = u32x4{e1, e2, e3, e4}; tw[r][3] = u32x4{A.z, A.w, B.x, B.y};
            PIN(tw[r][0]); PIN(tw[r][1]); PIN(tw[r][2]); PIN(tw[r][3]);
        }
        cons_wait(lds0, 5); cons_wait(lds0, 6);
        ST(5);
        const u32 tq = lds0 + L_CONV + kw * CONV_CH + lane * 16;
#pragma unroll
        for (int r = 0; r < 5; ++r) {
            if ((lane > 0 || r == 0) && (r < 4 || lane < 6)) {
#pragma unroll
                for (int c = 0; c < 4; ++c) sts16(tq + r * 1008 + c * TOEP_CP, tw[r][c]);
            }
        }
        asm volatile("s_waitcnt lgkmcnt(0)" ::: "memory");
        lds_add(lds0 + F_TOEP + kw * 4);
        WAITV(0);
        lds_set(lds0 + F_W + 4 + kw * 4);
    }
    const u64 epoch = ((u64)(u32)__builtin_amdgcn_readfirstlane(ep.y) << 32) | (u32)__builtin_amdgcn_readfirstlane(ep.x);
    const u64 tag = epoch + TAG_SALT;
    const u32 tlo = (u32)tag, thi = (u32)(tag >> 32);
    const u32 tbase = lds0 + L_CONV + chl * CONV_CH;
    const u32 ub = tbase + TOEP_CH;
    ST(6);
    lds_wait_ge(lds0 + F_TOEP + chl * 4, 128, lds_peek(lds0 + F_TOEP + chl * 4));
    ST(15);
    const int bb = lane & 15, gg = lane >> 4;
    const u32 aA = tbase + (bb & 3) * TOEP_CP + ((bb >> 2) + 2 * gg) * 8 + half * 256;
    const u32 aB = ub + (30 - 2 * bb + gg) * 16 + half * 256;
    f32x4 ac8[8] = {};
    if (!PRIOALL) __builtin_amdgcn_s_setprio(CPRIO);
    conv_body(aA, aB, ac8);
    ST(16);
    f32x4 acc[4];
    {
        const u32 xo = ub + U_SZ + lane * 16;
#pragma unroll
        for (int i = 0; i < 4; ++i) { const f32x4 give = half ? ac8[i] : ac8[4 + i]; acc[i] = half ? ac8[4 + i] : ac8[i]; sts16(xo + half * 4096 + i * 1024, __builtin_bit_cast(u32x4, give)); }
        asm volatile("s_waitcnt lgkmcnt(0)" ::: "memory");
        lds_set(lds0 + F_XCH + cw * 4);
        lds_wait(lds0 + F_XCH + (cw ^ 1) * 4);
        f32x4 got[4];
#pragma unroll
        for (int i = 0; i < 4; ++i) got[i] = __builtin_bit_cast(f32x4, lds16(xo + (half ^ 1) * 4096 + i * 1024));
        SB();
#pragma unroll
        for (int i = 0; i < 4; ++i) acc[i] = f32x4{add1(acc[i][0], got[i][0]), add1(acc[i][1], got[i][1]), add1(acc[i][2], got[i][2]), add1(acc[i][3], got[i][3])};
    }
    ST(8);
    {
        const float dd = bflo(dv);
        char* yb = ws + WS_YT + (size_t)chan * TP;
        u32x2 uq[4];
#pragma unroll
        for (int i = 0; i < 4; ++i) uq[i] = lds8(ub + (2044 - (256 * (4 * half + i) + 16 * bb + 4 * gg)) * 2);
        SB();
#pragma unroll
        for (int i = 0; i < 4; ++i) {
            const int qt = 4 * half + i;
            const int t = 256 * qt + 16 * bb + 4 * gg;
            const u32x2 uu = uq[i];
            const u32 c01 = pk2(acc[i][0], acc[i][1]), c23 = pk2(acc[i][2], acc[i][3]);
            const u32 d01 = pk2(mul1(bfhi(uu.y), dd), mul1(bflo(uu.y), dd)), d23 = pk2(mul1(bfhi(uu.x), dd), mul1(bflo(uu.x), dd));
            float y0 = add1(bflo(d01), bflo(c01)), y1 = add1(bfhi(d01), bfhi(c01));
            float y2 = add1(bflo(d23), bflo(c23)), y3 = add1(bfhi(d23), bfhi(c23));
            gst16c<0>((u32)(t * 4), yb, u32x4{pk2(y0, y1), tlo, pk2(y2, y3), tlo});
        }
        lds_arrive_last(lds0 + F_YCNT, 4);
    }
    if (cw < 2) {
        G2Ctx g2; make_g2(g2, p, q, 2 * cw, lane, lds0);
        const u32 gp = lds0 + F_GP + cw * 8, wf_ = lds0 + F_WF + cw * 8;
#define SERVE(C) do { if constexpr ((C) >= WC0) { const char* sb = g2_wsrc<C>(g2); \
            const u32x2 pr = *(volatile LAS u32x2*)gp; \
            if ((C) > WC0 && (u32)__builtin_amdgcn_readfirstlane(pr.x < pr.y ? pr.x : pr.y) < (C) - 4) { WAITV(0); sts8(wf_, u32x2{(C), (C)}); } \
            _Pragma("unroll") for (int i = 0; i < 2; ++i) { lds_wait_ge(gp + i * 4, (C) - 4, i ? pr.y : pr.x); setm0(g2_loc<C>(g2) + i * 4096); \
                dmai<0>(g2.voffw[0], sb + i * 32 * 896); dmai<1024>(g2.voffw[1], sb + i * 32 * 896); dmai<2048>(g2.voffw[2], sb + i * 32 * 896); dmai<3072>(g2.voffw[3], sb + i * 32 * 896); } \
            if constexpr ((C) > WC0 + 1) { WAITV(16); sts8(wf_, u32x2{(C) - 1, (C) - 1}); } } } while (0)
        SERVE(5); SERVE(6); SERVE(7); SERVE(8); SERVE(9); SERVE(10); SERVE(11); SERVE(12); SERVE(13);
#undef SERVE
        WAITV(8); sts8(wf_, u32x2{13u, 13u});
        WAITV(0); sts8(wf_, u32x2{14u, 14u});
        ST(9);
    } else {
        const int kw = cw - 2;
        lds_wait_ge(lds0 + F_YCNT, 4, lds_peek(lds0 + F_YCNT)); lds_wait_ge(lds0 + F_HCNT, 4, lds_peek(lds0 + F_HCNT));
        const u32 slab = slab_base(lds0, 2 * kw) + lane * 64;
        float xn[32];
        {
            const u32 rf = *(const LAS u32*)(lds0 + L_ROWF + lane * 4);
            const float nm = -bflo(rf); float ri = __builtin_amdgcn_rcpf(bfhi(rf));
            asm volatile("s_nop 1" : "+v"(ri));
#pragma unroll
            for (int j = 0; j < 8; ++j) {
                const u32 lo = xs[j >> 1][(j & 1) * 2], hi_ = xs[j >> 1][(j & 1) * 2 + 1];
                const f32x4 v = {bflo(lo), bfhi(lo), bflo(hi_), bfhi(hi_)};
                sts16(slab + (j >> 2) * 8192 + 4096 + (j & 3) * 16, __builtin_bit_cast(u32x4, v));
#pragma unroll
                for (int e = 0; e < 4; e += 2) {
                    const u32 dx = pk2(add1(v[e], nm), add1(v[e + 1], nm)), xr = pk2(mul1(bflo(dx), ri), mul1(bfhi(dx), ri));
                    xn[4 * j + e] = bflo(xr); xn[4 * j + e + 1] = bfhi(xr);
                }
            }
            if (kw == 0) *(LAS float*)(lds0 + L_R2 + lane * 4) = ri * ri;
            else *(LAS float*)(lds0 + L_B2F + lane * 4) = bflo(*(const LAS u16*)(lds0 + L_B2 + lane * 2));
        }
        const char* yb = ws + WS_YT + (size_t)(q.D0 + 32 * kw) * TP + q.T0 * 4;
        const u32 vo = (u32)(gg * TP + bb * 16);
        u32x4 y[8];
        probe<YSLP>((u32)(s31 * TP), yb, tlo);
        _Pragma("clang loop unroll(disable)") for (int spin = 0; spin < SPIN_CAP; ++spin) {
#pragma unroll
            for (int i = 0; i < 8; ++i) y[i] = gld16c<0>(vo, yb + i * 4 * TP);
            asm volatile("s_waitcnt vmcnt(0)" : "+v"(y[0]), "+v"(y[1]), "+v"(y[2]), "+v"(y[3]), "+v"(y[4]), "+v"(y[5]), "+v"(y[6]), "+v"(y[7]) :: "memory");
            u32 bad = 0u;
#pragma unroll
            for (int i = 0; i < 8; ++i) bad = chk(y[i].w, tlo, chk(y[i].y, tlo, bad));
            if (!__any(bad != 0u)) break;
            __builtin_amdgcn_s_sleep(4);
        }
        ST(9);
        const u32 ys = lds0 + L_CONV + kw * CONV_CH + TOEP_CH + U_SZ;
#pragma unroll
        for (int i = 0; i < 8; ++i) sts8(ys + (4 * i + gg) * 144 + bb * 8, u32x2{y[i].x, y[i].z});
        const int qq = (lane & 15) >> 2, pp = lane & 3;
        const u32 a = ys + qq * 144 + gg * 32 + pp * 8;
        u32x2 t[8];
#pragma unroll
        for (int j = 0; j < 8; ++j) t[j] = ldtr(a + j * 4 * 144);
        SB();
#pragma unroll
        for (int j = 0; j < 8; ++j)
        {
            const u32 s01 = pk2(mul1(bflo(t[j].x), xn[4 * j]), mul1(bfhi(t[j].x), xn[4 * j + 1])), s23 = pk2(mul1(bflo(t[j].y), xn[4 * j + 2]), mul1(bfhi(t[j].y), xn[4 * j + 3]));
            sts16(slab + (j >> 2) * 8192 + (j & 3) * 16, u32x4{s01 << 16, s01 & 0xffff0000u, s23 << 16, s23 & 0xffff0000u});
        }
        if (q.b == 0 && kw == 1) {
            _Pragma("clang loop unroll(disable)") for (int spin = 0; spin < SPIN_CAP; ++spin) {
                u32x4 f0 = gld16c<0>(lane * 16u, ws + WS_ALL), f1 = gld16c<1024>((lane < 48 ? lane : 0) * 16u, ws + WS_ALL);
                asm volatile("s_waitcnt vmcnt(0)" : "+v"(f0), "+v"(f1) :: "memory");
                bool ok = f0.x == tlo && f0.y == thi && f0.z == tlo && f0.w == thi && f1.x == tlo && f1.y == thi && f1.z == tlo && f1.w == thi;
                if (__all(ok)) break;
                __builtin_amdgcn_s_sleep(SLP);
            }
            if (lane == 0) { const u64 e1 = epoch + 1; gst8c<0>(0u, ws + WS_EPOCH, u32x2{(u32)e1, (u32)(e1 >> 32)}); }
        }
    }
    __builtin_amdgcn_s_setprio(0);
    BARL(); ST(10);
    final_epilogue(p, q, lds0);
    STAMP_OUT(cw + 4);
}

DI float dpp_xor_sum8(float v) {
    v += __uint_as_float(__builtin_amdgcn_mov_dpp(__float_as_uint(v), 0xB1, 0xf, 0xf, true));
    v += __uint_as_float(__builtin_amdgcn_mov_dpp(__float_as_uint(v), 0x4E, 0xf, 0xf, true));
    v += __uint_as_float(__builtin_amdgcn_mov_dpp(__float_as_uint(v), 0x141, 0xf, 0xf, true));
    return v;
}
DI void body_help(const Params& p, const int wave) {
    const u32 lds0 = (u32)(size_t)(LAS char*)smem;
    const int lane = threadIdx.x & 63, r8 = lane >> 3, c8 = lane & 7;
    const int xcd = blockIdx.x & 7, j = blockIdx.x >> 3;
    char* ws = p.ws;
    u32x2 ep = gld8c(0u, ws + WS_EPOCH);
    u32x4 d[14];
    const int ctile = (xcd >> 1) * 4 + j;
    const bool cols = wave >= 4;
    if (!cols) {
        const char* xb = (const char*)p.x + (size_t)(256 * xcd + 64 * j + 16 * wave) * 896;
#pragma unroll
        for (int i = 0; i < 14; ++i) d[i] = gld16((u32)((r8 + 8 * (i / 7)) * 896 + (i % 7) * 128 + c8 * 16), xb);
    } else {
        const char* wb = (const char*)p.w1 + (size_t)(112 * (wave - 4)) * 3584 + ((xcd & 1) * 14 + (ctile < 14 ? ctile : 0)) * 128;
#pragma unroll
        for (int i = 0; i < 14; ++i) d[i] = gld16((u32)((r8 + 8 * i) * 3584 + c8 * 16), wb);
    }
    setm0(lds0 + 16384 + wave * 4096);
#pragma unroll
    for (int c = 0; c < WEARLY; ++c) {
        int cc = c + (2 * xcd) % 14; if (cc >= 14) cc -= 14;
        const char* sb = (const char*)p.w2 + (size_t)cc * 114688 + (j * 8 + wave) * 3584;
        if (lane < 32) dmai<3072>(lane * 16u, sb);
        dmai<0>(lane * 16u, sb); dmai<1024>(lane * 16u, sb); dmai<2048>(lane * 16u, sb);
    }
    asm volatile("s_waitcnt vmcnt(%15)" : "+v"(ep), "+v"(d[0]), "+v"(d[1]), "+v"(d[2]), "+v"(d[3]), "+v"(d[4]), "+v"(d[5]), "+v"(d[6]), "+v"(d[7]), "+v"(d[8]), "+v"(d[9]),
                 "+v"(d[10]), "+v"(d[11]), "+v"(d[12]), "+v"(d[13]) : "i"(4 * WEARLY) : "memory");
    const u32 tlo = (u32)((((u64)ep.y << 32) | ep.x) + TAG_SALT);
    if (!cols) {
#pragma unroll
        for (int h = 0; h < 2; ++h) {
            float s0 = 0.f, s1 = 0.f, q0 = 0.f, q1 = 0.f;
#pragma unroll
            for (int i = 0; i < 7; ++i)
#pragma unroll
                for (int e = 0; e < 4; e += 2) {
                    s0 = dot2(d[7 * h + i][e], 0x3f803f80u, s0); q0 = dot2(d[7 * h + i][e], d[7 * h + i][e], q0);
                    s1 = dot2(d[7 * h + i][e + 1], 0x3f803f80u, s1); q1 = dot2(d[7 * h + i][e + 1], d[7 * h + i][e + 1], q1);
                }
            const float mean = dpp_xor_sum8(s0 + s1) * (1.f / 448.f);
            const float var = fmaxf(dpp_xor_sum8(q0 + q1) * (1.f / 448.f) - mean * mean, 0.f);
            const float sd = __builtin_amdgcn_sqrtf(rbf(rbf(var) + 1e-5f));
            if (c8 == 0) gst8c<0>((u32)(WS_RG + (256 * xcd + 64 * j + 16 * wave + 8 * h + r8) * 8), ws, u32x2{pk2(mean, sd), tlo});
        }
    } else {
        float a[8] = {};
        u32 klo = 0x00003f80u, khi = 0x3f800000u; PIN(klo); PIN(khi);
#pragma unroll
        for (int i = 0; i < 14; ++i)
#pragma unroll
            for (int e = 0; e < 4; ++e) { a[2 * e] = dot2(d[i][e], klo, a[2 * e]); a[2 * e + 1] = dot2(d[i][e], khi, a[2 * e + 1]); }
        const u32 o = lds0 + ((wave - 4) * 8 + r8) * 272 + c8 * 32;
        sts16(o, __builtin_bit_cast(u32x4, f32x4{a[0], a[1], a[2], a[3]})); sts16(o + 16, __builtin_bit_cast(u32x4, f32x4{a[4], a[5], a[6], a[7]}));
    }
    BARL();
    if (wave == 4 && ctile < 14) {
        float t0 = 0.f, t1 = 0.f;
#pragma unroll
        for (int i = 0; i < 32; i += 2) { t0 += *(const LAS float*)(lds0 + i * 272 + lane * 4); t1 += *(const LAS float*)(lds0 + (i + 1) * 272 + lane * 4); }
        gst8c<0>((u32)(WS_CG + (((xcd & 1) * 14 + ctile) * 64 + lane) * 8), ws, u32x2{pk2(t0 + t1, (t0 + t1) - rbf(t0 + t1)), tlo});
    }
    BARL();
    {
        const int crot = (2 * xcd) % 14;
        setm0(lds0 + 16384 + wave * 4096);
        for (int c = WEARLY; c < 14; ++c) {
            int cc = c + crot; if (cc >= 14) cc -= 14;
            const char* sb = (const char*)p.w2 + (size_t)cc * 114688 + (j * 8 + wave) * 3584;
            if (lane < 32) dmai<3072>(lane * 16u, sb);
            dmai<0>(lane * 16u, sb); dmai<1024>(lane * 16u, sb); dmai<2048>(lane * 16u, sb);
            WAITV(WDEPTH);
        }
        WAITV(0);
    }
}

__global__ __launch_bounds__(512, 2) void fused_kernel(Params p) {
    const int wave = __builtin_amdgcn_readfirstlane(threadIdx.x >> 6);
    if (blockIdx.x < NHELP) { body_help(p, wave); return; }
    if (threadIdx.x < 32) *(LAS u32*)((u32)(size_t)(LAS char*)smem + L_FLAG + threadIdx.x * 4) = 0u;
    else if (threadIdx.x < 72) *(LAS u32*)((u32)(size_t)(LAS char*)smem + F_CONS + ((threadIdx.x - 32) >> 2) * 144 + (threadIdx.x & 3) * 4) = 0u;
    BARL();
    if (wave < 4) body_g(p, wave); else body_c(p, wave - 4);
}

extern "C" void kernel_launch(void* const* d_in, const int* in_sizes, int n_in,
                              void* d_out, int out_size, void* d_ws, size_t ws_size,
                              hipStream_t stream) {
    Params p;
    p.x = (const u16*)d_in[0]; p.k = (const u16*)d_in[1]; p.v = (const u16*)d_in[2]; p.shk = (const u16*)d_in[3];
    p.ssmk = (const u16*)d_in[4]; p.ssmd = (const u16*)d_in[5]; p.w1 = (const u16*)d_in[7]; p.b1 = (const u16*)d_in[8];
    p.w2 = (const u16*)d_in[9]; p.b2 = (const u16*)d_in[10];
    p.out = (u16*)d_out; p.ws = (char*)d_ws;
    (void)hipFuncSetAttribute((const void*)fused_kernel, hipFuncAttributeMaxDynamicSharedMemorySize, LDS_BYTES);
    fused_kernel<<<NWG + NHELP, 512, LDS_BYTES, stream>>>(p);
}
```

```cpp
#define NOWARM
#include <hip/hip_runtime.h>
#include <stdint.h>
#include <type_traits>

typedef unsigned short u16;
typedef unsigned int u32;
typedef unsigned long long u64;
typedef u32 u32x4 __attribute__((ext_vector_type(4)));
typedef u32 u32x2 __attribute__((ext_vector_type(2)));
typedef float f32x4 __attribute__((ext_vector_type(4)));
typedef float f32x2 __attribute__((ext_vector_type(2)));
typedef float f32x16 __attribute__((ext_vector_type(16)));
typedef __bf16 bf16x8 __attribute__((ext_vector_type(8)));
typedef __bf16 bf16x2 __attribute__((ext_vector_type(2)));
typedef short s16x4 __attribute__((ext_vector_type(4)));
#define LAS __attribute__((address_space(3)))
#define GAS __attribute__((address_space(1)))
#define DI __device__ __forceinline__

constexpr int S_ = 2048, D_ = 448, F_ = 1792;
constexpr int NHELP = 32, NWG = 224;
constexpr u32 MB = 256;
constexpr u32 WS_EPOCH = 0, WS_ALL = 2048, WS_KVF = 4096, WS_HF = WS_KVF + 65536, WS_YF = WS_HF + 65536, WS_RG = WS_YF + 65536, WS_CG = WS_RG + 16384;
constexpr u32 TP = 4096 + 128;
constexpr u32 WS_H = 1u << 20, WS_KVT = 9u << 20, WS_YT = 11u << 20;
constexpr u32 STAMP_OFF = 16u << 20;
constexpr int NSTAMP = 22;
constexpr u32 L_PIECE = 16384, L_W = 65536, L_LOC0 = 131072, L_MISC = 147456, LDS_BYTES = 163840;
constexpr u32 KVRAW_SZ = 5632, L_KVS = L_LOC0 + 4 * KVRAW_SZ, L_B2 = L_KVS + 9216, L_FLAG = L_B2 + 128, L_ROWF = L_FLAG + 128, L_B2F = L_ROWF + 256, L_R2 = L_B2F + 256;
constexpr u32 L_SCR = L_LOC0 + 2 * KVRAW_SZ + 9728 + 64;
static_assert(L_R2 + 256 <= LDS_BYTES, "lds");
constexpr u32 F_TOEP = L_FLAG, F_HCNT = L_FLAG + 16, F_HRDY = L_FLAG + 20, F_KVRDY = L_FLAG + 24, F_YCNT = L_FLAG + 28, F_YRDY = L_FLAG + 32, F_S0 = L_FLAG + 36, F_KVCNT = L_FLAG + 40, F_W = L_FLAG + 48, F_LAND = L_FLAG + 64, F_XCH = L_FLAG + 96, F_KVS = L_FLAG + 124, F_CONS = L_KVS + 128;
constexpr u32 L_CONV = 16384, CONV_CH = 65536, TOEP_CP = 4160, TOEP_CH = 4 * TOEP_CP, U_SZ = 4608, YS_SZ = 2304;
constexpr u64 TAG_SALT = 0x9E3779B97F4A7C15ull;
constexpr int SPIN_CAP = 1 << 15;
#define SLP 2

#define ST(i) do {} while (0)
#define STP nullptr

DI void dma16(u32 voff, const void* sbase, u32 lds) {
    asm volatile("s_mov_b32 m0, %2\n\ts_nop 0\n\tglobal_load_lds_dwordx4 %0, %1" :: "v"(voff), "s"(sbase), "s"(lds) : "memory");
}
DI void setm0(u32 lds) { asm volatile("s_mov_b32 m0, %0\n\ts_nop 0" :: "s"(lds) : "memory"); }
template <int OFF> DI void dmai(u32 voff, const void* sbase) { asm volatile("global_load_lds_dwordx4 %0, %1 offset:%2" :: "v"(voff), "s"(sbase), "i"(OFF) : "memory"); }
DI void bdma16(u32 voff, u32x4 srd, u32 lds) {
    asm volatile("s_mov_b32 m0, %2\n\ts_nop 0\n\tbuffer_load_dwordx4 %0, %1, 0 offen lds" :: "v"(voff), "s"(srd), "s"(lds) : "memory");
}
#define SB() __builtin_amdgcn_sched_barrier(0)
template <int OFF> DI u32x2 alds8(u32 a) { u32x2 r; asm volatile("ds_read_b64 %0, %1 offset:%2" : "=v"(r) : "v"(a), "i"(OFF)); return r; }
template <int OFF> DI u32x4 alds16(u32 a) { u32x4 r; asm volatile("ds_read_b128 %0, %1 offset:%2" : "=v"(r) : "v"(a), "i"(OFF)); return r; }
DI u32x4 gld16(u32 voff, const void* sbase) { u32x4 r; asm volatile("global_load_dwordx4 %0, %1, %2" : "=v"(r) : "v"(voff), "s"(sbase) : "memory"); return r; }
template <int OFF> DI u32x4 gld16c(u32 voff, const void* sbase) { u32x4 r; asm volatile("global_load_dwordx4 %0, %1, %2 offset:%3 sc1" : "=v"(r) : "v"(voff), "s"(sbase), "i"(OFF) : "memory"); return r; }
DI u32x2 gld8c(u32 voff, const void* sbase) { u32x2 r; asm volatile("global_load_dwordx2 %0, %1, %2 sc1" : "=v"(r) : "v"(voff), "s"(sbase) : "memory"); return r; }
DI void touch(const void* addr, u32 lds) { asm volatile("s_mov_b32 m0, %1\n\ts_nop 0\n\tglobal_load_lds_dword %0, off" :: "v"(addr), "s"(lds) : "memory"); }
DI u32 gld2(u32 voff, const void* sbase) { u32 r; asm volatile("global_load_ushort %0, %1, %2" : "=v"(r) : "v"(voff), "s"(sbase) : "memory"); return r; }
DI u32x4 bld16(u32 voff, u32x4 srd) { u32x4 r; asm volatile("buffer_load_dwordx4 %0, %1, %2, 0 offen" : "=v"(r) : "v"(voff), "s"(srd) : "memory"); return r; }
template <int OFF> DI u32x4 gld16o(u32 voff, const void* sbase) { u32x4 r; asm volatile("global_load_dwordx4 %0, %1, %2 offset:%3" : "=v"(r) : "v"(voff), "s"(sbase), "i"(OFF) : "memory"); return r; }
template <int OFF> DI void gst16c(u32 voff, void* sbase, u32x4 v) { asm volatile("global_store_dwordx4 %0, %1, %2 offset:%3 sc1\n\ts_nop 1" :: "v"(voff), "v"(v), "s"(sbase), "i"(OFF) : "memory"); }
template <int OFF> DI void gst8c(u32 voff, void* sbase, u32x2 v) { asm volatile("global_store_dwordx2 %0, %1, %2 offset:%3 sc1" :: "v"(voff), "v"(v), "s"(sbase), "i"(OFF) : "memory"); }
#define WAITV(n) asm volatile("s_waitcnt vmcnt(%0)" :: "i"(n) : "memory")
#define WAITBAR(n) asm volatile("s_waitcnt vmcnt(%0) lgkmcnt(0)\n\ts_barrier" :: "i"(n) : "memory")
#define BARL() asm volatile("s_waitcnt lgkmcnt(0)\n\ts_barrier" ::: "memory")
#define PIN(x) asm volatile("" : "+v"(x))

DI u32x4 mksrd(const void* p, u32 bytes) { u64 a = (u64)p; return u32x4{(u32)a, (u32)(a >> 32) & 0xffffu, bytes, 0x00020000u}; }
DI float bflo(u32 x) { return __uint_as_float(x << 16); }
DI float bfhi(u32 x) { return __uint_as_float(x & 0xffff0000u); }
DI u32 pk2(float a, float b) { bf16x2 v = {(__bf16)a, (__bf16)b}; return __builtin_bit_cast(u32, v); }
DI float rbf(float a) { return bflo(pk2(a, a)); }
DI float relu(float a) { int i = __float_as_int(a); return __int_as_float(i > 0 ? i : 0); }
DI float fma1(float a, float b, float c) { float r; asm("v_fma_f32 %0, %1, %2, %3" : "=v"(r) : "v"(a), "v"(b), "v"(c)); return r; }
DI float mul1(float a, float b) { float r; asm("v_mul_f32 %0, %1, %2" : "=v"(r) : "v"(a), "v"(b)); return r; }
DI float add1(float a, float b) { float r; asm("v_add_f32 %0, %1, %2" : "=v"(r) : "v"(a), "v"(b)); return r; }
DI float dot2(u32 a, u32 b, float c) { return __builtin_amdgcn_fdot2_f32_bf16(__builtin_bit_cast(bf16x2, a), __builtin_bit_cast(bf16x2, b), c, false); }
DI u32x4 lds16(u32 a) { return *(const LAS u32x4*)a; }
DI u32x2 lds8(u32 a) { return *(const LAS u32x2*)a; }
DI void sts16(u32 a, u32x4 v) { *(LAS u32x4*)a = v; }
DI void sts8(u32 a, u32x2 v) { *(LAS u32x2*)a = v; }
DI u32x2 ldtr(u32 a) { return __builtin_bit_cast(u32x2, __builtin_amdgcn_ds_read_tr16_b64_v4i16((LAS s16x4*)a)); }
DI u32x4 cat(u32x2 a, u32x2 b) { return u32x4{a.x, a.y, b.x, b.y}; }
DI f32x16 mfma32(u32x4 a, u32x4 b, f32x16 c) { return __builtin_amdgcn_mfma_f32_32x32x16_bf16(__builtin_bit_cast(bf16x8, a), __builtin_bit_cast(bf16x8, b), c, 0, 0, 0); }
DI f32x4 mfma16(u32x4 a, u32x4 b, f32x4 c) { return __builtin_amdgcn_mfma_f32_16x16x32_bf16(__builtin_bit_cast(bf16x8, a), __builtin_bit_cast(bf16x8, b), c, 0, 0, 0); }
DI float xhalf_sum(float v) { auto r = __builtin_amdgcn_permlane32_swap(__float_as_uint(v), __float_as_uint(v), false, false); return __uint_as_float(r[0]) + __uint_as_float(r[1]); }

struct Params {
    const u16 *x, *k, *v, *shk, *ssmk, *ssmd, *w1, *b1, *w2, *b2;
    u16* out; char* ws;
};

DI void poll8(u32 voff, const void* sbase, bool need, u32 tlo, u32 thi) {
    _Pragma("clang loop unroll(disable)") for (int spin = 0; spin < SPIN_CAP; ++spin) {
        u32x2 f = gld8c(voff, sbase);
        asm volatile("s_waitcnt vmcnt(0)" : "+v"(f) :: "memory");
        bool ok = !need || (f.x == tlo && f.y == thi);
        if (__all(ok)) break;
        __builtin_amdgcn_s_sleep(SLP);
    }
}
DI void poll16(u32 voff, const void* sbase, u32 tlo, u32 thi) {
    _Pragma("clang loop unroll(disable)") for (int spin = 0; spin < SPIN_CAP; ++spin) {
        u32x4 f = gld16c<0>(voff, sbase);
        asm volatile("s_waitcnt vmcnt(0)" : "+v"(f) :: "memory");
        bool ok = f.x == tlo && f.y == thi && f.z == tlo && f.w == thi;
        if (__all(ok)) break;
        __builtin_amdgcn_s_sleep(SLP);
    }
}

DI bool lds_arrive_last(u32 addr, u32 n) {
    u32 old = 0;
    if ((threadIdx.x & 63) == 0) old = __hip_atomic_fetch_add((LAS u32*)addr, 1u, __ATOMIC_RELAXED, __HIP_MEMORY_SCOPE_WORKGROUP);
    return (u32)__builtin_amdgcn_readfirstlane(old) == n - 1;
}
DI void lds_wait(u32 addr) {
    _Pragma("clang loop unroll(disable)") for (int spin = 0; spin < (1 << 22); ++spin) {
        if (__builtin_amdgcn_readfirstlane(*(volatile LAS u32*)addr)) break;
        __builtin_amdgcn_s_sleep(1);
    }
    asm volatile("" ::: "memory");
}
DI void lds_add(u32 addr) { asm volatile("" ::: "memory"); __hip_atomic_fetch_add((LAS u32*)addr, 1u, __ATOMIC_RELAXED, __HIP_MEMORY_SCOPE_WORKGROUP); asm volatile("" ::: "memory"); }
DI u32 lds_peek(u32 addr) { return *(volatile LAS u32*)addr; }
DI void lds_wait_ge(u32 addr, u32 n, u32 first) {
    u32 v = first;
    _Pragma("clang loop unroll(disable)") for (int spin = 0; spin < (1 << 22) && (u32)__builtin_amdgcn_readfirstlane(v) < n; ++spin) { __builtin_amdgcn_s_sleep(1); v = lds_peek(addr); }
    asm volatile("" ::: "memory");
}
template <int t> DI void cons_set(u32 lds0, int g) { asm volatile("s_waitcnt lgkmcnt(0)\n\tds_write_b32 %0, %1 offset:%2" :: "v"(lds0 + 131072 + g * 4), "v"(1u), "i"(F_CONS + t * 144 - 131072) : "memory"); }
DI void cons_wait(u32 lds0, int t) {
    _Pragma("clang loop unroll(disable)") for (int spin = 0; spin < (1 << 22); ++spin) {
        const u32x4 v = *(volatile LAS u32x4*)(lds0 + F_CONS + t * 144);
        if (__builtin_amdgcn_readfirstlane(v.x & v.y & v.z & v.w)) break;
        __builtin_amdgcn_s_sleep(1);
    }
    asm volatile("" ::: "memory");
}
DI void lds_set(u32 addr) { asm volatile("" ::: "memory"); if ((threadIdx.x & 63) == 0) *(volatile LAS u32*)addr = 1u; }
constexpr u32 loc_of(int c) { return (c % 5) == 0 ? L_LOC0 : (c % 5) == 1 ? 3 * L_PIECE : (c % 5) == 2 ? L_W + 3 * L_PIECE : (c % 5) == 3 ? 0u : L_W; }

#define HDV 3
constexpr int HD = HDV, HS = HD + 1;
constexpr int RD = 4, WS_ = RD + 1;
constexpr int g2_younger(int c) {
    const int wneed = c + RD;
    bool sh = false, sw = false; int ah = 0, aw = 0;
    const bool wdone = wneed < 5 + RD || wneed >= 14;
    for (int i = 0; i < HD; ++i) { if (sh) ah += 4; if (i == c) sh = true; }
    for (int i = 0; i < c; ++i) {
        if (i + HD < 14) { if (sh) ah += 4; if (sw) aw += 4; if (i + HD == c) sh = true; }
        if (i >= 1 && i + RD + 4 < 14) { if (sh) ah += 4; if (sw) aw += 4; if (i + RD + 4 == wneed) sw = true; }
    }
    if (wdone) return ah;
    return ah < aw ? ah : aw;
}

struct G2Ctx { const char* w2b; const char* hb; int crot; u32 voffw[4], ldsg, vlane, ta[3][2]; };
template <int C> DI const char* g2_wsrc(const G2Ctx& x) { int c = C + x.crot; if (c >= 14) c -= 14; return x.w2b + (size_t)c * (128 * 896); }
template <int C> DI u32 g2_loc(const G2Ctx& x) { return loc_of(C) + x.ldsg; }
template <int C, int N> DI void g2_w1(const G2Ctx& x, const char* sb) { if constexpr (N == 0) setm0(g2_loc<C>(x)); dmai<N * 1024>(x.voffw[N], sb); }
template <int C> DI void g2_issue_w(const G2Ctx& x) { const char* sb = g2_wsrc<C>(x); g2_w1<C, 0>(x, sb); g2_w1<C, 1>(x, sb); g2_w1<C, 2>(x, sb); g2_w1<C, 3>(x, sb); }
template <int C> DI const char* g2_hsrc(const G2Ctx& x) { int c = C + x.crot; if (c >= 14) c -= 14; return x.hb + c * 8192; }
template <int N> DI u32x4 g2_h1(const G2Ctx& x, const char* sb) { return gld16c<(N & 1) * 1024>(x.vlane + (N >> 1) * 112 * 1024, sb); }
template <int C> DI void g2_issue_h(const G2Ctx& x, u32x4 (&hf)[HS][4]) {
    const char* sb = g2_hsrc<C>(x);
    hf[C % HS][0] = g2_h1<0>(x, sb); hf[C % HS][1] = g2_h1<1>(x, sb); hf[C % HS][2] = g2_h1<2>(x, sb); hf[C % HS][3] = g2_h1<3>(x, sb);
}
template <int C> DI void g2_read(const G2Ctx& x, u32x4 (&wf)[WS_][4]) {
    const u32 a0 = x.ta[loc_of(C) >> 16][0], a1 = x.ta[loc_of(C) >> 16][1];
    constexpr u32 lo = loc_of(C) & 0xffffu;
#pragma unroll
    for (int ks = 0; ks < 2; ++ks) {
        wf[C % WS_][ks * 2] = cat(ldtr(a0 + lo + ks * 2048), ldtr(a0 + lo + ks * 2048 + 1024));
        wf[C % WS_][ks * 2 + 1] = cat(ldtr(a1 + lo + ks * 2048), ldtr(a1 + lo + ks * 2048 + 1024));
    }
}
template <int C, int I> DI void g2_rd(const G2Ctx& x, u32x4 (&wf)[WS_][4]) {
    if constexpr (C < 14) {
        const u32 a = x.ta[loc_of(C) >> 16][I & 1]; constexpr u32 lo = (loc_of(C) & 0xffffu) + (I >> 1) * 2048;
        wf[C % WS_][I] = cat(ldtr(a + lo), ldtr(a + lo + 1024));
    }
}
template <int C> DI void g2_step(const G2Ctx& x, u32x4 (&hf)[HS][4], f32x16 (&acc)[2][2], u32x4 (&wf)[WS_][4], u64* stamps) {
    asm volatile("s_waitcnt vmcnt(%4) lgkmcnt(0)" : "+v"(hf[C % HS][0]), "+v"(hf[C % HS][1]), "+v"(hf[C % HS][2]), "+v"(hf[C % HS][3]) : "i"(g2_younger(C)) : "memory");
    if constexpr (C % 3 == 0) ST(14 + C / 3);
    SB();
    constexpr bool DH = C + HD < 14, DW = C >= 1 && C + RD + 4 < 14;
    const char* hs = g2_hsrc<(DH ? C + HD : 0)>(x);
    const char* wsrc = g2_wsrc<(DW ? C + RD + 4 : 0)>(x);
#define G2M(i) acc[((i) >> 1) & 1][(i) & 1] = mfma32(wf[C % WS_][((i) >> 2) * 2 + (((i) >> 1) & 1)], hf[C % HS][((i) & 1) * 2 + ((i) >> 2)], acc[((i) >> 1) & 1][(i) & 1])
    G2M(0); g2_rd<C + RD, 0>(x, wf); if constexpr (DH) hf[(C + HD) % HS][0] = g2_h1<0>(x, hs); SB();
    G2M(1); g2_rd<C + RD, 1>(x, wf); if constexpr (DH) hf[(C + HD) % HS][1] = g2_h1<1>(x, hs); SB();
    G2M(2); g2_rd<C + RD, 2>(x, wf); if constexpr (DH) hf[(C + HD) % HS][2] = g2_h1<2>(x, hs); SB();
    G2M(3); g2_rd<C + RD, 3>(x, wf); if constexpr (DH) hf[(C + HD) % HS][3] = g2_h1<3>(x, hs); SB();
    G2M(4); if constexpr (DW) g2_w1<C + RD + 4, 0>(x, wsrc); SB();
    G2M(5); if constexpr (DW) g2_w1<C + RD + 4, 1>(x, wsrc); SB();
    G2M(6); if constexpr (DW) g2_w1<C + RD + 4, 2>(x, wsrc); SB();
    G2M(7); if constexpr (DW) g2_w1<C + RD + 4, 3>(x, wsrc); SB();
#undef G2M
    if constexpr (C + 1 < 14) g2_step<C + 1>(x, hf, acc, wf, stamps);
}

struct CF { u32x2 a0[8], a1[8]; u32x4 b[8]; };
template <int S, int R> DI void cv_ld(CF& f, u32 aA, u32 aB) {
    f.a0[R] = alds8<R * 512 + S * 64>(aA); f.a1[R] = alds8<R * 512 + S * 64 + 8>(aA);
    f.b[R] = alds16<(224 - 32 * R + 4 * S) * 16>(aB);
}
DI void cv_wait(CF& f) {
    asm volatile("s_waitcnt lgkmcnt(0)" : "+v"(f.a0[0]), "+v"(f.a0[1]), "+v"(f.a0[2]), "+v"(f.a0[3]), "+v"(f.a0[4]), "+v"(f.a0[5]), "+v"(f.a0[6]), "+v"(f.a0[7]),
                 "+v"(f.a1[0]), "+v"(f.a1[1]), "+v"(f.a1[2]), "+v"(f.a1[3]), "+v"(f.a1[4]), "+v"(f.a1[5]), "+v"(f.a1[6]), "+v"(f.a1[7]),
                 "+v"(f.b[0]), "+v"(f.b[1]), "+v"(f.b[2]), "+v"(f.b[3]), "+v"(f.b[4]), "+v"(f.b[5]), "+v"(f.b[6]), "+v"(f.b[7]));
}
constexpr int cv_r(int m) { int r = 0; while (m >= 8 - r) { m -= 8 - r; ++r; } return r; }
constexpr int cv_c(int m) { int r = 0; while (m >= 8 - r) { m -= 8 - r; ++r; } return m; }
template <int S, int M> DI void cv_mm(CF& f, CF& n, u32 aA, u32 aB, f32x4 (&acc)[8]) {
    constexpr int r = cv_r(M), c = cv_c(M);
    acc[r + c] = mfma16(cat(f.a0[r], f.a1[r]), f.b[c], acc[r + c]);
    if constexpr ((M & 3) == 0 && M / 4 < 8) cv_ld<S + 1, M / 4>(n, aA, aB);
    SB();
    if constexpr (M + 1 < 36) cv_mm<S, M + 1>(f, n, aA, aB, acc);
}
DI void conv_body(u32 aA, u32 aB, f32x4 (&acc)[8]) {
    CF f, n;
    asm volatile("s_waitcnt lgkmcnt(0)" ::: "memory");
    cv_ld<0, 0>(f, aA, aB); cv_ld<0, 1>(f, aA, aB); cv_ld<0, 2>(f, aA, aB); cv_ld<0, 3>(f, aA, aB);
    cv_ld<0, 4>(f, aA, aB); cv_ld<0, 5>(f, aA, aB); cv_ld<0, 6>(f, aA, aB); cv_ld<0, 7>(f, aA, aB);
    _Pragma("clang loop unroll(disable)") for (int it = 0; it < 2; ++it) {
        cv_wait(f); SB();
        cv_mm<0, 0>(f, n, aA, aB, acc);
        cv_wait(n); SB();
        cv_mm<1, 0>(n, f, aA, aB, acc);
        aA += 128; aB += 128;
    }
    cv_wait(f);
    asm volatile("" ::: "memory");
}

#define ROTM 1
#define LDELAY 0
#define KDELAY 0
struct Geo { int bi, b, xcd, fh2, RB, CT, tb64, db, rot, crot; u32 R0, F0, T0, D0; };
DI Geo make_geo() {
    Geo g; g.b = blockIdx.x - NHELP; g.xcd = g.b & 7; const int bi = g.b >> 3; g.bi = bi;
    const int rq = g.xcd >> 1, rb = bi / 7, ct = bi - rb * 7;
    g.fh2 = g.xcd & 1; g.RB = 4 * rq + rb; g.CT = 7 * g.fh2 + ct; g.R0 = 128 * g.RB; g.F0 = 128 * g.CT;
    g.tb64 = 2 * g.RB + g.fh2; g.db = ct; g.T0 = 64 * g.tb64; g.D0 = 64 * g.db;

    g.rot = g.xcd >= 7 ? 0 : g.xcd;
     g.crot = (2 * ct + 7 * (rb & 1) + 3 * (rb >> 1) + rq + 5 * g.fh2) % 14;
    return g;
}
DI void make_g2(G2Ctx& g2, const Params& p, const Geo& q, int g, int lane, u32 lds0) {
    g2.w2b = (const char*)p.w2 + (size_t)(32 * g) * 896 + q.D0 * 2;
    g2.hb = p.ws + WS_H + (size_t)(q.tb64 * 2 * 112 + 2 * g) * 1024;
    g2.crot = q.crot;
#pragma unroll
    for (int n = 0; n < 4; ++n) g2.voffw[n] = (u32)((lane >> 3) * 896 + 16 * ((lane & 7) ^ (((lane >> 4) & 1) << 2)) + n * (8 * 896 - 1024));
    g2.ldsg = lds0 + g * 4096; g2.vlane = lane * 16u;
}
DI u32 slab_base(u32 lds0, int cw) { return lds0 + L_CONV + (cw >> 1) * CONV_CH + (cw & 1) * 8192; }
DI void final_epilogue(const Params& p, const Geo& q, u32 lds0) {
    int tz = threadIdx.x; PIN(tz);
    const int t = tz >> 3, c = tz & 7;
    const u32 pb = lds0 + ((t >> 5) ? 0u : 3 * L_PIECE) + ((t >> 4) & 1) * L_W + (t & 15) * 256;
    const u32 o0 = pb + 16 * ((2 * c) ^ (t & 7)), o1 = o0 ^ 16;
    f32x4 pa[4], pb_[4];
#pragma unroll
    for (int g = 0; g < 4; ++g) { pa[g] = __builtin_bit_cast(f32x4, lds16(o0 + g * 4096)); pb_[g] = __builtin_bit_cast(f32x4, lds16(o1 + g * 4096)); }
    const float r2 = *(const LAS float*)(lds0 + L_R2 + t * 4);
    const f32x4 b0 = __builtin_bit_cast(f32x4, lds16(lds0 + L_B2F + c * 32)), b1 = __builtin_bit_cast(f32x4, lds16(lds0 + L_B2F + c * 32 + 16));
    const u32 sb = slab_base(lds0, c >> 1) + t * 64 + (c & 1) * 32;
    const f32x4 sv0 = __builtin_bit_cast(f32x4, lds16(sb)), sv1 = __builtin_bit_cast(f32x4, lds16(sb + 16));
    const f32x4 xv0 = __builtin_bit_cast(f32x4, lds16(sb + 4096)), xv1 = __builtin_bit_cast(f32x4, lds16(sb + 4096 + 16));
    SB();
    const f32x4 s0 = (pa[0] + pa[1]) + (pa[2] + pa[3]), s1 = (pb_[0] + pb_[1]) + (pb_[2] + pb_[3]);
    f32x4 z0 = s0 * r2 + b0, z1 = s1 * r2 + b1;
    f32x4 e0 = z0 * -1.4426950408889634f, e1 = z1 * -1.4426950408889634f;
#pragma unroll
    for (int j = 0; j < 4; ++j) { e0[j] = __builtin_amdgcn_exp2f(e0[j]); e1[j] = __builtin_amdgcn_exp2f(e1[j]); }
    e0 += 1.f; e1 += 1.f;
#pragma unroll
    for (int j = 0; j < 4; ++j) { e0[j] = __builtin_amdgcn_rcpf(e0[j]); e1[j] = __builtin_amdgcn_rcpf(e1[j]); }
    z0 = z0 * e0; z1 = z1 * e1;
    z0 = z0 * sv0 + xv0; z1 = z1 * sv1 + xv1;
    const u32x4 res = {pk2(z0[0], z0[1]), pk2(z0[2], z0[3]), pk2(z1[0], z1[1]), pk2(z1[2], z1[3])};
    *(GAS u32x4*)((GAS char*)p.out + (size_t)(q.T0 + t) * 896 + q.D0 * 2 + c * 16) = res;
}
#define STAMP_DECL
#define STAMP_OUT(wave) do {} while (0)

extern __shared__ __attribute__((aligned(16))) char smem[];

struct CL { const char *xb, *wb; u32 vx[4], vw[4], sl; int rot; };
struct CSrc { const char *xs, *wsrc; };
template <int ST_> DI CSrc c_src(const CL& c) { int kc = ST_ + c.rot; if (kc >= 7) kc -= 7; return CSrc{c.xb + kc * 128, c.wb + (size_t)kc * 64 * 3584}; }
template <int ST_, int N> DI void c_dma1(const CL& c, const CSrc& s) {
    if constexpr (N == 0) setm0(c.sl + (ST_ & 3) * L_PIECE);
    if constexpr (N == 4) setm0(c.sl + (ST_ & 3) * L_PIECE + L_W);
    if constexpr (N < 4) dmai<N * 1024>(c.vx[N], s.xs); else dmai<(N - 4) * 1024>(c.vw[N - 4], s.wsrc);
}
template <int ST_> DI void c_dma(const CL& c) {
    const CSrc s = c_src<ST_>(c);
    c_dma1<ST_, 0>(c, s); c_dma1<ST_, 1>(c, s); c_dma1<ST_, 2>(c, s); c_dma1<ST_, 3>(c, s);
    c_dma1<ST_, 4>(c, s); c_dma1<ST_, 5>(c, s); c_dma1<ST_, 6>(c, s); c_dma1<ST_, 7>(c, s);
}
DI void make_cl(CL& c, const Params& p, const Geo& q, int cw, int lane, u32 lds0) {
    const int r8 = lane >> 3, c8 = lane & 7, r4 = lane >> 4, c16 = lane & 15;
#pragma unroll
    for (int n = 0; n < 4; ++n) {
        c.vx[n] = (u32)((32 * cw + r8) * 896 + 16 * (c8 ^ (((n & 1) << 2) | (r8 >> 1))) + n * (8 * 896 - 1024));
        c.vw[n] = (u32)((16 * cw + r4) * 3584 + 16 * (c16 ^ (r4 << 2)) + n * (4 * 3584 - 1024));
    }
    c.xb = (const char*)p.x + (size_t)q.R0 * 896; c.wb = (const char*)p.w1 + q.F0 * 2;
    c.sl = lds0 + cw * 4096; c.rot = q.rot;
}

template <int ST_> DI void l_dma(const CL& c) {
    const CSrc s = c_src<ST_>(c);
    const u32 sl = c.sl + (ST_ & 3) * L_PIECE;
    setm0(sl); dmai<0>(c.vx[0], s.xs); dmai<1024>(c.vx[1], s.xs); dmai<2048>(c.vx[2], s.xs); dmai<3072>(c.vx[3], s.xs);
    setm0(sl + L_W); dmai<0>(c.vw[0], s.wsrc); dmai<1024>(c.vw[1], s.wsrc); dmai<2048>(c.vw[2], s.wsrc); dmai<3072>(c.vw[3], s.wsrc);
    const char* x2 = s.xs + 32 * 896; const char* w2 = s.wsrc + 16 * 3584;
    setm0(sl + 4096); dmai<0>(c.vx[0], x2); dmai<1024>(c.vx[1], x2); dmai<2048>(c.vx[2], x2); dmai<3072>(c.vx[3], x2);
    setm0(sl + L_W + 4096); dmai<0>(c.vw[0], w2); dmai<1024>(c.vw[1], w2); dmai<2048>(c.vw[2], w2); dmai<3072>(c.vw[3], w2);
}

struct Fr { u32x4 x[4][2], w[4][2]; };
struct GA { u32 ax[4], aw[2]; };
template <int ST_> DI void g1_read(const GA& a, Fr& f) {
    constexpr u32 so = (ST_ & 3) * L_PIECE;
#pragma unroll
    for (int k = 0; k < 4; ++k) {
        f.w[k][0] = cat(ldtr(a.aw[0] + so + k * 4096), ldtr(a.aw[0] + so + k * 4096 + 1024));
        f.x[k][0] = lds16(a.ax[k] + so);
        f.w[k][1] = cat(ldtr(a.aw[1] + so + k * 4096), ldtr(a.aw[1] + so + k * 4096 + 1024));
        f.x[k][1] = lds16(a.ax[k] + so + 4096);
    }
}
template <int K0> DI void g1_mma(const Fr& f, f32x16 (&acc)[2][2]) {
#pragma unroll
    for (int k = K0; k < K0 + 2; ++k) {
        acc[0][0] = mfma32(f.w[k][0], f.x[k][0], acc[0][0]); acc[1][0] = mfma32(f.w[k][1], f.x[k][0], acc[1][0]);
        acc[0][1] = mfma32(f.w[k][0], f.x[k][1], acc[0][1]); acc[1][1] = mfma32(f.w[k][1], f.x[k][1], acc[1][1]);
    }
    SB();
}

template <int ST_, int I> DI void g1_rd(const GA& a, Fr& f) {
    constexpr u32 so = (ST_ & 3) * L_PIECE; constexpr int k = I >> 1, h = I & 1;
    f.w[k][h] = cat(ldtr(a.aw[h] + so + k * 4096), ldtr(a.aw[h] + so + k * 4096 + 1024));
    f.x[k][h] = lds16(a.ax[k] + so + h * 4096);
}
template <int K0, int I> DI void g1_m1(const Fr& f, f32x16 (&acc)[2][2]) {
    constexpr int k = K0 + (I >> 2), mt = I & 1, st = (I >> 1) & 1;
    acc[mt][st] = mfma32(f.w[k][mt], f.x[k][st], acc[mt][st]);
}
template <int ST_, bool CARRY> DI void g1_stage(const GA& a, const Fr& fp, Fr& fn, f32x16 (&acc)[2][2], u32 lds0, int g, u32& pk) {
    if constexpr (CARRY) { g1_m1<2, 0>(fp, acc); SB(); cons_set<ST_ - 1>(lds0, g); SB(); }
    lds_wait_ge(lds0 + F_LAND + 4 * ST_, ST_ == 0 ? 256 : (ST_ == 3 || ST_ == 6) ? 128 : 64, pk);
    if constexpr (CARRY) {
        g1_rd<ST_, 0>(a, fn); SB();
        g1_m1<2, 1>(fp, acc); SB(); g1_m1<2, 2>(fp, acc); g1_rd<ST_, 1>(a, fn); SB();
        g1_m1<2, 3>(fp, acc); SB(); g1_m1<2, 4>(fp, acc); g1_rd<ST_, 2>(a, fn); SB();
        g1_m1<2, 5>(fp, acc); g1_rd<ST_, 3>(a, fn); SB(); g1_m1<2, 6>(fp, acc); SB();
        g1_m1<2, 7>(fp, acc); SB();
    } else { g1_rd<ST_, 0>(a, fn); g1_rd<ST_, 1>(a, fn); g1_rd<ST_, 2>(a, fn); g1_rd<ST_, 3>(a, fn); SB(); }
    g1_m1<0, 0>(fn, acc); g1_rd<ST_, 4>(a, fn); SB(); g1_m1<0, 1>(fn, acc); SB();
    g1_m1<0, 2>(fn, acc); g1_rd<ST_, 5>(a, fn); SB(); g1_m1<0, 3>(fn, acc); g1_rd<ST_, 6>(a, fn); SB();
    g1_m1<0, 4>(fn, acc); g1_rd<ST_, 7>(a, fn); SB(); g1_m1<0, 5>(fn, acc); pk = lds_peek(lds0 + F_LAND + 4 * ST_ + 4); SB();
    g1_m1<0, 6>(fn, acc); SB(); g1_m1<0, 7>(fn, acc); SB();
}

DI void body_g(const Params& p, const int g) {
    STAMP_DECL
    const u32 lds0 = (u32)(size_t)(LAS char*)smem;
    const int lane = threadIdx.x & 63;
    const Geo q = make_geo();
    char* ws = p.ws;
    const int hi = lane >> 5, s31 = lane & 31;
    const int r8 = lane >> 3, c8 = lane & 7;
    const u32 raw = lds0 + L_LOC0 + g * KVRAW_SZ;
    {
        const u32 vkv = (u32)(r8 * 896 + c8 * 16);
        bdma16((u32)(((int)q.T0 + 16 * g - 8) * 896 + (int)q.D0 * 2) + vkv, mksrd(p.k, S_ * D_ * 2), raw);
        const char* kb = (const char*)p.k + (size_t)(q.T0 + 16 * g) * 896 + q.D0 * 2;
        const char* vb = (const char*)p.v + (size_t)(q.T0 + 16 * g) * 896 + q.D0 * 2;
        dmai<1024>(vkv, kb - 1024); dmai<2048>(vkv, kb + 8 * 896 - 2048); dmai<3072>(vkv, vb - 3072);
        setm0(raw + 4096);
        dmai<0>(vkv, vb + 8 * 896);
        if (lane < 32) dmai<1024>(lane * 16u, (const char*)p.shk + q.D0 * 8 - 1024);
        CL c; make_cl(c, p, q, g, lane, lds0);
        c_dma<0>(c);
    }
    u32x2 ep = gld8c(0u, ws + WS_EPOCH);
    const int fh = g & 1, sh = g >> 1;
    GA ga;
    {
        const int blk = (lane >> 4) & 1, qq = (lane & 15) >> 2, pp = lane & 3;
        const u32 Z = (u32)(hi ^ ((s31 >> 1) & 7));
#pragma unroll
        for (int k = 0; k < 4; ++k) ga.ax[k] = lds0 + (64 * sh + s31) * 128 + 16 * ((2 * k) ^ Z);
        ga.aw[0] = lds0 + L_W + (8 * hi + qq) * 256 + 16 * ((8 * fh + 2 * blk + (pp >> 1)) ^ (qq << 2)) + 8 * (pp & 1);
        ga.aw[1] = ga.aw[0] ^ 64;
    }
    WAITV(9);
    ST(1);
    {
        u32x4 wr[4], kr[5], vr[2];
#pragma unroll
        for (int e = 0; e < 4; ++e) wr[e] = lds16(raw + 5120 + c8 * 64 + e * 16);
#pragma unroll
        for (int j = 0; j < 5; ++j) kr[j] = lds16(raw + (5 + 2 * r8 + j) * 128 + c8 * 16);
        vr[0] = lds16(raw + 3072 + 2 * r8 * 128 + c8 * 16); vr[1] = lds16(raw + 3072 + (2 * r8 + 1) * 128 + c8 * 16);
        SB();
        u32x4 o0, o1;
#pragma unroll
        for (int e = 0; e < 4; ++e) {
            u32 pl[4], ph[4];
#pragma unroll
            for (int j = 0; j < 4; ++j) { pl[j] = __builtin_amdgcn_perm(kr[j][e], kr[j + 1][e], 0x05040100u); ph[j] = __builtin_amdgcn_perm(kr[j][e], kr[j + 1][e], 0x07060302u); }
            f32x2 a = {dot2(pl[0], wr[e].y, dot2(pl[2], wr[e].x, 0.f)), dot2(ph[0], wr[e].w, dot2(ph[2], wr[e].z, 0.f))};
            f32x2 b = {dot2(pl[1], wr[e].y, dot2(pl[3], wr[e].x, 0.f)), dot2(ph[1], wr[e].w, dot2(ph[3], wr[e].z, 0.f))};
            a *= f32x2{bflo(vr[0][e]), bfhi(vr[0][e])}; b *= f32x2{bflo(vr[1][e]), bfhi(vr[1][e])};
            o0[e] = pk2(a.x, a.y); o1[e] = pk2(b.x, b.y);
        }
        sts16(lds0 + L_KVS + (16 * g + 2 * r8) * 144 + c8 * 16, o0);
        sts16(lds0 + L_KVS + (16 * g + 2 * r8 + 1) * 144 + c8 * 16, o1);
        asm volatile("s_waitcnt lgkmcnt(0)" ::: "memory");
        lds_add(lds0 + F_KVS);
    }
    f32x16 acc[2][2] = {};
    Fr fa, fb;
    WAITV(1);
    lds_add(lds0 + F_LAND);
    u32 pk = lds_peek(lds0 + F_LAND);
    ST(2);
    g1_stage<0, false>(ga, fb, fa, acc, lds0, g, pk);
    g1_stage<1, true>(ga, fa, fb, acc, lds0, g, pk);
    g1_stage<2, true>(ga, fb, fa, acc, lds0, g, pk);
    asm volatile("s_waitcnt vmcnt(0)" : "+v"(ep) :: "memory");
    const u64 epoch = ((u64)(u32)__builtin_amdgcn_readfirstlane(ep.y) << 32) | (u32)__builtin_amdgcn_readfirstlane(ep.x);
    const u64 tag = epoch + TAG_SALT;
    const u32 tlo = (u32)tag, thi = (u32)(tag >> 32);
    g1_stage<3, true>(ga, fa, fb, acc, lds0, g, pk);
    ST(3);
    g1_stage<4, true>(ga, fb, fa, acc, lds0, g, pk);
    const u32 rgo = WS_RG + (q.R0 + 64 * sh + lane) * 8, cgo = WS_CG + (q.F0 + 64 * fh + lane) * 8;
    u32x2 rg = gld8c(rgo, ws), cg = gld8c(cgo, ws);
    u32 b1v = gld2((u32)((64 * fh + lane) * 2), (const char*)p.b1 + q.F0 * 2);
    g1_stage<5, true>(ga, fa, fb, acc, lds0, g, pk);
    g1_stage<6, true>(ga, fb, fa, acc, lds0, g, pk);
    cons_set<6>(lds0, g);
    ST(5);
    asm volatile("s_waitcnt vmcnt(0)" : "+v"(rg), "+v"(cg), "+v"(b1v) :: "memory");
    _Pragma("clang loop unroll(disable)") for (int spin = 0; spin < SPIN_CAP && !__all(rg.y == tlo && cg.y == tlo); ++spin) {
        rg = gld8c(rgo, ws); cg = gld8c(cgo, ws);
        asm volatile("s_waitcnt vmcnt(0)" : "+v"(rg), "+v"(cg) :: "memory");
    }
    if (fh == 0 && sh == q.fh2) *(LAS u32*)(lds0 + L_ROWF + lane * 4) = rg.x;
    g1_mma<2>(fa, acc);
    {
        u32x4 ax[2], bx[2];
        const auto rr = __builtin_amdgcn_permlane32_swap(rg.x, rg.x, false, false);
        const auto cc = __builtin_amdgcn_permlane32_swap(cg.x, cg.x, false, false);
        const auto bb = __builtin_amdgcn_permlane32_swap(b1v, b1v, false, false);
#pragma unroll
        for (int i = 0; i < 2; ++i) {
            const u32 nm = __builtin_amdgcn_perm(rr[i], rr[i], 0x01000100u) ^ 0x80008000u;
            ax[i] = u32x4{cc[i], bb[i], 0u, 0u};
            bx[i] = u32x4{hi ? 0u : nm, hi ? 0u : (rr[i] >> 16), 0u, 0u};
        }
#pragma unroll
        for (int st = 0; st < 2; ++st)
#pragma unroll
            for (int mt = 0; mt < 2; ++mt) acc[mt][st] = mfma32(ax[mt], bx[st], acc[mt][st]);
#pragma unroll
        for (int st = 0; st < 2; ++st) {
            char* hb = ws + WS_H + (size_t)((4 * q.RB + 2 * sh + st) * 112 + q.CT * 8 + fh * 4) * 1024;
#pragma unroll
            for (int mt = 0; mt < 2; ++mt)
#pragma unroll
                for (int t = 0; t < 2; ++t) {
                    u32x4 hv;
#pragma unroll
                    for (int j = 0; j < 4; ++j) {
                        f32x2 a = {acc[mt][st][8 * t + 2 * j], acc[mt][st][8 * t + 2 * j + 1]};
                        f32x2 u = {relu(a.x), relu(a.y)};
                        u = u * u;
                        hv[j] = pk2(u.x, u.y);
                    }
                    if (mt == 0 && t == 0) gst16c<0>(lane * 16u, hb, hv);
                    if (mt == 0 && t == 1) gst16c<1024>(lane * 16u, hb, hv);
                    if (mt == 1 && t == 0) gst16c<2048>(lane * 16u, hb, hv);
                    if (mt == 1 && t == 1) gst16c<3072>(lane * 16u, hb, hv);
                    SB();
                }
        }
    }
    ST(6);
    G2Ctx g2; make_g2(g2, p, q, g, lane, lds0);
    {
        const int blk = (lane >> 4) & 1, qq = (lane & 15) >> 2, pp = lane & 3;
        const u32 t0 = g2.ldsg + (4 * hi + qq) * 128 + 16 * ((2 * blk + (pp >> 1)) ^ ((qq >> 1) << 2)) + 8 * (pp & 1);
#pragma unroll
        for (int i = 0; i < 3; ++i) { g2.ta[i][0] = t0 + i * 65536; g2.ta[i][1] = (t0 ^ 64) + i * 65536; PIN(g2.ta[i][0]); PIN(g2.ta[i][1]); }
    }
    u32x4 wf[WS_][4];
    cons_wait(lds0, 4);
    asm volatile("s_waitcnt vmcnt(0) lgkmcnt(0)" ::: "memory"); ST(9);
    if (lds_arrive_last(lds0 + F_HCNT, 4)) { if (lane < 15) gst8c<0>(lane < 14 ? WS_HF + (q.RB * 14 + lane) * MB + q.CT * 8 : WS_ALL + q.b * 8, ws, u32x2{tlo, thi}); }
    {
        const u32 po = WS_HF + (q.RB * 14 + q.CT) * MB + (lane < 14 ? lane : 0) * 8;
        g2_issue_w<2>(g2); g2_issue_w<3>(g2);
        u32x2 f = {0u, 0u};
        if (g == 0) f = gld8c(po, ws);
        g2_issue_w<4>(g2);
        lds_wait(lds0 + F_W);
        g2_read<0>(g2, wf); g2_read<1>(g2, wf);
        asm volatile("s_waitcnt lgkmcnt(0)" ::: "memory");
        g2_issue_w<5>(g2); g2_issue_w<6>(g2);
        asm volatile("s_waitcnt vmcnt(12)" : "+v"(f) :: "memory");
        bool rdy = __all(f.x == tlo && f.y == thi);
        if (g == 0 && rdy) lds_set(lds0 + F_HRDY);
        g2_read<2>(g2, wf); g2_read<3>(g2, wf);
        asm volatile("s_waitcnt lgkmcnt(0)" ::: "memory");
        if (g == 0 && !rdy) f = gld8c(po, ws);
        g2_issue_w<7>(g2); g2_issue_w<8>(g2);
        if (g == 0) {
            if (!rdy) {
                asm volatile("s_waitcnt vmcnt(8)" : "+v"(f) :: "memory");
                if (!__all(f.x == tlo && f.y == thi)) poll8(po, ws, true, tlo, thi);
                lds_set(lds0 + F_HRDY);
            }
        } else lds_wait(lds0 + F_HRDY);
    }
    ST(7);
    {
        u32x4 hf[HS][4];
        g2_issue_h<0>(g2, hf); g2_issue_h<1>(g2, hf); if constexpr (HD > 2) g2_issue_h<2 % HS>(g2, hf); if constexpr (HD > 3) g2_issue_h<3 % HS>(g2, hf); if constexpr (HD > 4) g2_issue_h<4 % HS>(g2, hf);
        f32x16 a2[2][2] = {};
        g2_step<0>(g2, hf, a2, wf, STP);
        ST(8);
        const u32 pw = g2.ldsg + (s31 >> 4) * L_W + (s31 & 15) * 256;
#pragma unroll
        for (int s2 = 0; s2 < 2; ++s2)
#pragma unroll
            for (int dt = 0; dt < 2; ++dt)
#pragma unroll
                for (int gr = 0; gr < 4; ++gr) {
                    const u32 a = pw + (s2 ? 0u : 3 * L_PIECE) + 16 * ((8 * dt + 2 * gr + hi) ^ (s31 & 7));
                    sts16(a, __builtin_bit_cast(u32x4, f32x4{a2[dt][s2][4 * gr], a2[dt][s2][4 * gr + 1], a2[dt][s2][4 * gr + 2], a2[dt][s2][4 * gr + 3]}));
                }
    }
    BARL(); ST(10);
    final_epilogue(p, q, lds0);
    STAMP_OUT(g);
}

DI void body_c(const Params& p, const int cw) {
    STAMP_DECL
    const u32 lds0 = (u32)(size_t)(LAS char*)smem;
    const int lane = threadIdx.x & 63;
    const Geo q = make_geo();
    char* ws = p.ws;
    const int hi = lane >> 5, s31 = lane & 31;
    const int half = cw & 1, chl = cw >> 1, chan = 2 * q.b + chl;
    u32x2 ep;
    u32x4 xa, xb_; u32 dv;
    const char* xsl = (const char*)p.x + (size_t)q.T0 * 896 + (q.D0 + 16 * cw) * 2;
    if (cw < 2) {
        CL c, c2; make_cl(c, p, q, 0, lane, lds0); c2 = c; c2.xb += 64 * 896; c2.wb += 32 * 3584; c2.sl += 8192;
        ep = gld8c(0u, ws + WS_EPOCH);
        if (cw == 0) {
            l_dma<1>(c); l_dma<1>(c2); ST(1);
            WAITV(0); lds_add(lds0 + F_LAND + 4); ST(2);
            cons_wait(lds0, 0); l_dma<4>(c); l_dma<4>(c2); ST(19);
            WAITV(0); lds_add(lds0 + F_LAND + 16); ST(20);
        } else {
            l_dma<2>(c); l_dma<2>(c2); ST(1);
            WAITV(0); lds_add(lds0 + F_LAND + 8); ST(2);
            cons_wait(lds0, 1); l_dma<5>(c); l_dma<5>(c2); ST(19);
            WAITV(0); lds_add(lds0 + F_LAND + 20); ST(20);
        }
        asm volatile("s_waitcnt vmcnt(0)" : "+v"(ep) :: "memory");
        const u64 tg = (((u64)ep.y << 32) | ep.x) + TAG_SALT;
        if (cw == 1) { poll8(WS_KVF + q.b * MB + s31 * 8, ws, true, (u32)tg, (u32)(tg >> 32)); lds_set(lds0 + F_KVRDY); }
        else {
            cons_wait(lds0, 3);
#pragma unroll
            for (int i = 0; i < 4; ++i) { G2Ctx g2; make_g2(g2, p, q, i, lane, lds0); g2_issue_w<0>(g2); g2_issue_w<1>(g2); }
            lds_wait(lds0 + F_KVRDY);
        }
        ST(7);
        const char* kb2 = ws + WS_KVT + (size_t)(2 * q.b + cw) * TP;
        u32x4 u0 = gld16c<0>(lane * 16u, kb2), u1 = gld16c<1024>(lane * 16u, kb2), u2 = gld16c<2048>(lane * 16u, kb2), u3 = gld16c<3072>(lane * 16u, kb2);
        xa = gld16((u32)(lane * 896), xsl); xb_ = gld16((u32)(lane * 896 + 16), xsl); dv = gld2(0u, (const char*)p.ssmd + chan * 2);
        if (cw == 0) { WAITV(7); lds_set(lds0 + F_W); }
        cons_wait(lds0, 5); cons_wait(lds0, 6);
        const u32 uq_ = lds0 + L_CONV + cw * CONV_CH + TOEP_CH + lane * 16;
        if (lane < 32) sts16(uq_ + 4096, u32x4{0u, 0u, 0u, 0u});
        asm volatile("s_waitcnt vmcnt(0)" : "+v"(u0), "+v"(u1), "+v"(u2), "+v"(u3), "+v"(xa), "+v"(xb_), "+v"(dv) :: "memory");
        ST(14);
        sts16(uq_, u0); sts16(uq_ + 1024, u1); sts16(uq_ + 2048, u2); sts16(uq_ + 3072, u3);
        asm volatile("s_waitcnt lgkmcnt(0)" ::: "memory");
        lds_add(lds0 + F_TOEP + cw * 4);
    } else {
        const int kw = cw - 2;
        if (kw == 0 && lane < 8) dma16(lane * 16u, (const char*)p.b2 + q.D0 * 2, lds0 + L_B2);
        ep = gld8c(0u, ws + WS_EPOCH);
        CL c; make_cl(c, p, q, 2 * kw, lane, lds0);
        l_dma<3>(c);
        lds_wait_ge(lds0 + F_KVS, 256, lds_peek(lds0 + F_KVS));
        ST(2);
        {
            const int grp = lane >> 4, qq = (lane & 15) >> 2, pp = lane & 3;
            const u32 a = lds0 + L_KVS + (16 * grp + 8 * (pp >> 1) + 7 - qq) * 144 + kw * 64 + (pp & 1) * 8;
            const u32 vo = (u32)((q.D0 + 32 * kw + (lane & 7)) * TP + (2040 - (int)q.T0 - 8 * (lane >> 3)) * 2);
            u32x4 tr[4];
#pragma unroll
            for (int i = 0; i < 4; ++i) tr[i] = cat(ldtr(a + 16 * i), ldtr(a - 4 * 144 + 16 * i));
            SB();
#pragma unroll
            for (int i = 0; i < 4; ++i) gst16c<0>(vo, ws + WS_KVT + i * 8 * TP, tr[i]);
        }
        ST(20);
        asm volatile("s_waitcnt vmcnt(4)" : "+v"(ep) :: "memory");
        lds_add(lds0 + F_LAND + 12);
        WAITV(0);
        ST(19);
        const u64 tg = (((u64)ep.y << 32) | ep.x) + TAG_SALT;
        if (lds_arrive_last(lds0 + F_KVCNT, 2)) {
            if (lane < 32) gst8c<0>(WS_KVF + (q.db * 32 + lane) * MB + q.tb64 * 8, ws, u32x2{(u32)tg, (u32)(tg >> 32)});
        }
        cons_wait(lds0, 2);
        l_dma<6>(c);
        ST(3);
        u32x4 sk[5];
        const u32x4 sksrd = mksrd((const char*)p.ssmk + (size_t)(2 * q.b + kw) * 4096, 4096);
#pragma unroll
        for (int r = 0; r < 5; ++r) sk[r] = bld16((u32)((lane - 1 + 63 * r) * 16), sksrd);
        xa = gld16((u32)(lane * 896), xsl); xb_ = gld16((u32)(lane * 896 + 16), xsl); dv = gld2(0u, (const char*)p.ssmd + chan * 2);
        WAITV(8);
        lds_add(lds0 + F_LAND + 24);
        ST(21);
        asm volatile("s_waitcnt vmcnt(0)" : "+v"(sk[0]), "+v"(sk[1]), "+v"(sk[2]), "+v"(sk[3]), "+v"(sk[4]), "+v"(xa), "+v"(xb_), "+v"(dv) :: "memory");
        u32x4 tw[5][4];
#pragma unroll
        for (int r = 0; r < 5; ++r) {
            const u32x4 B = sk[r];
            u32x4 A;
#pragma unroll
            for (int j = 0; j < 4; ++j) A[j] = __builtin_amdgcn_update_dpp(0u, B[j], 0x138  , 0xf, 0xf, false);
            const u32 e0 = __builtin_amdgcn_alignbit(A.y, A.x, 16), e1 = __builtin_amdgcn_alignbit(A.z, A.y, 16), e2 = __builtin_amdgcn_alignbit(A.w, A.z, 16),
                      e3 = __builtin_amdgcn_alignbit(B.x, A.w, 16), e4 = __builtin_amdgcn_alignbit(B.y, B.x, 16);
            tw[r][0] = u32x4{e0, e1, e2, e3}; tw[r][1] = u32x4{A.y, A.z, A.w, B.x}; tw[r][2] = u32x4{e1, e2, e3, e4}; tw[r][3] = u32x4{A.z, A.w, B.x, B.y};
            PIN(tw[r][0]); PIN(tw[r][1]); PIN(tw[r][2]); PIN(tw[r][3]);
        }
        cons_wait(lds0, 5); cons_wait(lds0, 6);
        ST(5);
        const u32 tq = lds0 + L_CONV + kw * CONV_CH + lane * 16;
#pragma unroll
        for (int r = 0; r < 5; ++r) {
            if ((lane > 0 || r == 0) && (r < 4 || lane < 6)) {
#pragma unroll
                for (int c = 0; c < 4; ++c) sts16(tq + r * 1008 + c * TOEP_CP, tw[r][c]);
            }
        }
        asm volatile("s_waitcnt lgkmcnt(0)" ::: "memory");
        lds_add(lds0 + F_TOEP + kw * 4);
    }
    const u64 epoch = ((u64)(u32)__builtin_amdgcn_readfirstlane(ep.y) << 32) | (u32)__builtin_amdgcn_readfirstlane(ep.x);
    const u64 tag = epoch + TAG_SALT;
    const u32 tlo = (u32)tag, thi = (u32)(tag >> 32);
    const u32 tbase = lds0 + L_CONV + chl * CONV_CH;
    const u32 ub = tbase + TOEP_CH;
    ST(6);
    lds_wait_ge(lds0 + F_TOEP + chl * 4, 128, lds_peek(lds0 + F_TOEP + chl * 4));
    ST(15);
    const int bb = lane & 15, gg = lane >> 4;
    const u32 aA = tbase + (bb & 3) * TOEP_CP + ((bb >> 2) + 2 * gg) * 8 + half * 256;
    const u32 aB = ub + (30 - 2 * bb + gg) * 16 + half * 256;
    f32x4 ac8[8] = {};
    conv_body(aA, aB, ac8);
    ST(16);
    f32x4 acc[4];
    {
        const u32 xo = ub + U_SZ + lane * 16;
#pragma unroll
        for (int i = 0; i < 4; ++i) { const f32x4 give = half ? ac8[i] : ac8[4 + i]; acc[i] = half ? ac8[4 + i] : ac8[i]; sts16(xo + half * 4096 + i * 1024, __builtin_bit_cast(u32x4, give)); }
        asm volatile("s_waitcnt lgkmcnt(0)" ::: "memory");
        lds_set(lds0 + F_XCH + cw * 4);
        lds_wait(lds0 + F_XCH + (cw ^ 1) * 4);
        f32x4 got[4];
#pragma unroll
        for (int i = 0; i < 4; ++i) got[i] = __builtin_bit_cast(f32x4, lds16(xo + (half ^ 1) * 4096 + i * 1024));
        SB();
#pragma unroll
        for (int i = 0; i < 4; ++i) acc[i] = f32x4{add1(acc[i][0], got[i][0]), add1(acc[i][1], got[i][1]), add1(acc[i][2], got[i][2]), add1(acc[i][3], got[i][3])};
    }
    ST(8);
    {
        const float dd = bflo(dv);
        char* yb = ws + WS_YT + (size_t)chan * TP;
        u32x2 uq[4];
#pragma unroll
        for (int i = 0; i < 4; ++i) uq[i] = lds8(ub + (2044 - (256 * (4 * half + i) + 16 * bb + 4 * gg)) * 2);
        SB();
#pragma unroll
        for (int i = 0; i < 4; ++i) {
            const int qt = 4 * half + i;
            const int t = 256 * qt + 16 * bb + 4 * gg;
            const u32x2 uu = uq[i];
            const u32 c01 = pk2(acc[i][0], acc[i][1]), c23 = pk2(acc[i][2], acc[i][3]);
            float y0 = fma1(bfhi(uu.y), dd, bflo(c01)), y1 = fma1(bflo(uu.y), dd, bfhi(c01));
            float y2 = fma1(bfhi(uu.x), dd, bflo(c23)), y3 = fma1(bflo(uu.x), dd, bfhi(c23));
            gst8c<0>((u32)(t * 2), yb, u32x2{pk2(y0, y1), pk2(y2, y3)});
        }
        WAITV(0);
        if (lds_arrive_last(lds0 + F_YCNT, 4)) { if (lane < 32) gst8c<0>(WS_YF + (lane * 7 + (q.b >> 5)) * MB + (q.b & 31) * 8, ws, u32x2{tlo, thi}); }
    }
    _Pragma("clang loop unroll(disable)") for (int spin = 0; spin < (1 << 22); ++spin) { if (__builtin_amdgcn_readfirstlane(*(volatile LAS u32*)(lds0 + F_YCNT)) >= 4) break; __builtin_amdgcn_s_sleep(1); }
    asm volatile("" ::: "memory");
    const u32 slab = slab_base(lds0, cw) + lane * 64;
    f32x4 xn[4];
    {
        _Pragma("clang loop unroll(disable)") for (int spin = 0; spin < (1 << 22); ++spin) { if (__builtin_amdgcn_readfirstlane(*(volatile LAS u32*)(lds0 + F_HCNT)) >= 4) break; __builtin_amdgcn_s_sleep(1); }
        asm volatile("" ::: "memory");
        const u32 rf = *(const LAS u32*)(lds0 + L_ROWF + lane * 4);
        const float m = bflo(rf), ri = __builtin_amdgcn_rcpf(bfhi(rf)), nmri = -m * ri;
        const u32 xx[8] = {xa.x, xa.y, xa.z, xa.w, xb_.x, xb_.y, xb_.z, xb_.w};
#pragma unroll
        for (int j = 0; j < 4; ++j) {
            f32x4 v = {bflo(xx[2 * j]), bfhi(xx[2 * j]), bflo(xx[2 * j + 1]), bfhi(xx[2 * j + 1])};
            sts16(slab + 4096 + j * 16, __builtin_bit_cast(u32x4, v));
            xn[j] = f32x4{fma1(v[0], ri, nmri), fma1(v[1], ri, nmri), fma1(v[2], ri, nmri), fma1(v[3], ri, nmri)};
        }
        if (cw == 0) *(LAS float*)(lds0 + L_R2 + lane * 4) = ri * ri;
        if (cw == 1) *(LAS float*)(lds0 + L_B2F + lane * 4) = bflo(*(const LAS u16*)(lds0 + L_B2 + lane * 2));
    }
    if (q.b == 0 && cw == 3) {
        _Pragma("clang loop unroll(disable)") for (int spin = 0; spin < SPIN_CAP; ++spin) {
            u32x4 f0 = gld16c<0>(lane * 16u, ws + WS_ALL), f1 = gld16c<1024>((lane < 48 ? lane : 0) * 16u, ws + WS_ALL);
            asm volatile("s_waitcnt vmcnt(0)" : "+v"(f0), "+v"(f1) :: "memory");
            bool ok = f0.x == tlo && f0.y == thi && f0.z == tlo && f0.w == thi && f1.x == tlo && f1.y == thi && f1.z == tlo && f1.w == thi;
            if (__all(ok)) break;
            __builtin_amdgcn_s_sleep(SLP);
        }
        if (lane == 0) { const u64 e1 = epoch + 1; gst8c<0>(0u, ws + WS_EPOCH, u32x2{(u32)e1, (u32)(e1 >> 32)}); }
    }
    if (cw == 0) { poll8(WS_YF + (q.tb64 * 7 + q.db) * MB + s31 * 8, ws, true, tlo, thi); lds_set(lds0 + F_YRDY); }
    else lds_wait(lds0 + F_YRDY);
    ST(9);
    {
        const char* yb = ws + WS_YT + (size_t)(q.D0 + 16 * cw) * TP + q.T0 * 2;
        const u32 vo = (u32)((lane >> 3) * TP + (lane & 7) * 16);
        u32x4 y0 = gld16c<0>(vo, yb), y1 = gld16c<0>(vo, yb + 8 * TP);
        asm volatile("s_waitcnt vmcnt(0)" : "+v"(y0), "+v"(y1) :: "memory");
        const u32 ys = tbase + TOEP_CH + U_SZ + half * 4096;
        sts16(ys + (lane >> 3) * 144 + (lane & 7) * 16, y0);
        sts16(ys + (8 + (lane >> 3)) * 144 + (lane & 7) * 16, y1);
        const int qq = (lane & 15) >> 2, pp = lane & 3;
        const u32 a = ys + qq * 144 + gg * 32 + pp * 8;
        u32x2 t0 = ldtr(a), t1 = ldtr(a + 4 * 144), t2 = ldtr(a + 8 * 144), t3 = ldtr(a + 12 * 144);
        const u32 yy[8] = {t0.x, t0.y, t1.x, t1.y, t2.x, t2.y, t3.x, t3.y};
#pragma unroll
        for (int j = 0; j < 4; ++j) {
            f32x4 v = {bflo(yy[2 * j]), bfhi(yy[2 * j]), bflo(yy[2 * j + 1]), bfhi(yy[2 * j + 1])};
            sts16(slab + ((j ^ (lane & 3)) * 0 + j) * 16, __builtin_bit_cast(u32x4, f32x4{mul1(v[0], xn[j][0]), mul1(v[1], xn[j][1]), mul1(v[2], xn[j][2]), mul1(v[3], xn[j][3])}));
        }
    }
    BARL(); ST(10);
    final_epilogue(p, q, lds0);
    STAMP_OUT(cw + 4);
}

DI float dpp_xor_sum8(float v) {
    v += __uint_as_float(__builtin_amdgcn_mov_dpp(__float_as_uint(v), 0xB1, 0xf, 0xf, true));
    v += __uint_as_float(__builtin_amdgcn_mov_dpp(__float_as_uint(v), 0x4E, 0xf, 0xf, true));
    v += __uint_as_float(__builtin_amdgcn_mov_dpp(__float_as_uint(v), 0x141, 0xf, 0xf, true));
    return v;
}
DI void body_help(const Params& p, const int wave) {
    const u32 lds0 = (u32)(size_t)(LAS char*)smem;
    const int lane = threadIdx.x & 63, r8 = lane >> 3, c8 = lane & 7;
    const int xcd = blockIdx.x & 7, j = blockIdx.x >> 3;
    char* ws = p.ws;
    u32x2 ep = gld8c(0u, ws + WS_EPOCH);
    u32x4 d[14];
    const int ctile = (xcd >> 1) * 4 + j;
    const bool cols = wave >= 4;
    if (!cols) {
        const char* xb = (const char*)p.x + (size_t)(256 * xcd + 64 * j + 16 * wave) * 896;
#pragma unroll
        for (int i = 0; i < 14; ++i) d[i] = gld16((u32)((r8 + 8 * (i / 7)) * 896 + (i % 7) * 128 + c8 * 16), xb);
    } else {
        const char* wb = (const char*)p.w1 + (size_t)(112 * (wave - 4)) * 3584 + ((xcd & 1) * 14 + (ctile < 14 ? ctile : 0)) * 128;
#pragma unroll
        for (int i = 0; i < 14; ++i) d[i] = gld16((u32)((r8 + 8 * i) * 3584 + c8 * 16), wb);
    }
    asm volatile("s_waitcnt vmcnt(0)" : "+v"(ep), "+v"(d[0]), "+v"(d[1]), "+v"(d[2]), "+v"(d[3]), "+v"(d[4]), "+v"(d[5]), "+v"(d[6]), "+v"(d[7]), "+v"(d[8]), "+v"(d[9]),
                 "+v"(d[10]), "+v"(d[11]), "+v"(d[12]), "+v"(d[13]) :: "memory");
    const u32 tlo = (u32)((((u64)ep.y << 32) | ep.x) + TAG_SALT);
    if (!cols) {
#pragma unroll
        for (int h = 0; h < 2; ++h) {
            float s0 = 0.f, s1 = 0.f, q0 = 0.f, q1 = 0.f;
#pragma unroll
            for (int i = 0; i < 7; ++i)
#pragma unroll
                for (int e = 0; e < 4; e += 2) {
                    s0 = dot2(d[7 * h + i][e], 0x3f803f80u, s0); q0 = dot2(d[7 * h + i][e], d[7 * h + i][e], q0);
                    s1 = dot2(d[7 * h + i][e + 1], 0x3f803f80u, s1); q1 = dot2(d[7 * h + i][e + 1], d[7 * h + i][e + 1], q1);
                }
            const float mean = dpp_xor_sum8(s0 + s1) * (1.f / 448.f);
            const float var = fmaxf(dpp_xor_sum8(q0 + q1) * (1.f / 448.f) - mean * mean, 0.f);
            const float sd = __builtin_amdgcn_sqrtf(rbf(rbf(var) + 1e-5f));
            if (c8 == 0) gst8c<0>((u32)(WS_RG + (256 * xcd + 64 * j + 16 * wave + 8 * h + r8) * 8), ws, u32x2{pk2(mean, sd), tlo});
        }
    } else {
        float a[8] = {};
        u32 klo = 0x00003f80u, khi = 0x3f800000u; PIN(klo); PIN(khi);
#pragma unroll
        for (int i = 0; i < 14; ++i)
#pragma unroll
            for (int e = 0; e < 4; ++e) { a[2 * e] = dot2(d[i][e], klo, a[2 * e]); a[2 * e + 1] = dot2(d[i][e], khi, a[2 * e + 1]); }
        const u32 o = lds0 + ((wave - 4) * 8 + r8) * 272 + c8 * 32;
        sts16(o, __builtin_bit_cast(u32x4, f32x4{a[0], a[1], a[2], a[3]})); sts16(o + 16, __builtin_bit_cast(u32x4, f32x4{a[4], a[5], a[6], a[7]}));
    }
    BARL();
    if (wave == 4 && ctile < 14) {
        float t0 = 0.f, t1 = 0.f;
#pragma unroll
        for (int i = 0; i < 32; i += 2) { t0 += *(const LAS float*)(lds0 + i * 272 + lane * 4); t1 += *(const LAS float*)(lds0 + (i + 1) * 272 + lane * 4); }
        gst8c<0>((u32)(WS_CG + (((xcd & 1) * 14 + ctile) * 64 + lane) * 8), ws, u32x2{pk2(t0 + t1, (t0 + t1) - rbf(t0 + t1)), tlo});
    }
}

__global__ __launch_bounds__(512, 2) void fused_kernel(Params p) {
    const int wave = __builtin_amdgcn_readfirstlane(threadIdx.x >> 6);
    if (blockIdx.x < NHELP) { body_help(p, wave); return; }
    if (threadIdx.x < 32) *(LAS u32*)((u32)(size_t)(LAS char*)smem + L_FLAG + threadIdx.x * 4) = 0u;
    else if (threadIdx.x < 64) *(LAS u32*)((u32)(size_t)(LAS char*)smem + F_CONS + ((threadIdx.x - 32) >> 2) * 144 + (threadIdx.x & 3) * 4) = 0u;
    BARL();
    if (wave < 4) body_g(p, wave); else body_c(p, wave - 4);
}

extern "C" void kernel_launch(void* const* d_in, const int* in_sizes, int n_in,
                              void* d_out, int out_size, void* d_ws, size_t ws_size,
                              hipStream_t stream) {
    Params p;
    p.x = (const u16*)d_in[0]; p.k = (const u16*)d_in[1]; p.v = (const u16*)d_in[2]; p.shk = (const u16*)d_in[3];
    p.ssmk = (const u16*)d_in[4]; p.ssmd = (const u16*)d_in[5]; p.w1 = (const u16*)d_in[7]; p.b1 = (const u16*)d_in[8];
    p.w2 = (const u16*)d_in[9]; p.b2 = (const u16*)d_in[10];
    p.out = (u16*)d_out; p.ws = (char*)d_ws;
    (void)hipFuncSetAttribute((const void*)fused_kernel, hipFuncAttributeMaxDynamicSharedMemorySize, LDS_BYTES);
    fused_kernel<<<NWG + NHELP, 512, LDS_BYTES, stream>>>(p);
}
```

```cpp
#define CODE_LINES 272
#include <hip/hip_runtime.h>
#include <stdint.h>
#include <type_traits>

typedef unsigned short u16;
typedef unsigned int u32;
typedef unsigned long long u64;
typedef u32 u32x4 __attribute__((ext_vector_type(4)));
typedef u32 u32x2 __attribute__((ext_vector_type(2)));
typedef float f32x4 __attribute__((ext_vector_type(4)));
typedef float f32x2 __attribute__((ext_vector_type(2)));
typedef float f32x16 __attribute__((ext_vector_type(16)));
typedef __bf16 bf16x8 __attribute__((ext_vector_type(8)));
typedef __bf16 bf16x2 __attribute__((ext_vector_type(2)));
typedef short s16x4 __attribute__((ext_vector_type(4)));
#define LAS __attribute__((address_space(3)))
#define GAS __attribute__((address_space(1)))
#define DI __device__ __forceinline__

constexpr int S_ = 2048, D_ = 448, F_ = 1792;
constexpr int NHELP = 32, NWG = 224;
constexpr u32 MB = 256;
constexpr u32 WS_EPOCH = 0, WS_ALL = 2048, WS_KVF = 4096, WS_HF = WS_KVF + 65536, WS_YF = WS_HF + 65536, WS_RG = WS_YF + 65536, WS_CG = WS_RG + 16384;
constexpr u32 TP = 8192 + 128;
constexpr u32 WS_H = 1u << 20, WS_KVT = 9u << 20, WS_YT = 13u << 20;
constexpr u32 STAMP_OFF = 24u << 20;
constexpr int NSTAMP = 22;
constexpr u32 L_PIECE = 16384, L_W = 65536, L_LOC0 = 131072, L_MISC = 147456, LDS_BYTES = 163840;
constexpr u32 KVRAW_SZ = 5632, L_KVS = L_LOC0 + 4 * KVRAW_SZ, L_B2 = L_KVS + 9216, L_FLAG = L_B2 + 128, L_ROWF = L_FLAG + 128, L_B2F = L_ROWF + 256, L_R2 = L_B2F + 256;
constexpr u32 L_SCR = L_B2F;
static_assert(L_R2 + 256 <= LDS_BYTES, "lds");
constexpr u32 F_TOEP = L_FLAG, F_HCNT = L_FLAG + 16, F_HRDY = L_FLAG + 20, F_KVRDY = L_FLAG + 24, F_YCNT = L_FLAG + 28, F_YRDY = L_FLAG + 32, F_S0 = L_FLAG + 36, F_KVCNT = L_FLAG + 40, F_W = L_FLAG + 48, F_LAND = L_FLAG + 64, F_XCH = L_FLAG + 96, F_KVS = L_FLAG + 124, F_CONS = L_KVS + 128;
constexpr u32 F_GP = F_CONS + 8 * 144, F_WF = F_CONS + 9 * 144;
constexpr u32 L_CONV = 16384, CONV_CH = 65536, TOEP_CP = 4160, TOEP_CH = 4 * TOEP_CP, U_SZ = 4608, YS_SZ = 2304;
constexpr u64 TAG_SALT = 0x9E3779B97F4A7C15ull;
constexpr int SPIN_CAP = 1 << 15;
#define SLP 2

#define ST(i) do {} while (0)
#define STP nullptr

DI void dma16(u32 voff, const void* sbase, u32 lds) {
    asm volatile("s_mov_b32 m0, %2\n\ts_nop 0\n\tglobal_load_lds_dwordx4 %0, %1" :: "v"(voff), "s"(sbase), "s"(lds) : "memory");
}
DI void setm0(u32 lds) { asm volatile("s_mov_b32 m0, %0\n\ts_nop 0" :: "s"(lds) : "memory"); }
template <int OFF> DI void dmai(u32 voff, const void* sbase) { asm volatile("global_load_lds_dwordx4 %0, %1 offset:%2" :: "v"(voff), "s"(sbase), "i"(OFF) : "memory"); }
DI void bdma16(u32 voff, u32x4 srd, u32 lds) {
    asm volatile("s_mov_b32 m0, %2\n\ts_nop 0\n\tbuffer_load_dwordx4 %0, %1, 0 offen lds" :: "v"(voff), "s"(srd), "s"(lds) : "memory");
}
#define SB() __builtin_amdgcn_sched_barrier(0)
template <int OFF> DI u32x2 alds8(u32 a) { u32x2 r; asm volatile("ds_read_b64 %0, %1 offset:%2" : "=v"(r) : "v"(a), "i"(OFF)); return r; }
template <int OFF> DI u32x4 alds16(u32 a) { u32x4 r; asm volatile("ds_read_b128 %0, %1 offset:%2" : "=v"(r) : "v"(a), "i"(OFF)); return r; }
DI u32x4 gld16(u32 voff, const void* sbase) { u32x4 r; asm volatile("global_load_dwordx4 %0, %1, %2" : "=v"(r) : "v"(voff), "s"(sbase) : "memory"); return r; }
template <int OFF> DI u32x4 gld16c(u32 voff, const void* sbase) { u32x4 r; asm volatile("global_load_dwordx4 %0, %1, %2 offset:%3 sc1" : "=v"(r) : "v"(voff), "s"(sbase), "i"(OFF) : "memory"); return r; }
DI u32x2 gld8c(u32 voff, const void* sbase) { u32x2 r; asm volatile("global_load_dwordx2 %0, %1, %2 sc1" : "=v"(r) : "v"(voff), "s"(sbase) : "memory"); return r; }
DI void touch(const void* addr, u32 lds) { asm volatile("s_mov_b32 m0, %1\n\ts_nop 0\n\tglobal_load_lds_dword %0, off" :: "v"(addr), "s"(lds) : "memory"); }
DI u32 gld2(u32 voff, const void* sbase) { u32 r; asm volatile("global_load_ushort %0, %1, %2" : "=v"(r) : "v"(voff), "s"(sbase) : "memory"); return r; }
DI u32x4 bld16(u32 voff, u32x4 srd) { u32x4 r; asm volatile("buffer_load_dwordx4 %0, %1, %2, 0 offen" : "=v"(r) : "v"(voff), "s"(srd) : "memory"); return r; }
template <int OFF> DI u32x4 gld16o(u32 voff, const void* sbase) { u32x4 r; asm volatile("global_load_dwordx4 %0, %1, %2 offset:%3" : "=v"(r) : "v"(voff), "s"(sbase), "i"(OFF) : "memory"); return r; }
template <int OFF> DI void gst16c(u32 voff, void* sbase, u32x4 v) { asm volatile("global_store_dwordx4 %0, %1, %2 offset:%3 sc1\n\ts_nop 1" :: "v"(voff), "v"(v), "s"(sbase), "i"(OFF) : "memory"); }
template <int OFF> DI void gst8c(u32 voff, void* sbase, u32x2 v) { asm volatile("global_store_dwordx2 %0, %1, %2 offset:%3 sc1" :: "v"(voff), "v"(v), "s"(sbase), "i"(OFF) : "memory"); }
#define WAITV(n) asm volatile("s_waitcnt vmcnt(%0)" :: "i"(n) : "memory")
#define WAITBAR(n) asm volatile("s_waitcnt vmcnt(%0) lgkmcnt(0)\n\ts_barrier" :: "i"(n) : "memory")
#define BARL() asm volatile("s_waitcnt lgkmcnt(0)\n\ts_barrier" ::: "memory")
#define PIN(x) asm volatile("" : "+v"(x))

DI u32x4 mksrd(const void* p, u32 bytes) { u64 a = (u64)p; return u32x4{(u32)a, (u32)(a >> 32) & 0xffffu, bytes, 0x00020000u}; }
DI float bflo(u32 x) { return __uint_as_float(x << 16); }
DI float bfhi(u32 x) { return __uint_as_float(x & 0xffff0000u); }
DI u32 pk2(float a, float b) { bf16x2 v = {(__bf16)a, (__bf16)b}; return __builtin_bit_cast(u32, v); }
DI float rbf(float a) { return bflo(pk2(a, a)); }
DI float relu(float a) { int i = __float_as_int(a); return __int_as_float(i > 0 ? i : 0); }
DI float fma1(float a, float b, float c) { float r; asm("v_fma_f32 %0, %1, %2, %3" : "=v"(r) : "v"(a), "v"(b), "v"(c)); return r; }
DI float mul1(float a, float b) { float r; asm("v_mul_f32 %0, %1, %2" : "=v"(r) : "v"(a), "v"(b)); return r; }
DI float add1(float a, float b) { float r; asm("v_add_f32 %0, %1, %2" : "=v"(r) : "v"(a), "v"(b)); return r; }
DI float dot2(u32 a, u32 b, float c) { return __builtin_amdgcn_fdot2_f32_bf16(__builtin_bit_cast(bf16x2, a), __builtin_bit_cast(bf16x2, b), c, false); }
DI u32x4 lds16(u32 a) { return *(const LAS u32x4*)a; }
DI u32x2 lds8(u32 a) { return *(const LAS u32x2*)a; }
DI void sts16(u32 a, u32x4 v) { *(LAS u32x4*)a = v; }
DI void sts8(u32 a, u32x2 v) { *(LAS u32x2*)a = v; }
DI u32x2 ldtr(u32 a) { return __builtin_bit_cast(u32x2, __builtin_amdgcn_ds_read_tr16_b64_v4i16((LAS s16x4*)a)); }
DI u32x4 cat(u32x2 a, u32x2 b) { return u32x4{a.x, a.y, b.x, b.y}; }
DI f32x16 mfma32(u32x4 a, u32x4 b, f32x16 c) { return __builtin_amdgcn_mfma_f32_32x32x16_bf16(__builtin_bit_cast(bf16x8, a), __builtin_bit_cast(bf16x8, b), c, 0, 0, 0); }
DI f32x4 mfma16(u32x4 a, u32x4 b, f32x4 c) { return __builtin_amdgcn_mfma_f32_16x16x32_bf16(__builtin_bit_cast(bf16x8, a), __builtin_bit_cast(bf16x8, b), c, 0, 0, 0); }
DI float xhalf_sum(float v) { auto r = __builtin_amdgcn_permlane32_swap(__float_as_uint(v), __float_as_uint(v), false, false); return __uint_as_float(r[0]) + __uint_as_float(r[1]); }

struct Params {
    const u16 *x, *k, *v, *shk, *ssmk, *ssmd, *w1, *b1, *w2, *b2;
    u16* out; char* ws;
};

DI void poll8(u32 voff, const void* sbase, bool need, u32 tlo, u32 thi) {
    _Pragma("clang loop unroll(disable)") for (int spin = 0; spin < SPIN_CAP; ++spin) {
        u32x2 f = gld8c(voff, sbase);
        asm volatile("s_waitcnt vmcnt(0)" : "+v"(f) :: "memory");
        bool ok = !need || (f.x == tlo && f.y == thi);
        if (__all(ok)) break;
        __builtin_amdgcn_s_sleep(SLP);
    }
}
DI void poll16(u32 voff, const void* sbase, u32 tlo, u32 thi) {
    _Pragma("clang loop unroll(disable)") for (int spin = 0; spin < SPIN_CAP; ++spin) {
        u32x4 f = gld16c<0>(voff, sbase);
        asm volatile("s_waitcnt vmcnt(0)" : "+v"(f) :: "memory");
        bool ok = f.x == tlo && f.y == thi && f.z == tlo && f.w == thi;
        if (__all(ok)) break;
        __builtin_amdgcn_s_sleep(SLP);
    }
}

template <int SL> DI void probe(u32 voff, const void* sbase, u32 tag) {
    _Pragma("clang loop unroll(disable)") for (int spin = 0; spin < SPIN_CAP; ++spin) {
        u32x2 f = gld8c(voff, sbase);
        asm volatile("s_waitcnt vmcnt(0)" : "+v"(f) :: "memory");
        if (__all(f.y == tag)) break;
        __builtin_amdgcn_s_sleep(SL);
    }
}
DI bool lds_arrive_last(u32 addr, u32 n) {
    u32 old = 0;
    if ((threadIdx.x & 63) == 0) old = __hip_atomic_fetch_add((LAS u32*)addr, 1u, __ATOMIC_RELAXED, __HIP_MEMORY_SCOPE_WORKGROUP);
    return (u32)__builtin_amdgcn_readfirstlane(old) == n - 1;
}
DI void lds_wait(u32 addr) {
    _Pragma("clang loop unroll(disable)") for (int spin = 0; spin < (1 << 22); ++spin) {
        if (__builtin_amdgcn_readfirstlane(*(volatile LAS u32*)addr)) break;
        __builtin_amdgcn_s_sleep(1);
    }
    asm volatile("" ::: "memory");
}
DI void lds_add(u32 addr) { asm volatile("" ::: "memory"); __hip_atomic_fetch_add((LAS u32*)addr, 1u, __ATOMIC_RELAXED, __HIP_MEMORY_SCOPE_WORKGROUP); asm volatile("" ::: "memory"); }
DI u32 lds_peek(u32 addr) { return *(volatile LAS u32*)addr; }
DI void lds_wait_ge(u32 addr, u32 n, u32 first) {
    u32 v = first;
    _Pragma("clang loop unroll(disable)") for (int spin = 0; spin < (1 << 22) && (u32)__builtin_amdgcn_readfirstlane(v) < n; ++spin) { __builtin_amdgcn_s_sleep(1); v = lds_peek(addr); }
    asm volatile("" ::: "memory");
}
template <int t> DI void cons_set(u32 lds0, int g) { asm volatile("s_waitcnt lgkmcnt(0)\n\tds_write_b32 %0, %1 offset:%2" :: "v"(lds0 + 131072 + g * 4), "v"(1u), "i"(F_CONS + t * 144 - 131072) : "memory"); }
DI void cons_wait(u32 lds0, int t) {
    _Pragma("clang loop unroll(disable)") for (int spin = 0; spin < (1 << 22); ++spin) {
        const u32x4 v = *(volatile LAS u32x4*)(lds0 + F_CONS + t * 144);
        if (__builtin_amdgcn_readfirstlane(v.x & v.y & v.z & v.w)) break;
        __builtin_amdgcn_s_sleep(1);
    }
    asm volatile("" ::: "memory");
}
DI void lds_set(u32 addr) { asm volatile("" ::: "memory"); if ((threadIdx.x & 63) == 0) *(volatile LAS u32*)addr = 1u; }
constexpr u32 loc_of(int c) { return (c % 5) == 0 ? L_LOC0 : (c % 5) == 1 ? 3 * L_PIECE : (c % 5) == 2 ? L_W + 3 * L_PIECE : (c % 5) == 3 ? 0u : L_W; }

#define CPRIO 2
#define YSLP 4
#define YFIRST 0
#define PRIOALL 0
#define LW 2
#define WEARLY 0
#define WDEPTH 8
#define TW 0
#define WC0 14
#define RSLP 8
#define HWAIT 2
#define KW 0
#define CROTM 0
#define ROTM 1
#define LDELAY 0
#define KDELAY 0
#define HDV 4
constexpr int HD = HDV, HS = HD + 1;
constexpr int WS_ = 2;
constexpr int g2_younger(int c) {
    const int n = c + 1;
    if (n >= 14) return 63;
    bool sh = false, sw = n < LW || n >= WC0; int ah = 0, aw = sw ? 1000 : 0;
    for (int i = LW; i < 5; ++i) { if (sw) aw += 4; if (i == n) sw = true; }
    for (int i = 0; i < HD; ++i) { if (sh) ah += 4; if (sw) aw += 4; if (i == n) sh = true; }
    for (int i = 0; i < c; ++i) {
        if (i + HD < 14) { if (sh) ah += 4; if (sw) aw += 4; if (i + HD == n) sh = true; }
        if (i + 5 < WC0) { if (sh) ah += 4; if (sw) aw += 4; if (i + 5 == n) sw = true; }
    }
    return ah < aw ? ah : aw;
}

struct G2Ctx { const char* w2b; const char* hb; int crot, fh2; u32 voffw[4], ldsg, vlane, ta[3][2], smask, fl; };
template <int C> DI int g2_chunk(const G2Ctx& x) {
    int c = C + x.crot; if (c >= 14) c -= 14;
    return c;
}
template <int C> DI const char* g2_wsrc(const G2Ctx& x) { return x.w2b + (size_t)g2_chunk<C>(x) * (128 * 896); }
template <int C> DI u32 g2_loc(const G2Ctx& x) { return loc_of(C) + x.ldsg; }
template <int C, int N> DI void g2_w1(const G2Ctx& x, const char* sb) { if constexpr (N == 0) setm0(g2_loc<C>(x)); dmai<N * 1024>(x.voffw[N], sb); }
template <int C> DI void g2_issue_w(const G2Ctx& x) { const char* sb = g2_wsrc<C>(x); g2_w1<C, 0>(x, sb); g2_w1<C, 1>(x, sb); g2_w1<C, 2>(x, sb); g2_w1<C, 3>(x, sb); }
template <int C> DI const char* g2_hsrc(const G2Ctx& x) { return x.hb + g2_chunk<C>(x) * 8192; }
template <int N> DI u32x4 g2_h1(const G2Ctx& x, const char* sb) { return gld16c<(N & 1) * 1024>(x.vlane + (N >> 1) * 112 * 1024, sb); }
template <int C> DI void g2_issue_h(const G2Ctx& x, u32x4 (&hf)[HS][4]) {
    const char* sb = g2_hsrc<C>(x);
    hf[C % HS][0] = g2_h1<0>(x, sb); hf[C % HS][1] = g2_h1<1>(x, sb); hf[C % HS][2] = g2_h1<2>(x, sb); hf[C % HS][3] = g2_h1<3>(x, sb);
}
template <int C> DI void g2_read(const G2Ctx& x, u32x4 (&wf)[WS_][4]) {
    const u32 a0 = x.ta[loc_of(C) >> 16][0], a1 = x.ta[loc_of(C) >> 16][1];
    constexpr u32 lo = loc_of(C) & 0xffffu;
#pragma unroll
    for (int ks = 0; ks < 2; ++ks) {
        wf[C % WS_][ks * 2] = cat(ldtr(a0 + lo + ks * 2048), ldtr(a0 + lo + ks * 2048 + 1024));
        wf[C % WS_][ks * 2 + 1] = cat(ldtr(a1 + lo + ks * 2048), ldtr(a1 + lo + ks * 2048 + 1024));
    }
}
template <int C, int I> DI void g2_rd(const G2Ctx& x, u32x4 (&wf)[WS_][4]) {
    if constexpr (C < 14) {
        const u32 a = x.ta[loc_of(C) >> 16][I & 1]; constexpr u32 lo = (loc_of(C) & 0xffffu) + (I >> 1) * 2048;
        wf[C % WS_][I] = cat(ldtr(a + lo), ldtr(a + lo + 1024));
    }
}
DI u32 chk(u32 d, u32 m, u32 bad) { return __builtin_amdgcn_bitop3_b32(d, m, bad, 0xBE); }
template <int C> DI void g2_retry(const G2Ctx& x, u32x4 (&hf)[HS][4], u32 bad, u64* stamps) {
    if (__builtin_expect(!__any((bad & 0x80008000u) != 0u), 1)) return;
    _Pragma("clang loop unroll(disable)") for (int spin = 0; spin < SPIN_CAP; ++spin) {
        __builtin_amdgcn_s_sleep(RSLP);
        g2_issue_h<C>(x, hf);
        asm volatile("s_waitcnt vmcnt(0)" : "+v"(hf[C % HS][0]), "+v"(hf[C % HS][1]), "+v"(hf[C % HS][2]), "+v"(hf[C % HS][3]) :: "memory");
        bad = 0u;
#pragma unroll
        for (int i = 0; i < 16; ++i) { hf[C % HS][i >> 2][i & 3] ^= x.smask; bad |= hf[C % HS][i >> 2][i & 3]; }
        if (!__any((bad & 0x80008000u) != 0u)) break;
    }
}
template <int C> DI void g2_step(const G2Ctx& x, u32x4 (&hf)[HS][4], f32x16 (&acc)[2][2], u32x4 (&wf)[WS_][4], u32 pk, u64* stamps) {
    constexpr int N = (C + 1 < 14 ? C + 1 : C) % HS;
    asm volatile("s_waitcnt vmcnt(%4) lgkmcnt(0)" : "+v"(hf[N][0]), "+v"(hf[N][1]), "+v"(hf[N][2]), "+v"(hf[N][3]) : "i"(g2_younger(C)) : "memory");
    if constexpr (C % 3 == 0) ST(14 + C / 3);
    if constexpr (C + 5 >= WC0 && C + 5 < 14) asm volatile("ds_write_b32 %0, %1 offset:%2" :: "v"(x.fl), "v"(C + 1), "i"(F_GP - 131072) : "memory");
    if constexpr (C + 1 >= WC0 && C + 1 < 14) lds_wait_ge(x.fl + (F_WF - 131072), C + 2, pk);
    SB();
    constexpr bool DH = C + HD < 14, DW = C + 5 < WC0, DC = C + 1 < 14;
    const char* hs = g2_hsrc<(DH ? C + HD : 0)>(x);
    const char* wsrc = g2_wsrc<(DW ? C + 5 : 0)>(x);
    u32 bad = 0u;
#define G2M(i) acc[((i) >> 1) & 1][(i) & 1] = mfma32(wf[C % WS_][((i) >> 2) * 2 + (((i) >> 1) & 1)], hf[C % HS][((i) & 1) * 2 + ((i) >> 2)], acc[((i) >> 1) & 1][(i) & 1]); \
               if constexpr (DC) { hf[N][(i) >> 1][((i) & 1) * 2] ^= x.smask; hf[N][(i) >> 1][((i) & 1) * 2 + 1] ^= x.smask; bad = bad | hf[N][(i) >> 1][((i) & 1) * 2] | hf[N][(i) >> 1][((i) & 1) * 2 + 1]; }
    G2M(0); g2_rd<C + 1, 0>(x, wf); if constexpr (DH) hf[(C + HD) % HS][0] = g2_h1<0>(x, hs); SB();
    G2M(1); g2_rd<C + 1, 1>(x, wf); if constexpr (DH) hf[(C + HD) % HS][1] = g2_h1<1>(x, hs); SB();
    G2M(2); g2_rd<C + 1, 2>(x, wf); if constexpr (DH) hf[(C + HD) % HS][2] = g2_h1<2>(x, hs); SB();
    G2M(3); g2_rd<C + 1, 3>(x, wf); if constexpr (DH) hf[(C + HD) % HS][3] = g2_h1<3>(x, hs); SB();
    G2M(4); if constexpr (DW) g2_w1<C + 5, 0>(x, wsrc); SB();
    G2M(5); if constexpr (DW) g2_w1<C + 5, 1>(x, wsrc); SB();
    G2M(6); if constexpr (DW) g2_w1<C + 5, 2>(x, wsrc); SB();
    G2M(7); if constexpr (DW) g2_w1<C + 5, 3>(x, wsrc); if constexpr (C + 2 >= WC0 && C + 2 < 14) pk = lds_peek(x.fl + (F_WF - 131072)); SB();
#undef G2M
    if constexpr (DC) { g2_retry<C + 1>(x, hf, bad, stamps); g2_step<C + 1>(x, hf, acc, wf, pk, stamps); }
}

struct CF { u32x2 a0[8], a1[8]; u32x4 b[8]; };
template <int S, int R> DI void cv_ld(CF& f, u32 aA, u32 aB) {
    f.a0[R] = alds8<R * 512 + S * 64>(aA); f.a1[R] = alds8<R * 512 + S * 64 + 8>(aA);
    f.b[R] = alds16<(224 - 32 * R + 4 * S) * 16>(aB);
}
DI void cv_wait(CF& f) {
    asm volatile("s_waitcnt lgkmcnt(0)" : "+v"(f.a0[0]), "+v"(f.a0[1]), "+v"(f.a0[2]), "+v"(f.a0[3]), "+v"(f.a0[4]), "+v"(f.a0[5]), "+v"(f.a0[6]), "+v"(f.a0[7]),
                 "+v"(f.a1[0]), "+v"(f.a1[1]), "+v"(f.a1[2]), "+v"(f.a1[3]), "+v"(f.a1[4]), "+v"(f.a1[5]), "+v"(f.a1[6]), "+v"(f.a1[7]),
                 "+v"(f.b[0]), "+v"(f.b[1]), "+v"(f.b[2]), "+v"(f.b[3]), "+v"(f.b[4]), "+v"(f.b[5]), "+v"(f.b[6]), "+v"(f.b[7]));
}
constexpr int cv_r(int m) { int r = 0; while (m >= 8 - r) { m -= 8 - r; ++r; } return r; }
constexpr int cv_c(int m) { int r = 0; while (m >= 8 - r) { m -= 8 - r; ++r; } return m; }
template <int S, int M> DI void cv_mm(CF& f, CF& n, u32 aA, u32 aB, f32x4 (&acc)[8]) {
    constexpr int r = cv_r(M), c = cv_c(M);
    acc[r + c] = mfma16(cat(f.a0[r], f.a1[r]), f.b[c], acc[r + c]);
    if constexpr ((M & 3) == 0 && M / 4 < 8) cv_ld<S + 1, M / 4>(n, aA, aB);
    SB();
    if constexpr (M + 1 < 36) cv_mm<S, M + 1>(f, n, aA, aB, acc);
}
DI void conv_body(u32 aA, u32 aB, f32x4 (&acc)[8]) {
    CF f, n;
    asm volatile("s_waitcnt lgkmcnt(0)" ::: "memory");
    cv_ld<0, 0>(f, aA, aB); cv_ld<0, 1>(f, aA, aB); cv_ld<0, 2>(f, aA, aB); cv_ld<0, 3>(f, aA, aB);
    cv_ld<0, 4>(f, aA, aB); cv_ld<0, 5>(f, aA, aB); cv_ld<0, 6>(f, aA, aB); cv_ld<0, 7>(f, aA, aB);
    _Pragma("clang loop unroll(disable)") for (int it = 0; it < 2; ++it) {
        cv_wait(f); SB();
        cv_mm<0, 0>(f, n, aA, aB, acc);
        cv_wait(n); SB();
        cv_mm<1, 0>(n, f, aA, aB, acc);
        aA += 128; aB += 128;
    }
    cv_wait(f);
    asm volatile("" ::: "memory");
}

struct Geo { int bi, b, xcd, fh2, RB, CT, tb64, db, rot, crot; u32 R0, F0, T0, D0; };
DI Geo make_geo() {
    Geo g; g.b = blockIdx.x - NHELP; g.xcd = g.b & 7; const int bi = g.b >> 3; g.bi = bi;
    const int rq = g.xcd >> 1, rb = bi / 7, ct = bi - rb * 7;
    g.fh2 = g.xcd & 1; g.RB = 4 * rq + rb; g.CT = 7 * g.fh2 + ct; g.R0 = 128 * g.RB; g.F0 = 128 * g.CT;
    g.tb64 = 2 * g.RB + g.fh2; g.db = ct; g.T0 = 64 * g.tb64; g.D0 = 64 * g.db;

    g.rot = g.xcd >= 7 ? 0 : g.xcd;
     g.crot = CROTM == 4 ? (2 * g.xcd) % 14 : CROTM == 0 ? g.CT : CROTM == 1 ? (g.CT + 3 * rb + (rb >> 1)) % 14 : CROTM == 2 ? 2 * ct : (2 * ct + 4 * rb - (rb == 3)) % 14;
    return g;
}
DI void make_g2(G2Ctx& g2, const Params& p, const Geo& q, int g, int lane, u32 lds0) {
    g2.w2b = (const char*)p.w2 + (size_t)(32 * g) * 896 + q.D0 * 2;
    g2.hb = p.ws + WS_H + (size_t)(q.tb64 * 2 * 112 + 2 * g) * 1024;
    g2.crot = q.crot; g2.fh2 = q.fh2;
#pragma unroll
    for (int n = 0; n < 4; ++n) g2.voffw[n] = (u32)((lane >> 3) * 896 + 16 * ((lane & 7) ^ (((lane >> 4) & 1) << 2)) + n * (8 * 896 - 1024));
    g2.ldsg = lds0 + g * 4096; g2.vlane = lane * 16u;
}
DI u32 slab_base(u32 lds0, int cw) { return lds0 + L_CONV + (cw >> 1) * CONV_CH + (cw & 1) * 8192; }
DI void final_epilogue(const Params& p, const Geo& q, u32 lds0) {
    int tz = threadIdx.x; PIN(tz);
    const int t = tz >> 3, c = tz & 7;
    const u32 pb = lds0 + ((t >> 5) ? 0u : 3 * L_PIECE) + ((t >> 4) & 1) * L_W + (t & 15) * 256;
    const u32 o0 = pb + 16 * ((2 * c) ^ (t & 7)), o1 = o0 ^ 16;
    f32x4 pa[4], pb_[4];
#pragma unroll
    for (int g = 0; g < 4; ++g) { pa[g] = __builtin_bit_cast(f32x4, lds16(o0 + g * 4096)); pb_[g] = __builtin_bit_cast(f32x4, lds16(o1 + g * 4096)); }
    const float r2 = *(const LAS float*)(lds0 + L_R2 + t * 4);
    const f32x4 b0 = __builtin_bit_cast(f32x4, lds16(lds0 + L_B2F + c * 32)), b1 = __builtin_bit_cast(f32x4, lds16(lds0 + L_B2F + c * 32 + 16));
    const u32 sb = slab_base(lds0, c >> 1) + t * 64 + (c & 1) * 32;
    const f32x4 sv0 = __builtin_bit_cast(f32x4, lds16(sb)), sv1 = __builtin_bit_cast(f32x4, lds16(sb + 16));
    const f32x4 xv0 = __builtin_bit_cast(f32x4, lds16(sb + 4096)), xv1 = __builtin_bit_cast(f32x4, lds16(sb + 4096 + 16));
    SB();
    const f32x4 s0 = (pa[0] + pa[1]) + (pa[2] + pa[3]), s1 = (pb_[0] + pb_[1]) + (pb_[2] + pb_[3]);
    f32x4 z0 = s0 * r2 + b0, z1 = s1 * r2 + b1;
    f32x4 e0 = z0 * -1.4426950408889634f, e1 = z1 * -1.4426950408889634f;
#pragma unroll
    for (int j = 0; j < 4; ++j) { e0[j] = __builtin_amdgcn_exp2f(e0[j]); e1[j] = __builtin_amdgcn_exp2f(e1[j]); }
    e0 += 1.f; e1 += 1.f;
#pragma unroll
    for (int j = 0; j < 4; ++j) { e0[j] = __builtin_amdgcn_rcpf(e0[j]); e1[j] = __builtin_amdgcn_rcpf(e1[j]); }
    z0 = z0 * e0; z1 = z1 * e1;
    z0 = z0 * sv0 + xv0; z1 = z1 * sv1 + xv1;
    const u32x4 res = {pk2(z0[0], z0[1]), pk2(z0[2], z0[3]), pk2(z1[0], z1[1]), pk2(z1[2], z1[3])};
    *(GAS u32x4*)((GAS char*)p.out + (size_t)(q.T0 + t) * 896 + q.D0 * 2 + c * 16) = res;
}
#define STAMP_DECL
#define STAMP_OUT(wave) do {} while (0)

extern __shared__ __attribute__((aligned(16))) char smem[];

struct CL { const char *xb, *wb; u32 vx[4], vw[4], sl; int rot; };
struct CSrc { const char *xs, *wsrc; };
template <int ST_> DI CSrc c_src(const CL& c) { int kc = ST_ + c.rot; if (kc >= 7) kc -= 7; return CSrc{c.xb + kc * 128, c.wb + (size_t)kc * 64 * 3584}; }
template <int ST_, int N> DI void c_dma1(const CL& c, const CSrc& s) {
    if constexpr (N == 0) setm0(c.sl + (ST_ & 3) * L_PIECE);
    if constexpr (N == 4) setm0(c.sl + (ST_ & 3) * L_PIECE + L_W);
    if constexpr (N < 4) dmai<N * 1024>(c.vx[N], s.xs); else dmai<(N - 4) * 1024>(c.vw[N - 4], s.wsrc);
}
template <int ST_> DI void c_dma(const CL& c) {
    const CSrc s = c_src<ST_>(c);
    c_dma1<ST_, 0>(c, s); c_dma1<ST_, 1>(c, s); c_dma1<ST_, 2>(c, s); c_dma1<ST_, 3>(c, s);
    c_dma1<ST_, 4>(c, s); c_dma1<ST_, 5>(c, s); c_dma1<ST_, 6>(c, s); c_dma1<ST_, 7>(c, s);
}
DI void make_cl(CL& c, const Params& p, const Geo& q, int cw, int lane, u32 lds0) {
    const int r8 = lane >> 3, c8 = lane & 7, r4 = lane >> 4, c16 = lane & 15;
#pragma unroll
    for (int n = 0; n < 4; ++n) {
        c.vx[n] = (u32)((32 * cw + r8) * 896 + 16 * (c8 ^ (((n & 1) << 2) | (r8 >> 1))) + n * (8 * 896 - 1024));
        c.vw[n] = (u32)((16 * cw + r4) * 3584 + 16 * (c16 ^ (r4 << 2)) + n * (4 * 3584 - 1024));
    }
    c.xb = (const char*)p.x + (size_t)q.R0 * 896; c.wb = (const char*)p.w1 + q.F0 * 2;
    c.sl = lds0 + cw * 4096; c.rot = q.rot;
}

template <int ST_> DI void l_dma(const CL& c) {
    const CSrc s = c_src<ST_>(c);
    const u32 sl = c.sl + (ST_ & 3) * L_PIECE;
    setm0(sl); dmai<0>(c.vx[0], s.xs); dmai<1024>(c.vx[1], s.xs); dmai<2048>(c.vx[2], s.xs); dmai<3072>(c.vx[3], s.xs);
    setm0(sl + L_W); dmai<0>(c.vw[0], s.wsrc); dmai<1024>(c.vw[1], s.wsrc); dmai<2048>(c.vw[2], s.wsrc); dmai<3072>(c.vw[3], s.wsrc);
    const char* x2 = s.xs + 32 * 896; const char* w2 = s.wsrc + 16 * 3584;
    setm0(sl + 4096); dmai<0>(c.vx[0], x2); dmai<1024>(c.vx[1], x2); dmai<2048>(c.vx[2], x2); dmai<3072>(c.vx[3], x2);
    setm0(sl + L_W + 4096); dmai<0>(c.vw[0], w2); dmai<1024>(c.vw[1], w2); dmai<2048>(c.vw[2], w2); dmai<3072>(c.vw[3], w2);
}

struct Fr { u32x4 x[4][2], w[4][2]; };
struct GA { u32 ax[4], aw[2]; };
template <int ST_> DI void g1_read(const GA& a, Fr& f) {
    constexpr u32 so = (ST_ & 3) * L_PIECE;
#pragma unroll
    for (int k = 0; k < 4; ++k) {
        f.w[k][0] = cat(ldtr(a.aw[0] + so + k * 4096), ldtr(a.aw[0] + so + k * 4096 + 1024));
        f.x[k][0] = lds16(a.ax[k] + so);
        f.w[k][1] = cat(ldtr(a.aw[1] + so + k * 4096), ldtr(a.aw[1] + so + k * 4096 + 1024));
        f.x[k][1] = lds16(a.ax[k] + so + 4096);
    }
}
template <int K0> DI void g1_mma(const Fr& f, f32x16 (&acc)[2][2]) {
#pragma unroll
    for (int k = K0; k < K0 + 2; ++k) {
        acc[0][0] = mfma32(f.w[k][0], f.x[k][0], acc[0][0]); acc[1][0] = mfma32(f.w[k][1], f.x[k][0], acc[1][0]);
        acc[0][1] = mfma32(f.w[k][0], f.x[k][1], acc[0][1]); acc[1][1] = mfma32(f.w[k][1], f.x[k][1], acc[1][1]);
    }
    SB();
}

template <int ST_, int I> DI void g1_rd(const GA& a, Fr& f) {
    constexpr u32 so = (ST_ & 3) * L_PIECE; constexpr int k = I >> 1, h = I & 1;
    f.w[k][h] = cat(ldtr(a.aw[h] + so + k * 4096), ldtr(a.aw[h] + so + k * 4096 + 1024));
    f.x[k][h] = lds16(a.ax[k] + so + h * 4096);
}
template <int K0, int I> DI void g1_m1(const Fr& f, f32x16 (&acc)[2][2]) {
    constexpr int k = K0 + (I >> 2), mt = I & 1, st = (I >> 1) & 1;
    acc[mt][st] = mfma32(f.w[k][mt], f.x[k][st], acc[mt][st]);
}
template <int ST_, bool CARRY> DI void g1_stage(const GA& a, const Fr& fp, Fr& fn, f32x16 (&acc)[2][2], u32 lds0, int g, u32& pk) {
    if constexpr (CARRY) { g1_m1<2, 0>(fp, acc); SB(); cons_set<ST_ - 1>(lds0, g); SB(); }
    lds_wait_ge(lds0 + F_LAND + 4 * ST_, ST_ == 0 ? 256 : (ST_ == 3 || ST_ == 6) ? 128 : 64, pk);
    if constexpr (CARRY) {
        g1_rd<ST_, 0>(a, fn); SB();
        g1_m1<2, 1>(fp, acc); SB(); g1_m1<2, 2>(fp, acc); g1_rd<ST_, 1>(a, fn); SB();
        g1_m1<2, 3>(fp, acc); SB(); g1_m1<2, 4>(fp, acc); g1_rd<ST_, 2>(a, fn); SB();
        g1_m1<2, 5>(fp, acc); g1_rd<ST_, 3>(a, fn); SB(); g1_m1<2, 6>(fp, acc); SB();
        g1_m1<2, 7>(fp, acc); SB();
    } else { g1_rd<ST_, 0>(a, fn); g1_rd<ST_, 1>(a, fn); g1_rd<ST_, 2>(a, fn); g1_rd<ST_, 3>(a, fn); SB(); }
    g1_m1<0, 0>(fn, acc); g1_rd<ST_, 4>(a, fn); SB(); g1_m1<0, 1>(fn, acc); SB();
    g1_m1<0, 2>(fn, acc); g1_rd<ST_, 5>(a, fn); SB(); g1_m1<0, 3>(fn, acc); g1_rd<ST_, 6>(a, fn); SB();
    g1_m1<0, 4>(fn, acc); g1_rd<ST_, 7>(a, fn); SB(); g1_m1<0, 5>(fn, acc); pk = lds_peek(lds0 + F_LAND + 4 * ST_ + 4); SB();
    g1_m1<0, 6>(fn, acc); SB(); g1_m1<0, 7>(fn, acc); SB();
}

DI void body_g(const Params& p, const int g) {
    STAMP_DECL
    const u32 lds0 = (u32)(size_t)(LAS char*)smem;
    const int lane = threadIdx.x & 63;
    const Geo q = make_geo();
    char* ws = p.ws;
    const int hi = lane >> 5, s31 = lane & 31;
    const int r8 = lane >> 3, c8 = lane & 7;
    const u32 raw = lds0 + L_LOC0 + g * KVRAW_SZ;
    {
        const u32 vkv = (u32)(r8 * 896 + c8 * 16);
        bdma16((u32)(((int)q.T0 + 16 * g - 8) * 896 + (int)q.D0 * 2) + vkv, mksrd(p.k, S_ * D_ * 2), raw);
        const char* kb = (const char*)p.k + (size_t)(q.T0 + 16 * g) * 896 + q.D0 * 2;
        const char* vb = (const char*)p.v + (size_t)(q.T0 + 16 * g) * 896 + q.D0 * 2;
        dmai<1024>(vkv, kb - 1024); dmai<2048>(vkv, kb + 8 * 896 - 2048); dmai<3072>(vkv, vb - 3072);
        setm0(raw + 4096);
        dmai<0>(vkv, vb + 8 * 896);
        if (lane < 32) dmai<1024>(lane * 16u, (const char*)p.shk + q.D0 * 8 - 1024);
        CL c; make_cl(c, p, q, g, lane, lds0);
        c_dma<0>(c);
    }
    u32x2 ep = gld8c(0u, ws + WS_EPOCH);
    const int fh = g & 1, sh = g >> 1;
    GA ga;
    {
        const int blk = (lane >> 4) & 1, qq = (lane & 15) >> 2, pp = lane & 3;
        const u32 Z = (u32)(hi ^ ((s31 >> 1) & 7));
#pragma unroll
        for (int k = 0; k < 4; ++k) ga.ax[k] = lds0 + (64 * sh + s31) * 128 + 16 * ((2 * k) ^ Z);
        ga.aw[0] = lds0 + L_W + (8 * hi + qq) * 256 + 16 * ((8 * fh + 2 * blk + (pp >> 1)) ^ (qq << 2)) + 8 * (pp & 1);
        ga.aw[1] = ga.aw[0] ^ 64;
    }
    WAITV(9);
    {
        u32x4 wr[4], kr[5], vr[2];
#pragma unroll
        for (int e = 0; e < 4; ++e) wr[e] = lds16(raw + 5120 + c8 * 64 + e * 16);
#pragma unroll
        for (int j = 0; j < 5; ++j) kr[j] = lds16(raw + (5 + 2 * r8 + j) * 128 + c8 * 16);
        vr[0] = lds16(raw + 3072 + 2 * r8 * 128 + c8 * 16); vr[1] = lds16(raw + 3072 + (2 * r8 + 1) * 128 + c8 * 16);
        SB();
        u32x4 o0, o1;
#pragma unroll
        for (int e = 0; e < 4; ++e) {
            u32 pl[4], ph[4];
#pragma unroll
            for (int j = 0; j < 4; ++j) { pl[j] = __builtin_amdgcn_perm(kr[j][e], kr[j + 1][e], 0x05040100u); ph[j] = __builtin_amdgcn_perm(kr[j][e], kr[j + 1][e], 0x07060302u); }
            f32x2 a = {dot2(pl[0], wr[e].y, dot2(pl[2], wr[e].x, 0.f)), dot2(ph[0], wr[e].w, dot2(ph[2], wr[e].z, 0.f))};
            f32x2 b = {dot2(pl[1], wr[e].y, dot2(pl[3], wr[e].x, 0.f)), dot2(ph[1], wr[e].w, dot2(ph[3], wr[e].z, 0.f))};
            const u32 ar = pk2(a.x, a.y), br = pk2(b.x, b.y);
            a = f32x2{bflo(ar), bfhi(ar)} * f32x2{bflo(vr[0][e]), bfhi(vr[0][e])}; b = f32x2{bflo(br), bfhi(br)} * f32x2{bflo(vr[1][e]), bfhi(vr[1][e])};
            o0[e] = pk2(a.x, a.y); o1[e] = pk2(b.x, b.y);
        }
        sts16(lds0 + L_KVS + (16 * g + 2 * r8) * 144 + c8 * 16, o0);
        sts16(lds0 + L_KVS + (16 * g + 2 * r8 + 1) * 144 + c8 * 16, o1);
        asm volatile("s_waitcnt lgkmcnt(0)" ::: "memory");
        lds_add(lds0 + F_KVS);
    }
    f32x16 acc[2][2] = {};
    Fr fa, fb;
    WAITV(1);
    lds_add(lds0 + F_LAND);
    u32 pk = lds_peek(lds0 + F_LAND);
    ST(2);
    g1_stage<0, false>(ga, fb, fa, acc, lds0, g, pk);
    g1_stage<1, true>(ga, fa, fb, acc, lds0, g, pk);
    g1_stage<2, true>(ga, fb, fa, acc, lds0, g, pk);
    asm volatile("s_waitcnt vmcnt(0)" : "+v"(ep) :: "memory");
    const u64 epoch = ((u64)(u32)__builtin_amdgcn_readfirstlane(ep.y) << 32) | (u32)__builtin_amdgcn_readfirstlane(ep.x);
    const u64 tag = epoch + TAG_SALT;
    const u32 tlo = (u32)tag, thi = (u32)(tag >> 32);
    const u32 smask = ((lane ^ (u32)epoch) & 1u) ? 0x80008000u : 0u;
    g1_stage<3, true>(ga, fa, fb, acc, lds0, g, pk);
    ST(3);
    const u32 rgo = WS_RG + (q.R0 + 64 * sh + lane) * 8, cgo = WS_CG + (q.F0 + 64 * fh + lane) * 8;
    u32x2 rg = gld8c(rgo, ws), cg = gld8c(cgo, ws);
    u32 b1v = gld2((u32)((64 * fh + lane) * 2), (const char*)p.b1 + q.F0 * 2);
    g1_stage<4, true>(ga, fb, fa, acc, lds0, g, pk);
    asm volatile("s_waitcnt vmcnt(0)" : "+v"(rg), "+v"(cg), "+v"(b1v) :: "memory");
    if (!__all(rg.y == tlo && cg.y == tlo)) {
        rg = gld8c(rgo, ws); cg = gld8c(cgo, ws);
    }
    g1_stage<5, true>(ga, fa, fb, acc, lds0, g, pk);
    asm volatile("s_waitcnt vmcnt(0)" : "+v"(rg), "+v"(cg) :: "memory");
    if (!__all(rg.y == tlo && cg.y == tlo)) {
        rg = gld8c(rgo, ws); cg = gld8c(cgo, ws);
    }
    g1_stage<6, true>(ga, fb, fa, acc, lds0, g, pk);
    cons_set<6>(lds0, g);
    ST(5);
    asm volatile("s_waitcnt vmcnt(0)" : "+v"(rg), "+v"(cg), "+v"(b1v) :: "memory");
    _Pragma("clang loop unroll(disable)") for (int spin = 0; spin < SPIN_CAP && !__all(rg.y == tlo && cg.y == tlo); ++spin) {
        rg = gld8c(rgo, ws); cg = gld8c(cgo, ws);
        asm volatile("s_waitcnt vmcnt(0)" : "+v"(rg), "+v"(cg) :: "memory");
    }
    if (fh == 0 && sh == q.fh2) *(LAS u32*)(lds0 + L_ROWF + lane * 4) = rg.x;
    g1_mma<2>(fa, acc);
    {
        u32x4 ax[2], bx[2];
        const auto rr = __builtin_amdgcn_permlane32_swap(rg.x, rg.x, false, false);
        const auto cc = __builtin_amdgcn_permlane32_swap(cg.x, cg.x, false, false);
        const auto bb = __builtin_amdgcn_permlane32_swap(b1v, b1v, false, false);
#pragma unroll
        for (int i = 0; i < 2; ++i) {
            const u32 nm = __builtin_amdgcn_perm(rr[i], rr[i], 0x01000100u) ^ 0x80008000u;
            ax[i] = u32x4{cc[i], bb[i], 0u, 0u};
            bx[i] = u32x4{hi ? 0u : nm, hi ? 0u : (rr[i] >> 16), 0u, 0u};
        }
#pragma unroll
        for (int st = 0; st < 2; ++st)
#pragma unroll
            for (int mt = 0; mt < 2; ++mt) acc[mt][st] = mfma32(ax[mt], bx[st], acc[mt][st]);
#pragma unroll
        for (int st = 0; st < 2; ++st) {
            char* hb = ws + WS_H + (size_t)((4 * q.RB + 2 * sh + st) * 112 + q.CT * 8 + fh * 4) * 1024;
#pragma unroll
            for (int mt = 0; mt < 2; ++mt)
#pragma unroll
                for (int t = 0; t < 2; ++t) {
                    u32x4 hv;
#pragma unroll
                    for (int j = 0; j < 4; ++j) {
                        f32x2 a = {acc[mt][st][8 * t + 2 * j], acc[mt][st][8 * t + 2 * j + 1]};
                        f32x2 u = {relu(a.x), relu(a.y)};
                        u = u * u;
                        hv[j] = pk2(u.x, u.y) ^ smask;
                    }
                    if (mt == 0 && t == 0) gst16c<0>(lane * 16u, hb, hv);
                    if (mt == 0 && t == 1) gst16c<1024>(lane * 16u, hb, hv);
                    if (mt == 1 && t == 0) gst16c<2048>(lane * 16u, hb, hv);
                    if (mt == 1 && t == 1) gst16c<3072>(lane * 16u, hb, hv);
                    SB();
                }
        }
    }
    ST(6);
    G2Ctx g2; make_g2(g2, p, q, g, lane, lds0);
    {
        const int blk = (lane >> 4) & 1, qq = (lane & 15) >> 2, pp = lane & 3;
        const u32 t0 = g2.ldsg + (4 * hi + qq) * 128 + 16 * ((2 * blk + (pp >> 1)) ^ ((qq >> 1) << 2)) + 8 * (pp & 1);
#pragma unroll
        for (int i = 0; i < 3; ++i) { g2.ta[i][0] = t0 + i * 65536; g2.ta[i][1] = (t0 ^ 64) + i * 65536; PIN(g2.ta[i][0]); PIN(g2.ta[i][1]); }
    }
    g2.smask = smask; g2.fl = lds0 + 131072 + g * 4;
    u32x4 wf[WS_][4];
    cons_wait(lds0, 4);
    asm volatile("s_waitcnt lgkmcnt(0)" ::: "memory");
    {
        u32x4 hf[HS][4];
        if (lds_arrive_last(lds0 + F_HCNT, 4)) { if (lane == 0) gst8c<0>(WS_ALL + q.b * 8, ws, u32x2{tlo, thi}); }
        if constexpr (LW <= 2) g2_issue_w<2>(g2);
        if constexpr (LW <= 3) g2_issue_w<3>(g2);
        if constexpr (LW <= 4) g2_issue_w<4>(g2);
        if constexpr (HWAIT == 1) WAITV(12);
        if constexpr (HWAIT == 2) WAITV(0);
        g2_issue_h<0>(g2, hf); g2_issue_h<1>(g2, hf); if constexpr (HD > 2) g2_issue_h<2 % HS>(g2, hf); if constexpr (HD > 3) g2_issue_h<3 % HS>(g2, hf); if constexpr (HD > 4) g2_issue_h<4 % HS>(g2, hf);
        if constexpr (HD > 5) g2_issue_h<5 % HS>(g2, hf);
        ST(9);
        lds_wait(lds0 + F_W);
        if constexpr (KW) lds_wait(lds0 + F_W + 4 + (g >> 1) * 4);
        g2_read<0>(g2, wf);
        asm volatile("s_waitcnt vmcnt(%4)" : "+v"(hf[0][0]), "+v"(hf[0][1]), "+v"(hf[0][2]), "+v"(hf[0][3]) : "i"(4 * (HD - 1)) : "memory");
        {
            u32 bad = 0u;
#pragma unroll
            for (int i = 0; i < 16; ++i) { hf[0][i >> 2][i & 3] ^= smask; bad |= hf[0][i >> 2][i & 3]; }
            g2_retry<0>(g2, hf, bad, STP);
        }
        ST(7);
        f32x16 a2[2][2] = {};
        g2_step<0>(g2, hf, a2, wf, 0u, STP);
        ST(8);
        const u32 pw = g2.ldsg + (s31 >> 4) * L_W + (s31 & 15) * 256;
#pragma unroll
        for (int s2 = 0; s2 < 2; ++s2)
#pragma unroll
            for (int dt = 0; dt < 2; ++dt)
#pragma unroll
                for (int gr = 0; gr < 4; ++gr) {
                    const u32 a = pw + (s2 ? 0u : 3 * L_PIECE) + 16 * ((8 * dt + 2 * gr + hi) ^ (s31 & 7));
                    sts16(a, __builtin_bit_cast(u32x4, f32x4{a2[dt][s2][4 * gr], a2[dt][s2][4 * gr + 1], a2[dt][s2][4 * gr + 2], a2[dt][s2][4 * gr + 3]}));
                }
    }
    BARL(); ST(10);
    final_epilogue(p, q, lds0);
    STAMP_OUT(g);
}

DI void body_c(const Params& p, const int cw) {
    STAMP_DECL
    const u32 lds0 = (u32)(size_t)(LAS char*)smem;
    const int lane = threadIdx.x & 63;
    const Geo q = make_geo();
    char* ws = p.ws;
    const int hi = lane >> 5, s31 = lane & 31;
    const int half = cw & 1, chl = cw >> 1, chan = 2 * q.b + chl;
    if (PRIOALL) __builtin_amdgcn_s_setprio(CPRIO);
    u32x2 ep;
    u32x4 xs[4]; u32 dv;
    if (cw < 2) {
        CL c, c2; make_cl(c, p, q, 0, lane, lds0); c2 = c; c2.xb += 64 * 896; c2.wb += 32 * 3584; c2.sl += 8192;
        ep = gld8c(0u, ws + WS_EPOCH);
        if (cw == 0) {
            l_dma<1>(c); l_dma<1>(c2); ST(1);
            WAITV(0); lds_add(lds0 + F_LAND + 4); ST(2);
            cons_wait(lds0, 0); l_dma<4>(c); l_dma<4>(c2); ST(19);
            WAITV(0); lds_add(lds0 + F_LAND + 16); ST(20);
        } else {
            l_dma<2>(c); l_dma<2>(c2); ST(1);
            WAITV(0); lds_add(lds0 + F_LAND + 8); ST(2);
            cons_wait(lds0, 1); l_dma<5>(c); l_dma<5>(c2); ST(19);
            WAITV(0); lds_add(lds0 + F_LAND + 20); ST(20);
        }
        asm volatile("s_waitcnt vmcnt(0)" : "+v"(ep) :: "memory");
        const u32 tg32 = (u32)((((u64)ep.y << 32) | ep.x) + TAG_SALT);
        if (cw == 1) {
            G2Ctx g2; make_g2(g2, p, q, 0, lane, lds0);
            cons_wait(lds0, 3);
#define W4(C) do { const char* sb = g2_wsrc<C>(g2); _Pragma("unroll") for (int i = 0; i < 4; ++i) { setm0(g2_loc<C>(g2) + i * 4096); \
                dmai<0>(g2.voffw[0], sb + i * 32 * 896); dmai<1024>(g2.voffw[1], sb + i * 32 * 896); dmai<2048>(g2.voffw[2], sb + i * 32 * 896); dmai<3072>(g2.voffw[3], sb + i * 32 * 896); } } while (0)
            W4(0); W4(1);
            if constexpr (LW > 2) { WAITV(16); W4(2); }
            if constexpr (LW > 3) { cons_wait(lds0, 4); WAITV(16); W4(3); }
            if constexpr (LW > 4) { WAITV(16); W4(4); }
#undef W4
            dv = gld2(0u, (const char*)p.ssmd + chan * 2);
            WAITV(1);
            lds_set(lds0 + F_W);
            asm volatile("s_waitcnt vmcnt(0)" : "+v"(dv) :: "memory");
            ST(7);
        } else {
            const char* kb2 = ws + WS_KVT + (size_t)(2 * q.b) * TP;
            u32x4 u[16];
            probe<2>((u32)(hi * TP + s31 * 256), kb2, tg32);
            _Pragma("clang loop unroll(disable)") for (int spin = 0; spin < SPIN_CAP; ++spin) {
#pragma unroll
                for (int c = 0; c < 4; ++c) {
                    const char* sb = kb2 + (c >> 1) * TP + (c & 1) * 4096;
                    u[4 * c] = gld16c<0>(lane * 16u, sb); u[4 * c + 1] = gld16c<1024>(lane * 16u, sb); u[4 * c + 2] = gld16c<2048>(lane * 16u, sb); u[4 * c + 3] = gld16c<3072>(lane * 16u, sb);
                }
                asm volatile("s_waitcnt vmcnt(0)" : "+v"(u[0]), "+v"(u[1]), "+v"(u[2]), "+v"(u[3]), "+v"(u[4]), "+v"(u[5]), "+v"(u[6]), "+v"(u[7]), "+v"(u[8]), "+v"(u[9]), "+v"(u[10]),
                             "+v"(u[11]), "+v"(u[12]), "+v"(u[13]), "+v"(u[14]), "+v"(u[15]) :: "memory");
                u32 bad = 0u;
#pragma unroll
                for (int i = 0; i < 16; ++i) bad = chk(u[i].w, tg32, chk(u[i].y, tg32, bad));
                if (!__any(bad != 0u)) break;
                __builtin_amdgcn_s_sleep(4);
            }
            ST(7);
            dv = gld2(0u, (const char*)p.ssmd + chan * 2);
            cons_wait(lds0, 5); cons_wait(lds0, 6);
            ST(14);
            const u32 uq_ = lds0 + L_CONV + TOEP_CH + lane * 8;
#pragma unroll
            for (int i = 0; i < 16; ++i) sts8(uq_ + (i >> 3) * CONV_CH + (i & 7) * 512, u32x2{u[i].x, u[i].z});
            sts8(uq_ + 4096, u32x2{0u, 0u}); sts8(uq_ + CONV_CH + 4096, u32x2{0u, 0u});
            asm volatile("s_waitcnt lgkmcnt(0)" ::: "memory");
            lds_add(lds0 + F_TOEP); lds_add(lds0 + F_TOEP + 4);
            asm volatile("s_waitcnt vmcnt(0)" : "+v"(dv) :: "memory");
        }
    } else {
        const int kw = cw - 2;
        if (kw == 0 && lane < 8) dma16(lane * 16u, (const char*)p.b2 + q.D0 * 2, lds0 + L_B2);
        ep = gld8c(0u, ws + WS_EPOCH);
        CL c; make_cl(c, p, q, 2 * kw, lane, lds0);
        l_dma<3>(c);
        lds_wait_ge(lds0 + F_KVS, 256, lds_peek(lds0 + F_KVS));
        ST(2);
        asm volatile("s_waitcnt vmcnt(16)" : "+v"(ep) :: "memory");
        const u32 tg32 = (u32)((((u64)ep.y << 32) | ep.x) + TAG_SALT);
        {
            const int grp = lane >> 4, qq = (lane & 15) >> 2, pp = lane & 3;
            const u32 a = lds0 + L_KVS + (16 * grp + 4 * pp + 3 - qq) * 144 + kw * 64;
            const u32 vo = (u32)((q.D0 + 32 * kw + (lane & 3)) * TP + (2044 - (int)q.T0 - 4 * (lane >> 2)) * 4);
            u32x2 tr[8];
#pragma unroll
            for (int i = 0; i < 8; ++i) tr[i] = ldtr(a + 8 * i);
            SB();
#pragma unroll
            for (int i = 0; i < 8; ++i) gst16c<0>(vo, ws + WS_KVT + i * 4 * TP, u32x4{tr[i].x, tg32, tr[i].y, tg32});
        }
        ST(20);
        WAITV(8);
        lds_add(lds0 + F_LAND + 12);
        ST(19);
#pragma unroll
        for (int i = 0; i < TW; ++i) { const int L = ((2 * i + kw) * 28 + q.bi) * 64 + lane; if (2 * i + kw < 7) touch((const char*)p.w2 + (u32)(12543 - L) * 128u, lds0 + L_SCR); }
        cons_wait(lds0, 2);
        l_dma<6>(c);
        ST(3);
        u32x4 sk[5];
        const u32x4 sksrd = mksrd((const char*)p.ssmk + (size_t)(2 * q.b + kw) * 4096, 4096);
#pragma unroll
        for (int r = 0; r < 5; ++r) sk[r] = bld16((u32)((lane - 1 + 63 * r) * 16), sksrd);
        { const char* xsl = (const char*)p.x + (size_t)q.T0 * 896 + (q.D0 + 32 * kw) * 2;
          xs[0] = gld16o<0>((u32)(lane * 896), xsl); xs[1] = gld16o<16>((u32)(lane * 896), xsl); xs[2] = gld16o<32>((u32)(lane * 896), xsl); xs[3] = gld16o<48>((u32)(lane * 896), xsl); }
        dv = gld2(0u, (const char*)p.ssmd + chan * 2);
        WAITV(10);
        lds_add(lds0 + F_LAND + 24);
        ST(21);
        if constexpr (KW) {
            G2Ctx ga_, gb_; make_g2(ga_, p, q, 2 * kw, lane, lds0); gb_ = ga_; gb_.w2b += 32 * 896; gb_.ldsg += 4096;
            cons_wait(lds0, 3); g2_issue_w<2>(ga_); g2_issue_w<2>(gb_);
            cons_wait(lds0, 4); g2_issue_w<3>(ga_); g2_issue_w<3>(gb_); g2_issue_w<4>(ga_); g2_issue_w<4>(gb_);
        }
        asm volatile("s_waitcnt vmcnt(0)" : "+v"(sk[0]), "+v"(sk[1]), "+v"(sk[2]), "+v"(sk[3]), "+v"(sk[4]), "+v"(xs[0]), "+v"(xs[1]), "+v"(xs[2]), "+v"(xs[3]), "+v"(dv) :: "memory");
        u32x4 tw[5][4];
#pragma unroll
        for (int r = 0; r < 5; ++r) {
            const u32x4 B = sk[r];
            u32x4 A;
#pragma unroll
            for (int j = 0; j < 4; ++j) A[j] = __builtin_amdgcn_update_dpp(0u, B[j], 0x138  , 0xf, 0xf, false);
            const u32 e0 = __builtin_amdgcn_alignbit(A.y, A.x, 16), e1 = __builtin_amdgcn_alignbit(A.z, A.y, 16), e2 = __builtin_amdgcn_alignbit(A.w, A.z, 16),
                      e3 = __builtin_amdgcn_alignbit(B.x, A.w, 16), e4 = __builtin_amdgcn_alignbit(B.y, B.x, 16);
            tw[r][0] = u32x4{e0, e1, e2, e3}; tw[r][1] = u32x4{A.y, A.z, A.w, B.x}; tw[r][2] = u32x4{e1, e2, e3, e4}; tw[r][3] = u32x4{A.z, A.w, B.x, B.y};
            PIN(tw[r][0]); PIN(tw[r][1]); PIN(tw[r][2]); PIN(tw[r][3]);
        }
        cons_wait(lds0, 5); cons_wait(lds0, 6);
        ST(5);
        const u32 tq = lds0 + L_CONV + kw * CONV_CH + lane * 16;
#pragma unroll
        for (int r = 0; r < 5; ++r) {
            if ((lane > 0 || r == 0) && (r < 4 || lane < 6)) {
#pragma unroll
                for (int c = 0; c < 4; ++c) sts16(tq + r * 1008 + c * TOEP_CP, tw[r][c]);
            }
        }
        asm volatile("s_waitcnt lgkmcnt(0)" ::: "memory");
        lds_add(lds0 + F_TOEP + kw * 4);
        WAITV(0);
        lds_set(lds0 + F_W + 4 + kw * 4);
    }
    const u64 epoch = ((u64)(u32)__builtin_amdgcn_readfirstlane(ep.y) << 32) | (u32)__builtin_amdgcn_readfirstlane(ep.x);
    const u64 tag = epoch + TAG_SALT;
    const u32 tlo = (u32)tag, thi = (u32)(tag >> 32);
    const u32 tbase = lds0 + L_CONV + chl * CONV_CH;
    const u32 ub = tbase + TOEP_CH;
    ST(6);
    lds_wait_ge(lds0 + F_TOEP + chl * 4, 128, lds_peek(lds0 + F_TOEP + chl * 4));
    ST(15);
    const int bb = lane & 15, gg = lane >> 4;
    const u32 aA = tbase + (bb & 3) * TOEP_CP + ((bb >> 2) + 2 * gg) * 8 + half * 256;
    const u32 aB = ub + (30 - 2 * bb + gg) * 16 + half * 256;
    f32x4 ac8[8] = {};
    if (!PRIOALL) __builtin_amdgcn_s_setprio(CPRIO);
    conv_body(aA, aB, ac8);
    ST(16);
    f32x4 acc[4];
    {
        const u32 xo = ub + U_SZ + lane * 16;
#pragma unroll
        for (int i = 0; i < 4; ++i) { const f32x4 give = half ? ac8[i] : ac8[4 + i]; acc[i] = half ? ac8[4 + i] : ac8[i]; sts16(xo + half * 4096 + i * 1024, __builtin_bit_cast(u32x4, give)); }
        asm volatile("s_waitcnt lgkmcnt(0)" ::: "memory");
        lds_set(lds0 + F_XCH + cw * 4);
        lds_wait(lds0 + F_XCH + (cw ^ 1) * 4);
        f32x4 got[4];
#pragma unroll
        for (int i = 0; i < 4; ++i) got[i] = __builtin_bit_cast(f32x4, lds16(xo + (half ^ 1) * 4096 + i * 1024));
        SB();
#pragma unroll
        for (int i = 0; i < 4; ++i) acc[i] = f32x4{add1(acc[i][0], got[i][0]), add1(acc[i][1], got[i][1]), add1(acc[i][2], got[i][2]), add1(acc[i][3], got[i][3])};
    }
    ST(8);
    {
        const float dd = bflo(dv);
        char* yb = ws + WS_YT + (size_t)chan * TP;
        u32x2 uq[4];
#pragma unroll
        for (int i = 0; i < 4; ++i) uq[i] = lds8(ub + (2044 - (256 * (4 * half + i) + 16 * bb + 4 * gg)) * 2);
        SB();
#pragma unroll
        for (int i = 0; i < 4; ++i) {
            const int qt = 4 * half + i;
            const int t = 256 * qt + 16 * bb + 4 * gg;
            const u32x2 uu = uq[i];
            const u32 c01 = pk2(acc[i][0], acc[i][1]), c23 = pk2(acc[i][2], acc[i][3]);
            const u32 d01 = pk2(mul1(bfhi(uu.y), dd), mul1(bflo(uu.y), dd)), d23 = pk2(mul1(bfhi(uu.x), dd), mul1(bflo(uu.x), dd));
            float y0 = add1(bflo(d01), bflo(c01)), y1 = add1(bfhi(d01), bfhi(c01));
            float y2 = add1(bflo(d23), bflo(c23)), y3 = add1(bfhi(d23), bfhi(c23));
            gst16c<0>((u32)(t * 4), yb, u32x4{pk2(y0, y1), tlo, pk2(y2, y3), tlo});
        }
        lds_arrive_last(lds0 + F_YCNT, 4);
    }
    if (cw < 2) {
        G2Ctx g2; make_g2(g2, p, q, 2 * cw, lane, lds0);
        const u32 gp = lds0 + F_GP + cw * 8, wf_ = lds0 + F_WF + cw * 8;
#define SERVE(C) do { if constexpr ((C) >= WC0) { const char* sb = g2_wsrc<C>(g2); \
            const u32x2 pr = *(volatile LAS u32x2*)gp; \
            if ((C) > WC0 && (u32)__builtin_amdgcn_readfirstlane(pr.x < pr.y ? pr.x : pr.y) < (C) - 4) { WAITV(0); sts8(wf_, u32x2{(C), (C)}); } \
            _Pragma("unroll") for (int i = 0; i < 2; ++i) { lds_wait_ge(gp + i * 4, (C) - 4, i ? pr.y : pr.x); setm0(g2_loc<C>(g2) + i * 4096); \
                dmai<0>(g2.voffw[0], sb + i * 32 * 896); dmai<1024>(g2.voffw[1], sb + i * 32 * 896); dmai<2048>(g2.voffw[2], sb + i * 32 * 896); dmai<3072>(g2.voffw[3], sb + i * 32 * 896); } \
            if constexpr ((C) > WC0 + 1) { WAITV(16); sts8(wf_, u32x2{(C) - 1, (C) - 1}); } } } while (0)
        SERVE(5); SERVE(6); SERVE(7); SERVE(8); SERVE(9); SERVE(10); SERVE(11); SERVE(12); SERVE(13);
#undef SERVE
        WAITV(8); sts8(wf_, u32x2{13u, 13u});
        WAITV(0); sts8(wf_, u32x2{14u, 14u});
        ST(9);
    } else {
        const int kw = cw - 2;
        lds_wait_ge(lds0 + F_YCNT, 4, lds_peek(lds0 + F_YCNT)); lds_wait_ge(lds0 + F_HCNT, 4, lds_peek(lds0 + F_HCNT));
        const u32 slab = slab_base(lds0, 2 * kw) + lane * 64;
        float xn[32];
        {
            const u32 rf = *(const LAS u32*)(lds0 + L_ROWF + lane * 4);
            const float nm = -bflo(rf); float ri = __builtin_amdgcn_rcpf(bfhi(rf));
            asm volatile("s_nop 1" : "+v"(ri));
#pragma unroll
            for (int j = 0; j < 8; ++j) {
                const u32 lo = xs[j >> 1][(j & 1) * 2], hi_ = xs[j >> 1][(j & 1) * 2 + 1];
                const f32x4 v = {bflo(lo), bfhi(lo), bflo(hi_), bfhi(hi_)};
                sts16(slab + (j >> 2) * 8192 + 4096 + (j & 3) * 16, __builtin_bit_cast(u32x4, v));
#pragma unroll
                for (int e = 0; e < 4; e += 2) {
                    const u32 dx = pk2(add1(v[e], nm), add1(v[e + 1], nm)), xr = pk2(mul1(bflo(dx), ri), mul1(bfhi(dx), ri));
                    xn[4 * j + e] = bflo(xr); xn[4 * j + e + 1] = bfhi(xr);
                }
            }
            if (kw == 0) *(LAS float*)(lds0 + L_R2 + lane * 4) = ri * ri;
            else *(LAS float*)(lds0 + L_B2F + lane * 4) = bflo(*(const LAS u16*)(lds0 + L_B2 + lane * 2));
        }
        const char* yb = ws + WS_YT + (size_t)(q.D0 + 32 * kw) * TP + q.T0 * 4;
        const u32 vo = (u32)(gg * TP + bb * 16);
        u32x4 y[8];
        probe<YSLP>((u32)(s31 * TP), yb, tlo);
        _Pragma("clang loop unroll(disable)") for (int spin = 0; spin < SPIN_CAP; ++spin) {
#pragma unroll
            for (int i = 0; i < 8; ++i) y[i] = gld16c<0>(vo, yb + i * 4 * TP);
            asm volatile("s_waitcnt vmcnt(0)" : "+v"(y[0]), "+v"(y[1]), "+v"(y[2]), "+v"(y[3]), "+v"(y[4]), "+v"(y[5]), "+v"(y[6]), "+v"(y[7]) :: "memory");
            u32 bad = 0u;
#pragma unroll
            for (int i = 0; i < 8; ++i) bad = chk(y[i].w, tlo, chk(y[i].y, tlo, bad));
            if (!__any(bad != 0u)) break;
            __builtin_amdgcn_s_sleep(4);
        }
        ST(9);
        const u32 ys = lds0 + L_CONV + kw * CONV_CH + TOEP_CH + U_SZ;
#pragma unroll
        for (int i = 0; i < 8; ++i) sts8(ys + (4 * i + gg) * 144 + bb * 8, u32x2{y[i].x, y[i].z});
        const int qq = (lane & 15) >> 2, pp = lane & 3;
        const u32 a = ys + qq * 144 + gg * 32 + pp * 8;
        u32x2 t[8];
#pragma unroll
        for (int j = 0; j < 8; ++j) t[j] = ldtr(a + j * 4 * 144);
        SB();
#pragma unroll
        for (int j = 0; j < 8; ++j)
        {
            const u32 s01 = pk2(mul1(bflo(t[j].x), xn[4 * j]), mul1(bfhi(t[j].x), xn[4 * j + 1])), s23 = pk2(mul1(bflo(t[j].y), xn[4 * j + 2]), mul1(bfhi(t[j].y), xn[4 * j + 3]));
            sts16(slab + (j >> 2) * 8192 + (j & 3) * 16, u32x4{s01 << 16, s01 & 0xffff0000u, s23 << 16, s23 & 0xffff0000u});
        }
        if (q.b == 0 && kw == 1) {
            _Pragma("clang loop unroll(disable)") for (int spin = 0; spin < SPIN_CAP; ++spin) {
                u32x4 f0 = gld16c<0>(lane * 16u, ws + WS_ALL), f1 = gld16c<1024>((lane < 48 ? lane : 0) * 16u, ws + WS_ALL);
                asm volatile("s_waitcnt vmcnt(0)" : "+v"(f0), "+v"(f1) :: "memory");
                bool ok = f0.x == tlo && f0.y == thi && f0.z == tlo && f0.w == thi && f1.x == tlo && f1.y == thi && f1.z == tlo && f1.w == thi;
                if (__all(ok)) break;
                __builtin_amdgcn_s_sleep(SLP);
            }
            if (lane == 0) { const u64 e1 = epoch + 1; gst8c<0>(0u, ws + WS_EPOCH, u32x2{(u32)e1, (u32)(e1 >> 32)}); }
        }
    }
    __builtin_amdgcn_s_setprio(0);
    BARL(); ST(10);
    final_epilogue(p, q, lds0);
    STAMP_OUT(cw + 4);
}

DI float dpp_xor_sum8(float v) {
    v += __uint_as_float(__builtin_amdgcn_mov_dpp(__float_as_uint(v), 0xB1, 0xf, 0xf, true));
    v += __uint_as_float(__builtin_amdgcn_mov_dpp(__float_as_uint(v), 0x4E, 0xf, 0xf, true));
    v += __uint_as_float(__builtin_amdgcn_mov_dpp(__float_as_uint(v), 0x141, 0xf, 0xf, true));
    return v;
}
DI void body_help(const Params& p, const int wave) {
    const u32 lds0 = (u32)(size_t)(LAS char*)smem;
    const int lane = threadIdx.x & 63, r8 = lane >> 3, c8 = lane & 7;
    const int xcd = blockIdx.x & 7, j = blockIdx.x >> 3;
    char* ws = p.ws;
    u32x2 ep = gld8c(0u, ws + WS_EPOCH);
    u32x4 d[14];
    const int ctile = (xcd >> 1) * 4 + j;
    const bool cols = wave >= 4;
    if (!cols) {
        const char* xb = (const char*)p.x + (size_t)(256 * xcd + 64 * j + 16 * wave) * 896;
#pragma unroll
        for (int i = 0; i < 14; ++i) d[i] = gld16((u32)((r8 + 8 * (i / 7)) * 896 + (i % 7) * 128 + c8 * 16), xb);
    } else {
        const char* wb = (const char*)p.w1 + (size_t)(112 * (wave - 4)) * 3584 + ((xcd & 1) * 14 + (ctile < 14 ? ctile : 0)) * 128;
#pragma unroll
        for (int i = 0; i < 14; ++i) d[i] = gld16((u32)((r8 + 8 * i) * 3584 + c8 * 16), wb);
    }
    setm0(lds0 + 16384 + wave * 4096);
#pragma unroll
    for (int c = 0; c < WEARLY; ++c) {
        int cc = c + (2 * xcd) % 14; if (cc >= 14) cc -= 14;
        const char* sb = (const char*)p.w2 + (size_t)cc * 114688 + (j * 8 + wave) * 3584;
        if (lane < 32) dmai<3072>(lane * 16u, sb);
        dmai<0>(lane * 16u, sb); dmai<1024>(lane * 16u, sb); dmai<2048>(lane * 16u, sb);
    }
    asm volatile("s_waitcnt vmcnt(%15)" : "+v"(ep), "+v"(d[0]), "+v"(d[1]), "+v"(d[2]), "+v"(d[3]), "+v"(d[4]), "+v"(d[5]), "+v"(d[6]), "+v"(d[7]), "+v"(d[8]), "+v"(d[9]),
                 "+v"(d[10]), "+v"(d[11]), "+v"(d[12]), "+v"(d[13]) : "i"(4 * WEARLY) : "memory");
    const u32 tlo = (u32)((((u64)ep.y << 32) | ep.x) + TAG_SALT);
    if (!cols) {
#pragma unroll
        for (int h = 0; h < 2; ++h) {
            float s0 = 0.f, s1 = 0.f, q0 = 0.f, q1 = 0.f;
#pragma unroll
            for (int i = 0; i < 7; ++i)
#pragma unroll
                for (int e = 0; e < 4; e += 2) {
                    s0 = dot2(d[7 * h + i][e], 0x3f803f80u, s0); q0 = dot2(d[7 * h + i][e], d[7 * h + i][e], q0);
                    s1 = dot2(d[7 * h + i][e + 1], 0x3f803f80u, s1); q1 = dot2(d[7 * h + i][e + 1], d[7 * h + i][e + 1], q1);
                }
            const float mean = dpp_xor_sum8(s0 + s1) * (1.f / 448.f);
            const float var = fmaxf(dpp_xor_sum8(q0 + q1) * (1.f / 448.f) - mean * mean, 0.f);
            const float sd = __builtin_amdgcn_sqrtf(rbf(rbf(var) + 1e-5f));
            if (c8 == 0) gst8c<0>((u32)(WS_RG + (256 * xcd + 64 * j + 16 * wave + 8 * h + r8) * 8), ws, u32x2{pk2(mean, sd), tlo});
        }
    } else {
        float a[8] = {};
        u32 klo = 0x00003f80u, khi = 0x3f800000u; PIN(klo); PIN(khi);
#pragma unroll
        for (int i = 0; i < 14; ++i)
#pragma unroll
            for (int e = 0; e < 4; ++e) { a[2 * e] = dot2(d[i][e], klo, a[2 * e]); a[2 * e + 1] = dot2(d[i][e], khi, a[2 * e + 1]); }
        const u32 o = lds0 + ((wave - 4) * 8 + r8) * 272 + c8 * 32;
        sts16(o, __builtin_bit_cast(u32x4, f32x4{a[0], a[1], a[2], a[3]})); sts16(o + 16, __builtin_bit_cast(u32x4, f32x4{a[4], a[5], a[6], a[7]}));
    }
    BARL();
    if (wave == 4 && ctile < 14) {
        float t0 = 0.f, t1 = 0.f;
#pragma unroll
        for (int i = 0; i < 32; i += 2) { t0 += *(const LAS float*)(lds0 + i * 272 + lane * 4); t1 += *(const LAS float*)(lds0 + (i + 1) * 272 + lane * 4); }
        gst8c<0>((u32)(WS_CG + (((xcd & 1) * 14 + ctile) * 64 + lane) * 8), ws, u32x2{pk2(t0 + t1, (t0 + t1) - rbf(t0 + t1)), tlo});
    }
    BARL();
    {
        const int crot = (2 * xcd) % 14;
        setm0(lds0 + 16384 + wave * 4096);
        for (int c = WEARLY; c < 14; ++c) {
            int cc = c + crot; if (cc >= 14) cc -= 14;
            const char* sb = (const char*)p.w2 + (size_t)cc * 114688 + (j * 8 + wave) * 3584;
            if (lane < 32) dmai<3072>(lane * 16u, sb);
            dmai<0>(lane * 16u, sb); dmai<1024>(lane * 16u, sb); dmai<2048>(lane * 16u, sb);
            WAITV(WDEPTH);
        }
        WAITV(0);
    }
}

__global__ __launch_bounds__(512, 2) void fused_kernel(Params p) {
    {
        u64 pc; asm volatile("s_getpc_b64 %0" : "=s"(pc));
        if (threadIdx.x < CODE_LINES) touch((const char*)(pc & ~127ull) + threadIdx.x * 128, (u32)(size_t)(LAS char*)smem + L_B2F);
    }
    const int wave = __builtin_amdgcn_readfirstlane(threadIdx.x >> 6);
    if (blockIdx.x < NHELP) { body_help(p, wave); return; }
    if (threadIdx.x < 32) *(LAS u32*)((u32)(size_t)(LAS char*)smem + L_FLAG + threadIdx.x * 4) = 0u;
    else if (threadIdx.x < 72) *(LAS u32*)((u32)(size_t)(LAS char*)smem + F_CONS + ((threadIdx.x - 32) >> 2) * 144 + (threadIdx.x & 3) * 4) = 0u;
    BARL();
    if (wave < 4) body_g(p, wave); else body_c(p, wave - 4);
}

extern "C" void kernel_launch(void* const* d_in, const int* in_sizes, int n_in,
                              void* d_out, int out_size, void* d_ws, size_t ws_size,
                              hipStream_t stream) {
    Params p;
    p.x = (const u16*)d_in[0]; p.k = (const u16*)d_in[1]; p.v = (const u16*)d_in[2]; p.shk = (const u16*)d_in[3];
    p.ssmk = (const u16*)d_in[4]; p.ssmd = (const u16*)d_in[5]; p.w1 = (const u16*)d_in[7]; p.b1 = (const u16*)d_in[8];
    p.w2 = (const u16*)d_in[9]; p.b2 = (const u16*)d_in[10];
    p.out = (u16*)d_out; p.ws = (char*)d_ws;
    (void)hipFuncSetAttribute((const void*)fused_kernel, hipFuncAttributeMaxDynamicSharedMemorySize, LDS_BYTES);
    fused_kernel<<<NWG + NHELP, 512, LDS_BYTES, stream>>>(p);
}
```

```cpp
#define DIAG 0
#define CODE_LINES 272
#include <hip/hip_runtime.h>
#include <stdint.h>
#include <type_traits>

typedef unsigned short u16;
typedef unsigned int u32;
typedef unsigned long long u64;
typedef u32 u32x4 __attribute__((ext_vector_type(4)));
typedef u32 u32x2 __attribute__((ext_vector_type(2)));
typedef float f32x4 __attribute__((ext_vector_type(4)));
typedef float f32x2 __attribute__((ext_vector_type(2)));
typedef float f32x16 __attribute__((ext_vector_type(16)));
typedef __bf16 bf16x8 __attribute__((ext_vector_type(8)));
typedef __bf16 bf16x2 __attribute__((ext_vector_type(2)));
typedef short s16x4 __attribute__((ext_vector_type(4)));
#define LAS __attribute__((address_space(3)))
#define GAS __attribute__((address_space(1)))
#define DI __device__ __forceinline__

constexpr int S_ = 2048, D_ = 448, F_ = 1792;
constexpr int NHELP = 32, NWG = 224;
constexpr u32 MB = 256;
constexpr u32 WS_EPOCH = 0, WS_ALL = 2048, WS_KVF = 4096, WS_HF = WS_KVF + 65536, WS_YF = WS_HF + 65536, WS_RG = WS_YF + 65536, WS_CG = WS_RG + 16384;
constexpr u32 TP = 8192 + 128;
constexpr u32 WS_H = 1u << 20, WS_KVT = 9u << 20, WS_YT = 13u << 20;
constexpr u32 STAMP_OFF = 24u << 20;
constexpr int NSTAMP = 22;
constexpr u32 L_PIECE = 16384, L_W = 65536, L_LOC0 = 131072, L_MISC = 147456, LDS_BYTES = 163840;
constexpr u32 KVRAW_SZ = 5632, L_KVS = L_LOC0 + 4 * KVRAW_SZ, L_B2 = L_KVS + 9216, L_FLAG = L_B2 + 128, L_ROWF = L_FLAG + 128, L_B2F = L_ROWF + 256, L_R2 = L_B2F + 256;
constexpr u32 L_SCR = L_B2F;
static_assert(L_R2 + 256 <= LDS_BYTES, "lds");
constexpr u32 F_TOEP = L_FLAG, F_HCNT = L_FLAG + 16, F_HRDY = L_FLAG + 20, F_KVRDY = L_FLAG + 24, F_YCNT = L_FLAG + 28, F_YRDY = L_FLAG + 32, F_S0 = L_FLAG + 36, F_KVCNT = L_FLAG + 40, F_W = L_FLAG + 48, F_LAND = L_FLAG + 64, F_XCH = L_FLAG + 96, F_KVS = L_FLAG + 124, F_CONS = L_KVS + 128;
constexpr u32 F_GP = F_CONS + 8 * 144, F_WF = F_CONS + 9 * 144;
constexpr u32 L_CONV = 16384, CONV_CH = 65536, TOEP_CP = 4160, TOEP_CH = 4 * TOEP_CP, U_SZ = 4608, YS_SZ = 2304;
constexpr u64 TAG_SALT = 0x9E3779B97F4A7C15ull;
constexpr int SPIN_CAP = 1 << 15;
#define SLP 2

#define ST(i) do {} while (0)
#define STP nullptr

DI void dma16(u32 voff, const void* sbase, u32 lds) {
    asm volatile("s_mov_b32 m0, %2\n\ts_nop 0\n\tglobal_load_lds_dwordx4 %0, %1" :: "v"(voff), "s"(sbase), "s"(lds) : "memory");
}
DI void setm0(u32 lds) { asm volatile("s_mov_b32 m0, %0\n\ts_nop 0" :: "s"(lds) : "memory"); }
template <int OFF> DI void dmai(u32 voff, const void* sbase) { asm volatile("global_load_lds_dwordx4 %0, %1 offset:%2" :: "v"(voff), "s"(sbase), "i"(OFF) : "memory"); }
DI void bdma16(u32 voff, u32x4 srd, u32 lds) {
    asm volatile("s_mov_b32 m0, %2\n\ts_nop 0\n\tbuffer_load_dwordx4 %0, %1, 0 offen lds" :: "v"(voff), "s"(srd), "s"(lds) : "memory");
}
#define SB() __builtin_amdgcn_sched_barrier(0)
template <int OFF> DI u32x2 alds8(u32 a) { u32x2 r; asm volatile("ds_read_b64 %0, %1 offset:%2" : "=v"(r) : "v"(a), "i"(OFF)); return r; }
template <int OFF> DI u32x4 alds16(u32 a) { u32x4 r; asm volatile("ds_read_b128 %0, %1 offset:%2" : "=v"(r) : "v"(a), "i"(OFF)); return r; }
DI u32x4 gld16(u32 voff, const void* sbase) { u32x4 r; asm volatile("global_load_dwordx4 %0, %1, %2" : "=v"(r) : "v"(voff), "s"(sbase) : "memory"); return r; }
template <int OFF> DI u32x4 gld16c(u32 voff, const void* sbase) { u32x4 r; asm volatile("global_load_dwordx4 %0, %1, %2 offset:%3 sc1" : "=v"(r) : "v"(voff), "s"(sbase), "i"(OFF) : "memory"); return r; }
DI u32x2 gld8c(u32 voff, const void* sbase) { u32x2 r; asm volatile("global_load_dwordx2 %0, %1, %2 sc1" : "=v"(r) : "v"(voff), "s"(sbase) : "memory"); return r; }
DI void touch(const void* addr, u32 lds) { asm volatile("s_mov_b32 m0, %1\n\ts_nop 0\n\tglobal_load_lds_dword %0, off" :: "v"(addr), "s"(lds) : "memory"); }
DI u32 gld2(u32 voff, const void* sbase) { u32 r; asm volatile("global_load_ushort %0, %1, %2" : "=v"(r) : "v"(voff), "s"(sbase) : "memory"); return r; }
DI u32x4 bld16(u32 voff, u32x4 srd) { u32x4 r; asm volatile("buffer_load_dwordx4 %0, %1, %2, 0 offen" : "=v"(r) : "v"(voff), "s"(srd) : "memory"); return r; }
template <int OFF> DI u32x4 gld16o(u32 voff, const void* sbase) { u32x4 r; asm volatile("global_load_dwordx4 %0, %1, %2 offset:%3" : "=v"(r) : "v"(voff), "s"(sbase), "i"(OFF) : "memory"); return r; }
template <int OFF> DI void gst16c(u32 voff, void* sbase, u32x4 v) { asm volatile("global_store_dwordx4 %0, %1, %2 offset:%3 sc1\n\ts_nop 1" :: "v"(voff), "v"(v), "s"(sbase), "i"(OFF) : "memory"); }
template <int OFF> DI void gst8c(u32 voff, void* sbase, u32x2 v) { asm volatile("global_store_dwordx2 %0, %1, %2 offset:%3 sc1" :: "v"(voff), "v"(v), "s"(sbase), "i"(OFF) : "memory"); }
#define WAITV(n) asm volatile("s_waitcnt vmcnt(%0)" :: "i"(n) : "memory")
#define WAITBAR(n) asm volatile("s_waitcnt vmcnt(%0) lgkmcnt(0)\n\ts_barrier" :: "i"(n) : "memory")
#define BARL() asm volatile("s_waitcnt lgkmcnt(0)\n\ts_barrier" ::: "memory")
#define PIN(x) asm volatile("" : "+v"(x))

DI u32x4 mksrd(const void* p, u32 bytes) { u64 a = (u64)p; return u32x4{(u32)a, (u32)(a >> 32) & 0xffffu, bytes, 0x00020000u}; }
DI float bflo(u32 x) { return __uint_as_float(x << 16); }
DI float bfhi(u32 x) { return __uint_as_float(x & 0xffff0000u); }
DI u32 pk2(float a, float b) { bf16x2 v = {(__bf16)a, (__bf16)b}; return __builtin_bit_cast(u32, v); }
DI float rbf(float a) { return bflo(pk2(a, a)); }
DI float relu(float a) { int i = __float_as_int(a); return __int_as_float(i > 0 ? i : 0); }
DI float fma1(float a, float b, float c) { float r; asm("v_fma_f32 %0, %1, %2, %3" : "=v"(r) : "v"(a), "v"(b), "v"(c)); return r; }
DI float mul1(float a, float b) { float r; asm("v_mul_f32 %0, %1, %2" : "=v"(r) : "v"(a), "v"(b)); return r; }
DI float add1(float a, float b) { float r; asm("v_add_f32 %0, %1, %2" : "=v"(r) : "v"(a), "v"(b)); return r; }
DI float dot2(u32 a, u32 b, float c) { return __builtin_amdgcn_fdot2_f32_bf16(__builtin_bit_cast(bf16x2, a), __builtin_bit_cast(bf16x2, b), c, false); }
DI u32x4 lds16(u32 a) { return *(const LAS u32x4*)a; }
DI u32x2 lds8(u32 a) { return *(const LAS u32x2*)a; }
DI void sts16(u32 a, u32x4 v) { *(LAS u32x4*)a = v; }
DI void sts8(u32 a, u32x2 v) { *(LAS u32x2*)a = v; }
DI u32x2 ldtr(u32 a) { return __builtin_bit_cast(u32x2, __builtin_amdgcn_ds_read_tr16_b64_v4i16((LAS s16x4*)a)); }
DI u32x4 cat(u32x2 a, u32x2 b) { return u32x4{a.x, a.y, b.x, b.y}; }
DI f32x16 mfma32(u32x4 a, u32x4 b, f32x16 c) { return __builtin_amdgcn_mfma_f32_32x32x16_bf16(__builtin_bit_cast(bf16x8, a), __builtin_bit_cast(bf16x8, b), c, 0, 0, 0); }
DI f32x4 mfma16(u32x4 a, u32x4 b, f32x4 c) { return __builtin_amdgcn_mfma_f32_16x16x32_bf16(__builtin_bit_cast(bf16x8, a), __builtin_bit_cast(bf16x8, b), c, 0, 0, 0); }
DI float xhalf_sum(float v) { auto r = __builtin_amdgcn_permlane32_swap(__float_as_uint(v), __float_as_uint(v), false, false); return __uint_as_float(r[0]) + __uint_as_float(r[1]); }

struct Params {
    const u16 *x, *k, *v, *shk, *ssmk, *ssmd, *w1, *b1, *w2, *b2;
    u16* out; char* ws;
};

DI void poll8(u32 voff, const void* sbase, bool need, u32 tlo, u32 thi) {
    _Pragma("clang loop unroll(disable)") for (int spin = 0; spin < SPIN_CAP; ++spin) {
        u32x2 f = gld8c(voff, sbase);
        asm volatile("s_waitcnt vmcnt(0)" : "+v"(f) :: "memory");
        bool ok = !need || (f.x == tlo && f.y == thi);
        if (__all(ok)) break;
        __builtin_amdgcn_s_sleep(SLP);
    }
}
DI void poll16(u32 voff, const void* sbase, u32 tlo, u32 thi) {
    _Pragma("clang loop unroll(disable)") for (int spin = 0; spin < SPIN_CAP; ++spin) {
        u32x4 f = gld16c<0>(voff, sbase);
        asm volatile("s_waitcnt vmcnt(0)" : "+v"(f) :: "memory");
        bool ok = f.x == tlo && f.y == thi && f.z == tlo && f.w == thi;
        if (__all(ok)) break;
        __builtin_amdgcn_s_sleep(SLP);
    }
}

template <int SL> DI void probe(u32 voff, const void* sbase, u32 tag) {
    _Pragma("clang loop unroll(disable)") for (int spin = 0; spin < SPIN_CAP; ++spin) {
        u32x2 f = gld8c(voff, sbase);
        asm volatile("s_waitcnt vmcnt(0)" : "+v"(f) :: "memory");
        if (__all(f.y == tag)) break;
        __builtin_amdgcn_s_sleep(SL);
    }
}
DI bool lds_arrive_last(u32 addr, u32 n) {
    u32 old = 0;
    if ((threadIdx.x & 63) == 0) old = __hip_atomic_fetch_add((LAS u32*)addr, 1u, __ATOMIC_RELAXED, __HIP_MEMORY_SCOPE_WORKGROUP);
    return (u32)__builtin_amdgcn_readfirstlane(old) == n - 1;
}
DI void lds_wait(u32 addr) {
    _Pragma("clang loop unroll(disable)") for (int spin = 0; spin < (1 << 22); ++spin) {
        if (__builtin_amdgcn_readfirstlane(*(volatile LAS u32*)addr)) break;
        __builtin_amdgcn_s_sleep(1);
    }
    asm volatile("" ::: "memory");
}
DI void lds_add(u32 addr) { asm volatile("" ::: "memory"); __hip_atomic_fetch_add((LAS u32*)addr, 1u, __ATOMIC_RELAXED, __HIP_MEMORY_SCOPE_WORKGROUP); asm volatile("" ::: "memory"); }
DI u32 lds_peek(u32 addr) { return *(volatile LAS u32*)addr; }
DI void lds_wait_ge(u32 addr, u32 n, u32 first) {
    u32 v = first;
    _Pragma("clang loop unroll(disable)") for (int spin = 0; spin < (1 << 22) && (u32)__builtin_amdgcn_readfirstlane(v) < n; ++spin) { __builtin_amdgcn_s_sleep(1); v = lds_peek(addr); }
    asm volatile("" ::: "memory");
}
template <int t> DI void cons_set(u32 lds0, int g) { asm volatile("s_waitcnt lgkmcnt(0)\n\tds_write_b32 %0, %1 offset:%2" :: "v"(lds0 + 131072 + g * 4), "v"(1u), "i"(F_CONS + t * 144 - 131072) : "memory"); }
DI void cons_wait(u32 lds0, int t) {
    _Pragma("clang loop unroll(disable)") for (int spin = 0; spin < (1 << 22); ++spin) {
        const u32x4 v = *(volatile LAS u32x4*)(lds0 + F_CONS + t * 144);
        if (__builtin_amdgcn_readfirstlane(v.x & v.y & v.z & v.w)) break;
        __builtin_amdgcn_s_sleep(1);
    }
    asm volatile("" ::: "memory");
}
DI void lds_set(u32 addr) { asm volatile("" ::: "memory"); if ((threadIdx.x & 63) == 0) *(volatile LAS u32*)addr = 1u; }
constexpr u32 loc_of(int c) { return (c % 5) == 0 ? L_LOC0 : (c % 5) == 1 ? 3 * L_PIECE : (c % 5) == 2 ? L_W + 3 * L_PIECE : (c % 5) == 3 ? 0u : L_W; }

#define CPRIO 2
#define YSLP 4
#define YFIRST 0
#define PRIOALL 0
#define LW 2
#define WEARLY 0
#define WDEPTH 8
#define TW 0
#define WC0 14
#define RSLP 8
#define HWAIT 2
#define KW 0
#define CROTM 0
#define ROTM 1
#define LDELAY 0
#define KDELAY 0
#define HDV 4
constexpr int HD = HDV, HS = HD + 1;
constexpr int WS_ = 2;
constexpr int g2_younger(int c) {
    const int n = c + 1;
    if (n >= 14) return 63;
    bool sh = false, sw = n < LW || n >= WC0; int ah = 0, aw = sw ? 1000 : 0;
    for (int i = LW; i < 5; ++i) { if (sw) aw += 4; if (i == n) sw = true; }
    for (int i = 0; i < HD; ++i) { if (sh) ah += 4; if (sw) aw += 4; if (i == n) sh = true; }
    for (int i = 0; i < c; ++i) {
        if (i + HD < 14) { if (sh) ah += 4; if (sw) aw += 4; if (i + HD == n) sh = true; }
        if (i + 5 < WC0) { if (sh) ah += 4; if (sw) aw += 4; if (i + 5 == n) sw = true; }
    }
    return ah < aw ? ah : aw;
}

struct G2Ctx { const char* w2b; const char* hb; int crot, fh2; u32 voffw[4], ldsg, vlane, ta[3][2], smask, fl; };
template <int C> DI int g2_chunk(const G2Ctx& x) {
    int c = C + x.crot; if (c >= 14) c -= 14;
    return c;
}
template <int C> DI const char* g2_wsrc(const G2Ctx& x) { return x.w2b + (size_t)g2_chunk<C>(x) * (128 * 896); }
template <int C> DI u32 g2_loc(const G2Ctx& x) { return loc_of(C) + x.ldsg; }
template <int C, int N> DI void g2_w1(const G2Ctx& x, const char* sb) { if constexpr (N == 0) setm0(g2_loc<C>(x)); dmai<N * 1024>(x.voffw[N], sb); }
template <int C> DI void g2_issue_w(const G2Ctx& x) { const char* sb = g2_wsrc<C>(x); g2_w1<C, 0>(x, sb); g2_w1<C, 1>(x, sb); g2_w1<C, 2>(x, sb); g2_w1<C, 3>(x, sb); }
template <int C> DI const char* g2_hsrc(const G2Ctx& x) { return x.hb + g2_chunk<C>(x) * 8192; }
template <int N> DI u32x4 g2_h1(const G2Ctx& x, const char* sb) { return gld16c<(N & 1) * 1024>(x.vlane + (N >> 1) * 112 * 1024, sb); }
template <int C> DI void g2_issue_h(const G2Ctx& x, u32x4 (&hf)[HS][4]) {
    const char* sb = g2_hsrc<C>(x);
    hf[C % HS][0] = g2_h1<0>(x, sb); hf[C % HS][1] = g2_h1<1>(x, sb); hf[C % HS][2] = g2_h1<2>(x, sb); hf[C % HS][3] = g2_h1<3>(x, sb);
}
template <int C> DI void g2_read(const G2Ctx& x, u32x4 (&wf)[WS_][4]) {
    const u32 a0 = x.ta[loc_of(C) >> 16][0], a1 = x.ta[loc_of(C) >> 16][1];
    constexpr u32 lo = loc_of(C) & 0xffffu;
#pragma unroll
    for (int ks = 0; ks < 2; ++ks) {
        wf[C % WS_][ks * 2] = cat(ldtr(a0 + lo + ks * 2048), ldtr(a0 + lo + ks * 2048 + 1024));
        wf[C % WS_][ks * 2 + 1] = cat(ldtr(a1 + lo + ks * 2048), ldtr(a1 + lo + ks * 2048 + 1024));
    }
}
template <int C, int I> DI void g2_rd(const G2Ctx& x, u32x4 (&wf)[WS_][4]) {
    if constexpr (C < 14) {
        const u32 a = x.ta[loc_of(C) >> 16][I & 1]; constexpr u32 lo = (loc_of(C) & 0xffffu) + (I >> 1) * 2048;
        wf[C % WS_][I] = cat(ldtr(a + lo), ldtr(a + lo + 1024));
    }
}
DI u32 chk(u32 d, u32 m, u32 bad) { return __builtin_amdgcn_bitop3_b32(d, m, bad, 0xBE); }
template <int C> DI void g2_retry(const G2Ctx& x, u32x4 (&hf)[HS][4], u32 bad, u64* stamps) {
    if (__builtin_expect(!__any((bad & 0x80008000u) != 0u), 1)) return;
    _Pragma("clang loop unroll(disable)") for (int spin = 0; spin < SPIN_CAP; ++spin) {
        __builtin_amdgcn_s_sleep(RSLP);
        g2_issue_h<C>(x, hf);
        asm volatile("s_waitcnt vmcnt(0)" : "+v"(hf[C % HS][0]), "+v"(hf[C % HS][1]), "+v"(hf[C % HS][2]), "+v"(hf[C % HS][3]) :: "memory");
        bad = 0u;
#pragma unroll
        for (int i = 0; i < 16; ++i) { hf[C % HS][i >> 2][i & 3] ^= x.smask; bad |= hf[C % HS][i >> 2][i & 3]; }
        if (!__any((bad & 0x80008000u) != 0u)) break;
    }
}
template <int C> DI void g2_step(const G2Ctx& x, u32x4 (&hf)[HS][4], f32x16 (&acc)[2][2], u32x4 (&wf)[WS_][4], u32 pk, u64* stamps) {
    constexpr int N = (C + 1 < 14 ? C + 1 : C) % HS;
    asm volatile("s_waitcnt vmcnt(%4) lgkmcnt(0)" : "+v"(hf[N][0]), "+v"(hf[N][1]), "+v"(hf[N][2]), "+v"(hf[N][3]) : "i"(g2_younger(C)) : "memory");
    if constexpr (C % 3 == 0) ST(14 + C / 3);
    if constexpr (C + 5 >= WC0 && C + 5 < 14) asm volatile("ds_write_b32 %0, %1 offset:%2" :: "v"(x.fl), "v"(C + 1), "i"(F_GP - 131072) : "memory");
    if constexpr (C + 1 >= WC0 && C + 1 < 14) lds_wait_ge(x.fl + (F_WF - 131072), C + 2, pk);
    SB();
    constexpr bool DH = C + HD < 14, DW = C + 5 < WC0, DC = C + 1 < 14;
    const char* hs = g2_hsrc<(DH ? C + HD : 0)>(x);
    const char* wsrc = g2_wsrc<(DW ? C + 5 : 0)>(x);
    u32 bad = 0u;
#define G2M(i) acc[((i) >> 1) & 1][(i) & 1] = mfma32(wf[C % WS_][((i) >> 2) * 2 + (((i) >> 1) & 1)], hf[C % HS][((i) & 1) * 2 + ((i) >> 2)], acc[((i) >> 1) & 1][(i) & 1]); \
               if constexpr (DC) { hf[N][(i) >> 1][((i) & 1) * 2] ^= x.smask; hf[N][(i) >> 1][((i) & 1) * 2 + 1] ^= x.smask; bad = bad | hf[N][(i) >> 1][((i) & 1) * 2] | hf[N][(i) >> 1][((i) & 1) * 2 + 1]; }
    G2M(0); g2_rd<C + 1, 0>(x, wf); if constexpr (DH) hf[(C + HD) % HS][0] = g2_h1<0>(x, hs); SB();
    G2M(1); g2_rd<C + 1, 1>(x, wf); if constexpr (DH) hf[(C + HD) % HS][1] = g2_h1<1>(x, hs); SB();
    G2M(2); g2_rd<C + 1, 2>(x, wf); if constexpr (DH) hf[(C + HD) % HS][2] = g2_h1<2>(x, hs); SB();
    G2M(3); g2_rd<C + 1, 3>(x, wf); if constexpr (DH) hf[(C + HD) % HS][3] = g2_h1<3>(x, hs); SB();
    G2M(4); if constexpr (DW) g2_w1<C + 5, 0>(x, wsrc); SB();
    G2M(5); if constexpr (DW) g2_w1<C + 5, 1>(x, wsrc); SB();
    G2M(6); if constexpr (DW) g2_w1<C + 5, 2>(x, wsrc); SB();
    G2M(7); if constexpr (DW) g2_w1<C + 5, 3>(x, wsrc); if constexpr (C + 2 >= WC0 && C + 2 < 14) pk = lds_peek(x.fl + (F_WF - 131072)); SB();
#undef G2M
    if constexpr (DC) { g2_retry<C + 1>(x, hf, bad, stamps); g2_step<C + 1>(x, hf, acc, wf, pk, stamps); }
}

struct CF { u32x2 a0[8], a1[8]; u32x4 b[8]; };
template <int S, int R> DI void cv_ld(CF& f, u32 aA, u32 aB) {
    f.a0[R] = alds8<R * 512 + S * 64>(aA); f.a1[R] = alds8<R * 512 + S * 64 + 8>(aA);
    f.b[R] = alds16<(224 - 32 * R + 4 * S) * 16>(aB);
}
DI void cv_wait(CF& f) {
    asm volatile("s_waitcnt lgkmcnt(0)" : "+v"(f.a0[0]), "+v"(f.a0[1]), "+v"(f.a0[2]), "+v"(f.a0[3]), "+v"(f.a0[4]), "+v"(f.a0[5]), "+v"(f.a0[6]), "+v"(f.a0[7]),
                 "+v"(f.a1[0]), "+v"(f.a1[1]), "+v"(f.a1[2]), "+v"(f.a1[3]), "+v"(f.a1[4]), "+v"(f.a1[5]), "+v"(f.a1[6]), "+v"(f.a1[7]),
                 "+v"(f.b[0]), "+v"(f.b[1]), "+v"(f.b[2]), "+v"(f.b[3]), "+v"(f.b[4]), "+v"(f.b[5]), "+v"(f.b[6]), "+v"(f.b[7]));
}
constexpr int cv_r(int m) { int r = 0; while (m >= 8 - r) { m -= 8 - r; ++r; } return r; }
constexpr int cv_c(int m) { int r = 0; while (m >= 8 - r) { m -= 8 - r; ++r; } return m; }
template <int S, int M> DI void cv_mm(CF& f, CF& n, u32 aA, u32 aB, f32x4 (&acc)[8]) {
    constexpr int r = cv_r(M), c = cv_c(M);
    acc[r + c] = mfma16(cat(f.a0[r], f.a1[r]), f.b[c], acc[r + c]);
    if constexpr ((M & 3) == 0 && M / 4 < 8) cv_ld<S + 1, M / 4>(n, aA, aB);
    SB();
    if constexpr (M + 1 < 36) cv_mm<S, M + 1>(f, n, aA, aB, acc);
}
DI void conv_body(u32 aA, u32 aB, f32x4 (&acc)[8]) {
    CF f, n;
    asm volatile("s_waitcnt lgkmcnt(0)" ::: "memory");
    cv_ld<0, 0>(f, aA, aB); cv_ld<0, 1>(f, aA, aB); cv_ld<0, 2>(f, aA, aB); cv_ld<0, 3>(f, aA, aB);
    cv_ld<0, 4>(f, aA, aB); cv_ld<0, 5>(f, aA, aB); cv_ld<0, 6>(f, aA, aB); cv_ld<0, 7>(f, aA, aB);
    _Pragma("clang loop unroll(disable)") for (int it = 0; it < 2; ++it) {
        cv_wait(f); SB();
        cv_mm<0, 0>(f, n, aA, aB, acc);
        cv_wait(n); SB();
        cv_mm<1, 0>(n, f, aA, aB, acc);
        aA += 128; aB += 128;
    }
    cv_wait(f);
    asm volatile("" ::: "memory");
}

struct Geo { int bi, b, xcd, fh2, RB, CT, tb64, db, rot, crot; u32 R0, F0, T0, D0; };
DI Geo make_geo() {
    Geo g; g.b = blockIdx.x - NHELP; g.xcd = g.b & 7; const int bi = g.b >> 3; g.bi = bi;
    const int rq = g.xcd >> 1, rb = bi / 7, ct = bi - rb * 7;
    g.fh2 = g.xcd & 1; g.RB = 4 * rq + rb; g.CT = 7 * g.fh2 + ct; g.R0 = 128 * g.RB; g.F0 = 128 * g.CT;
    g.tb64 = 2 * g.RB + g.fh2; g.db = ct; g.T0 = 64 * g.tb64; g.D0 = 64 * g.db;

    g.rot = g.xcd >= 7 ? 0 : g.xcd;
     g.crot = CROTM == 4 ? (2 * g.xcd) % 14 : CROTM == 0 ? g.CT : CROTM == 1 ? (g.CT + 3 * rb + (rb >> 1)) % 14 : CROTM == 2 ? 2 * ct : (2 * ct + 4 * rb - (rb == 3)) % 14;
    return g;
}
DI void make_g2(G2Ctx& g2, const Params& p, const Geo& q, int g, int lane, u32 lds0) {
    g2.w2b = (const char*)p.w2 + (size_t)(32 * g) * 896 + q.D0 * 2;
    g2.hb = p.ws + WS_H + (size_t)(q.tb64 * 2 * 112 + 2 * g) * 1024;
    g2.crot = q.crot; g2.fh2 = q.fh2;
#pragma unroll
    for (int n = 0; n < 4; ++n) g2.voffw[n] = (u32)((lane >> 3) * 896 + 16 * ((lane & 7) ^ (((lane >> 4) & 1) << 2)) + n * (8 * 896 - 1024));
    g2.ldsg = lds0 + g * 4096; g2.vlane = lane * 16u;
}
DI u32 slab_base(u32 lds0, int cw) { return lds0 + L_CONV + (cw >> 1) * CONV_CH + (cw & 1) * 8192; }
DI void final_epilogue(const Params& p, const Geo& q, u32 lds0) {
    int tz = threadIdx.x; PIN(tz);
    const int t = tz >> 3, c = tz & 7;
    const u32 pb = lds0 + ((t >> 5) ? 0u : 3 * L_PIECE) + ((t >> 4) & 1) * L_W + (t & 15) * 256;
    const u32 o0 = pb + 16 * ((2 * c) ^ (t & 7)), o1 = o0 ^ 16;
    f32x4 pa[4], pb_[4];
#pragma unroll
    for (int g = 0; g < 4; ++g) { pa[g] = __builtin_bit_cast(f32x4, lds16(o0 + g * 4096)); pb_[g] = __builtin_bit_cast(f32x4, lds16(o1 + g * 4096)); }
    const float r2 = *(const LAS float*)(lds0 + L_R2 + t * 4);
    const f32x4 b0 = __builtin_bit_cast(f32x4, lds16(lds0 + L_B2F + c * 32)), b1 = __builtin_bit_cast(f32x4, lds16(lds0 + L_B2F + c * 32 + 16));
    const u32 sb = slab_base(lds0, c >> 1) + t * 64 + (c & 1) * 32;
    const f32x4 sv0 = __builtin_bit_cast(f32x4, lds16(sb)), sv1 = __builtin_bit_cast(f32x4, lds16(sb + 16));
    const f32x4 xv0 = __builtin_bit_cast(f32x4, lds16(sb + 4096)), xv1 = __builtin_bit_cast(f32x4, lds16(sb + 4096 + 16));
    SB();
    const f32x4 s0 = (pa[0] + pa[1]) + (pa[2] + pa[3]), s1 = (pb_[0] + pb_[1]) + (pb_[2] + pb_[3]);
    f32x4 z0 = s0 * r2 + b0, z1 = s1 * r2 + b1;
    f32x4 e0 = z0 * -1.4426950408889634f, e1 = z1 * -1.4426950408889634f;
#pragma unroll
    for (int j = 0; j < 4; ++j) { e0[j] = __builtin_amdgcn_exp2f(e0[j]); e1[j] = __builtin_amdgcn_exp2f(e1[j]); }
    e0 += 1.f; e1 += 1.f;
#pragma unroll
    for (int j = 0; j < 4; ++j) { e0[j] = __builtin_amdgcn_rcpf(e0[j]); e1[j] = __builtin_amdgcn_rcpf(e1[j]); }
    z0 = z0 * e0; z1 = z1 * e1;
    z0 = z0 * sv0 + xv0; z1 = z1 * sv1 + xv1;
    const u32x4 res = {pk2(z0[0], z0[1]), pk2(z0[2], z0[3]), pk2(z1[0], z1[1]), pk2(z1[2], z1[3])};
    *(GAS u32x4*)((GAS char*)p.out + (size_t)(q.T0 + t) * 896 + q.D0 * 2 + c * 16) = res;
}
#define STAMP_DECL
#define STAMP_OUT(wave) do {} while (0)

extern __shared__ __attribute__((aligned(16))) char smem[];

struct CL { const char *xb, *wb; u32 vx[4], vw[4], sl; int rot; };
struct CSrc { const char *xs, *wsrc; };
template <int ST_> DI CSrc c_src(const CL& c) { int kc = ST_ + c.rot; if (kc >= 7) kc -= 7; return CSrc{c.xb + kc * 128, c.wb + (size_t)kc * 64 * 3584}; }
template <int ST_, int N> DI void c_dma1(const CL& c, const CSrc& s) {
    if constexpr (N == 0) setm0(c.sl + (ST_ & 3) * L_PIECE);
    if constexpr (N == 4) setm0(c.sl + (ST_ & 3) * L_PIECE + L_W);
    if constexpr (N < 4) dmai<N * 1024>(c.vx[N], s.xs); else dmai<(N - 4) * 1024>(c.vw[N - 4], s.wsrc);
}
template <int ST_> DI void c_dma(const CL& c) {
    const CSrc s = c_src<ST_>(c);
    c_dma1<ST_, 0>(c, s); c_dma1<ST_, 1>(c, s); c_dma1<ST_, 2>(c, s); c_dma1<ST_, 3>(c, s);
    c_dma1<ST_, 4>(c, s); c_dma1<ST_, 5>(c, s); c_dma1<ST_, 6>(c, s); c_dma1<ST_, 7>(c, s);
}
DI void make_cl(CL& c, const Params& p, const Geo& q, int cw, int lane, u32 lds0) {
    const int r8 = lane >> 3, c8 = lane & 7, r4 = lane >> 4, c16 = lane & 15;
#pragma unroll
    for (int n = 0; n < 4; ++n) {
        c.vx[n] = (u32)((32 * cw + r8) * 896 + 16 * (c8 ^ (((n & 1) << 2) | (r8 >> 1))) + n * (8 * 896 - 1024));
        c.vw[n] = (u32)((16 * cw + r4) * 3584 + 16 * (c16 ^ (r4 << 2)) + n * (4 * 3584 - 1024));
    }
    c.xb = (const char*)p.x + (size_t)q.R0 * 896; c.wb = (const char*)p.w1 + q.F0 * 2;
    c.sl = lds0 + cw * 4096; c.rot = q.rot;
}

template <int ST_> DI void l_dma(const CL& c) {
    const CSrc s = c_src<ST_>(c);
    const u32 sl = c.sl + (ST_ & 3) * L_PIECE;
    setm0(sl); dmai<0>(c.vx[0], s.xs); dmai<1024>(c.vx[1], s.xs); dmai<2048>(c.vx[2], s.xs); dmai<3072>(c.vx[3], s.xs);
    setm0(sl + L_W); dmai<0>(c.vw[0], s.wsrc); dmai<1024>(c.vw[1], s.wsrc); dmai<2048>(c.vw[2], s.wsrc); dmai<3072>(c.vw[3], s.wsrc);
    const char* x2 = s.xs + 32 * 896; const char* w2 = s.wsrc + 16 * 3584;
    setm0(sl + 4096); dmai<0>(c.vx[0], x2); dmai<1024>(c.vx[1], x2); dmai<2048>(c.vx[2], x2); dmai<3072>(c.vx[3], x2);
    setm0(sl + L_W + 4096); dmai<0>(c.vw[0], w2); dmai<1024>(c.vw[1], w2); dmai<2048>(c.vw[2], w2); dmai<3072>(c.vw[3], w2);
}

struct Fr { u32x4 x[4][2], w[4][2]; };
struct GA { u32 ax[4], aw[2]; };
template <int ST_> DI void g1_read(const GA& a, Fr& f) {
    constexpr u32 so = (ST_ & 3) * L_PIECE;
#pragma unroll
    for (int k = 0; k < 4; ++k) {
        f.w[k][0] = cat(ldtr(a.aw[0] + so + k * 4096), ldtr(a.aw[0] + so + k * 4096 + 1024));
        f.x[k][0] = lds16(a.ax[k] + so);
        f.w[k][1] = cat(ldtr(a.aw[1] + so + k * 4096), ldtr(a.aw[1] + so + k * 4096 + 1024));
        f.x[k][1] = lds16(a.ax[k] + so + 4096);
    }
}
template <int K0> DI void g1_mma(const Fr& f, f32x16 (&acc)[2][2]) {
#pragma unroll
    for (int k = K0; k < K0 + 2; ++k) {
        acc[0][0] = mfma32(f.w[k][0], f.x[k][0], acc[0][0]); acc[1][0] = mfma32(f.w[k][1], f.x[k][0], acc[1][0]);
        acc[0][1] = mfma32(f.w[k][0], f.x[k][1], acc[0][1]); acc[1][1] = mfma32(f.w[k][1], f.x[k][1], acc[1][1]);
    }
    SB();
}

template <int ST_, int I> DI void g1_rd(const GA& a, Fr& f) {
    constexpr u32 so = (ST_ & 3) * L_PIECE; constexpr int k = I >> 1, h = I & 1;
    f.w[k][h] = cat(ldtr(a.aw[h] + so + k * 4096), ldtr(a.aw[h] + so + k * 4096 + 1024));
    f.x[k][h] = lds16(a.ax[k] + so + h * 4096);
}
template <int K0, int I> DI void g1_m1(const Fr& f, f32x16 (&acc)[2][2]) {
    constexpr int k = K0 + (I >> 2), mt = I & 1, st = (I >> 1) & 1;
    acc[mt][st] = mfma32(f.w[k][mt], f.x[k][st], acc[mt][st]);
}
template <int ST_, bool CARRY> DI void g1_stage(const GA& a, const Fr& fp, Fr& fn, f32x16 (&acc)[2][2], u32 lds0, int g, u32& pk) {
    if constexpr (CARRY) { g1_m1<2, 0>(fp, acc); SB(); cons_set<ST_ - 1>(lds0, g); SB(); }
    lds_wait_ge(lds0 + F_LAND + 4 * ST_, ST_ == 0 ? 256 : (ST_ == 3 || ST_ == 6) ? 128 : 64, pk);
    if constexpr (CARRY) {
        g1_rd<ST_, 0>(a, fn); SB();
        g1_m1<2, 1>(fp, acc); SB(); g1_m1<2, 2>(fp, acc); g1_rd<ST_, 1>(a, fn); SB();
        g1_m1<2, 3>(fp, acc); SB(); g1_m1<2, 4>(fp, acc); g1_rd<ST_, 2>(a, fn); SB();
        g1_m1<2, 5>(fp, acc); g1_rd<ST_, 3>(a, fn); SB(); g1_m1<2, 6>(fp, acc); SB();
        g1_m1<2, 7>(fp, acc); SB();
    } else { g1_rd<ST_, 0>(a, fn); g1_rd<ST_, 1>(a, fn); g1_rd<ST_, 2>(a, fn); g1_rd<ST_, 3>(a, fn); SB(); }
    g1_m1<0, 0>(fn, acc); g1_rd<ST_, 4>(a, fn); SB(); g1_m1<0, 1>(fn, acc); SB();
    g1_m1<0, 2>(fn, acc); g1_rd<ST_, 5>(a, fn); SB(); g1_m1<0, 3>(fn, acc); g1_rd<ST_, 6>(a, fn); SB();
    g1_m1<0, 4>(fn, acc); g1_rd<ST_, 7>(a, fn); SB(); g1_m1<0, 5>(fn, acc); pk = lds_peek(lds0 + F_LAND + 4 * ST_ + 4); SB();
    g1_m1<0, 6>(fn, acc); SB(); g1_m1<0, 7>(fn, acc); SB();
}

DI void body_g(const Params& p, const int g) {
    STAMP_DECL
    const u64 dg0 = __builtin_amdgcn_s_memrealtime();
    const u32 lds0 = (u32)(size_t)(LAS char*)smem;
    const int lane = threadIdx.x & 63;
    const Geo q = make_geo();
    char* ws = p.ws;
    const int hi = lane >> 5, s31 = lane & 31;
    const int r8 = lane >> 3, c8 = lane & 7;
    const u32 raw = lds0 + L_LOC0 + g * KVRAW_SZ;
    {
        const u32 vkv = (u32)(r8 * 896 + c8 * 16);
        bdma16((u32)(((int)q.T0 + 16 * g - 8) * 896 + (int)q.D0 * 2) + vkv, mksrd(p.k, S_ * D_ * 2), raw);
        const char* kb = (const char*)p.k + (size_t)(q.T0 + 16 * g) * 896 + q.D0 * 2;
        const char* vb = (const char*)p.v + (size_t)(q.T0 + 16 * g) * 896 + q.D0 * 2;
        dmai<1024>(vkv, kb - 1024); dmai<2048>(vkv, kb + 8 * 896 - 2048); dmai<3072>(vkv, vb - 3072);
        setm0(raw + 4096);
        dmai<0>(vkv, vb + 8 * 896);
        if (lane < 32) dmai<1024>(lane * 16u, (const char*)p.shk + q.D0 * 8 - 1024);
        CL c; make_cl(c, p, q, g, lane, lds0);
        c_dma<0>(c);
    }
    u32x2 ep = gld8c(0u, ws + WS_EPOCH);
    const int fh = g & 1, sh = g >> 1;
    GA ga;
    {
        const int blk = (lane >> 4) & 1, qq = (lane & 15) >> 2, pp = lane & 3;
        const u32 Z = (u32)(hi ^ ((s31 >> 1) & 7));
#pragma unroll
        for (int k = 0; k < 4; ++k) ga.ax[k] = lds0 + (64 * sh + s31) * 128 + 16 * ((2 * k) ^ Z);
        ga.aw[0] = lds0 + L_W + (8 * hi + qq) * 256 + 16 * ((8 * fh + 2 * blk + (pp >> 1)) ^ (qq << 2)) + 8 * (pp & 1);
        ga.aw[1] = ga.aw[0] ^ 64;
    }
    WAITV(9);
    {
        u32x4 wr[4], kr[5], vr[2];
#pragma unroll
        for (int e = 0; e < 4; ++e) wr[e] = lds16(raw + 5120 + c8 * 64 + e * 16);
#pragma unroll
        for (int j = 0; j < 5; ++j) kr[j] = lds16(raw + (5 + 2 * r8 + j) * 128 + c8 * 16);
        vr[0] = lds16(raw + 3072 + 2 * r8 * 128 + c8 * 16); vr[1] = lds16(raw + 3072 + (2 * r8 + 1) * 128 + c8 * 16);
        SB();
        u32x4 o0, o1;
#pragma unroll
        for (int e = 0; e < 4; ++e) {
            u32 pl[4], ph[4];
#pragma unroll
            for (int j = 0; j < 4; ++j) { pl[j] = __builtin_amdgcn_perm(kr[j][e], kr[j + 1][e], 0x05040100u); ph[j] = __builtin_amdgcn_perm(kr[j][e], kr[j + 1][e], 0x07060302u); }
            f32x2 a = {dot2(pl[0], wr[e].y, dot2(pl[2], wr[e].x, 0.f)), dot2(ph[0], wr[e].w, dot2(ph[2], wr[e].z, 0.f))};
            f32x2 b = {dot2(pl[1], wr[e].y, dot2(pl[3], wr[e].x, 0.f)), dot2(ph[1], wr[e].w, dot2(ph[3], wr[e].z, 0.f))};
            const u32 ar = pk2(a.x, a.y), br = pk2(b.x, b.y);
            a = f32x2{bflo(ar), bfhi(ar)} * f32x2{bflo(vr[0][e]), bfhi(vr[0][e])}; b = f32x2{bflo(br), bfhi(br)} * f32x2{bflo(vr[1][e]), bfhi(vr[1][e])};
            o0[e] = pk2(a.x, a.y); o1[e] = pk2(b.x, b.y);
        }
        sts16(lds0 + L_KVS + (16 * g + 2 * r8) * 144 + c8 * 16, o0);
        sts16(lds0 + L_KVS + (16 * g + 2 * r8 + 1) * 144 + c8 * 16, o1);
        asm volatile("s_waitcnt lgkmcnt(0)" ::: "memory");
        lds_add(lds0 + F_KVS);
    }
    f32x16 acc[2][2] = {};
    Fr fa, fb;
    WAITV(1);
    lds_add(lds0 + F_LAND);
    u32 pk = lds_peek(lds0 + F_LAND);
    ST(2);
    g1_stage<0, false>(ga, fb, fa, acc, lds0, g, pk);
    g1_stage<1, true>(ga, fa, fb, acc, lds0, g, pk);
    g1_stage<2, true>(ga, fb, fa, acc, lds0, g, pk);
    asm volatile("s_waitcnt vmcnt(0)" : "+v"(ep) :: "memory");
    const u64 epoch = ((u64)(u32)__builtin_amdgcn_readfirstlane(ep.y) << 32) | (u32)__builtin_amdgcn_readfirstlane(ep.x);
    const u64 tag = epoch + TAG_SALT;
    const u32 tlo = (u32)tag, thi = (u32)(tag >> 32);
    const u32 smask = ((lane ^ (u32)epoch) & 1u) ? 0x80008000u : 0u;
    g1_stage<3, true>(ga, fa, fb, acc, lds0, g, pk);
    ST(3);
    const u32 rgo = WS_RG + (q.R0 + 64 * sh + lane) * 8, cgo = WS_CG + (q.F0 + 64 * fh + lane) * 8;
    u32x2 rg = gld8c(rgo, ws), cg = gld8c(cgo, ws);
    u32 b1v = gld2((u32)((64 * fh + lane) * 2), (const char*)p.b1 + q.F0 * 2);
    g1_stage<4, true>(ga, fb, fa, acc, lds0, g, pk);
    asm volatile("s_waitcnt vmcnt(0)" : "+v"(rg), "+v"(cg), "+v"(b1v) :: "memory");
    if (!__all(rg.y == tlo && cg.y == tlo)) {
        rg = gld8c(rgo, ws); cg = gld8c(cgo, ws);
    }
    g1_stage<5, true>(ga, fa, fb, acc, lds0, g, pk);
    asm volatile("s_waitcnt vmcnt(0)" : "+v"(rg), "+v"(cg) :: "memory");
    if (!__all(rg.y == tlo && cg.y == tlo)) {
        rg = gld8c(rgo, ws); cg = gld8c(cgo, ws);
    }
    g1_stage<6, true>(ga, fb, fa, acc, lds0, g, pk);
    cons_set<6>(lds0, g);
    ST(5);
    asm volatile("s_waitcnt vmcnt(0)" : "+v"(rg), "+v"(cg), "+v"(b1v) :: "memory");
    _Pragma("clang loop unroll(disable)") for (int spin = 0; spin < SPIN_CAP && !__all(rg.y == tlo && cg.y == tlo); ++spin) {
        rg = gld8c(rgo, ws); cg = gld8c(cgo, ws);
        asm volatile("s_waitcnt vmcnt(0)" : "+v"(rg), "+v"(cg) :: "memory");
    }
    if (fh == 0 && sh == q.fh2) *(LAS u32*)(lds0 + L_ROWF + lane * 4) = rg.x;
    g1_mma<2>(fa, acc);
    {
        u32x4 ax[2], bx[2];
        const auto rr = __builtin_amdgcn_permlane32_swap(rg.x, rg.x, false, false);
        const auto cc = __builtin_amdgcn_permlane32_swap(cg.x, cg.x, false, false);
        const auto bb = __builtin_amdgcn_permlane32_swap(b1v, b1v, false, false);
#pragma unroll
        for (int i = 0; i < 2; ++i) {
            const u32 nm = __builtin_amdgcn_perm(rr[i], rr[i], 0x01000100u) ^ 0x80008000u;
            ax[i] = u32x4{cc[i], bb[i], 0u, 0u};
            bx[i] = u32x4{hi ? 0u : nm, hi ? 0u : (rr[i] >> 16), 0u, 0u};
        }
#pragma unroll
        for (int st = 0; st < 2; ++st)
#pragma unroll
            for (int mt = 0; mt < 2; ++mt) acc[mt][st] = mfma32(ax[mt], bx[st], acc[mt][st]);
#pragma unroll
        for (int st = 0; st < 2; ++st) {
            char* hb = ws + WS_H + (size_t)((4 * q.RB + 2 * sh + st) * 112 + q.CT * 8 + fh * 4) * 1024;
#pragma unroll
            for (int mt = 0; mt < 2; ++mt)
#pragma unroll
                for (int t = 0; t < 2; ++t) {
                    u32x4 hv;
#pragma unroll
                    for (int j = 0; j < 4; ++j) {
                        f32x2 a = {acc[mt][st][8 * t + 2 * j], acc[mt][st][8 * t + 2 * j + 1]};
                        f32x2 u = {relu(a.x), relu(a.y)};
                        u = u * u;
                        hv[j] = pk2(u.x, u.y) ^ smask;
                    }
                    if (mt == 0 && t == 0) gst16c<0>(lane * 16u, hb, hv);
                    if (mt == 0 && t == 1) gst16c<1024>(lane * 16u, hb, hv);
                    if (mt == 1 && t == 0) gst16c<2048>(lane * 16u, hb, hv);
                    if (mt == 1 && t == 1) gst16c<3072>(lane * 16u, hb, hv);
                    SB();
                }
        }
    }
    ST(6);
    G2Ctx g2; make_g2(g2, p, q, g, lane, lds0);
    {
        const int blk = (lane >> 4) & 1, qq = (lane & 15) >> 2, pp = lane & 3;
        const u32 t0 = g2.ldsg + (4 * hi + qq) * 128 + 16 * ((2 * blk + (pp >> 1)) ^ ((qq >> 1) << 2)) + 8 * (pp & 1);
#pragma unroll
        for (int i = 0; i < 3; ++i) { g2.ta[i][0] = t0 + i * 65536; g2.ta[i][1] = (t0 ^ 64) + i * 65536; PIN(g2.ta[i][0]); PIN(g2.ta[i][1]); }
    }
    g2.smask = smask; g2.fl = lds0 + 131072 + g * 4;
    u32x4 wf[WS_][4];
    cons_wait(lds0, 4);
    asm volatile("s_waitcnt lgkmcnt(0)" ::: "memory");
    {
        u32x4 hf[HS][4];
        if (lds_arrive_last(lds0 + F_HCNT, 4)) { if (lane == 0) gst8c<0>(WS_ALL + q.b * 8, ws, u32x2{tlo, thi}); }
        if constexpr (LW <= 2) g2_issue_w<2>(g2);
        if constexpr (LW <= 3) g2_issue_w<3>(g2);
        if constexpr (LW <= 4) g2_issue_w<4>(g2);
        if constexpr (HWAIT == 1) WAITV(12);
        if constexpr (HWAIT == 2) WAITV(0);
        g2_issue_h<0>(g2, hf); g2_issue_h<1>(g2, hf); if constexpr (HD > 2) g2_issue_h<2 % HS>(g2, hf); if constexpr (HD > 3) g2_issue_h<3 % HS>(g2, hf); if constexpr (HD > 4) g2_issue_h<4 % HS>(g2, hf);
        if constexpr (HD > 5) g2_issue_h<5 % HS>(g2, hf);
        ST(9);
        lds_wait(lds0 + F_W);
        if constexpr (KW) lds_wait(lds0 + F_W + 4 + (g >> 1) * 4);
        g2_read<0>(g2, wf);
        asm volatile("s_waitcnt vmcnt(%4)" : "+v"(hf[0][0]), "+v"(hf[0][1]), "+v"(hf[0][2]), "+v"(hf[0][3]) : "i"(4 * (HD - 1)) : "memory");
        {
            u32 bad = 0u;
#pragma unroll
            for (int i = 0; i < 16; ++i) { hf[0][i >> 2][i & 3] ^= smask; bad |= hf[0][i >> 2][i & 3]; }
            g2_retry<0>(g2, hf, bad, STP);
        }
        ST(7);
        f32x16 a2[2][2] = {};
        g2_step<0>(g2, hf, a2, wf, 0u, STP);
        ST(8);
        const u32 pw = g2.ldsg + (s31 >> 4) * L_W + (s31 & 15) * 256;
#pragma unroll
        for (int s2 = 0; s2 < 2; ++s2)
#pragma unroll
            for (int dt = 0; dt < 2; ++dt)
#pragma unroll
                for (int gr = 0; gr < 4; ++gr) {
                    const u32 a = pw + (s2 ? 0u : 3 * L_PIECE) + 16 * ((8 * dt + 2 * gr + hi) ^ (s31 & 7));
                    sts16(a, __builtin_bit_cast(u32x4, f32x4{a2[dt][s2][4 * gr], a2[dt][s2][4 * gr + 1], a2[dt][s2][4 * gr + 2], a2[dt][s2][4 * gr + 3]}));
                }
    }
    BARL(); ST(10);
    final_epilogue(p, q, lds0);
    if (g == 0 && (DIAG == 2 || (DIAG == 1 && q.b == 100) || (DIAG == 5 && q.b == 0))) { const u64 e = __builtin_amdgcn_s_memrealtime(); const u64 e2 = e + 3 * (e - dg0); while (__builtin_amdgcn_s_memrealtime() < e2) __builtin_amdgcn_s_sleep(1); }
}

DI void body_c(const Params& p, const int cw) {
    STAMP_DECL
    const u64 dg0 = __builtin_amdgcn_s_memrealtime();
    const u32 lds0 = (u32)(size_t)(LAS char*)smem;
    const int lane = threadIdx.x & 63;
    const Geo q = make_geo();
    char* ws = p.ws;
    const int hi = lane >> 5, s31 = lane & 31;
    const int half = cw & 1, chl = cw >> 1, chan = 2 * q.b + chl;
    if (PRIOALL) __builtin_amdgcn_s_setprio(CPRIO);
    u32x2 ep;
    u32x4 xs[4]; u32 dv;
    if (cw < 2) {
        CL c, c2; make_cl(c, p, q, 0, lane, lds0); c2 = c; c2.xb += 64 * 896; c2.wb += 32 * 3584; c2.sl += 8192;
        ep = gld8c(0u, ws + WS_EPOCH);
        if (cw == 0) {
            l_dma<1>(c); l_dma<1>(c2); ST(1);
            WAITV(0); lds_add(lds0 + F_LAND + 4); ST(2);
            cons_wait(lds0, 0); l_dma<4>(c); l_dma<4>(c2); ST(19);
            WAITV(0); lds_add(lds0 + F_LAND + 16); ST(20);
        } else {
            l_dma<2>(c); l_dma<2>(c2); ST(1);
            WAITV(0); lds_add(lds0 + F_LAND + 8); ST(2);
            cons_wait(lds0, 1); l_dma<5>(c); l_dma<5>(c2); ST(19);
            WAITV(0); lds_add(lds0 + F_LAND + 20); ST(20);
        }
        asm volatile("s_waitcnt vmcnt(0)" : "+v"(ep) :: "memory");
        const u32 tg32 = (u32)((((u64)ep.y << 32) | ep.x) + TAG_SALT);
        if (cw == 1) {
            G2Ctx g2; make_g2(g2, p, q, 0, lane, lds0);
            cons_wait(lds0, 3);
#define W4(C) do { const char* sb = g2_wsrc<C>(g2); _Pragma("unroll") for (int i = 0; i < 4; ++i) { setm0(g2_loc<C>(g2) + i * 4096); \
                dmai<0>(g2.voffw[0], sb + i * 32 * 896); dmai<1024>(g2.voffw[1], sb + i * 32 * 896); dmai<2048>(g2.voffw[2], sb + i * 32 * 896); dmai<3072>(g2.voffw[3], sb + i * 32 * 896); } } while (0)
            W4(0); W4(1);
            if constexpr (LW > 2) { WAITV(16); W4(2); }
            if constexpr (LW > 3) { cons_wait(lds0, 4); WAITV(16); W4(3); }
            if constexpr (LW > 4) { WAITV(16); W4(4); }
#undef W4
            dv = gld2(0u, (const char*)p.ssmd + chan * 2);
            WAITV(1);
            lds_set(lds0 + F_W);
            asm volatile("s_waitcnt vmcnt(0)" : "+v"(dv) :: "memory");
            ST(7);
        } else {
            const char* kb2 = ws + WS_KVT + (size_t)(2 * q.b) * TP;
            u32x4 u[16];
            probe<2>((u32)(hi * TP + s31 * 256), kb2, tg32);
            _Pragma("clang loop unroll(disable)") for (int spin = 0; spin < SPIN_CAP; ++spin) {
#pragma unroll
                for (int c = 0; c < 4; ++c) {
                    const char* sb = kb2 + (c >> 1) * TP + (c & 1) * 4096;
                    u[4 * c] = gld16c<0>(lane * 16u, sb); u[4 * c + 1] = gld16c<1024>(lane * 16u, sb); u[4 * c + 2] = gld16c<2048>(lane * 16u, sb); u[4 * c + 3] = gld16c<3072>(lane * 16u, sb);
                }
                asm volatile("s_waitcnt vmcnt(0)" : "+v"(u[0]), "+v"(u[1]), "+v"(u[2]), "+v"(u[3]), "+v"(u[4]), "+v"(u[5]), "+v"(u[6]), "+v"(u[7]), "+v"(u[8]), "+v"(u[9]), "+v"(u[10]),
                             "+v"(u[11]), "+v"(u[12]), "+v"(u[13]), "+v"(u[14]), "+v"(u[15]) :: "memory");
                u32 bad = 0u;
#pragma unroll
                for (int i = 0; i < 16; ++i) bad = chk(u[i].w, tg32, chk(u[i].y, tg32, bad));
                if (!__any(bad != 0u)) break;
                __builtin_amdgcn_s_sleep(4);
            }
            ST(7);
            dv = gld2(0u, (const char*)p.ssmd + chan * 2);
            cons_wait(lds0, 5); cons_wait(lds0, 6);
            ST(14);
            const u32 uq_ = lds0 + L_CONV + TOEP_CH + lane * 8;
#pragma unroll
            for (int i = 0; i < 16; ++i) sts8(uq_ + (i >> 3) * CONV_CH + (i & 7) * 512, u32x2{u[i].x, u[i].z});
            sts8(uq_ + 4096, u32x2{0u, 0u}); sts8(uq_ + CONV_CH + 4096, u32x2{0u, 0u});
            asm volatile("s_waitcnt lgkmcnt(0)" ::: "memory");
            lds_add(lds0 + F_TOEP); lds_add(lds0 + F_TOEP + 4);
            asm volatile("s_waitcnt vmcnt(0)" : "+v"(dv) :: "memory");
        }
    } else {
        const int kw = cw - 2;
        if (kw == 0 && lane < 8) dma16(lane * 16u, (const char*)p.b2 + q.D0 * 2, lds0 + L_B2);
        ep = gld8c(0u, ws + WS_EPOCH);
        CL c; make_cl(c, p, q, 2 * kw, lane, lds0);
        l_dma<3>(c);
        lds_wait_ge(lds0 + F_KVS, 256, lds_peek(lds0 + F_KVS));
        ST(2);
        asm volatile("s_waitcnt vmcnt(16)" : "+v"(ep) :: "memory");
        const u32 tg32 = (u32)((((u64)ep.y << 32) | ep.x) + TAG_SALT);
        {
            const int grp = lane >> 4, qq = (lane & 15) >> 2, pp = lane & 3;
            const u32 a = lds0 + L_KVS + (16 * grp + 4 * pp + 3 - qq) * 144 + kw * 64;
            const u32 vo = (u32)((q.D0 + 32 * kw + (lane & 3)) * TP + (2044 - (int)q.T0 - 4 * (lane >> 2)) * 4);
            u32x2 tr[8];
#pragma unroll
            for (int i = 0; i < 8; ++i) tr[i] = ldtr(a + 8 * i);
            SB();
#pragma unroll
            for (int i = 0; i < 8; ++i) gst16c<0>(vo, ws + WS_KVT + i * 4 * TP, u32x4{tr[i].x, tg32, tr[i].y, tg32});
        }
        ST(20);
        WAITV(8);
        lds_add(lds0 + F_LAND + 12);
        ST(19);
#pragma unroll
        for (int i = 0; i < TW; ++i) { const int L = ((2 * i + kw) * 28 + q.bi) * 64 + lane; if (2 * i + kw < 7) touch((const char*)p.w2 + (u32)(12543 - L) * 128u, lds0 + L_SCR); }
        cons_wait(lds0, 2);
        l_dma<6>(c);
        ST(3);
        u32x4 sk[5];
        const u32x4 sksrd = mksrd((const char*)p.ssmk + (size_t)(2 * q.b + kw) * 4096, 4096);
#pragma unroll
        for (int r = 0; r < 5; ++r) sk[r] = bld16((u32)((lane - 1 + 63 * r) * 16), sksrd);
        { const char* xsl = (const char*)p.x + (size_t)q.T0 * 896 + (q.D0 + 32 * kw) * 2;
          xs[0] = gld16o<0>((u32)(lane * 896), xsl); xs[1] = gld16o<16>((u32)(lane * 896), xsl); xs[2] = gld16o<32>((u32)(lane * 896), xsl); xs[3] = gld16o<48>((u32)(lane * 896), xsl); }
        dv = gld2(0u, (const char*)p.ssmd + chan * 2);
        WAITV(10);
        lds_add(lds0 + F_LAND + 24);
        ST(21);
        if constexpr (KW) {
            G2Ctx ga_, gb_; make_g2(ga_, p, q, 2 * kw, lane, lds0); gb_ = ga_; gb_.w2b += 32 * 896; gb_.ldsg += 4096;
            cons_wait(lds0, 3); g2_issue_w<2>(ga_); g2_issue_w<2>(gb_);
            cons_wait(lds0, 4); g2_issue_w<3>(ga_); g2_issue_w<3>(gb_); g2_issue_w<4>(ga_); g2_issue_w<4>(gb_);
        }
        asm volatile("s_waitcnt vmcnt(0)" : "+v"(sk[0]), "+v"(sk[1]), "+v"(sk[2]), "+v"(sk[3]), "+v"(sk[4]), "+v"(xs[0]), "+v"(xs[1]), "+v"(xs[2]), "+v"(xs[3]), "+v"(dv) :: "memory");
        u32x4 tw[5][4];
#pragma unroll
        for (int r = 0; r < 5; ++r) {
            const u32x4 B = sk[r];
            u32x4 A;
#pragma unroll
            for (int j = 0; j < 4; ++j) A[j] = __builtin_amdgcn_update_dpp(0u, B[j], 0x138  , 0xf, 0xf, false);
            const u32 e0 = __builtin_amdgcn_alignbit(A.y, A.x, 16), e1 = __builtin_amdgcn_alignbit(A.z, A.y, 16), e2 = __builtin_amdgcn_alignbit(A.w, A.z, 16),
                      e3 = __builtin_amdgcn_alignbit(B.x, A.w, 16), e4 = __builtin_amdgcn_alignbit(B.y, B.x, 16);
            tw[r][0] = u32x4{e0, e1, e2, e3}; tw[r][1] = u32x4{A.y, A.z, A.w, B.x}; tw[r][2] = u32x4{e1, e2, e3, e4}; tw[r][3] = u32x4{A.z, A.w, B.x, B.y};
            PIN(tw[r][0]); PIN(tw[r][1]); PIN(tw[r][2]); PIN(tw[r][3]);
        }
        cons_wait(lds0, 5); cons_wait(lds0, 6);
        ST(5);
        const u32 tq = lds0 + L_CONV + kw * CONV_CH + lane * 16;
#pragma unroll
        for (int r = 0; r < 5; ++r) {
            if ((lane > 0 || r == 0) && (r < 4 || lane < 6)) {
#pragma unroll
                for (int c = 0; c < 4; ++c) sts16(tq + r * 1008 + c * TOEP_CP, tw[r][c]);
            }
        }
        asm volatile("s_waitcnt lgkmcnt(0)" ::: "memory");
        lds_add(lds0 + F_TOEP + kw * 4);
        WAITV(0);
        lds_set(lds0 + F_W + 4 + kw * 4);
    }
    const u64 epoch = ((u64)(u32)__builtin_amdgcn_readfirstlane(ep.y) << 32) | (u32)__builtin_amdgcn_readfirstlane(ep.x);
    const u64 tag = epoch + TAG_SALT;
    const u32 tlo = (u32)tag, thi = (u32)(tag >> 32);
    const u32 tbase = lds0 + L_CONV + chl * CONV_CH;
    const u32 ub = tbase + TOEP_CH;
    ST(6);
    lds_wait_ge(lds0 + F_TOEP + chl * 4, 128, lds_peek(lds0 + F_TOEP + chl * 4));
    ST(15);
    const int bb = lane & 15, gg = lane >> 4;
    const u32 aA = tbase + (bb & 3) * TOEP_CP + ((bb >> 2) + 2 * gg) * 8 + half * 256;
    const u32 aB = ub + (30 - 2 * bb + gg) * 16 + half * 256;
    f32x4 ac8[8] = {};
    if (!PRIOALL) __builtin_amdgcn_s_setprio(CPRIO);
    conv_body(aA, aB, ac8);
    ST(16);
    f32x4 acc[4];
    {
        const u32 xo = ub + U_SZ + lane * 16;
#pragma unroll
        for (int i = 0; i < 4; ++i) { const f32x4 give = half ? ac8[i] : ac8[4 + i]; acc[i] = half ? ac8[4 + i] : ac8[i]; sts16(xo + half * 4096 + i * 1024, __builtin_bit_cast(u32x4, give)); }
        asm volatile("s_waitcnt lgkmcnt(0)" ::: "memory");
        lds_set(lds0 + F_XCH + cw * 4);
        lds_wait(lds0 + F_XCH + (cw ^ 1) * 4);
        f32x4 got[4];
#pragma unroll
        for (int i = 0; i < 4; ++i) got[i] = __builtin_bit_cast(f32x4, lds16(xo + (half ^ 1) * 4096 + i * 1024));
        SB();
#pragma unroll
        for (int i = 0; i < 4; ++i) acc[i] = f32x4{add1(acc[i][0], got[i][0]), add1(acc[i][1], got[i][1]), add1(acc[i][2], got[i][2]), add1(acc[i][3], got[i][3])};
    }
    ST(8);
    {
        const float dd = bflo(dv);
        char* yb = ws + WS_YT + (size_t)chan * TP;
        u32x2 uq[4];
#pragma unroll
        for (int i = 0; i < 4; ++i) uq[i] = lds8(ub + (2044 - (256 * (4 * half + i) + 16 * bb + 4 * gg)) * 2);
        SB();
#pragma unroll
        for (int i = 0; i < 4; ++i) {
            const int qt = 4 * half + i;
            const int t = 256 * qt + 16 * bb + 4 * gg;
            const u32x2 uu = uq[i];
            const u32 c01 = pk2(acc[i][0], acc[i][1]), c23 = pk2(acc[i][2], acc[i][3]);
            const u32 d01 = pk2(mul1(bfhi(uu.y), dd), mul1(bflo(uu.y), dd)), d23 = pk2(mul1(bfhi(uu.x), dd), mul1(bflo(uu.x), dd));
            float y0 = add1(bflo(d01), bflo(c01)), y1 = add1(bfhi(d01), bfhi(c01));
            float y2 = add1(bflo(d23), bflo(c23)), y3 = add1(bfhi(d23), bfhi(c23));
            gst16c<0>((u32)(t * 4), yb, u32x4{pk2(y0, y1), tlo, pk2(y2, y3), tlo});
        }
        lds_arrive_last(lds0 + F_YCNT, 4);
    }
    if (cw < 2) {
        G2Ctx g2; make_g2(g2, p, q, 2 * cw, lane, lds0);
        const u32 gp = lds0 + F_GP + cw * 8, wf_ = lds0 + F_WF + cw * 8;
#define SERVE(C) do { if constexpr ((C) >= WC0) { const char* sb = g2_wsrc<C>(g2); \
            const u32x2 pr = *(volatile LAS u32x2*)gp; \
            if ((C) > WC0 && (u32)__builtin_amdgcn_readfirstlane(pr.x < pr.y ? pr.x : pr.y) < (C) - 4) { WAITV(0); sts8(wf_, u32x2{(C), (C)}); } \
            _Pragma("unroll") for (int i = 0; i < 2; ++i) { lds_wait_ge(gp + i * 4, (C) - 4, i ? pr.y : pr.x); setm0(g2_loc<C>(g2) + i * 4096); \
                dmai<0>(g2.voffw[0], sb + i * 32 * 896); dmai<1024>(g2.voffw[1], sb + i * 32 * 896); dmai<2048>(g2.voffw[2], sb + i * 32 * 896); dmai<3072>(g2.voffw[3], sb + i * 32 * 896); } \
            if constexpr ((C) > WC0 + 1) { WAITV(16); sts8(wf_, u32x2{(C) - 1, (C) - 1}); } } } while (0)
        SERVE(5); SERVE(6); SERVE(7); SERVE(8); SERVE(9); SERVE(10); SERVE(11); SERVE(12); SERVE(13);
#undef SERVE
        WAITV(8); sts8(wf_, u32x2{13u, 13u});
        WAITV(0); sts8(wf_, u32x2{14u, 14u});
        ST(9);
    } else {
        const int kw = cw - 2;
        lds_wait_ge(lds0 + F_YCNT, 4, lds_peek(lds0 + F_YCNT)); lds_wait_ge(lds0 + F_HCNT, 4, lds_peek(lds0 + F_HCNT));
        const u32 slab = slab_base(lds0, 2 * kw) + lane * 64;
        float xn[32];
        {
            const u32 rf = *(const LAS u32*)(lds0 + L_ROWF + lane * 4);
            const float nm = -bflo(rf); float ri = __builtin_amdgcn_rcpf(bfhi(rf));
            asm volatile("s_nop 1" : "+v"(ri));
#pragma unroll
            for (int j = 0; j < 8; ++j) {
                const u32 lo = xs[j >> 1][(j & 1) * 2], hi_ = xs[j >> 1][(j & 1) * 2 + 1];
                const f32x4 v = {bflo(lo), bfhi(lo), bflo(hi_), bfhi(hi_)};
                sts16(slab + (j >> 2) * 8192 + 4096 + (j & 3) * 16, __builtin_bit_cast(u32x4, v));
#pragma unroll
                for (int e = 0; e < 4; e += 2) {
                    const u32 dx = pk2(add1(v[e], nm), add1(v[e + 1], nm)), xr = pk2(mul1(bflo(dx), ri), mul1(bfhi(dx), ri));
                    xn[4 * j + e] = bflo(xr); xn[4 * j + e + 1] = bfhi(xr);
                }
            }
            if (kw == 0) *(LAS float*)(lds0 + L_R2 + lane * 4) = ri * ri;
            else *(LAS float*)(lds0 + L_B2F + lane * 4) = bflo(*(const LAS u16*)(lds0 + L_B2 + lane * 2));
        }
        const char* yb = ws + WS_YT + (size_t)(q.D0 + 32 * kw) * TP + q.T0 * 4;
        const u32 vo = (u32)(gg * TP + bb * 16);
        u32x4 y[8];
        probe<YSLP>((u32)(s31 * TP), yb, tlo);
        _Pragma("clang loop unroll(disable)") for (int spin = 0; spin < SPIN_CAP; ++spin) {
#pragma unroll
            for (int i = 0; i < 8; ++i) y[i] = gld16c<0>(vo, yb + i * 4 * TP);
            asm volatile("s_waitcnt vmcnt(0)" : "+v"(y[0]), "+v"(y[1]), "+v"(y[2]), "+v"(y[3]), "+v"(y[4]), "+v"(y[5]), "+v"(y[6]), "+v"(y[7]) :: "memory");
            u32 bad = 0u;
#pragma unroll
            for (int i = 0; i < 8; ++i) bad = chk(y[i].w, tlo, chk(y[i].y, tlo, bad));
            if (!__any(bad != 0u)) break;
            __builtin_amdgcn_s_sleep(4);
        }
        ST(9);
        const u32 ys = lds0 + L_CONV + kw * CONV_CH + TOEP_CH + U_SZ;
#pragma unroll
        for (int i = 0; i < 8; ++i) sts8(ys + (4 * i + gg) * 144 + bb * 8, u32x2{y[i].x, y[i].z});
        const int qq = (lane & 15) >> 2, pp = lane & 3;
        const u32 a = ys + qq * 144 + gg * 32 + pp * 8;
        u32x2 t[8];
#pragma unroll
        for (int j = 0; j < 8; ++j) t[j] = ldtr(a + j * 4 * 144);
        SB();
#pragma unroll
        for (int j = 0; j < 8; ++j)
        {
            const u32 s01 = pk2(mul1(bflo(t[j].x), xn[4 * j]), mul1(bfhi(t[j].x), xn[4 * j + 1])), s23 = pk2(mul1(bflo(t[j].y), xn[4 * j + 2]), mul1(bfhi(t[j].y), xn[4 * j + 3]));
            sts16(slab + (j >> 2) * 8192 + (j & 3) * 16, u32x4{s01 << 16, s01 & 0xffff0000u, s23 << 16, s23 & 0xffff0000u});
        }
        if (q.b == 0 && kw == 1) {
            _Pragma("clang loop unroll(disable)") for (int spin = 0; spin < SPIN_CAP; ++spin) {
                u32x4 f0 = gld16c<0>(lane * 16u, ws + WS_ALL), f1 = gld16c<1024>((lane < 48 ? lane : 0) * 16u, ws + WS_ALL);
                asm volatile("s_waitcnt vmcnt(0)" : "+v"(f0), "+v"(f1) :: "memory");
                bool ok = f0.x == tlo && f0.y == thi && f0.z == tlo && f0.w == thi && f1.x == tlo && f1.y == thi && f1.z == tlo && f1.w == thi;
                if (__all(ok)) break;
                __builtin_amdgcn_s_sleep(SLP);
            }
            if (lane == 0) { const u64 e1 = epoch + 1; gst8c<0>(0u, ws + WS_EPOCH, u32x2{(u32)e1, (u32)(e1 >> 32)}); }
        }
    }
    __builtin_amdgcn_s_setprio(0);
    BARL(); ST(10);
    final_epilogue(p, q, lds0);
    if (DIAG == 3) { const u64 e = __builtin_amdgcn_s_memrealtime(); const u64 e2 = e + 3 * (e - dg0); while (__builtin_amdgcn_s_memrealtime() < e2) __builtin_amdgcn_s_sleep(1); }
}

DI float dpp_xor_sum8(float v) {
    v += __uint_as_float(__builtin_amdgcn_mov_dpp(__float_as_uint(v), 0xB1, 0xf, 0xf, true));
    v += __uint_as_float(__builtin_amdgcn_mov_dpp(__float_as_uint(v), 0x4E, 0xf, 0xf, true));
    v += __uint_as_float(__builtin_amdgcn_mov_dpp(__float_as_uint(v), 0x141, 0xf, 0xf, true));
    return v;
}
DI void body_help(const Params& p, const int wave) {
    const u64 dg0 = __builtin_amdgcn_s_memrealtime();
    const u32 lds0 = (u32)(size_t)(LAS char*)smem;
    const int lane = threadIdx.x & 63, r8 = lane >> 3, c8 = lane & 7;
    const int xcd = blockIdx.x & 7, j = blockIdx.x >> 3;
    char* ws = p.ws;
    u32x2 ep = gld8c(0u, ws + WS_EPOCH);
    u32x4 d[14];
    const int ctile = (xcd >> 1) * 4 + j;
    const bool cols = wave >= 4;
    if (!cols) {
        const char* xb = (const char*)p.x + (size_t)(256 * xcd + 64 * j + 16 * wave) * 896;
#pragma unroll
        for (int i = 0; i < 14; ++i) d[i] = gld16((u32)((r8 + 8 * (i / 7)) * 896 + (i % 7) * 128 + c8 * 16), xb);
    } else {
        const char* wb = (const char*)p.w1 + (size_t)(112 * (wave - 4)) * 3584 + ((xcd & 1) * 14 + (ctile < 14 ? ctile : 0)) * 128;
#pragma unroll
        for (int i = 0; i < 14; ++i) d[i] = gld16((u32)((r8 + 8 * i) * 3584 + c8 * 16), wb);
    }
    setm0(lds0 + 16384 + wave * 4096);
#pragma unroll
    for (int c = 0; c < WEARLY; ++c) {
        int cc = c + (2 * xcd) % 14; if (cc >= 14) cc -= 14;
        const char* sb = (const char*)p.w2 + (size_t)cc * 114688 + (j * 8 + wave) * 3584;
        if (lane < 32) dmai<3072>(lane * 16u, sb);
        dmai<0>(lane * 16u, sb); dmai<1024>(lane * 16u, sb); dmai<2048>(lane * 16u, sb);
    }
    asm volatile("s_waitcnt vmcnt(%15)" : "+v"(ep), "+v"(d[0]), "+v"(d[1]), "+v"(d[2]), "+v"(d[3]), "+v"(d[4]), "+v"(d[5]), "+v"(d[6]), "+v"(d[7]), "+v"(d[8]), "+v"(d[9]),
                 "+v"(d[10]), "+v"(d[11]), "+v"(d[12]), "+v"(d[13]) : "i"(4 * WEARLY) : "memory");
    const u32 tlo = (u32)((((u64)ep.y << 32) | ep.x) + TAG_SALT);
    if (!cols) {
#pragma unroll
        for (int h = 0; h < 2; ++h) {
            float s0 = 0.f, s1 = 0.f, q0 = 0.f, q1 = 0.f;
#pragma unroll
            for (int i = 0; i < 7; ++i)
#pragma unroll
                for (int e = 0; e < 4; e += 2) {
                    s0 = dot2(d[7 * h + i][e], 0x3f803f80u, s0); q0 = dot2(d[7 * h + i][e], d[7 * h + i][e], q0);
                    s1 = dot2(d[7 * h + i][e + 1], 0x3f803f80u, s1); q1 = dot2(d[7 * h + i][e + 1], d[7 * h + i][e + 1], q1);
                }
            const float mean = dpp_xor_sum8(s0 + s1) * (1.f / 448.f);
            const float var = fmaxf(dpp_xor_sum8(q0 + q1) * (1.f / 448.f) - mean * mean, 0.f);
            const float sd = __builtin_amdgcn_sqrtf(rbf(rbf(var) + 1e-5f));
            if (c8 == 0) gst8c<0>((u32)(WS_RG + (256 * xcd + 64 * j + 16 * wave + 8 * h + r8) * 8), ws, u32x2{pk2(mean, sd), tlo});
        }
    } else {
        float a[8] = {};
        u32 klo = 0x00003f80u, khi = 0x3f800000u; PIN(klo); PIN(khi);
#pragma unroll
        for (int i = 0; i < 14; ++i)
#pragma unroll
            for (int e = 0; e < 4; ++e) { a[2 * e] = dot2(d[i][e], klo, a[2 * e]); a[2 * e + 1] = dot2(d[i][e], khi, a[2 * e + 1]); }
        const u32 o = lds0 + ((wave - 4) * 8 + r8) * 272 + c8 * 32;
        sts16(o, __builtin_bit_cast(u32x4, f32x4{a[0], a[1], a[2], a[3]})); sts16(o + 16, __builtin_bit_cast(u32x4, f32x4{a[4], a[5], a[6], a[7]}));
    }
    BARL();
    if (wave == 4 && ctile < 14) {
        float t0 = 0.f, t1 = 0.f;
#pragma unroll
        for (int i = 0; i < 32; i += 2) { t0 += *(const LAS float*)(lds0 + i * 272 + lane * 4); t1 += *(const LAS float*)(lds0 + (i + 1) * 272 + lane * 4); }
        gst8c<0>((u32)(WS_CG + (((xcd & 1) * 14 + ctile) * 64 + lane) * 8), ws, u32x2{pk2(t0 + t1, (t0 + t1) - rbf(t0 + t1)), tlo});
    }
    BARL();
    {
        const int crot = (2 * xcd) % 14;
        setm0(lds0 + 16384 + wave * 4096);
        for (int c = WEARLY; c < 14; ++c) {
            int cc = c + crot; if (cc >= 14) cc -= 14;
            const char* sb = (const char*)p.w2 + (size_t)cc * 114688 + (j * 8 + wave) * 3584;
            if (lane < 32) dmai<3072>(lane * 16u, sb);
            dmai<0>(lane * 16u, sb); dmai<1024>(lane * 16u, sb); dmai<2048>(lane * 16u, sb);
            WAITV(WDEPTH);
        }
        WAITV(0);
    }
    if (DIAG == 4) { const u64 e = __builtin_amdgcn_s_memrealtime(); const u64 e2 = e + 3 * (e - dg0); while (__builtin_amdgcn_s_memrealtime() < e2) __builtin_amdgcn_s_sleep(1); }
}

__global__ __launch_bounds__(512, 2) void fused_kernel(Params p) {
    {
        u64 pc; asm volatile("s_getpc_b64 %0" : "=s"(pc));
        if (threadIdx.x < CODE_LINES) touch((const char*)(pc & ~127ull) + threadIdx.x * 128, (u32)(size_t)(LAS char*)smem + L_B2F);
    }
    const int wave = __builtin_amdgcn_readfirstlane(threadIdx.x >> 6);
    if (blockIdx.x < NHELP) { body_help(p, wave); return; }
    if (threadIdx.x < 32) *(LAS u32*)((u32)(size_t)(LAS char*)smem + L_FLAG + threadIdx.x * 4) = 0u;
    else if (threadIdx.x < 72) *(LAS u32*)((u32)(size_t)(LAS char*)smem + F_CONS + ((threadIdx.x - 32) >> 2) * 144 + (threadIdx.x & 3) * 4) = 0u;
    BARL();
    if (wave < 4) body_g(p, wave); else body_c(p, wave - 4);
}

extern "C" void kernel_launch(void* const* d_in, const int* in_sizes, int n_in,
                              void* d_out, int out_size, void* d_ws, size_t ws_size,
                              hipStream_t stream) {
    Params p;
    p.x = (const u16*)d_in[0]; p.k = (const u16*)d_in[1]; p.v = (const u16*)d_in[2]; p.shk = (const u16*)d_in[3];
    p.ssmk = (const u16*)d_in[4]; p.ssmd = (const u16*)d_in[5]; p.w1 = (const u16*)d_in[7]; p.b1 = (const u16*)d_in[8];
    p.w2 = (const u16*)d_in[9]; p.b2 = (const u16*)d_in[10];
    p.out = (u16*)d_out; p.ws = (char*)d_ws;
    (void)hipFuncSetAttribute((const void*)fused_kernel, hipFuncAttributeMaxDynamicSharedMemorySize, LDS_BYTES);
    fused_kernel<<<NWG + NHELP, 512, LDS_BYTES, stream>>>(p);
}
```

```cpp
#define CODE_LINES 268
#include <hip/hip_runtime.h>
#include <stdint.h>
#include <type_traits>

typedef unsigned short u16;
typedef unsigned int u32;
typedef unsigned long long u64;
typedef u32 u32x4 __attribute__((ext_vector_type(4)));
typedef u32 u32x2 __attribute__((ext_vector_type(2)));
typedef float f32x4 __attribute__((ext_vector_type(4)));
typedef float f32x2 __attribute__((ext_vector_type(2)));
typedef float f32x16 __attribute__((ext_vector_type(16)));
typedef __bf16 bf16x8 __attribute__((ext_vector_type(8)));
typedef __bf16 bf16x2 __attribute__((ext_vector_type(2)));
typedef short s16x4 __attribute__((ext_vector_type(4)));
#define LAS __attribute__((address_space(3)))
#define GAS __attribute__((address_space(1)))
#define DI __device__ __forceinline__

constexpr int S_ = 2048, D_ = 448, F_ = 1792;
constexpr int NHELP = 32, NWG = 224;
constexpr u32 WS_EPOCH = 0, WS_ALL = 2048, WS_RG = 4096, WS_CG = WS_RG + 16384;
constexpr u32 TP = 8192 + 128;
constexpr u32 WS_H = 1u << 20, WS_KVT = 9u << 20, WS_YT = 13u << 20;
constexpr u32 L_PIECE = 16384, L_W = 65536, L_LOC0 = 131072, LDS_BYTES = 163840;
constexpr u32 KVRAW_SZ = 5632, L_KVS = L_LOC0 + 4 * KVRAW_SZ, L_B2 = L_KVS + 9216, L_FLAG = L_B2 + 128, L_ROWF = L_FLAG + 128, L_B2F = L_ROWF + 256, L_R2 = L_B2F + 256;
static_assert(L_R2 + 256 <= LDS_BYTES, "lds");
constexpr u32 F_TOEP = L_FLAG, F_HCNT = L_FLAG + 16, F_YCNT = L_FLAG + 28, F_W = L_FLAG + 48, F_LAND = L_FLAG + 64, F_XCH = L_FLAG + 96, F_KVS = L_FLAG + 124;
constexpr u32 F_CONS = L_KVS + 128;
constexpr u32 L_CONV = 16384, CONV_CH = 65536, TOEP_CP = 4160, TOEP_CH = 4 * TOEP_CP, U_SZ = 4608;
constexpr u64 TAG_SALT = 0x9E3779B97F4A7C15ull;
constexpr int SPIN_CAP = 1 << 15;


DI void dma16(u32 voff, const void* sbase, u32 lds) {
    asm volatile("s_mov_b32 m0, %2\n\ts_nop 0\n\tglobal_load_lds_dwordx4 %0, %1" :: "v"(voff), "s"(sbase), "s"(lds) : "memory");
}
DI void setm0(u32 lds) { asm volatile("s_mov_b32 m0, %0\n\ts_nop 0" :: "s"(lds) : "memory"); }
template <int OFF> DI void dmai(u32 voff, const void* sbase) { asm volatile("global_load_lds_dwordx4 %0, %1 offset:%2" :: "v"(voff), "s"(sbase), "i"(OFF) : "memory"); }
DI void bdma16(u32 voff, u32x4 srd, u32 lds) {
    asm volatile("s_mov_b32 m0, %2\n\ts_nop 0\n\tbuffer_load_dwordx4 %0, %1, 0 offen lds" :: "v"(voff), "s"(srd), "s"(lds) : "memory");
}
#define SB() __builtin_amdgcn_sched_barrier(0)
template <int OFF> DI u32x2 alds8(u32 a) { u32x2 r; asm volatile("ds_read_b64 %0, %1 offset:%2" : "=v"(r) : "v"(a), "i"(OFF)); return r; }
template <int OFF> DI u32x4 alds16(u32 a) { u32x4 r; asm volatile("ds_read_b128 %0, %1 offset:%2" : "=v"(r) : "v"(a), "i"(OFF)); return r; }
DI u32x4 gld16(u32 voff, const void* sbase) { u32x4 r; asm volatile("global_load_dwordx4 %0, %1, %2" : "=v"(r) : "v"(voff), "s"(sbase) : "memory"); return r; }
template <int OFF> DI u32x4 gld16c(u32 voff, const void* sbase) { u32x4 r; asm volatile("global_load_dwordx4 %0, %1, %2 offset:%3 sc1" : "=v"(r) : "v"(voff), "s"(sbase), "i"(OFF) : "memory"); return r; }
DI u32x2 gld8c(u32 voff, const void* sbase) { u32x2 r; asm volatile("global_load_dwordx2 %0, %1, %2 sc1" : "=v"(r) : "v"(voff), "s"(sbase) : "memory"); return r; }
DI void touch(const void* addr, u32 lds) { asm volatile("s_mov_b32 m0, %1\n\ts_nop 0\n\tglobal_load_lds_dword %0, off" :: "v"(addr), "s"(lds) : "memory"); }
DI u32 gld2(u32 voff, const void* sbase) { u32 r; asm volatile("global_load_ushort %0, %1, %2" : "=v"(r) : "v"(voff), "s"(sbase) : "memory"); return r; }
DI u32x4 bld16(u32 voff, u32x4 srd) { u32x4 r; asm volatile("buffer_load_dwordx4 %0, %1, %2, 0 offen" : "=v"(r) : "v"(voff), "s"(srd) : "memory"); return r; }
template <int OFF> DI u32x4 gld16o(u32 voff, const void* sbase) { u32x4 r; asm volatile("global_load_dwordx4 %0, %1, %2 offset:%3" : "=v"(r) : "v"(voff), "s"(sbase), "i"(OFF) : "memory"); return r; }
template <int OFF> DI void gst16c(u32 voff, void* sbase, u32x4 v) { asm volatile("global_store_dwordx4 %0, %1, %2 offset:%3 sc1\n\ts_nop 1" :: "v"(voff), "v"(v), "s"(sbase), "i"(OFF) : "memory"); }
template <int OFF> DI void gst8c(u32 voff, void* sbase, u32x2 v) { asm volatile("global_store_dwordx2 %0, %1, %2 offset:%3 sc1" :: "v"(voff), "v"(v), "s"(sbase), "i"(OFF) : "memory"); }
#define WAITV(n) asm volatile("s_waitcnt vmcnt(%0)" :: "i"(n) : "memory")
#define WAITBAR(n) asm volatile("s_waitcnt vmcnt(%0) lgkmcnt(0)\n\ts_barrier" :: "i"(n) : "memory")
#define BARL() asm volatile("s_waitcnt lgkmcnt(0)\n\ts_barrier" ::: "memory")
#define PIN(x) asm volatile("" : "+v"(x))

DI u32x4 mksrd(const void* p, u32 bytes) { u64 a = (u64)p; return u32x4{(u32)a, (u32)(a >> 32) & 0xffffu, bytes, 0x00020000u}; }
DI float bflo(u32 x) { return __uint_as_float(x << 16); }
DI float bfhi(u32 x) { return __uint_as_float(x & 0xffff0000u); }
DI u32 pk2(float a, float b) { bf16x2 v = {(__bf16)a, (__bf16)b}; return __builtin_bit_cast(u32, v); }
DI float rbf(float a) { return bflo(pk2(a, a)); }
DI float relu(float a) { int i = __float_as_int(a); return __int_as_float(i > 0 ? i : 0); }
DI float mul1(float a, float b) { float r; asm("v_mul_f32 %0, %1, %2" : "=v"(r) : "v"(a), "v"(b)); return r; }
DI float add1(float a, float b) { float r; asm("v_add_f32 %0, %1, %2" : "=v"(r) : "v"(a), "v"(b)); return r; }
DI float dot2(u32 a, u32 b, float c) { return __builtin_amdgcn_fdot2_f32_bf16(__builtin_bit_cast(bf16x2, a), __builtin_bit_cast(bf16x2, b), c, false); }
DI u32x4 lds16(u32 a) { return *(const LAS u32x4*)a; }
DI u32x2 lds8(u32 a) { return *(const LAS u32x2*)a; }
DI void sts16(u32 a, u32x4 v) { *(LAS u32x4*)a = v; }
DI void sts8(u32 a, u32x2 v) { *(LAS u32x2*)a = v; }
DI u32x2 ldtr(u32 a) { return __builtin_bit_cast(u32x2, __builtin_amdgcn_ds_read_tr16_b64_v4i16((LAS s16x4*)a)); }
DI u32x4 cat(u32x2 a, u32x2 b) { return u32x4{a.x, a.y, b.x, b.y}; }
DI f32x16 mfma32(u32x4 a, u32x4 b, f32x16 c) { return __builtin_amdgcn_mfma_f32_32x32x16_bf16(__builtin_bit_cast(bf16x8, a), __builtin_bit_cast(bf16x8, b), c, 0, 0, 0); }
DI f32x4 mfma16(u32x4 a, u32x4 b, f32x4 c) { return __builtin_amdgcn_mfma_f32_16x16x32_bf16(__builtin_bit_cast(bf16x8, a), __builtin_bit_cast(bf16x8, b), c, 0, 0, 0); }

struct Params {
    const u16 *x, *k, *v, *shk, *ssmk, *ssmd, *w1, *b1, *w2, *b2;
    u16* out; char* ws;
};

template <int SL> DI void probe(u32 voff, const void* sbase, u32 tag) {
    _Pragma("clang loop unroll(disable)") for (int spin = 0; spin < SPIN_CAP; ++spin) {
        u32x2 f = gld8c(voff, sbase);
        asm volatile("s_waitcnt vmcnt(0)" : "+v"(f) :: "memory");
        if (__all(f.y == tag)) break;
        __builtin_amdgcn_s_sleep(SL);
    }
}
DI bool lds_arrive_last(u32 addr, u32 n) {
    u32 old = 0;
    if ((threadIdx.x & 63) == 0) old = __hip_atomic_fetch_add((LAS u32*)addr, 1u, __ATOMIC_RELAXED, __HIP_MEMORY_SCOPE_WORKGROUP);
    return (u32)__builtin_amdgcn_readfirstlane(old) == n - 1;
}
DI void lds_wait(u32 addr) {
    _Pragma("clang loop unroll(disable)") for (int spin = 0; spin < (1 << 22); ++spin) {
        if (__builtin_amdgcn_readfirstlane(*(volatile LAS u32*)addr)) break;
        __builtin_amdgcn_s_sleep(1);
    }
    asm volatile("" ::: "memory");
}
DI void lds_add(u32 addr) { asm volatile("" ::: "memory"); __hip_atomic_fetch_add((LAS u32*)addr, 1u, __ATOMIC_RELAXED, __HIP_MEMORY_SCOPE_WORKGROUP); asm volatile("" ::: "memory"); }
DI u32 lds_peek(u32 addr) { return *(volatile LAS u32*)addr; }
DI void lds_wait_ge(u32 addr, u32 n, u32 first) {
    u32 v = first;
    _Pragma("clang loop unroll(disable)") for (int spin = 0; spin < (1 << 22) && (u32)__builtin_amdgcn_readfirstlane(v) < n; ++spin) { __builtin_amdgcn_s_sleep(1); v = lds_peek(addr); }
    asm volatile("" ::: "memory");
}
template <int t> DI void cons_set(u32 lds0, int g) { asm volatile("s_waitcnt lgkmcnt(0)\n\tds_write_b32 %0, %1 offset:%2" :: "v"(lds0 + 131072 + g * 4), "v"(1u), "i"(F_CONS + t * 144 - 131072) : "memory"); }
DI void cons_wait(u32 lds0, int t) {
    _Pragma("clang loop unroll(disable)") for (int spin = 0; spin < (1 << 22); ++spin) {
        const u32x4 v = *(volatile LAS u32x4*)(lds0 + F_CONS + t * 144);
        if (__builtin_amdgcn_readfirstlane(v.x & v.y & v.z & v.w)) break;
        __builtin_amdgcn_s_sleep(1);
    }
    asm volatile("" ::: "memory");
}
DI void lds_set(u32 addr) { asm volatile("" ::: "memory"); if ((threadIdx.x & 63) == 0) *(volatile LAS u32*)addr = 1u; }
constexpr u32 loc_of(int c) { return (c % 5) == 0 ? L_LOC0 : (c % 5) == 1 ? 3 * L_PIECE : (c % 5) == 2 ? L_W + 3 * L_PIECE : (c % 5) == 3 ? 0u : L_W; }

constexpr int HD = 4, HS = HD + 1;
constexpr int WS_ = 2;
constexpr int g2_younger(int c) {
    const int n = c + 1;
    if (n >= 14) return 63;
    bool sh = false, sw = n < 5; int ah = 0, aw = sw ? 1000 : 0;
    for (int i = 0; i < HD; ++i) { if (sh) ah += 4; if (i == n) sh = true; }
    for (int i = 0; i < c; ++i) {
        if (i + HD < 14) { if (sh) ah += 4; if (sw) aw += 4; if (i + HD == n) sh = true; }
        if (i + 5 < 14) { if (sh) ah += 4; if (sw) aw += 4; if (i + 5 == n) sw = true; }
    }
    return ah < aw ? ah : aw;
}

struct G2Ctx { const char* w2b; const char* hb; int crot; u32 voffw[4], ldsg, vlane, ta[3][2], smask; };
template <int C> DI int g2_chunk(const G2Ctx& x) {
    int c = C + x.crot; if (c >= 14) c -= 14;
    return c;
}
template <int C> DI const char* g2_wsrc(const G2Ctx& x) { return x.w2b + (size_t)g2_chunk<C>(x) * (128 * 896); }
template <int C> DI u32 g2_loc(const G2Ctx& x) { return loc_of(C) + x.ldsg; }
template <int C, int N> DI void g2_w1(const G2Ctx& x, const char* sb) { if constexpr (N == 0) setm0(g2_loc<C>(x)); dmai<N * 1024>(x.voffw[N], sb); }
template <int C> DI void g2_issue_w(const G2Ctx& x) { const char* sb = g2_wsrc<C>(x); g2_w1<C, 0>(x, sb); g2_w1<C, 1>(x, sb); g2_w1<C, 2>(x, sb); g2_w1<C, 3>(x, sb); }
template <int C> DI const char* g2_hsrc(const G2Ctx& x) { return x.hb + g2_chunk<C>(x) * 8192; }
template <int N> DI u32x4 g2_h1(const G2Ctx& x, const char* sb) { return gld16c<(N & 1) * 1024>(x.vlane + (N >> 1) * 112 * 1024, sb); }
template <int C> DI void g2_issue_h(const G2Ctx& x, u32x4 (&hf)[HS][4]) {
    const char* sb = g2_hsrc<C>(x);
    hf[C % HS][0] = g2_h1<0>(x, sb); hf[C % HS][1] = g2_h1<1>(x, sb); hf[C % HS][2] = g2_h1<2>(x, sb); hf[C % HS][3] = g2_h1<3>(x, sb);
}
template <int C> DI void g2_read(const G2Ctx& x, u32x4 (&wf)[WS_][4]) {
    const u32 a0 = x.ta[loc_of(C) >> 16][0], a1 = x.ta[loc_of(C) >> 16][1];
    constexpr u32 lo = loc_of(C) & 0xffffu;
#pragma unroll
    for (int ks = 0; ks < 2; ++ks) {
        wf[C % WS_][ks * 2] = cat(ldtr(a0 + lo + ks * 2048), ldtr(a0 + lo + ks * 2048 + 1024));
        wf[C % WS_][ks * 2 + 1] = cat(ldtr(a1 + lo + ks * 2048), ldtr(a1 + lo + ks * 2048 + 1024));
    }
}
template <int C, int I> DI void g2_rd(const G2Ctx& x, u32x4 (&wf)[WS_][4]) {
    if constexpr (C < 14) {
        const u32 a = x.ta[loc_of(C) >> 16][I & 1]; constexpr u32 lo = (loc_of(C) & 0xffffu) + (I >> 1) * 2048;
        wf[C % WS_][I] = cat(ldtr(a + lo), ldtr(a + lo + 1024));
    }
}
DI u32 chk(u32 d, u32 m, u32 bad) { return __builtin_amdgcn_bitop3_b32(d, m, bad, 0xBE); }
template <int C> DI void g2_retry(const G2Ctx& x, u32x4 (&hf)[HS][4], u32 bad) {
    if (__builtin_expect(!__any((bad & 0x80008000u) != 0u), 1)) return;
    _Pragma("clang loop unroll(disable)") for (int spin = 0; spin < SPIN_CAP; ++spin) {
        __builtin_amdgcn_s_sleep(8);
        g2_issue_h<C>(x, hf);
        asm volatile("s_waitcnt vmcnt(0)" : "+v"(hf[C % HS][0]), "+v"(hf[C % HS][1]), "+v"(hf[C % HS][2]), "+v"(hf[C % HS][3]) :: "memory");
        bad = 0u;
#pragma unroll
        for (int i = 0; i < 16; ++i) { hf[C % HS][i >> 2][i & 3] ^= x.smask; bad |= hf[C % HS][i >> 2][i & 3]; }
        if (!__any((bad & 0x80008000u) != 0u)) break;
    }
}
template <int C> DI void g2_step(const G2Ctx& x, u32x4 (&hf)[HS][4], f32x16 (&acc)[2][2], u32x4 (&wf)[WS_][4]) {
    constexpr int N = (C + 1 < 14 ? C + 1 : C) % HS;
    asm volatile("s_waitcnt vmcnt(%4) lgkmcnt(0)" : "+v"(hf[N][0]), "+v"(hf[N][1]), "+v"(hf[N][2]), "+v"(hf[N][3]) : "i"(g2_younger(C)) : "memory");
    SB();
    constexpr bool DH = C + HD < 14, DW = C + 5 < 14, DC = C + 1 < 14;
    const char* hs = g2_hsrc<(DH ? C + HD : 0)>(x);
    const char* wsrc = g2_wsrc<(DW ? C + 5 : 0)>(x);
    u32 bad = 0u;
#define G2M(i) acc[((i) >> 1) & 1][(i) & 1] = mfma32(wf[C % WS_][((i) >> 2) * 2 + (((i) >> 1) & 1)], hf[C % HS][((i) & 1) * 2 + ((i) >> 2)], acc[((i) >> 1) & 1][(i) & 1]); \
               if constexpr (DC) { hf[N][(i) >> 1][((i) & 1) * 2] ^= x.smask; hf[N][(i) >> 1][((i) & 1) * 2 + 1] ^= x.smask; bad = bad | hf[N][(i) >> 1][((i) & 1) * 2] | hf[N][(i) >> 1][((i) & 1) * 2 + 1]; }
    G2M(0); g2_rd<C + 1, 0>(x, wf); if constexpr (DH) hf[(C + HD) % HS][0] = g2_h1<0>(x, hs); SB();
    G2M(1); g2_rd<C + 1, 1>(x, wf); if constexpr (DH) hf[(C + HD) % HS][1] = g2_h1<1>(x, hs); SB();
    G2M(2); g2_rd<C + 1, 2>(x, wf); if constexpr (DH) hf[(C + HD) % HS][2] = g2_h1<2>(x, hs); SB();
    G2M(3); g2_rd<C + 1, 3>(x, wf); if constexpr (DH) hf[(C + HD) % HS][3] = g2_h1<3>(x, hs); SB();
    G2M(4); if constexpr (DW) g2_w1<C + 5, 0>(x, wsrc); SB();
    G2M(5); if constexpr (DW) g2_w1<C + 5, 1>(x, wsrc); SB();
    G2M(6); if constexpr (DW) g2_w1<C + 5, 2>(x, wsrc); SB();
    G2M(7); if constexpr (DW) g2_w1<C + 5, 3>(x, wsrc); SB();
#undef G2M
    if constexpr (DC) { g2_retry<C + 1>(x, hf, bad); g2_step<C + 1>(x, hf, acc, wf); }
}

struct CF { u32x2 a0[8], a1[8]; u32x4 b[8]; };
template <int S, int R> DI void cv_ld(CF& f, u32 aA, u32 aB) {
    f.a0[R] = alds8<R * 512 + S * 64>(aA); f.a1[R] = alds8<R * 512 + S * 64 + 8>(aA);
    f.b[R] = alds16<(224 - 32 * R + 4 * S) * 16>(aB);
}
DI void cv_wait(CF& f) {
    asm volatile("s_waitcnt lgkmcnt(0)" : "+v"(f.a0[0]), "+v"(f.a0[1]), "+v"(f.a0[2]), "+v"(f.a0[3]), "+v"(f.a0[4]), "+v"(f.a0[5]), "+v"(f.a0[6]), "+v"(f.a0[7]),
                 "+v"(f.a1[0]), "+v"(f.a1[1]), "+v"(f.a1[2]), "+v"(f.a1[3]), "+v"(f.a1[4]), "+v"(f.a1[5]), "+v"(f.a1[6]), "+v"(f.a1[7]),
                 "+v"(f.b[0]), "+v"(f.b[1]), "+v"(f.b[2]), "+v"(f.b[3]), "+v"(f.b[4]), "+v"(f.b[5]), "+v"(f.b[6]), "+v"(f.b[7]));
}
constexpr int cv_r(int m) { int r = 0; while (m >= 8 - r) { m -= 8 - r; ++r; } return r; }
constexpr int cv_c(int m) { int r = 0; while (m >= 8 - r) { m -= 8 - r; ++r; } return m; }
template <int S, int M> DI void cv_mm(CF& f, CF& n, u32 aA, u32 aB, f32x4 (&acc)[8]) {
    constexpr int r = cv_r(M), c = cv_c(M);
    acc[r + c] = mfma16(cat(f.a0[r], f.a1[r]), f.b[c], acc[r + c]);
    if constexpr ((M & 3) == 0 && M / 4 < 8) cv_ld<S + 1, M / 4>(n, aA, aB);
    SB();
    if constexpr (M + 1 < 36) cv_mm<S, M + 1>(f, n, aA, aB, acc);
}
DI void conv_body(u32 aA, u32 aB, f32x4 (&acc)[8]) {
    CF f, n;
    asm volatile("s_waitcnt lgkmcnt(0)" ::: "memory");
    cv_ld<0, 0>(f, aA, aB); cv_ld<0, 1>(f, aA, aB); cv_ld<0, 2>(f, aA, aB); cv_ld<0, 3>(f, aA, aB);
    cv_ld<0, 4>(f, aA, aB); cv_ld<0, 5>(f, aA, aB); cv_ld<0, 6>(f, aA, aB); cv_ld<0, 7>(f, aA, aB);
    _Pragma("clang loop unroll(disable)") for (int it = 0; it < 2; ++it) {
        cv_wait(f); SB();
        cv_mm<0, 0>(f, n, aA, aB, acc);
        cv_wait(n); SB();
        cv_mm<1, 0>(n, f, aA, aB, acc);
        aA += 128; aB += 128;
    }
    cv_wait(f);
    asm volatile("" ::: "memory");
}

struct Geo { int b, xcd, fh2, RB, CT, tb64, db, rot, crot; u32 R0, F0, T0, D0; };
DI Geo make_geo() {
    Geo g; g.b = blockIdx.x - NHELP; g.xcd = g.b & 7; const int bi = g.b >> 3;
    const int rq = g.xcd >> 1, rb = bi / 7, ct = bi - rb * 7;
    g.fh2 = g.xcd & 1; g.RB = 4 * rq + rb; g.CT = 7 * g.fh2 + ct; g.R0 = 128 * g.RB; g.F0 = 128 * g.CT;
    g.tb64 = 2 * g.RB + g.fh2; g.db = ct; g.T0 = 64 * g.tb64; g.D0 = 64 * g.db;
    g.rot = g.xcd >= 7 ? 0 : g.xcd; g.crot = g.CT;
    return g;
}
DI void make_g2(G2Ctx& g2, const Params& p, const Geo& q, int g, int lane, u32 lds0) {
    g2.w2b = (const char*)p.w2 + (size_t)(32 * g) * 896 + q.D0 * 2;
    g2.hb = p.ws + WS_H + (size_t)(q.tb64 * 2 * 112 + 2 * g) * 1024;
    g2.crot = q.crot;
#pragma unroll
    for (int n = 0; n < 4; ++n) g2.voffw[n] = (u32)((lane >> 3) * 896 + 16 * ((lane & 7) ^ (((lane >> 4) & 1) << 2)) + n * (8 * 896 - 1024));
    g2.ldsg = lds0 + g * 4096; g2.vlane = lane * 16u;
}
DI u32 slab_base(u32 lds0, int cw) { return lds0 + L_CONV + (cw >> 1) * CONV_CH + (cw & 1) * 8192; }
DI void final_epilogue(const Params& p, const Geo& q, u32 lds0) {
    int tz = threadIdx.x; PIN(tz);
    const int t = tz >> 3, c = tz & 7;
    const u32 pb = lds0 + ((t >> 5) ? 0u : 3 * L_PIECE) + ((t >> 4) & 1) * L_W + (t & 15) * 256;
    const u32 o0 = pb + 16 * ((2 * c) ^ (t & 7)), o1 = o0 ^ 16;
    f32x4 pa[4], pb_[4];
#pragma unroll
    for (int g = 0; g < 4; ++g) { pa[g] = __builtin_bit_cast(f32x4, lds16(o0 + g * 4096)); pb_[g] = __builtin_bit_cast(f32x4, lds16(o1 + g * 4096)); }
    const float r2 = *(const LAS float*)(lds0 + L_R2 + t * 4);
    const f32x4 b0 = __builtin_bit_cast(f32x4, lds16(lds0 + L_B2F + c * 32)), b1 = __builtin_bit_cast(f32x4, lds16(lds0 + L_B2F + c * 32 + 16));
    const u32 sb = slab_base(lds0, c >> 1) + t * 64 + (c & 1) * 32;
    const f32x4 sv0 = __builtin_bit_cast(f32x4, lds16(sb)), sv1 = __builtin_bit_cast(f32x4, lds16(sb + 16));
    const f32x4 xv0 = __builtin_bit_cast(f32x4, lds16(sb + 4096)), xv1 = __builtin_bit_cast(f32x4, lds16(sb + 4096 + 16));
    SB();
    const f32x4 s0 = (pa[0] + pa[1]) + (pa[2] + pa[3]), s1 = (pb_[0] + pb_[1]) + (pb_[2] + pb_[3]);
    f32x4 z0 = s0 * r2 + b0, z1 = s1 * r2 + b1, e0, e1;
#pragma unroll
    for (int j = 0; j < 4; ++j) { e0[j] = __builtin_amdgcn_exp2f(z0[j]); e1[j] = __builtin_amdgcn_exp2f(z1[j]); }
    e0 += 1.f; e1 += 1.f;
#pragma unroll
    for (int j = 0; j < 4; ++j) { e0[j] = __builtin_amdgcn_rcpf(e0[j]); e1[j] = __builtin_amdgcn_rcpf(e1[j]); }
    z0 = z0 * e0; z1 = z1 * e1;
    z0 = z0 * sv0 + xv0; z1 = z1 * sv1 + xv1;
    const u32x4 res = {pk2(z0[0], z0[1]), pk2(z0[2], z0[3]), pk2(z1[0], z1[1]), pk2(z1[2], z1[3])};
    gst16c<0>((u32)((q.T0 + t) * 896 + q.D0 * 2 + c * 16), p.out, res);
}

extern __shared__ __attribute__((aligned(16))) char smem[];

struct CL { const char *xb, *wb; u32 vx[4], vw[4], sl; int rot; };
struct CSrc { const char *xs, *wsrc; };
template <int ST_> DI CSrc c_src(const CL& c) { int kc = ST_ + c.rot; if (kc >= 7) kc -= 7; return CSrc{c.xb + kc * 128, c.wb + (size_t)kc * 64 * 3584}; }
template <int ST_, int N> DI void c_dma1(const CL& c, const CSrc& s) {
    if constexpr (N == 0) setm0(c.sl + (ST_ & 3) * L_PIECE);
    if constexpr (N == 4) setm0(c.sl + (ST_ & 3) * L_PIECE + L_W);
    if constexpr (N < 4) dmai<N * 1024>(c.vx[N], s.xs); else dmai<(N - 4) * 1024>(c.vw[N - 4], s.wsrc);
}
template <int ST_> DI void c_dma(const CL& c) {
    const CSrc s = c_src<ST_>(c);
    c_dma1<ST_, 0>(c, s); c_dma1<ST_, 1>(c, s); c_dma1<ST_, 2>(c, s); c_dma1<ST_, 3>(c, s);
    c_dma1<ST_, 4>(c, s); c_dma1<ST_, 5>(c, s); c_dma1<ST_, 6>(c, s); c_dma1<ST_, 7>(c, s);
}
DI void make_cl(CL& c, const Params& p, const Geo& q, int cw, int lane, u32 lds0) {
    const int r8 = lane >> 3, c8 = lane & 7, r4 = lane >> 4, c16 = lane & 15;
#pragma unroll
    for (int n = 0; n < 4; ++n) {
        c.vx[n] = (u32)((32 * cw + r8) * 896 + 16 * (c8 ^ (((n & 1) << 2) | (r8 >> 1))) + n * (8 * 896 - 1024));
        c.vw[n] = (u32)((16 * cw + r4) * 3584 + 16 * (c16 ^ (r4 << 2)) + n * (4 * 3584 - 1024));
    }
    c.xb = (const char*)p.x + (size_t)q.R0 * 896; c.wb = (const char*)p.w1 + q.F0 * 2;
    c.sl = lds0 + cw * 4096; c.rot = q.rot;
}

template <int ST_> DI void l_dma(const CL& c) {
    const CSrc s = c_src<ST_>(c);
    const u32 sl = c.sl + (ST_ & 3) * L_PIECE;
    setm0(sl); dmai<0>(c.vx[0], s.xs); dmai<1024>(c.vx[1], s.xs); dmai<2048>(c.vx[2], s.xs); dmai<3072>(c.vx[3], s.xs);
    setm0(sl + L_W); dmai<0>(c.vw[0], s.wsrc); dmai<1024>(c.vw[1], s.wsrc); dmai<2048>(c.vw[2], s.wsrc); dmai<3072>(c.vw[3], s.wsrc);
    const char* x2 = s.xs + 32 * 896; const char* w2 = s.wsrc + 16 * 3584;
    setm0(sl + 4096); dmai<0>(c.vx[0], x2); dmai<1024>(c.vx[1], x2); dmai<2048>(c.vx[2], x2); dmai<3072>(c.vx[3], x2);
    setm0(sl + L_W + 4096); dmai<0>(c.vw[0], w2); dmai<1024>(c.vw[1], w2); dmai<2048>(c.vw[2], w2); dmai<3072>(c.vw[3], w2);
}

struct Fr { u32x4 x[4][2], w[4][2]; };
struct GA { u32 ax[4], aw[2]; };
template <int K0> DI void g1_mma(const Fr& f, f32x16 (&acc)[2][2]) {
#pragma unroll
    for (int k = K0; k < K0 + 2; ++k) {
        acc[0][0] = mfma32(f.w[k][0], f.x[k][0], acc[0][0]); acc[1][0] = mfma32(f.w[k][1], f.x[k][0], acc[1][0]);
        acc[0][1] = mfma32(f.w[k][0], f.x[k][1], acc[0][1]); acc[1][1] = mfma32(f.w[k][1], f.x[k][1], acc[1][1]);
    }
    SB();
}

template <int ST_, int I> DI void g1_rd(const GA& a, Fr& f) {
    constexpr u32 so = (ST_ & 3) * L_PIECE; constexpr int k = I >> 1, h = I & 1;
    f.w[k][h] = cat(ldtr(a.aw[h] + so + k * 4096), ldtr(a.aw[h] + so + k * 4096 + 1024));
    f.x[k][h] = lds16(a.ax[k] + so + h * 4096);
}
template <int K0, int I> DI void g1_m1(const Fr& f, f32x16 (&acc)[2][2]) {
    constexpr int k = K0 + (I >> 2), mt = I & 1, st = (I >> 1) & 1;
    acc[mt][st] = mfma32(f.w[k][mt], f.x[k][st], acc[mt][st]);
}
template <int ST_, bool CARRY> DI void g1_stage(const GA& a, const Fr& fp, Fr& fn, f32x16 (&acc)[2][2], u32 lds0, int g, u32& pk) {
    if constexpr (CARRY) { g1_m1<2, 0>(fp, acc); SB(); cons_set<ST_ - 1>(lds0, g); SB(); }
    lds_wait_ge(lds0 + F_LAND + 4 * ST_, ST_ == 0 ? 256 : (ST_ == 3 || ST_ == 6) ? 128 : 64, pk);
    if constexpr (CARRY) {
        g1_rd<ST_, 0>(a, fn); SB();
        g1_m1<2, 1>(fp, acc); SB(); g1_m1<2, 2>(fp, acc); g1_rd<ST_, 1>(a, fn); SB();
        g1_m1<2, 3>(fp, acc); SB(); g1_m1<2, 4>(fp, acc); g1_rd<ST_, 2>(a, fn); SB();
        g1_m1<2, 5>(fp, acc); g1_rd<ST_, 3>(a, fn); SB(); g1_m1<2, 6>(fp, acc); SB();
        g1_m1<2, 7>(fp, acc); SB();
    } else { g1_rd<ST_, 0>(a, fn); g1_rd<ST_, 1>(a, fn); g1_rd<ST_, 2>(a, fn); g1_rd<ST_, 3>(a, fn); SB(); }
    g1_m1<0, 0>(fn, acc); g1_rd<ST_, 4>(a, fn); SB(); g1_m1<0, 1>(fn, acc); SB();
    g1_m1<0, 2>(fn, acc); g1_rd<ST_, 5>(a, fn); SB(); g1_m1<0, 3>(fn, acc); g1_rd<ST_, 6>(a, fn); SB();
    g1_m1<0, 4>(fn, acc); g1_rd<ST_, 7>(a, fn); SB(); g1_m1<0, 5>(fn, acc); pk = lds_peek(lds0 + F_LAND + 4 * ST_ + 4); SB();
    g1_m1<0, 6>(fn, acc); SB(); g1_m1<0, 7>(fn, acc); SB();
}

DI void body_g(const Params& p, const int g) {
    const u32 lds0 = (u32)(size_t)(LAS char*)smem;
    const int lane = threadIdx.x & 63;
    const Geo q = make_geo();
    char* ws = p.ws;
    const int hi = lane >> 5, s31 = lane & 31;
    const int r8 = lane >> 3, c8 = lane & 7;
    const u32 raw = lds0 + L_LOC0 + g * KVRAW_SZ;
    {
        const u32 vkv = (u32)(r8 * 896 + c8 * 16);
        bdma16((u32)(((int)q.T0 + 16 * g - 8) * 896 + (int)q.D0 * 2) + vkv, mksrd(p.k, S_ * D_ * 2), raw);
        const char* kb = (const char*)p.k + (size_t)(q.T0 + 16 * g) * 896 + q.D0 * 2;
        const char* vb = (const char*)p.v + (size_t)(q.T0 + 16 * g) * 896 + q.D0 * 2;
        dmai<1024>(vkv, kb - 1024); dmai<2048>(vkv, kb + 8 * 896 - 2048); dmai<3072>(vkv, vb - 3072);
        setm0(raw + 4096);
        dmai<0>(vkv, vb + 8 * 896);
        if (lane < 32) dmai<1024>(lane * 16u, (const char*)p.shk + q.D0 * 8 - 1024);
        CL c; make_cl(c, p, q, g, lane, lds0);
        c_dma<0>(c);
    }
    u32x2 ep = gld8c(0u, ws + WS_EPOCH);
    const int fh = g & 1, sh = g >> 1;
    GA ga;
    {
        const int blk = (lane >> 4) & 1, qq = (lane & 15) >> 2, pp = lane & 3;
        const u32 Z = (u32)(hi ^ ((s31 >> 1) & 7));
#pragma unroll
        for (int k = 0; k < 4; ++k) ga.ax[k] = lds0 + (64 * sh + s31) * 128 + 16 * ((2 * k) ^ Z);
        ga.aw[0] = lds0 + L_W + (8 * hi + qq) * 256 + 16 * ((8 * fh + 2 * blk + (pp >> 1)) ^ (qq << 2)) + 8 * (pp & 1);
        ga.aw[1] = ga.aw[0] ^ 64;
    }
    WAITV(9);
    {
        u32x4 wr[4], kr[5], vr[2];
#pragma unroll
        for (int e = 0; e < 4; ++e) wr[e] = lds16(raw + 5120 + c8 * 64 + e * 16);
#pragma unroll
        for (int j = 0; j < 5; ++j) kr[j] = lds16(raw + (5 + 2 * r8 + j) * 128 + c8 * 16);
        vr[0] = lds16(raw + 3072 + 2 * r8 * 128 + c8 * 16); vr[1] = lds16(raw + 3072 + (2 * r8 + 1) * 128 + c8 * 16);
        SB();
        u32x4 o0, o1;
#pragma unroll
        for (int e = 0; e < 4; ++e) {
            u32 pl[4], ph[4];
#pragma unroll
            for (int j = 0; j < 4; ++j) { pl[j] = __builtin_amdgcn_perm(kr[j][e], kr[j + 1][e], 0x05040100u); ph[j] = __builtin_amdgcn_perm(kr[j][e], kr[j + 1][e], 0x07060302u); }
            f32x2 a = {dot2(pl[0], wr[e].y, dot2(pl[2], wr[e].x, 0.f)), dot2(ph[0], wr[e].w, dot2(ph[2], wr[e].z, 0.f))};
            f32x2 b = {dot2(pl[1], wr[e].y, dot2(pl[3], wr[e].x, 0.f)), dot2(ph[1], wr[e].w, dot2(ph[3], wr[e].z, 0.f))};
            const u32 ar = pk2(a.x, a.y), br = pk2(b.x, b.y);
            a = f32x2{bflo(ar), bfhi(ar)} * f32x2{bflo(vr[0][e]), bfhi(vr[0][e])}; b = f32x2{bflo(br), bfhi(br)} * f32x2{bflo(vr[1][e]), bfhi(vr[1][e])};
            o0[e] = pk2(a.x, a.y); o1[e] = pk2(b.x, b.y);
        }
        sts16(lds0 + L_KVS + (16 * g + 2 * r8) * 144 + c8 * 16, o0);
        sts16(lds0 + L_KVS + (16 * g + 2 * r8 + 1) * 144 + c8 * 16, o1);
        asm volatile("s_waitcnt lgkmcnt(0)" ::: "memory");
        lds_add(lds0 + F_KVS);
    }
    f32x16 acc[2][2] = {};
    Fr fa, fb;
    WAITV(1);
    lds_add(lds0 + F_LAND);
    u32 pk = lds_peek(lds0 + F_LAND);
    g1_stage<0, false>(ga, fb, fa, acc, lds0, g, pk);
    g1_stage<1, true>(ga, fa, fb, acc, lds0, g, pk);
    g1_stage<2, true>(ga, fb, fa, acc, lds0, g, pk);
    asm volatile("s_waitcnt vmcnt(0)" : "+v"(ep) :: "memory");
    const u64 epoch = ((u64)(u32)__builtin_amdgcn_readfirstlane(ep.y) << 32) | (u32)__builtin_amdgcn_readfirstlane(ep.x);
    const u64 tag = epoch + TAG_SALT;
    const u32 tlo = (u32)tag, thi = (u32)(tag >> 32);
    const u32 smask = ((lane ^ (u32)epoch) & 1u) ? 0x80008000u : 0u;
    g1_stage<3, true>(ga, fa, fb, acc, lds0, g, pk);
    const u32 rgo = WS_RG + (q.R0 + 64 * sh + lane) * 8, cgo = WS_CG + (q.F0 + 64 * fh + lane) * 8;
    u32x2 rg = gld8c(rgo, ws), cg = gld8c(cgo, ws);
    u32 b1v = gld2((u32)((64 * fh + lane) * 2), (const char*)p.b1 + q.F0 * 2);
    G2Ctx g2; make_g2(g2, p, q, g, lane, lds0);
    {
        const int blk = (lane >> 4) & 1, qq = (lane & 15) >> 2, pp = lane & 3;
        const u32 t0 = g2.ldsg + (4 * hi + qq) * 128 + 16 * ((2 * blk + (pp >> 1)) ^ ((qq >> 1) << 2)) + 8 * (pp & 1);
#pragma unroll
        for (int i = 0; i < 3; ++i) { g2.ta[i][0] = t0 + i * 65536; g2.ta[i][1] = (t0 ^ 64) + i * 65536; PIN(g2.ta[i][0]); PIN(g2.ta[i][1]); }
#pragma unroll
        for (int i = 0; i < 4; ++i) PIN(g2.voffw[i]);
    }
    g2.smask = smask;
    g1_stage<4, true>(ga, fb, fa, acc, lds0, g, pk);
    g1_stage<5, true>(ga, fa, fb, acc, lds0, g, pk);
    g1_stage<6, true>(ga, fb, fa, acc, lds0, g, pk);
    cons_set<6>(lds0, g);
    asm volatile("s_waitcnt vmcnt(0)" : "+v"(rg), "+v"(cg), "+v"(b1v) :: "memory");
    _Pragma("clang loop unroll(disable)") for (int spin = 0; spin < SPIN_CAP && !__all(rg.y == tlo && cg.y == tlo); ++spin) {
        rg = gld8c(rgo, ws); cg = gld8c(cgo, ws);
        asm volatile("s_waitcnt vmcnt(0)" : "+v"(rg), "+v"(cg) :: "memory");
    }
    if (fh == 0 && sh == q.fh2) *(LAS u32*)(lds0 + L_ROWF + lane * 4) = rg.x;
    g1_mma<2>(fa, acc);
    {
        u32x4 ax[2], bx[2];
        const auto rr = __builtin_amdgcn_permlane32_swap(rg.x, rg.x, false, false);
        const auto cc = __builtin_amdgcn_permlane32_swap(cg.x, cg.x, false, false);
        const auto bb = __builtin_amdgcn_permlane32_swap(b1v, b1v, false, false);
#pragma unroll
        for (int i = 0; i < 2; ++i) {
            const u32 nm = __builtin_amdgcn_perm(rr[i], rr[i], 0x01000100u) ^ 0x80008000u;
            ax[i] = u32x4{cc[i], bb[i], 0u, 0u};
            bx[i] = u32x4{hi ? 0u : nm, hi ? 0u : (rr[i] >> 16), 0u, 0u};
        }
#pragma unroll
        for (int st = 0; st < 2; ++st)
#pragma unroll
            for (int mt = 0; mt < 2; ++mt) acc[mt][st] = mfma32(ax[mt], bx[st], acc[mt][st]);
#pragma unroll
        for (int st = 0; st < 2; ++st) {
            char* hb = ws + WS_H + (size_t)((4 * q.RB + 2 * sh + st) * 112 + q.CT * 8 + fh * 4) * 1024;
#pragma unroll
            for (int mt = 0; mt < 2; ++mt)
#pragma unroll
                for (int t = 0; t < 2; ++t) {
                    u32x4 hv;
#pragma unroll
                    for (int j = 0; j < 4; ++j) {
                        f32x2 a = {acc[mt][st][8 * t + 2 * j], acc[mt][st][8 * t + 2 * j + 1]};
                        f32x2 u = {relu(a.x), relu(a.y)};
                        u = u * u;
                        hv[j] = (pk2(u.x, u.y) & 0x7fff7fffu) | smask;
                    }
                    if (mt == 0 && t == 0) gst16c<0>(lane * 16u, hb, hv);
                    if (mt == 0 && t == 1) gst16c<1024>(lane * 16u, hb, hv);
                    if (mt == 1 && t == 0) gst16c<2048>(lane * 16u, hb, hv);
                    if (mt == 1 && t == 1) gst16c<3072>(lane * 16u, hb, hv);
                    SB();
                }
        }
    }
    u32x4 wf[WS_][4];
    cons_wait(lds0, 4);
    asm volatile("s_waitcnt lgkmcnt(0)" ::: "memory");
    {
        u32x4 hf[HS][4];
        if (lds_arrive_last(lds0 + F_HCNT, 4)) { if (lane == 0) gst8c<0>(WS_ALL + q.b * 8, ws, u32x2{tlo, thi}); }
        g2_issue_w<2>(g2); g2_issue_w<3>(g2); g2_issue_w<4>(g2);
        WAITV(0);
        g2_issue_h<0>(g2, hf); g2_issue_h<1>(g2, hf); g2_issue_h<2>(g2, hf); g2_issue_h<3>(g2, hf);
        lds_wait(lds0 + F_W);
        g2_read<0>(g2, wf);
        asm volatile("s_waitcnt vmcnt(%4)" : "+v"(hf[0][0]), "+v"(hf[0][1]), "+v"(hf[0][2]), "+v"(hf[0][3]) : "i"(4 * (HD - 1)) : "memory");
        {
            u32 bad = 0u;
#pragma unroll
            for (int i = 0; i < 16; ++i) { hf[0][i >> 2][i & 3] ^= smask; bad |= hf[0][i >> 2][i & 3]; }
            g2_retry<0>(g2, hf, bad);
        }
        f32x16 a2[2][2] = {};
        g2_step<0>(g2, hf, a2, wf);
        const u32 pw = g2.ldsg + (s31 >> 4) * L_W + (s31 & 15) * 256;
#pragma unroll
        for (int s2 = 0; s2 < 2; ++s2)
#pragma unroll
            for (int dt = 0; dt < 2; ++dt)
#pragma unroll
                for (int gr = 0; gr < 4; ++gr) {
                    const u32 a = pw + (s2 ? 0u : 3 * L_PIECE) + 16 * ((8 * dt + 2 * gr + hi) ^ (s31 & 7));
                    sts16(a, __builtin_bit_cast(u32x4, f32x4{a2[dt][s2][4 * gr], a2[dt][s2][4 * gr + 1], a2[dt][s2][4 * gr + 2], a2[dt][s2][4 * gr + 3]}));
                }
    }
    BARL();
    final_epilogue(p, q, lds0);
}

DI void body_c(const Params& p, const int cw) {
    const u32 lds0 = (u32)(size_t)(LAS char*)smem;
    const int lane = threadIdx.x & 63;
    const Geo q = make_geo();
    char* ws = p.ws;
    const int hi = lane >> 5, s31 = lane & 31;
    const int half = cw & 1, chl = cw >> 1, chan = 2 * q.b + chl;
    u32x2 ep;
    u32x4 xs[4]; u32 dv;
    if (cw < 2) {
        CL c, c2; make_cl(c, p, q, 0, lane, lds0); c2 = c; c2.xb += 64 * 896; c2.wb += 32 * 3584; c2.sl += 8192;
        ep = gld8c(0u, ws + WS_EPOCH);
        if (cw == 0) {
            l_dma<1>(c); l_dma<1>(c2);
            WAITV(0); lds_add(lds0 + F_LAND + 4);
            cons_wait(lds0, 0); l_dma<4>(c); l_dma<4>(c2);
            WAITV(0); lds_add(lds0 + F_LAND + 16);
        } else {
            l_dma<2>(c); l_dma<2>(c2);
            WAITV(0); lds_add(lds0 + F_LAND + 8);
            cons_wait(lds0, 1); l_dma<5>(c); l_dma<5>(c2);
            WAITV(0); lds_add(lds0 + F_LAND + 20);
        }
        asm volatile("s_waitcnt vmcnt(0)" : "+v"(ep) :: "memory");
        const u32 tg32 = (u32)((((u64)ep.y << 32) | ep.x) + TAG_SALT);
        if (cw == 1) {
            G2Ctx g2; make_g2(g2, p, q, 0, lane, lds0);
            cons_wait(lds0, 3);
#define W4(C) do { const char* sb = g2_wsrc<C>(g2); _Pragma("unroll") for (int i = 0; i < 4; ++i) { setm0(g2_loc<C>(g2) + i * 4096); \
                dmai<0>(g2.voffw[0], sb + i * 32 * 896); dmai<1024>(g2.voffw[1], sb + i * 32 * 896); dmai<2048>(g2.voffw[2], sb + i * 32 * 896); dmai<3072>(g2.voffw[3], sb + i * 32 * 896); } } while (0)
            W4(0); W4(1);
#undef W4
            dv = gld2(0u, (const char*)p.ssmd + chan * 2);
            WAITV(1);
            lds_set(lds0 + F_W);
            asm volatile("s_waitcnt vmcnt(0)" : "+v"(dv) :: "memory");
        } else {
            const char* kb2 = ws + WS_KVT + (size_t)(2 * q.b) * TP;
            u32x4 u[16];
            probe<2>((u32)(hi * TP + s31 * 256), kb2, tg32);
            _Pragma("clang loop unroll(disable)") for (int spin = 0; spin < SPIN_CAP; ++spin) {
#pragma unroll
                for (int c = 0; c < 4; ++c) {
                    const char* sb = kb2 + (c >> 1) * TP + (c & 1) * 4096;
                    u[4 * c] = gld16c<0>(lane * 16u, sb); u[4 * c + 1] = gld16c<1024>(lane * 16u, sb); u[4 * c + 2] = gld16c<2048>(lane * 16u, sb); u[4 * c + 3] = gld16c<3072>(lane * 16u, sb);
                }
                asm volatile("s_waitcnt vmcnt(0)" : "+v"(u[0]), "+v"(u[1]), "+v"(u[2]), "+v"(u[3]), "+v"(u[4]), "+v"(u[5]), "+v"(u[6]), "+v"(u[7]), "+v"(u[8]), "+v"(u[9]), "+v"(u[10]),
                             "+v"(u[11]), "+v"(u[12]), "+v"(u[13]), "+v"(u[14]), "+v"(u[15]) :: "memory");
                u32 bad = 0u;
#pragma unroll
                for (int i = 0; i < 16; ++i) bad = chk(u[i].w, tg32, chk(u[i].y, tg32, bad));
                if (!__any(bad != 0u)) break;
                __builtin_amdgcn_s_sleep(4);
            }
            dv = gld2(0u, (const char*)p.ssmd + chan * 2);
            cons_wait(lds0, 5); cons_wait(lds0, 6);
            const u32 uq_ = lds0 + L_CONV + TOEP_CH + lane * 8;
#pragma unroll
            for (int i = 0; i < 16; ++i) sts8(uq_ + (i >> 3) * CONV_CH + (i & 7) * 512, u32x2{u[i].x, u[i].z});
            sts8(uq_ + 4096, u32x2{0u, 0u}); sts8(uq_ + CONV_CH + 4096, u32x2{0u, 0u});
            asm volatile("s_waitcnt lgkmcnt(0)" ::: "memory");
            lds_add(lds0 + F_TOEP); lds_add(lds0 + F_TOEP + 4);
            asm volatile("s_waitcnt vmcnt(0)" : "+v"(dv) :: "memory");
        }
    } else {
        const int kw = cw - 2;
        if (kw == 0 && lane < 8) dma16(lane * 16u, (const char*)p.b2 + q.D0 * 2, lds0 + L_B2);
        ep = gld8c(0u, ws + WS_EPOCH);
        CL c; make_cl(c, p, q, 2 * kw, lane, lds0);
        l_dma<3>(c);
        lds_wait_ge(lds0 + F_KVS, 256, lds_peek(lds0 + F_KVS));
        asm volatile("s_waitcnt vmcnt(16)" : "+v"(ep) :: "memory");
        const u32 tg32 = (u32)((((u64)ep.y << 32) | ep.x) + TAG_SALT);
        {
            const int grp = lane >> 4, qq = (lane & 15) >> 2, pp = lane & 3;
            const u32 a = lds0 + L_KVS + (16 * grp + 4 * pp + 3 - qq) * 144 + kw * 64;
            const u32 vo = (u32)((q.D0 + 32 * kw + (lane & 3)) * TP + (2044 - (int)q.T0 - 4 * (lane >> 2)) * 4);
            u32x2 tr[8];
#pragma unroll
            for (int i = 0; i < 8; ++i) tr[i] = ldtr(a + 8 * i);
            SB();
#pragma unroll
            for (int i = 0; i < 8; ++i) gst16c<0>(vo, ws + WS_KVT + i * 4 * TP, u32x4{tr[i].x, tg32, tr[i].y, tg32});
        }
        WAITV(8);
        lds_add(lds0 + F_LAND + 12);
        cons_wait(lds0, 2);
        l_dma<6>(c);
        u32x4 sk[5];
        const u32x4 sksrd = mksrd((const char*)p.ssmk + (size_t)(2 * q.b + kw) * 4096, 4096);
#pragma unroll
        for (int r = 0; r < 5; ++r) sk[r] = bld16((u32)((lane - 1 + 63 * r) * 16), sksrd);
        { const char* xsl = (const char*)p.x + (size_t)q.T0 * 896 + (q.D0 + 32 * kw) * 2;
          xs[0] = gld16o<0>((u32)(lane * 896), xsl); xs[1] = gld16o<16>((u32)(lane * 896), xsl); xs[2] = gld16o<32>((u32)(lane * 896), xsl); xs[3] = gld16o<48>((u32)(lane * 896), xsl); }
        dv = gld2(0u, (const char*)p.ssmd + chan * 2);
        WAITV(10);
        lds_add(lds0 + F_LAND + 24);
        asm volatile("s_waitcnt vmcnt(0)" : "+v"(sk[0]), "+v"(sk[1]), "+v"(sk[2]), "+v"(sk[3]), "+v"(sk[4]), "+v"(xs[0]), "+v"(xs[1]), "+v"(xs[2]), "+v"(xs[3]), "+v"(dv) :: "memory");
        u32x4 tw[5][4];
#pragma unroll
        for (int r = 0; r < 5; ++r) {
            const u32x4 B = sk[r];
            u32x4 A;
#pragma unroll
            for (int j = 0; j < 4; ++j) A[j] = __builtin_amdgcn_update_dpp(0u, B[j], 0x138  , 0xf, 0xf, false);
            const u32 e0 = __builtin_amdgcn_alignbit(A.y, A.x, 16), e1 = __builtin_amdgcn_alignbit(A.z, A.y, 16), e2 = __builtin_amdgcn_alignbit(A.w, A.z, 16),
                      e3 = __builtin_amdgcn_alignbit(B.x, A.w, 16), e4 = __builtin_amdgcn_alignbit(B.y, B.x, 16);
            tw[r][0] = u32x4{e0, e1, e2, e3}; tw[r][1] = u32x4{A.y, A.z, A.w, B.x}; tw[r][2] = u32x4{e1, e2, e3, e4}; tw[r][3] = u32x4{A.z, A.w, B.x, B.y};
            PIN(tw[r][0]); PIN(tw[r][1]); PIN(tw[r][2]); PIN(tw[r][3]);
        }
        cons_wait(lds0, 5); cons_wait(lds0, 6);
        const u32 tq = lds0 + L_CONV + kw * CONV_CH + lane * 16;
#pragma unroll
        for (int r = 0; r < 5; ++r) {
            if ((lane > 0 || r == 0) && (r < 4 || lane < 6)) {
#pragma unroll
                for (int c = 0; c < 4; ++c) sts16(tq + r * 1008 + c * TOEP_CP, tw[r][c]);
            }
        }
        asm volatile("s_waitcnt lgkmcnt(0)" ::: "memory");
        lds_add(lds0 + F_TOEP + kw * 4);
    }
    const u64 epoch = ((u64)(u32)__builtin_amdgcn_readfirstlane(ep.y) << 32) | (u32)__builtin_amdgcn_readfirstlane(ep.x);
    const u64 tag = epoch + TAG_SALT;
    const u32 tlo = (u32)tag, thi = (u32)(tag >> 32);
    const u32 tbase = lds0 + L_CONV + chl * CONV_CH;
    const u32 ub = tbase + TOEP_CH;
    lds_wait_ge(lds0 + F_TOEP + chl * 4, 128, lds_peek(lds0 + F_TOEP + chl * 4));
    const int bb = lane & 15, gg = lane >> 4;
    const u32 aA = tbase + (bb & 3) * TOEP_CP + ((bb >> 2) + 2 * gg) * 8 + half * 256;
    const u32 aB = ub + (30 - 2 * bb + gg) * 16 + half * 256;
    f32x4 ac8[8] = {};
    __builtin_amdgcn_s_setprio(2);
    conv_body(aA, aB, ac8);
    f32x4 acc[4];
    {
        const u32 xo = ub + U_SZ + lane * 16;
#pragma unroll
        for (int i = 0; i < 4; ++i) { const f32x4 give = half ? ac8[i] : ac8[4 + i]; acc[i] = half ? ac8[4 + i] : ac8[i]; sts16(xo + half * 4096 + i * 1024, __builtin_bit_cast(u32x4, give)); }
        asm volatile("s_waitcnt lgkmcnt(0)" ::: "memory");
        lds_set(lds0 + F_XCH + cw * 4);
        lds_wait(lds0 + F_XCH + (cw ^ 1) * 4);
        f32x4 got[4];
#pragma unroll
        for (int i = 0; i < 4; ++i) got[i] = __builtin_bit_cast(f32x4, lds16(xo + (half ^ 1) * 4096 + i * 1024));
        SB();
#pragma unroll
        for (int i = 0; i < 4; ++i) acc[i] = f32x4{add1(acc[i][0], got[i][0]), add1(acc[i][1], got[i][1]), add1(acc[i][2], got[i][2]), add1(acc[i][3], got[i][3])};
    }
    {
        const float dd = bflo(dv);
        char* yb = ws + WS_YT + (size_t)chan * TP;
        u32x2 uq[4];
#pragma unroll
        for (int i = 0; i < 4; ++i) uq[i] = lds8(ub + (2044 - (256 * (4 * half + i) + 16 * bb + 4 * gg)) * 2);
        SB();
#pragma unroll
        for (int i = 0; i < 4; ++i) {
            const int qt = 4 * half + i;
            const int t = 256 * qt + 16 * bb + 4 * gg;
            const u32x2 uu = uq[i];
            const u32 c01 = pk2(acc[i][0], acc[i][1]), c23 = pk2(acc[i][2], acc[i][3]);
            const u32 d01 = pk2(mul1(bfhi(uu.y), dd), mul1(bflo(uu.y), dd)), d23 = pk2(mul1(bfhi(uu.x), dd), mul1(bflo(uu.x), dd));
            float y0 = add1(bflo(d01), bflo(c01)), y1 = add1(bfhi(d01), bfhi(c01));
            float y2 = add1(bflo(d23), bflo(c23)), y3 = add1(bfhi(d23), bfhi(c23));
            gst16c<0>((u32)(t * 4), yb, u32x4{pk2(y0, y1), tlo, pk2(y2, y3), tlo});
        }
        lds_arrive_last(lds0 + F_YCNT, 4);
    }
    if (cw >= 2) {
        const int kw = cw - 2;
        lds_wait_ge(lds0 + F_YCNT, 4, lds_peek(lds0 + F_YCNT)); lds_wait_ge(lds0 + F_HCNT, 4, lds_peek(lds0 + F_HCNT));
        const u32 slab = slab_base(lds0, 2 * kw) + lane * 64;
        float xn[32];
        {
            const u32 rf = *(const LAS u32*)(lds0 + L_ROWF + lane * 4);
            const float nm = -bflo(rf); float ri = __builtin_amdgcn_rcpf(bfhi(rf));
            asm volatile("s_nop 1" : "+v"(ri));
#pragma unroll
            for (int j = 0; j < 8; ++j) {
                const u32 lo = xs[j >> 1][(j & 1) * 2], hi_ = xs[j >> 1][(j & 1) * 2 + 1];
                const f32x4 v = {bflo(lo), bfhi(lo), bflo(hi_), bfhi(hi_)};
                sts16(slab + (j >> 2) * 8192 + 4096 + (j & 3) * 16, __builtin_bit_cast(u32x4, v));
#pragma unroll
                for (int e = 0; e < 4; e += 2) {
                    const u32 dx = pk2(add1(v[e], nm), add1(v[e + 1], nm)), xr = pk2(mul1(bflo(dx), ri), mul1(bfhi(dx), ri));
                    xn[4 * j + e] = bflo(xr); xn[4 * j + e + 1] = bfhi(xr);
                }
            }
            if (kw == 0) *(LAS float*)(lds0 + L_R2 + lane * 4) = -1.4426950408889634f * ri * ri;
            else *(LAS float*)(lds0 + L_B2F + lane * 4) = -1.4426950408889634f * bflo(*(const LAS u16*)(lds0 + L_B2 + lane * 2));
        }
        const char* yb = ws + WS_YT + (size_t)(q.D0 + 32 * kw) * TP + q.T0 * 4;
        const u32 vo = (u32)(gg * TP + bb * 16);
        u32x4 y[8];
        probe<4>((u32)(s31 * TP), yb, tlo);
        _Pragma("clang loop unroll(disable)") for (int spin = 0; spin < SPIN_CAP; ++spin) {
#pragma unroll
            for (int i = 0; i < 8; ++i) y[i] = gld16c<0>(vo, yb + i * 4 * TP);
            asm volatile("s_waitcnt vmcnt(0)" : "+v"(y[0]), "+v"(y[1]), "+v"(y[2]), "+v"(y[3]), "+v"(y[4]), "+v"(y[5]), "+v"(y[6]), "+v"(y[7]) :: "memory");
            u32 bad = 0u;
#pragma unroll
            for (int i = 0; i < 8; ++i) bad = chk(y[i].w, tlo, chk(y[i].y, tlo, bad));
            if (!__any(bad != 0u)) break;
            __builtin_amdgcn_s_sleep(4);
        }
        const u32 ys = lds0 + L_CONV + kw * CONV_CH + TOEP_CH + U_SZ;
#pragma unroll
        for (int i = 0; i < 8; ++i) sts8(ys + (4 * i + gg) * 144 + bb * 8, u32x2{y[i].x, y[i].z});
        const int qq = (lane & 15) >> 2, pp = lane & 3;
        const u32 a = ys + qq * 144 + gg * 32 + pp * 8;
        u32x2 t[8];
        const float nln2 = -0.6931471805599453f;
#pragma unroll
        for (int j = 0; j < 8; ++j) t[j] = ldtr(a + j * 4 * 144);
        SB();
#pragma unroll
        for (int j = 0; j < 8; ++j)
        {
            const u32 s01 = pk2(mul1(bflo(t[j].x), xn[4 * j]), mul1(bfhi(t[j].x), xn[4 * j + 1])), s23 = pk2(mul1(bflo(t[j].y), xn[4 * j + 2]), mul1(bfhi(t[j].y), xn[4 * j + 3]));
            sts16(slab + (j >> 2) * 8192 + (j & 3) * 16, __builtin_bit_cast(u32x4, f32x4{mul1(bflo(s01), nln2), mul1(bfhi(s01), nln2), mul1(bflo(s23), nln2), mul1(bfhi(s23), nln2)}));
        }
        if (q.b == 0 && kw == 1) {
            _Pragma("clang loop unroll(disable)") for (int spin = 0; spin < SPIN_CAP; ++spin) {
                u32x4 f0 = gld16c<0>(lane * 16u, ws + WS_ALL), f1 = gld16c<1024>((lane < 48 ? lane : 0) * 16u, ws + WS_ALL);
                asm volatile("s_waitcnt vmcnt(0)" : "+v"(f0), "+v"(f1) :: "memory");
                bool ok = f0.x == tlo && f0.y == thi && f0.z == tlo && f0.w == thi && f1.x == tlo && f1.y == thi && f1.z == tlo && f1.w == thi;
                if (__all(ok)) break;
                __builtin_amdgcn_s_sleep(2);
            }
            if (lane == 0) { const u64 e1 = epoch + 1; gst8c<0>(0u, ws + WS_EPOCH, u32x2{(u32)e1, (u32)(e1 >> 32)}); }
        }
    }
    __builtin_amdgcn_s_setprio(0);
    BARL();
    final_epilogue(p, q, lds0);
}

DI float dpp_xor_sum8(float v) {
    v += __uint_as_float(__builtin_amdgcn_mov_dpp(__float_as_uint(v), 0xB1, 0xf, 0xf, true));
    v += __uint_as_float(__builtin_amdgcn_mov_dpp(__float_as_uint(v), 0x4E, 0xf, 0xf, true));
    v += __uint_as_float(__builtin_amdgcn_mov_dpp(__float_as_uint(v), 0x141, 0xf, 0xf, true));
    return v;
}
DI void body_help(const Params& p, const int wave) {
    const u32 lds0 = (u32)(size_t)(LAS char*)smem;
    const int lane = threadIdx.x & 63, r8 = lane >> 3, c8 = lane & 7;
    const int xcd = blockIdx.x & 7, j = blockIdx.x >> 3;
    char* ws = p.ws;
    u32x2 ep = gld8c(0u, ws + WS_EPOCH);
    u32x4 d[14];
    const int ctile = (xcd >> 1) * 4 + j;
    const bool cols = wave >= 4;
    if (!cols) {
        const char* xb = (const char*)p.x + (size_t)(256 * xcd + 64 * j + 16 * wave) * 896;
#pragma unroll
        for (int i = 0; i < 14; ++i) d[i] = gld16((u32)((r8 + 8 * (i / 7)) * 896 + (i % 7) * 128 + c8 * 16), xb);
    } else {
        const char* wb = (const char*)p.w1 + (size_t)(112 * (wave - 4)) * 3584 + ((xcd & 1) * 14 + (ctile < 14 ? ctile : 0)) * 128;
#pragma unroll
        for (int i = 0; i < 14; ++i) d[i] = gld16((u32)((r8 + 8 * i) * 3584 + c8 * 16), wb);
    }
    asm volatile("s_waitcnt vmcnt(0)" : "+v"(ep), "+v"(d[0]), "+v"(d[1]), "+v"(d[2]), "+v"(d[3]), "+v"(d[4]), "+v"(d[5]), "+v"(d[6]), "+v"(d[7]), "+v"(d[8]), "+v"(d[9]),
                 "+v"(d[10]), "+v"(d[11]), "+v"(d[12]), "+v"(d[13]) :: "memory");
    const u32 tlo = (u32)((((u64)ep.y << 32) | ep.x) + TAG_SALT);
    if (!cols) {
#pragma unroll
        for (int h = 0; h < 2; ++h) {
            float s0 = 0.f, s1 = 0.f, q0 = 0.f, q1 = 0.f;
#pragma unroll
            for (int i = 0; i < 7; ++i)
#pragma unroll
                for (int e = 0; e < 4; e += 2) {
                    s0 = dot2(d[7 * h + i][e], 0x3f803f80u, s0); q0 = dot2(d[7 * h + i][e], d[7 * h + i][e], q0);
                    s1 = dot2(d[7 * h + i][e + 1], 0x3f803f80u, s1); q1 = dot2(d[7 * h + i][e + 1], d[7 * h + i][e + 1], q1);
                }
            const float mean = dpp_xor_sum8(s0 + s1) * (1.f / 448.f);
            const float var = fmaxf(dpp_xor_sum8(q0 + q1) * (1.f / 448.f) - mean * mean, 0.f);
            const float sd = __builtin_amdgcn_sqrtf(rbf(rbf(var) + 1e-5f));
            if (c8 == 0) gst8c<0>((u32)(WS_RG + (256 * xcd + 64 * j + 16 * wave + 8 * h + r8) * 8), ws, u32x2{pk2(mean, sd), tlo});
        }
    } else {
        float a[8] = {};
        u32 klo = 0x00003f80u, khi = 0x3f800000u; PIN(klo); PIN(khi);
#pragma unroll
        for (int i = 0; i < 14; ++i)
#pragma unroll
            for (int e = 0; e < 4; ++e) { a[2 * e] = dot2(d[i][e], klo, a[2 * e]); a[2 * e + 1] = dot2(d[i][e], khi, a[2 * e + 1]); }
        const u32 o = lds0 + ((wave - 4) * 8 + r8) * 272 + c8 * 32;
        sts16(o, __builtin_bit_cast(u32x4, f32x4{a[0], a[1], a[2], a[3]})); sts16(o + 16, __builtin_bit_cast(u32x4, f32x4{a[4], a[5], a[6], a[7]}));
    }
    BARL();
    if (wave == 4 && ctile < 14) {
        float t0 = 0.f, t1 = 0.f;
#pragma unroll
        for (int i = 0; i < 32; i += 2) { t0 += *(const LAS float*)(lds0 + i * 272 + lane * 4); t1 += *(const LAS float*)(lds0 + (i + 1) * 272 + lane * 4); }
        gst8c<0>((u32)(WS_CG + (((xcd & 1) * 14 + ctile) * 64 + lane) * 8), ws, u32x2{pk2(t0 + t1, (t0 + t1) - rbf(t0 + t1)), tlo});
    }
    BARL();
    {
        const int crot = (2 * xcd) % 14;
        setm0(lds0 + 16384 + wave * 4096);
        for (int c = 0; c < 14; ++c) {
            int cc = c + crot; if (cc >= 14) cc -= 14;
            const char* sb = (const char*)p.w2 + (size_t)cc * 114688 + (j * 8 + wave) * 3584;
            if (lane < 32) dmai<3072>(lane * 16u, sb);
            dmai<0>(lane * 16u, sb); dmai<1024>(lane * 16u, sb); dmai<2048>(lane * 16u, sb);
            WAITV(8);
        }
        WAITV(0);
    }
}

__global__ __launch_bounds__(512, 2) void fused_kernel(Params p) {
    {
        u64 pc; asm volatile("s_getpc_b64 %0" : "=s"(pc));
        if (threadIdx.x < CODE_LINES) touch((const char*)(pc & ~127ull) + threadIdx.x * 128, (u32)(size_t)(LAS char*)smem + L_B2F);
    }
    const int wave = __builtin_amdgcn_readfirstlane(threadIdx.x >> 6);
    if (blockIdx.x < NHELP) { body_help(p, wave); return; }
    if (threadIdx.x < 32) *(LAS u32*)((u32)(size_t)(LAS char*)smem + L_FLAG + threadIdx.x * 4) = 0u;
    else if (threadIdx.x < 64) *(LAS u32*)((u32)(size_t)(LAS char*)smem + F_CONS + ((threadIdx.x - 32) >> 2) * 144 + (threadIdx.x & 3) * 4) = 0u;
    BARL();
    if (wave < 4) body_g(p, wave); else body_c(p, wave - 4);
}

extern "C" void kernel_launch(void* const* d_in, const int* in_sizes, int n_in,
                              void* d_out, int out_size, void* d_ws, size_t ws_size,
                              hipStream_t stream) {
    Params p;
    p.x = (const u16*)d_in[0]; p.k = (const u16*)d_in[1]; p.v = (const u16*)d_in[2]; p.shk = (const u16*)d_in[3];
    p.ssmk = (const u16*)d_in[4]; p.ssmd = (const u16*)d_in[5]; p.w1 = (const u16*)d_in[7]; p.b1 = (const u16*)d_in[8];
    p.w2 = (const u16*)d_in[9]; p.b2 = (const u16*)d_in[10];
    p.out = (u16*)d_out; p.ws = (char*)d_ws;
    (void)hipFuncSetAttribute((const void*)fused_kernel, hipFuncAttributeMaxDynamicSharedMemorySize, LDS_BYTES);
    fused_kernel<<<NWG + NHELP, 512, LDS_BYTES, stream>>>(p);
}
```

```cpp
#include <hip/hip_runtime.h>
#include <stdint.h>
#pragma clang diagnostic ignored "-Wint-to-pointer-cast"

typedef unsigned short u16;
typedef unsigned int u32;
typedef unsigned long long u64;
typedef u32 u32x4 __attribute__((ext_vector_type(4)));
typedef u32 u32x2 __attribute__((ext_vector_type(2)));
typedef float f32x4 __attribute__((ext_vector_type(4)));
typedef float f32x2 __attribute__((ext_vector_type(2)));
typedef float f32x16 __attribute__((ext_vector_type(16)));
typedef __bf16 bf16x8 __attribute__((ext_vector_type(8)));
typedef __bf16 bf16x2 __attribute__((ext_vector_type(2)));
typedef short s16x4 __attribute__((ext_vector_type(4)));
#define LAS __attribute__((address_space(3)))
#define GAS __attribute__((address_space(1)))
#define DI __device__ __forceinline__

constexpr int S_ = 2048, D_ = 448;
constexpr int NHELP = 32, NWG = 224;
constexpr int CODE_LINES = 264;
constexpr u32 WS_EPOCH = 0, WS_ALL = 2048, WS_RG = 4096, WS_CG = WS_RG + 16384;
constexpr u32 TP = 8192 + 128;
constexpr u32 WS_H = 1u << 20, WS_KVT = 9u << 20, WS_YT = 13u << 20;
constexpr u32 L_PIECE = 16384, L_W = 65536, L_LOC0 = 131072, LDS_BYTES = 163840;
constexpr u32 KVRAW_SZ = 5632, L_KVS = L_LOC0 + 4 * KVRAW_SZ, L_B2 = L_KVS + 9216, L_FLAG = L_B2 + 128, L_ROWF = L_FLAG + 128, L_B2F = L_ROWF + 256, L_R2 = L_B2F + 256;
static_assert(L_R2 + 256 <= LDS_BYTES, "lds");
constexpr u32 F_TOEP = L_FLAG, F_HCNT = L_FLAG + 16, F_YCNT = L_FLAG + 28, F_W = L_FLAG + 48, F_LAND = L_FLAG + 64, F_XCH = L_FLAG + 96, F_KVS = L_FLAG + 124;
constexpr u32 F_CONS = L_KVS + 128;
constexpr u32 L_CONV = 16384, CONV_CH = 65536, TOEP_CP = 4160, TOEP_CH = 4 * TOEP_CP, U_SZ = 4608;
constexpr u64 TAG_SALT = 0x9E3779B97F4A7C15ull;
constexpr int SPIN_CAP = 1 << 15;

DI void dma16(u32 voff, const void* sbase, u32 lds) {
    asm volatile("s_mov_b32 m0, %2\n\ts_nop 0\n\tglobal_load_lds_dwordx4 %0, %1" :: "v"(voff), "s"(sbase), "s"(lds) : "memory");
}
DI void setm0(u32 lds) { asm volatile("s_mov_b32 m0, %0\n\ts_nop 0" :: "s"(lds) : "memory"); }
template <int OFF> DI void dmai(u32 voff, const void* sbase) { asm volatile("global_load_lds_dwordx4 %0, %1 offset:%2" :: "v"(voff), "s"(sbase), "i"(OFF) : "memory"); }
DI void bdma16(u32 voff, u32x4 srd, u32 lds) {
    asm volatile("s_mov_b32 m0, %2\n\ts_nop 0\n\tbuffer_load_dwordx4 %0, %1, 0 offen lds" :: "v"(voff), "s"(srd), "s"(lds) : "memory");
}
#define SB() __builtin_amdgcn_sched_barrier(0)
template <int OFF> DI u32x2 alds8(u32 a) { u32x2 r; asm volatile("ds_read_b64 %0, %1 offset:%2" : "=v"(r) : "v"(a), "i"(OFF)); return r; }
template <int OFF> DI u32x4 alds16(u32 a) { u32x4 r; asm volatile("ds_read_b128 %0, %1 offset:%2" : "=v"(r) : "v"(a), "i"(OFF)); return r; }
DI u32x4 gld16(u32 voff, const void* sbase) { u32x4 r; asm volatile("global_load_dwordx4 %0, %1, %2" : "=v"(r) : "v"(voff), "s"(sbase) : "memory"); return r; }
template <int OFF> DI u32x4 gld16c(u32 voff, const void* sbase) { u32x4 r; asm volatile("global_load_dwordx4 %0, %1, %2 offset:%3 sc1" : "=v"(r) : "v"(voff), "s"(sbase), "i"(OFF) : "memory"); return r; }
DI u32x2 gld8c(u32 voff, const void* sbase) { u32x2 r; asm volatile("global_load_dwordx2 %0, %1, %2 sc1" : "=v"(r) : "v"(voff), "s"(sbase) : "memory"); return r; }
DI void touch(const void* addr, u32 lds) { asm volatile("s_mov_b32 m0, %1\n\ts_nop 0\n\tglobal_load_lds_dword %0, off" :: "v"(addr), "s"(lds) : "memory"); }
DI u32 gld2(u32 voff, const void* sbase) { u32 r; asm volatile("global_load_ushort %0, %1, %2" : "=v"(r) : "v"(voff), "s"(sbase) : "memory"); return r; }
DI u32x4 bld16(u32 voff, u32x4 srd) { u32x4 r; asm volatile("buffer_load_dwordx4 %0, %1, %2, 0 offen" : "=v"(r) : "v"(voff), "s"(srd) : "memory"); return r; }
template <int OFF> DI u32x4 gld16o(u32 voff, const void* sbase) { u32x4 r; asm volatile("global_load_dwordx4 %0, %1, %2 offset:%3" : "=v"(r) : "v"(voff), "s"(sbase), "i"(OFF) : "memory"); return r; }
template <int OFF> DI void gst16c(u32 voff, void* sbase, u32x4 v) { asm volatile("global_store_dwordx4 %0, %1, %2 offset:%3 sc1\n\ts_nop 1" :: "v"(voff), "v"(v), "s"(sbase), "i"(OFF) : "memory"); }
template <int OFF> DI void gst8c(u32 voff, void* sbase, u32x2 v) { asm volatile("global_store_dwordx2 %0, %1, %2 offset:%3 sc1" :: "v"(voff), "v"(v), "s"(sbase), "i"(OFF) : "memory"); }
#define WAITV(n) asm volatile("s_waitcnt vmcnt(%0)" :: "i"(n) : "memory")
#define WAITBAR(n) asm volatile("s_waitcnt vmcnt(%0) lgkmcnt(0)\n\ts_barrier" :: "i"(n) : "memory")
#define BARL() asm volatile("s_waitcnt lgkmcnt(0)\n\ts_barrier" ::: "memory")
#define PIN(x) asm volatile("" : "+v"(x))

DI u32x4 mksrd(const void* p, u32 bytes) { u64 a = (u64)p; return u32x4{(u32)a, (u32)(a >> 32) & 0xffffu, bytes, 0x00020000u}; }
DI float bflo(u32 x) { return __uint_as_float(x << 16); }
DI float bfhi(u32 x) { return __uint_as_float(x & 0xffff0000u); }
DI u32 pk2(float a, float b) { bf16x2 v = {(__bf16)a, (__bf16)b}; return __builtin_bit_cast(u32, v); }
DI float rbf(float a) { return bflo(pk2(a, a)); }
DI float relu(float a) { int i = __float_as_int(a); return __int_as_float(i > 0 ? i : 0); }
DI float mul1(float a, float b) { float r; asm("v_mul_f32 %0, %1, %2" : "=v"(r) : "v"(a), "v"(b)); return r; }
DI float add1(float a, float b) { float r; asm("v_add_f32 %0, %1, %2" : "=v"(r) : "v"(a), "v"(b)); return r; }
DI float dot2(u32 a, u32 b, float c) { return __builtin_amdgcn_fdot2_f32_bf16(__builtin_bit_cast(bf16x2, a), __builtin_bit_cast(bf16x2, b), c, false); }
DI u32x4 lds16(u32 a) { return *(const LAS u32x4*)a; }
DI u32x2 lds8(u32 a) { return *(const LAS u32x2*)a; }
DI void sts16(u32 a, u32x4 v) { *(LAS u32x4*)a = v; }
DI void sts8(u32 a, u32x2 v) { *(LAS u32x2*)a = v; }
DI u32x2 ldtr(u32 a) { return __builtin_bit_cast(u32x2, __builtin_amdgcn_ds_read_tr16_b64_v4i16((LAS s16x4*)a)); }
DI u32x4 cat(u32x2 a, u32x2 b) { return u32x4{a.x, a.y, b.x, b.y}; }
DI f32x16 mfma32(u32x4 a, u32x4 b, f32x16 c) { return __builtin_amdgcn_mfma_f32_32x32x16_bf16(__builtin_bit_cast(bf16x8, a), __builtin_bit_cast(bf16x8, b), c, 0, 0, 0); }
DI f32x4 mfma16(u32x4 a, u32x4 b, f32x4 c) { return __builtin_amdgcn_mfma_f32_16x16x32_bf16(__builtin_bit_cast(bf16x8, a), __builtin_bit_cast(bf16x8, b), c, 0, 0, 0); }

struct Params {
    const u16 *x, *k, *v, *shk, *ssmk, *ssmd, *w1, *b1, *w2, *b2;
    u16* out; char* ws;
};

template <int SL> DI void probe(u32 voff, const void* sbase, u32 tag) {
    _Pragma("clang loop unroll(disable)") for (int spin = 0; spin < SPIN_CAP; ++spin) {
        u32x2 f = gld8c(voff, sbase);
        asm volatile("s_waitcnt vmcnt(0)" : "+v"(f) :: "memory");
        if (__all(f.y == tag)) break;
        __builtin_amdgcn_s_sleep(SL);
    }
}
DI bool lds_arrive_last(u32 addr, u32 n) {
    u32 old = 0;
    if ((threadIdx.x & 63) == 0) old = __hip_atomic_fetch_add((LAS u32*)addr, 1u, __ATOMIC_RELAXED, __HIP_MEMORY_SCOPE_WORKGROUP);
    return (u32)__builtin_amdgcn_readfirstlane(old) == n - 1;
}
DI void lds_wait(u32 addr) {
    _Pragma("clang loop unroll(disable)") for (int spin = 0; spin < (1 << 22); ++spin) {
        if (__builtin_amdgcn_readfirstlane(*(volatile LAS u32*)addr)) break;
        __builtin_amdgcn_s_sleep(1);
    }
    asm volatile("" ::: "memory");
}
DI void lds_add(u32 addr) { asm volatile("" ::: "memory"); __hip_atomic_fetch_add((LAS u32*)addr, 1u, __ATOMIC_RELAXED, __HIP_MEMORY_SCOPE_WORKGROUP); asm volatile("" ::: "memory"); }
DI u32 lds_peek(u32 addr) { return *(volatile LAS u32*)addr; }
DI void lds_wait_ge(u32 addr, u32 n, u32 first) {
    u32 v = first;
    _Pragma("clang loop unroll(disable)") for (int spin = 0; spin < (1 << 22) && (u32)__builtin_amdgcn_readfirstlane(v) < n; ++spin) { __builtin_amdgcn_s_sleep(1); v = lds_peek(addr); }
    asm volatile("" ::: "memory");
}
template <int t> DI void cons_set(u32 lds0, int g) { asm volatile("s_waitcnt lgkmcnt(0)\n\tds_write_b32 %0, %1 offset:%2" :: "v"(lds0 + 131072 + g * 4), "v"(1u), "i"(F_CONS + t * 144 - 131072) : "memory"); }
DI void cons_wait(u32 lds0, int t) {
    _Pragma("clang loop unroll(disable)") for (int spin = 0; spin < (1 << 22); ++spin) {
        const u32x4 v = *(volatile LAS u32x4*)(lds0 + F_CONS + t * 144);
        if (__builtin_amdgcn_readfirstlane(v.x & v.y & v.z & v.w)) break;
        __builtin_amdgcn_s_sleep(1);
    }
    asm volatile("" ::: "memory");
}
DI void lds_set(u32 addr) { asm volatile("" ::: "memory"); if ((threadIdx.x & 63) == 0) *(volatile LAS u32*)addr = 1u; }
constexpr u32 loc_of(int c) { return (c % 5) == 0 ? L_LOC0 : (c % 5) == 1 ? 3 * L_PIECE : (c % 5) == 2 ? L_W + 3 * L_PIECE : (c % 5) == 3 ? 0u : L_W; }

constexpr int HD = 4, HS = HD + 1;
constexpr int WS_ = 2;
constexpr int g2_younger(int c) {
    const int n = c + 1;
    if (n >= 14) return 63;
    bool sh = false, sw = n < 5; int ah = 0, aw = sw ? 1000 : 0;
    for (int i = 0; i < HD; ++i) { if (sh) ah += 4; if (i == n) sh = true; }
    for (int i = 0; i < c; ++i) {
        if (i + HD < 14) { if (sh) ah += 4; if (sw) aw += 4; if (i + HD == n) sh = true; }
        if (i + 5 < 14) { if (sh) ah += 4; if (sw) aw += 4; if (i + 5 == n) sw = true; }
    }
    return ah < aw ? ah : aw;
}

struct G2Ctx { const char* w2b; const char* hb; int crot; u32 voffw[4], ldsg, vlane, ta[3][2], smask; };
template <int C> DI int g2_chunk(const G2Ctx& x) {
    int c = C + x.crot; if (c >= 14) c -= 14;
    return c;
}
template <int C> DI const char* g2_wsrc(const G2Ctx& x) { return x.w2b + (size_t)g2_chunk<C>(x) * (128 * 896); }
template <int C> DI u32 g2_loc(const G2Ctx& x) { return loc_of(C) + x.ldsg; }
template <int C, int N> DI void g2_w1(const G2Ctx& x, const char* sb) { if constexpr (N == 0) setm0(g2_loc<C>(x)); dmai<N * 1024>(x.voffw[N], sb); }
template <int C> DI void g2_issue_w(const G2Ctx& x) { const char* sb = g2_wsrc<C>(x); g2_w1<C, 0>(x, sb); g2_w1<C, 1>(x, sb); g2_w1<C, 2>(x, sb); g2_w1<C, 3>(x, sb); }
template <int C> DI const char* g2_hsrc(const G2Ctx& x) { return x.hb + g2_chunk<C>(x) * 8192; }
template <int N> DI u32x4 g2_h1(const G2Ctx& x, const char* sb) { return gld16c<(N & 1) * 1024>(x.vlane + (N >> 1) * 112 * 1024, sb); }
template <int C> DI void g2_issue_h(const G2Ctx& x, u32x4 (&hf)[HS][4]) {
    const char* sb = g2_hsrc<C>(x);
    hf[C % HS][0] = g2_h1<0>(x, sb); hf[C % HS][1] = g2_h1<1>(x, sb); hf[C % HS][2] = g2_h1<2>(x, sb); hf[C % HS][3] = g2_h1<3>(x, sb);
}
template <int C> DI void g2_read(const G2Ctx& x, u32x4 (&wf)[WS_][4]) {
    const u32 a0 = x.ta[loc_of(C) >> 16][0], a1 = x.ta[loc_of(C) >> 16][1];
    constexpr u32 lo = loc_of(C) & 0xffffu;
#pragma unroll
    for (int ks = 0; ks < 2; ++ks) {
        wf[C % WS_][ks * 2] = cat(ldtr(a0 + lo + ks * 2048), ldtr(a0 + lo + ks * 2048 + 1024));
        wf[C % WS_][ks * 2 + 1] = cat(ldtr(a1 + lo + ks * 2048), ldtr(a1 + lo + ks * 2048 + 1024));
    }
}
template <int C, int I> DI void g2_rd(const G2Ctx& x, u32x4 (&wf)[WS_][4]) {
    if constexpr (C < 14) {
        const u32 a = x.ta[loc_of(C) >> 16][I & 1]; constexpr u32 lo = (loc_of(C) & 0xffffu) + (I >> 1) * 2048;
        wf[C % WS_][I] = cat(ldtr(a + lo), ldtr(a + lo + 1024));
    }
}
DI u32 chk(u32 d, u32 m, u32 bad) { return __builtin_amdgcn_bitop3_b32(d, m, bad, 0xBE); }
template <int C> DI void g2_retry(const G2Ctx& x, u32x4 (&hf)[HS][4], u32 bad) {
    if (__builtin_expect(!__any((bad & 0x80008000u) != 0u), 1)) return;
    _Pragma("clang loop unroll(disable)") for (int spin = 0; spin < SPIN_CAP; ++spin) {
        __builtin_amdgcn_s_sleep(8);
        g2_issue_h<C>(x, hf);
        asm volatile("s_waitcnt vmcnt(0)" : "+v"(hf[C % HS][0]), "+v"(hf[C % HS][1]), "+v"(hf[C % HS][2]), "+v"(hf[C % HS][3]) :: "memory");
        bad = 0u;
#pragma unroll
        for (int i = 0; i < 16; ++i) { hf[C % HS][i >> 2][i & 3] ^= x.smask; bad |= hf[C % HS][i >> 2][i & 3]; }
        if (!__any((bad & 0x80008000u) != 0u)) break;
    }
}
template <int C> DI void g2_step(const G2Ctx& x, u32x4 (&hf)[HS][4], f32x16 (&acc)[2][2], u32x4 (&wf)[WS_][4]) {
    constexpr int N = (C + 1 < 14 ? C + 1 : C) % HS;
    asm volatile("s_waitcnt vmcnt(%4) lgkmcnt(0)" : "+v"(hf[N][0]), "+v"(hf[N][1]), "+v"(hf[N][2]), "+v"(hf[N][3]) : "i"(g2_younger(C)) : "memory");
    SB();
    constexpr bool DH = C + HD < 14, DW = C + 5 < 14, DC = C + 1 < 14;
    const char* hs = g2_hsrc<(DH ? C + HD : 0)>(x);
    const char* wsrc = g2_wsrc<(DW ? C + 5 : 0)>(x);
    u32 bad = 0u;
#define G2M(i) acc[((i) >> 1) & 1][(i) & 1] = mfma32(wf[C % WS_][((i) >> 2) * 2 + (((i) >> 1) & 1)], hf[C % HS][((i) & 1) * 2 + ((i) >> 2)], acc[((i) >> 1) & 1][(i) & 1]); \
               if constexpr (DC) { hf[N][(i) >> 1][((i) & 1) * 2] ^= x.smask; hf[N][(i) >> 1][((i) & 1) * 2 + 1] ^= x.smask; bad = bad | hf[N][(i) >> 1][((i) & 1) * 2] | hf[N][(i) >> 1][((i) & 1) * 2 + 1]; }
    G2M(0); g2_rd<C + 1, 0>(x, wf); if constexpr (DH) hf[(C + HD) % HS][0] = g2_h1<0>(x, hs); SB();
    G2M(1); g2_rd<C + 1, 1>(x, wf); if constexpr (DH) hf[(C + HD) % HS][1] = g2_h1<1>(x, hs); SB();
    G2M(2); g2_rd<C + 1, 2>(x, wf); if constexpr (DH) hf[(C + HD) % HS][2] = g2_h1<2>(x, hs); SB();
    G2M(3); g2_rd<C + 1, 3>(x, wf); if constexpr (DH) hf[(C + HD) % HS][3] = g2_h1<3>(x, hs); SB();
    G2M(4); if constexpr (DW) g2_w1<C + 5, 0>(x, wsrc); SB();
    G2M(5); if constexpr (DW) g2_w1<C + 5, 1>(x, wsrc); SB();
    G2M(6); if constexpr (DW) g2_w1<C + 5, 2>(x, wsrc); SB();
    G2M(7); if constexpr (DW) g2_w1<C + 5, 3>(x, wsrc); SB();
#undef G2M
    if constexpr (DC) { g2_retry<C + 1>(x, hf, bad); g2_step<C + 1>(x, hf, acc, wf); }
}

struct CF { u32x2 a0[8], a1[8]; u32x4 b[8]; };
template <int S, int R> DI void cv_ld(CF& f, u32 aA, u32 aB) {
    f.a0[R] = alds8<R * 512 + S * 64>(aA); f.a1[R] = alds8<R * 512 + S * 64 + 8>(aA);
    f.b[R] = alds16<(224 - 32 * R + 4 * S) * 16>(aB);
}
DI void cv_wait(CF& f) {
    asm volatile("s_waitcnt lgkmcnt(0)" : "+v"(f.a0[0]), "+v"(f.a0[1]), "+v"(f.a0[2]), "+v"(f.a0[3]), "+v"(f.a0[4]), "+v"(f.a0[5]), "+v"(f.a0[6]), "+v"(f.a0[7]),
                 "+v"(f.a1[0]), "+v"(f.a1[1]), "+v"(f.a1[2]), "+v"(f.a1[3]), "+v"(f.a1[4]), "+v"(f.a1[5]), "+v"(f.a1[6]), "+v"(f.a1[7]),
                 "+v"(f.b[0]), "+v"(f.b[1]), "+v"(f.b[2]), "+v"(f.b[3]), "+v"(f.b[4]), "+v"(f.b[5]), "+v"(f.b[6]), "+v"(f.b[7]));
}
constexpr int cv_r(int m) { int r = 0; while (m >= 8 - r) { m -= 8 - r; ++r; } return r; }
constexpr int cv_c(int m) { int r = 0; while (m >= 8 - r) { m -= 8 - r; ++r; } return m; }
template <int S, int M> DI void cv_mm(CF& f, CF& n, u32 aA, u32 aB, f32x4 (&acc)[8]) {
    constexpr int r = cv_r(M), c = cv_c(M);
    acc[r + c] = mfma16(cat(f.a0[r], f.a1[r]), f.b[c], acc[r + c]);
    if constexpr ((M & 3) == 0 && M / 4 < 8) cv_ld<S + 1, M / 4>(n, aA, aB);
    SB();
    if constexpr (M + 1 < 36) cv_mm<S, M + 1>(f, n, aA, aB, acc);
}
DI void conv_body(u32 aA, u32 aB, f32x4 (&acc)[8]) {
    CF f, n;
    asm volatile("s_waitcnt lgkmcnt(0)" ::: "memory");
    cv_ld<0, 0>(f, aA, aB); cv_ld<0, 1>(f, aA, aB); cv_ld<0, 2>(f, aA, aB); cv_ld<0, 3>(f, aA, aB);
    cv_ld<0, 4>(f, aA, aB); cv_ld<0, 5>(f, aA, aB); cv_ld<0, 6>(f, aA, aB); cv_ld<0, 7>(f, aA, aB);
    _Pragma("clang loop unroll(disable)") for (int it = 0; it < 2; ++it) {
        cv_wait(f); SB();
        cv_mm<0, 0>(f, n, aA, aB, acc);
        cv_wait(n); SB();
        cv_mm<1, 0>(n, f, aA, aB, acc);
        aA += 128; aB += 128;
    }
    cv_wait(f);
    asm volatile("" ::: "memory");
}

struct Geo { int b, xcd, fh2, RB, CT, tb64, db, rot, crot; u32 R0, F0, T0, D0; };
DI Geo make_geo() {
    Geo g; g.b = blockIdx.x - NHELP; g.xcd = g.b & 7; const int bi = g.b >> 3;
    const int rq = g.xcd >> 1, rb = bi / 7, ct = bi - rb * 7;
    g.fh2 = g.xcd & 1; g.RB = 4 * rq + rb; g.CT = 7 * g.fh2 + ct; g.R0 = 128 * g.RB; g.F0 = 128 * g.CT;
    g.tb64 = 2 * g.RB + g.fh2; g.db = ct; g.T0 = 64 * g.tb64; g.D0 = 64 * g.db;
    g.rot = g.xcd >= 7 ? 0 : g.xcd; g.crot = g.CT;
    return g;
}
DI void make_g2(G2Ctx& g2, const Params& p, const Geo& q, int g, int lane, u32 lds0) {
    g2.w2b = (const char*)p.w2 + (size_t)(32 * g) * 896 + q.D0 * 2;
    g2.hb = p.ws + WS_H + (size_t)(q.tb64 * 2 * 112 + 2 * g) * 1024;
    g2.crot = q.crot;
#pragma unroll
    for (int n = 0; n < 4; ++n) g2.voffw[n] = (u32)((lane >> 3) * 896 + 16 * ((lane & 7) ^ (((lane >> 4) & 1) << 2)) + n * (8 * 896 - 1024));
    g2.ldsg = lds0 + g * 4096; g2.vlane = lane * 16u;
}
DI u32 slab_base(u32 lds0, int cw) { return lds0 + L_CONV + (cw >> 1) * CONV_CH + (cw & 1) * 8192; }
DI void final_epilogue(const Params& p, const Geo& q, u32 lds0) {
    int tz = threadIdx.x; PIN(tz);
    const int t = tz >> 3, c = tz & 7;
    const u32 pb = lds0 + ((t >> 5) ? 0u : 3 * L_PIECE) + ((t >> 4) & 1) * L_W + (t & 15) * 256;
    const u32 o0 = pb + 16 * ((2 * c) ^ (t & 7)), o1 = o0 ^ 16;
    f32x4 pa[4], pb_[4];
#pragma unroll
    for (int g = 0; g < 4; ++g) { pa[g] = __builtin_bit_cast(f32x4, lds16(o0 + g * 4096)); pb_[g] = __builtin_bit_cast(f32x4, lds16(o1 + g * 4096)); }
    const float r2 = *(const LAS float*)(lds0 + L_R2 + t * 4);
    const f32x4 b0 = __builtin_bit_cast(f32x4, lds16(lds0 + L_B2F + c * 32)), b1 = __builtin_bit_cast(f32x4, lds16(lds0 + L_B2F + c * 32 + 16));
    const u32 sb = slab_base(lds0, c >> 1) + t * 64 + (c & 1) * 32;
    const f32x4 sv0 = __builtin_bit_cast(f32x4, lds16(sb)), sv1 = __builtin_bit_cast(f32x4, lds16(sb + 16));
    const f32x4 xv0 = __builtin_bit_cast(f32x4, lds16(sb + 4096)), xv1 = __builtin_bit_cast(f32x4, lds16(sb + 4096 + 16));
    SB();
    const f32x4 s0 = (pa[0] + pa[1]) + (pa[2] + pa[3]), s1 = (pb_[0] + pb_[1]) + (pb_[2] + pb_[3]);
    f32x4 z0 = s0 * r2 + b0, z1 = s1 * r2 + b1, e0, e1;
#pragma unroll
    for (int j = 0; j < 4; ++j) { e0[j] = __builtin_amdgcn_exp2f(z0[j]); e1[j] = __builtin_amdgcn_exp2f(z1[j]); }
    e0 += 1.f; e1 += 1.f;
#pragma unroll
    for (int j = 0; j < 4; ++j) { e0[j] = __builtin_amdgcn_rcpf(e0[j]); e1[j] = __builtin_amdgcn_rcpf(e1[j]); }
    z0 = z0 * e0; z1 = z1 * e1;
    z0 = z0 * sv0 + xv0; z1 = z1 * sv1 + xv1;
    const u32x4 res = {pk2(z0[0], z0[1]), pk2(z0[2], z0[3]), pk2(z1[0], z1[1]), pk2(z1[2], z1[3])};
    *(GAS u32x4*)((GAS char*)p.out + (size_t)(q.T0 + t) * 896 + q.D0 * 2 + c * 16) = res;
}

extern __shared__ __attribute__((aligned(16))) char smem[];

struct CL { const char *xb, *wb; u32 vx[4], vw[4], sl; int rot; };
struct CSrc { const char *xs, *wsrc; };
template <int ST_> DI CSrc c_src(const CL& c) { int kc = ST_ + c.rot; if (kc >= 7) kc -= 7; return CSrc{c.xb + kc * 128, c.wb + (size_t)kc * 64 * 3584}; }
template <int ST_, int N> DI void c_dma1(const CL& c, const CSrc& s) {
    if constexpr (N == 0) setm0(c.sl + (ST_ & 3) * L_PIECE);
    if constexpr (N == 4) setm0(c.sl + (ST_ & 3) * L_PIECE + L_W);
    if constexpr (N < 4) dmai<N * 1024>(c.vx[N], s.xs); else dmai<(N - 4) * 1024>(c.vw[N - 4], s.wsrc);
}
template <int ST_> DI void c_dma(const CL& c) {
    const CSrc s = c_src<ST_>(c);
    c_dma1<ST_, 0>(c, s); c_dma1<ST_, 1>(c, s); c_dma1<ST_, 2>(c, s); c_dma1<ST_, 3>(c, s);
    c_dma1<ST_, 4>(c, s); c_dma1<ST_, 5>(c, s); c_dma1<ST_, 6>(c, s); c_dma1<ST_, 7>(c, s);
}
DI void make_cl(CL& c, const Params& p, const Geo& q, int cw, int lane, u32 lds0) {
    const int r8 = lane >> 3, c8 = lane & 7, r4 = lane >> 4, c16 = lane & 15;
#pragma unroll
    for (int n = 0; n < 4; ++n) {
        c.vx[n] = (u32)((32 * cw + r8) * 896 + 16 * (c8 ^ (((n & 1) << 2) | (r8 >> 1))) + n * (8 * 896 - 1024));
        c.vw[n] = (u32)((16 * cw + r4) * 3584 + 16 * (c16 ^ (r4 << 2)) + n * (4 * 3584 - 1024));
    }
    c.xb = (const char*)p.x + (size_t)q.R0 * 896; c.wb = (const char*)p.w1 + q.F0 * 2;
    c.sl = lds0 + cw * 4096; c.rot = q.rot;
}

template <int ST_> DI void l_dma(const CL& c) {
    const CSrc s = c_src<ST_>(c);
    const u32 sl = c.sl + (ST_ & 3) * L_PIECE;
    setm0(sl); dmai<0>(c.vx[0], s.xs); dmai<1024>(c.vx[1], s.xs); dmai<2048>(c.vx[2], s.xs); dmai<3072>(c.vx[3], s.xs);
    setm0(sl + L_W); dmai<0>(c.vw[0], s.wsrc); dmai<1024>(c.vw[1], s.wsrc); dmai<2048>(c.vw[2], s.wsrc); dmai<3072>(c.vw[3], s.wsrc);
    const char* x2 = s.xs + 32 * 896; const char* w2 = s.wsrc + 16 * 3584;
    setm0(sl + 4096); dmai<0>(c.vx[0], x2); dmai<1024>(c.vx[1], x2); dmai<2048>(c.vx[2], x2); dmai<3072>(c.vx[3], x2);
    setm0(sl + L_W + 4096); dmai<0>(c.vw[0], w2); dmai<1024>(c.vw[1], w2); dmai<2048>(c.vw[2], w2); dmai<3072>(c.vw[3], w2);
}

struct Fr { u32x4 x[4][2], w[4][2]; };
struct GA { u32 ax[4], aw[2]; };
template <int K0> DI void g1_mma(const Fr& f, f32x16 (&acc)[2][2]) {
#pragma unroll
    for (int k = K0; k < K0 + 2; ++k) {
        acc[0][0] = mfma32(f.w[k][0], f.x[k][0], acc[0][0]); acc[1][0] = mfma32(f.w[k][1], f.x[k][0], acc[1][0]);
        acc[0][1] = mfma32(f.w[k][0], f.x[k][1], acc[0][1]); acc[1][1] = mfma32(f.w[k][1], f.x[k][1], acc[1][1]);
    }
    SB();
}

template <int ST_, int I> DI void g1_rd(const GA& a, Fr& f) {
    constexpr u32 so = (ST_ & 3) * L_PIECE; constexpr int k = I >> 1, h = I & 1;
    f.w[k][h] = cat(ldtr(a.aw[h] + so + k * 4096), ldtr(a.aw[h] + so + k * 4096 + 1024));
    f.x[k][h] = lds16(a.ax[k] + so + h * 4096);
}
template <int K0, int I> DI void g1_m1(const Fr& f, f32x16 (&acc)[2][2]) {
    constexpr int k = K0 + (I >> 2), mt = I & 1, st = (I >> 1) & 1;
    acc[mt][st] = mfma32(f.w[k][mt], f.x[k][st], acc[mt][st]);
}
template <int ST_, bool CARRY> DI void g1_stage(const GA& a, const Fr& fp, Fr& fn, f32x16 (&acc)[2][2], u32 lds0, int g, u32& pk) {
    if constexpr (CARRY) { g1_m1<2, 0>(fp, acc); SB(); cons_set<ST_ - 1>(lds0, g); SB(); }
    lds_wait_ge(lds0 + F_LAND + 4 * ST_, ST_ == 0 ? 256 : (ST_ == 3 || ST_ == 6) ? 128 : 64, pk);
    if constexpr (CARRY) {
        g1_rd<ST_, 0>(a, fn); SB();
        g1_m1<2, 1>(fp, acc); SB(); g1_m1<2, 2>(fp, acc); g1_rd<ST_, 1>(a, fn); SB();
        g1_m1<2, 3>(fp, acc); SB(); g1_m1<2, 4>(fp, acc); g1_rd<ST_, 2>(a, fn); SB();
        g1_m1<2, 5>(fp, acc); g1_rd<ST_, 3>(a, fn); SB(); g1_m1<2, 6>(fp, acc); SB();
        g1_m1<2, 7>(fp, acc); SB();
    } else { g1_rd<ST_, 0>(a, fn); g1_rd<ST_, 1>(a, fn); g1_rd<ST_, 2>(a, fn); g1_rd<ST_, 3>(a, fn); SB(); }
    g1_m1<0, 0>(fn, acc); g1_rd<ST_, 4>(a, fn); SB(); g1_m1<0, 1>(fn, acc); SB();
    g1_m1<0, 2>(fn, acc); g1_rd<ST_, 5>(a, fn); SB(); g1_m1<0, 3>(fn, acc); g1_rd<ST_, 6>(a, fn); SB();
    g1_m1<0, 4>(fn, acc); g1_rd<ST_, 7>(a, fn); SB(); g1_m1<0, 5>(fn, acc); pk = lds_peek(lds0 + F_LAND + 4 * ST_ + 4); SB();
    g1_m1<0, 6>(fn, acc); SB(); g1_m1<0, 7>(fn, acc); SB();
}

DI void body_g(const Params& p, const int g) {
    const u32 lds0 = (u32)(size_t)(LAS char*)smem;
    const int lane = threadIdx.x & 63;
    const Geo q = make_geo();
    char* ws = p.ws;
    const int hi = lane >> 5, s31 = lane & 31;
    const int r8 = lane >> 3, c8 = lane & 7;
    const u32 raw = lds0 + L_LOC0 + g * KVRAW_SZ;
    {
        const u32 vkv = (u32)(r8 * 896 + c8 * 16);
        bdma16((u32)(((int)q.T0 + 16 * g - 8) * 896 + (int)q.D0 * 2) + vkv, mksrd(p.k, S_ * D_ * 2), raw);
        const char* kb = (const char*)p.k + (size_t)(q.T0 + 16 * g) * 896 + q.D0 * 2;
        const char* vb = (const char*)p.v + (size_t)(q.T0 + 16 * g) * 896 + q.D0 * 2;
        dmai<1024>(vkv, kb - 1024); dmai<2048>(vkv, kb + 8 * 896 - 2048); dmai<3072>(vkv, vb - 3072);
        setm0(raw + 4096);
        dmai<0>(vkv, vb + 8 * 896);
        if (lane < 32) dmai<1024>(lane * 16u, (const char*)p.shk + q.D0 * 8 - 1024);
        CL c; make_cl(c, p, q, g, lane, lds0);
        c_dma<0>(c);
    }
    u32x2 ep = gld8c(0u, ws + WS_EPOCH);
    const int fh = g & 1, sh = g >> 1;
    GA ga;
    {
        const int blk = (lane >> 4) & 1, qq = (lane & 15) >> 2, pp = lane & 3;
        const u32 Z = (u32)(hi ^ ((s31 >> 1) & 7));
#pragma unroll
        for (int k = 0; k < 4; ++k) ga.ax[k] = lds0 + (64 * sh + s31) * 128 + 16 * ((2 * k) ^ Z);
        ga.aw[0] = lds0 + L_W + (8 * hi + qq) * 256 + 16 * ((8 * fh + 2 * blk + (pp >> 1)) ^ (qq << 2)) + 8 * (pp & 1);
        ga.aw[1] = ga.aw[0] ^ 64;
    }
    WAITV(9);
    {
        u32x4 wr[4], kr[5], vr[2];
#pragma unroll
        for (int e = 0; e < 4; ++e) wr[e] = lds16(raw + 5120 + c8 * 64 + e * 16);
#pragma unroll
        for (int j = 0; j < 5; ++j) kr[j] = lds16(raw + (5 + 2 * r8 + j) * 128 + c8 * 16);
        vr[0] = lds16(raw + 3072 + 2 * r8 * 128 + c8 * 16); vr[1] = lds16(raw + 3072 + (2 * r8 + 1) * 128 + c8 * 16);
        SB();
        u32x4 o0, o1;
#pragma unroll
        for (int e = 0; e < 4; ++e) {
            u32 pl[4], ph[4];
#pragma unroll
            for (int j = 0; j < 4; ++j) { pl[j] = __builtin_amdgcn_perm(kr[j][e], kr[j + 1][e], 0x05040100u); ph[j] = __builtin_amdgcn_perm(kr[j][e], kr[j + 1][e], 0x07060302u); }
            f32x2 a = {dot2(pl[0], wr[e].y, dot2(pl[2], wr[e].x, 0.f)), dot2(ph[0], wr[e].w, dot2(ph[2], wr[e].z, 0.f))};
            f32x2 b = {dot2(pl[1], wr[e].y, dot2(pl[3], wr[e].x, 0.f)), dot2(ph[1], wr[e].w, dot2(ph[3], wr[e].z, 0.f))};
            const u32 ar = pk2(a.x, a.y), br = pk2(b.x, b.y);
            a = f32x2{bflo(ar), bfhi(ar)} * f32x2{bflo(vr[0][e]), bfhi(vr[0][e])}; b = f32x2{bflo(br), bfhi(br)} * f32x2{bflo(vr[1][e]), bfhi(vr[1][e])};
            o0[e] = pk2(a.x, a.y); o1[e] = pk2(b.x, b.y);
        }
        sts16(lds0 + L_KVS + (16 * g + 2 * r8) * 144 + c8 * 16, o0);
        sts16(lds0 + L_KVS + (16 * g + 2 * r8 + 1) * 144 + c8 * 16, o1);
        asm volatile("s_waitcnt lgkmcnt(0)" ::: "memory");
        lds_add(lds0 + F_KVS);
    }
    f32x16 acc[2][2] = {};
    Fr fa, fb;
    WAITV(1);
    lds_add(lds0 + F_LAND);
    u32 pk = lds_peek(lds0 + F_LAND);
    g1_stage<0, false>(ga, fb, fa, acc, lds0, g, pk);
    g1_stage<1, true>(ga, fa, fb, acc, lds0, g, pk);
    g1_stage<2, true>(ga, fb, fa, acc, lds0, g, pk);
    asm volatile("s_waitcnt vmcnt(0)" : "+v"(ep) :: "memory");
    const u64 epoch = ((u64)(u32)__builtin_amdgcn_readfirstlane(ep.y) << 32) | (u32)__builtin_amdgcn_readfirstlane(ep.x);
    const u64 tag = epoch + TAG_SALT;
    const u32 tlo = (u32)tag, thi = (u32)(tag >> 32);
    const u32 smask = ((lane ^ (u32)epoch) & 1u) ? 0x80008000u : 0u;
    g1_stage<3, true>(ga, fa, fb, acc, lds0, g, pk);
    const u32 rgo = WS_RG + (q.R0 + 64 * sh + lane) * 8, cgo = WS_CG + (q.F0 + 64 * fh + lane) * 8;
    u32x2 rg = gld8c(rgo, ws), cg = gld8c(cgo, ws);
    u32 b1v = gld2((u32)((64 * fh + lane) * 2), (const char*)p.b1 + q.F0 * 2);
    G2Ctx g2; make_g2(g2, p, q, g, lane, lds0);
    {
        const int blk = (lane >> 4) & 1, qq = (lane & 15) >> 2, pp = lane & 3;
        const u32 t0 = g2.ldsg + (4 * hi + qq) * 128 + 16 * ((2 * blk + (pp >> 1)) ^ ((qq >> 1) << 2)) + 8 * (pp & 1);
#pragma unroll
        for (int i = 0; i < 3; ++i) { g2.ta[i][0] = t0 + i * 65536; g2.ta[i][1] = (t0 ^ 64) + i * 65536; PIN(g2.ta[i][0]); PIN(g2.ta[i][1]); }
#pragma unroll
        for (int i = 0; i < 4; ++i) PIN(g2.voffw[i]);
    }
    g2.smask = smask;
    g1_stage<4, true>(ga, fb, fa, acc, lds0, g, pk);
    g1_stage<5, true>(ga, fa, fb, acc, lds0, g, pk);
    g1_stage<6, true>(ga, fb, fa, acc, lds0, g, pk);
    cons_set<6>(lds0, g);
    asm volatile("s_waitcnt vmcnt(0)" : "+v"(rg), "+v"(cg), "+v"(b1v) :: "memory");
    _Pragma("clang loop unroll(disable)") for (int spin = 0; spin < SPIN_CAP && !__all(rg.y == tlo && cg.y == tlo); ++spin) {
        rg = gld8c(rgo, ws); cg = gld8c(cgo, ws);
        asm volatile("s_waitcnt vmcnt(0)" : "+v"(rg), "+v"(cg) :: "memory");
    }
    if (fh == 0 && sh == q.fh2) *(LAS u32*)(lds0 + L_ROWF + lane * 4) = rg.x;
    g1_mma<2>(fa, acc);
    {
        u32x4 ax[2], bx[2];
        const auto rr = __builtin_amdgcn_permlane32_swap(rg.x, rg.x, false, false);
        const auto cc = __builtin_amdgcn_permlane32_swap(cg.x, cg.x, false, false);
        const auto bb = __builtin_amdgcn_permlane32_swap(b1v, b1v, false, false);
#pragma unroll
        for (int i = 0; i < 2; ++i) {
            const u32 nm = __builtin_amdgcn_perm(rr[i], rr[i], 0x01000100u) ^ 0x80008000u;
            ax[i] = u32x4{cc[i], bb[i], 0u, 0u};
            bx[i] = u32x4{hi ? 0u : nm, hi ? 0u : (rr[i] >> 16), 0u, 0u};
        }
#pragma unroll
        for (int st = 0; st < 2; ++st)
#pragma unroll
            for (int mt = 0; mt < 2; ++mt) acc[mt][st] = mfma32(ax[mt], bx[st], acc[mt][st]);
#pragma unroll
        for (int st = 0; st < 2; ++st) {
            char* hb = ws + WS_H + (size_t)((4 * q.RB + 2 * sh + st) * 112 + q.CT * 8 + fh * 4) * 1024;
#pragma unroll
            for (int mt = 0; mt < 2; ++mt)
#pragma unroll
                for (int t = 0; t < 2; ++t) {
                    u32x4 hv;
#pragma unroll
                    for (int j = 0; j < 4; ++j) {
                        f32x2 a = {acc[mt][st][8 * t + 2 * j], acc[mt][st][8 * t + 2 * j + 1]};
                        f32x2 u = {relu(a.x), relu(a.y)};
                        u = f32x2{mul1(u.x, u.x), mul1(u.y, u.y)};
                        hv[j] = (pk2(u.x, u.y) & 0x7fff7fffu) | smask;
                    }
                    if (mt == 0 && t == 0) gst16c<0>(lane * 16u, hb, hv);
                    if (mt == 0 && t == 1) gst16c<1024>(lane * 16u, hb, hv);
                    if (mt == 1 && t == 0) gst16c<2048>(lane * 16u, hb, hv);
                    if (mt == 1 && t == 1) gst16c<3072>(lane * 16u, hb, hv);
                    SB();
                }
        }
    }
    u32x4 wf[WS_][4];
    cons_wait(lds0, 4);
    asm volatile("s_waitcnt lgkmcnt(0)" ::: "memory");
    {
        u32x4 hf[HS][4];
        if (lds_arrive_last(lds0 + F_HCNT, 4)) { if (lane == 0) gst8c<0>(WS_ALL + q.b * 8, ws, u32x2{tlo, thi}); }
        g2_issue_w<2>(g2); g2_issue_w<3>(g2); g2_issue_w<4>(g2);
        WAITV(0);
        g2_issue_h<0>(g2, hf); g2_issue_h<1>(g2, hf); g2_issue_h<2>(g2, hf); g2_issue_h<3>(g2, hf);
        lds_wait(lds0 + F_W);
        g2_read<0>(g2, wf);
        asm volatile("s_waitcnt vmcnt(%4)" : "+v"(hf[0][0]), "+v"(hf[0][1]), "+v"(hf[0][2]), "+v"(hf[0][3]) : "i"(4 * (HD - 1)) : "memory");
        {
            u32 bad = 0u;
#pragma unroll
            for (int i = 0; i < 16; ++i) { hf[0][i >> 2][i & 3] ^= smask; bad |= hf[0][i >> 2][i & 3]; }
            g2_retry<0>(g2, hf, bad);
        }
        f32x16 a2[2][2] = {};
        g2_step<0>(g2, hf, a2, wf);
        const u32 pw = g2.ldsg + (s31 >> 4) * L_W + (s31 & 15) * 256;
#pragma unroll
        for (int s2 = 0; s2 < 2; ++s2)
#pragma unroll
            for (int dt = 0; dt < 2; ++dt)
#pragma unroll
                for (int gr = 0; gr < 4; ++gr) {
                    const u32 a = pw + (s2 ? 0u : 3 * L_PIECE) + 16 * ((8 * dt + 2 * gr + hi) ^ (s31 & 7));
                    sts16(a, __builtin_bit_cast(u32x4, f32x4{a2[dt][s2][4 * gr], a2[dt][s2][4 * gr + 1], a2[dt][s2][4 * gr + 2], a2[dt][s2][4 * gr + 3]}));
                }
    }
    BARL();
    final_epilogue(p, q, lds0);
}

DI void body_c(const Params& p, const int cw) {
    const u32 lds0 = (u32)(size_t)(LAS char*)smem;
    const int lane = threadIdx.x & 63;
    const Geo q = make_geo();
    char* ws = p.ws;
    const int hi = lane >> 5, s31 = lane & 31;
    const int half = cw & 1, chl = cw >> 1, chan = 2 * q.b + chl;
    u32x2 ep;
    u32x4 xs[4]; u32 dv;
    if (cw < 2) {
        CL c, c2; make_cl(c, p, q, 0, lane, lds0); c2 = c; c2.xb += 64 * 896; c2.wb += 32 * 3584; c2.sl += 8192;
        ep = gld8c(0u, ws + WS_EPOCH);
        if (cw == 0) {
            l_dma<1>(c); l_dma<1>(c2);
            WAITV(0); lds_add(lds0 + F_LAND + 4);
            cons_wait(lds0, 0); l_dma<4>(c); l_dma<4>(c2);
            WAITV(0); lds_add(lds0 + F_LAND + 16);
        } else {
            l_dma<2>(c); l_dma<2>(c2);
            WAITV(0); lds_add(lds0 + F_LAND + 8);
            cons_wait(lds0, 1); l_dma<5>(c); l_dma<5>(c2);
            WAITV(0); lds_add(lds0 + F_LAND + 20);
        }
        asm volatile("s_waitcnt vmcnt(0)" : "+v"(ep) :: "memory");
        const u32 tg32 = (u32)((((u64)ep.y << 32) | ep.x) + TAG_SALT);
        if (cw == 1) {
            G2Ctx g2; make_g2(g2, p, q, 0, lane, lds0);
            cons_wait(lds0, 3);
#define W4(C) do { const char* sb = g2_wsrc<C>(g2); _Pragma("unroll") for (int i = 0; i < 4; ++i) { setm0(g2_loc<C>(g2) + i * 4096); \
                dmai<0>(g2.voffw[0], sb + i * 32 * 896); dmai<1024>(g2.voffw[1], sb + i * 32 * 896); dmai<2048>(g2.voffw[2], sb + i * 32 * 896); dmai<3072>(g2.voffw[3], sb + i * 32 * 896); } } while (0)
            W4(0); W4(1);
#undef W4
            dv = gld2(0u, (const char*)p.ssmd + chan * 2);
            WAITV(1);
            lds_set(lds0 + F_W);
            asm volatile("s_waitcnt vmcnt(0)" : "+v"(dv) :: "memory");
        } else {
            const char* kb2 = ws + WS_KVT + (size_t)(2 * q.b) * TP;
            u32x4 u[16];
            probe<2>((u32)(hi * TP + s31 * 256), kb2, tg32);
            _Pragma("clang loop unroll(disable)") for (int spin = 0; spin < SPIN_CAP; ++spin) {
#pragma unroll
                for (int c = 0; c < 4; ++c) {
                    const char* sb = kb2 + (c >> 1) * TP + (c & 1) * 4096;
                    u[4 * c] = gld16c<0>(lane * 16u, sb); u[4 * c + 1] = gld16c<1024>(lane * 16u, sb); u[4 * c + 2] = gld16c<2048>(lane * 16u, sb); u[4 * c + 3] = gld16c<3072>(lane * 16u, sb);
                }
                asm volatile("s_waitcnt vmcnt(0)" : "+v"(u[0]), "+v"(u[1]), "+v"(u[2]), "+v"(u[3]), "+v"(u[4]), "+v"(u[5]), "+v"(u[6]), "+v"(u[7]), "+v"(u[8]), "+v"(u[9]), "+v"(u[10]),
                             "+v"(u[11]), "+v"(u[12]), "+v"(u[13]), "+v"(u[14]), "+v"(u[15]) :: "memory");
                u32 bad = 0u;
#pragma unroll
                for (int i = 0; i < 16; ++i) bad = chk(u[i].w, tg32, chk(u[i].y, tg32, bad));
                if (!__any(bad != 0u)) break;
                __builtin_amdgcn_s_sleep(4);
            }
            dv = gld2(0u, (const char*)p.ssmd + chan * 2);
            cons_wait(lds0, 5); cons_wait(lds0, 6);
            const u32 uq_ = lds0 + L_CONV + TOEP_CH + lane * 8;
#pragma unroll
            for (int i = 0; i < 16; ++i) sts8(uq_ + (i >> 3) * CONV_CH + (i & 7) * 512, u32x2{u[i].x, u[i].z});
            sts8(uq_ + 4096, u32x2{0u, 0u}); sts8(uq_ + CONV_CH + 4096, u32x2{0u, 0u});
            asm volatile("s_waitcnt lgkmcnt(0)" ::: "memory");
            lds_add(lds0 + F_TOEP); lds_add(lds0 + F_TOEP + 4);
            asm volatile("s_waitcnt vmcnt(0)" : "+v"(dv) :: "memory");
        }
    } else {
        const int kw = cw - 2;
        if (kw == 0 && lane < 8) dma16(lane * 16u, (const char*)p.b2 + q.D0 * 2, lds0 + L_B2);
        ep = gld8c(0u, ws + WS_EPOCH);
        CL c; make_cl(c, p, q, 2 * kw, lane, lds0);
        l_dma<3>(c);
        lds_wait_ge(lds0 + F_KVS, 256, lds_peek(lds0 + F_KVS));
        asm volatile("s_waitcnt vmcnt(16)" : "+v"(ep) :: "memory");
        const u32 tg32 = (u32)((((u64)ep.y << 32) | ep.x) + TAG_SALT);
        {
            const int grp = lane >> 4, qq = (lane & 15) >> 2, pp = lane & 3;
            const u32 a = lds0 + L_KVS + (16 * grp + 4 * pp + 3 - qq) * 144 + kw * 64;
            const u32 vo = (u32)((q.D0 + 32 * kw + (lane & 3)) * TP + (2044 - (int)q.T0 - 4 * (lane >> 2)) * 4);
            u32x2 tr[8];
#pragma unroll
            for (int i = 0; i < 8; ++i) tr[i] = ldtr(a + 8 * i);
            SB();
#pragma unroll
            for (int i = 0; i < 8; ++i) gst16c<0>(vo, ws + WS_KVT + i * 4 * TP, u32x4{tr[i].x, tg32, tr[i].y, tg32});
        }
        WAITV(8);
        lds_add(lds0 + F_LAND + 12);
        cons_wait(lds0, 2);
        l_dma<6>(c);
        u32x4 sk[5];
        const u32x4 sksrd = mksrd((const char*)p.ssmk + (size_t)(2 * q.b + kw) * 4096, 4096);
#pragma unroll
        for (int r = 0; r < 5; ++r) sk[r] = bld16((u32)((lane - 1 + 63 * r) * 16), sksrd);
        { const char* xsl = (const char*)p.x + (size_t)q.T0 * 896 + (q.D0 + 32 * kw) * 2;
          xs[0] = gld16o<0>((u32)(lane * 896), xsl); xs[1] = gld16o<16>((u32)(lane * 896), xsl); xs[2] = gld16o<32>((u32)(lane * 896), xsl); xs[3] = gld16o<48>((u32)(lane * 896), xsl); }
        dv = gld2(0u, (const char*)p.ssmd + chan * 2);
        WAITV(10);
        lds_add(lds0 + F_LAND + 24);
        asm volatile("s_waitcnt vmcnt(0)" : "+v"(sk[0]), "+v"(sk[1]), "+v"(sk[2]), "+v"(sk[3]), "+v"(sk[4]), "+v"(xs[0]), "+v"(xs[1]), "+v"(xs[2]), "+v"(xs[3]), "+v"(dv) :: "memory");
        u32x4 tw[5][4];
#pragma unroll
        for (int r = 0; r < 5; ++r) {
            const u32x4 B = sk[r];
            u32x4 A;
#pragma unroll
            for (int j = 0; j < 4; ++j) A[j] = __builtin_amdgcn_update_dpp(0u, B[j], 0x138  , 0xf, 0xf, false);
            const u32 e0 = __builtin_amdgcn_alignbit(A.y, A.x, 16), e1 = __builtin_amdgcn_alignbit(A.z, A.y, 16), e2 = __builtin_amdgcn_alignbit(A.w, A.z, 16),
                      e3 = __builtin_amdgcn_alignbit(B.x, A.w, 16), e4 = __builtin_amdgcn_alignbit(B.y, B.x, 16);
            tw[r][0] = u32x4{e0, e1, e2, e3}; tw[r][1] = u32x4{A.y, A.z, A.w, B.x}; tw[r][2] = u32x4{e1, e2, e3, e4}; tw[r][3] = u32x4{A.z, A.w, B.x, B.y};
            PIN(tw[r][0]); PIN(tw[r][1]); PIN(tw[r][2]); PIN(tw[r][3]);
        }
        cons_wait(lds0, 5); cons_wait(lds0, 6);
        const u32 tq = lds0 + L_CONV + kw * CONV_CH + lane * 16;
#pragma unroll
        for (int r = 0; r < 5; ++r) {
            if ((lane > 0 || r == 0) && (r < 4 || lane < 6)) {
#pragma unroll
                for (int c = 0; c < 4; ++c) sts16(tq + r * 1008 + c * TOEP_CP, tw[r][c]);
            }
        }
        asm volatile("s_waitcnt lgkmcnt(0)" ::: "memory");
        lds_add(lds0 + F_TOEP + kw * 4);
    }
    const u64 epoch = ((u64)(u32)__builtin_amdgcn_readfirstlane(ep.y) << 32) | (u32)__builtin_amdgcn_readfirstlane(ep.x);
    const u64 tag = epoch + TAG_SALT;
    const u32 tlo = (u32)tag, thi = (u32)(tag >> 32);
    const u32 tbase = lds0 + L_CONV + chl * CONV_CH;
    const u32 ub = tbase + TOEP_CH;
    lds_wait_ge(lds0 + F_TOEP + chl * 4, 128, lds_peek(lds0 + F_TOEP + chl * 4));
    const int bb = lane & 15, gg = lane >> 4;
    const u32 aA = tbase + (bb & 3) * TOEP_CP + ((bb >> 2) + 2 * gg) * 8 + half * 256;
    const u32 aB = ub + (30 - 2 * bb + gg) * 16 + half * 256;
    f32x4 ac8[8] = {};
    __builtin_amdgcn_s_setprio(2);
    conv_body(aA, aB, ac8);
    f32x4 acc[4];
    {
        const u32 xo = ub + U_SZ + lane * 16;
#pragma unroll
        for (int i = 0; i < 4; ++i) { const f32x4 give = half ? ac8[i] : ac8[4 + i]; acc[i] = half ? ac8[4 + i] : ac8[i]; sts16(xo + half * 4096 + i * 1024, __builtin_bit_cast(u32x4, give)); }
        asm volatile("s_waitcnt lgkmcnt(0)" ::: "memory");
        lds_set(lds0 + F_XCH + cw * 4);
        lds_wait(lds0 + F_XCH + (cw ^ 1) * 4);
        f32x4 got[4];
#pragma unroll
        for (int i = 0; i < 4; ++i) got[i] = __builtin_bit_cast(f32x4, lds16(xo + (half ^ 1) * 4096 + i * 1024));
        SB();
#pragma unroll
        for (int i = 0; i < 4; ++i) acc[i] = f32x4{add1(acc[i][0], got[i][0]), add1(acc[i][1], got[i][1]), add1(acc[i][2], got[i][2]), add1(acc[i][3], got[i][3])};
    }
    {
        const float dd = bflo(dv);
        char* yb = ws + WS_YT + (size_t)chan * TP;
        u32x2 uq[4];
#pragma unroll
        for (int i = 0; i < 4; ++i) uq[i] = lds8(ub + (2044 - (256 * (4 * half + i) + 16 * bb + 4 * gg)) * 2);
        SB();
#pragma unroll
        for (int i = 0; i < 4; ++i) {
            const int qt = 4 * half + i;
            const int t = 256 * qt + 16 * bb + 4 * gg;
            const u32x2 uu = uq[i];
            const u32 c01 = pk2(acc[i][0], acc[i][1]), c23 = pk2(acc[i][2], acc[i][3]);
            const u32 d01 = pk2(mul1(bfhi(uu.y), dd), mul1(bflo(uu.y), dd)), d23 = pk2(mul1(bfhi(uu.x), dd), mul1(bflo(uu.x), dd));
            float y0 = add1(bflo(d01), bflo(c01)), y1 = add1(bfhi(d01), bfhi(c01));
            float y2 = add1(bflo(d23), bflo(c23)), y3 = add1(bfhi(d23), bfhi(c23));
            gst16c<0>((u32)(t * 4), yb, u32x4{pk2(y0, y1), tlo, pk2(y2, y3), tlo});
        }
        lds_arrive_last(lds0 + F_YCNT, 4);
    }
    if (cw >= 2) {
        const int kw = cw - 2;
        lds_wait_ge(lds0 + F_YCNT, 4, lds_peek(lds0 + F_YCNT)); lds_wait_ge(lds0 + F_HCNT, 4, lds_peek(lds0 + F_HCNT));
        __builtin_amdgcn_s_setprio(0);
        const u32 slab = slab_base(lds0, 2 * kw) + lane * 64;
        float xn[32];
        {
            const u32 rf = *(const LAS u32*)(lds0 + L_ROWF + lane * 4);
            const float nm = -bflo(rf); float ri = __builtin_amdgcn_rcpf(bfhi(rf));
            asm volatile("s_nop 1" : "+v"(ri));
#pragma unroll
            for (int j = 0; j < 8; ++j) {
                const u32 lo = xs[j >> 1][(j & 1) * 2], hi_ = xs[j >> 1][(j & 1) * 2 + 1];
                const f32x4 v = {bflo(lo), bfhi(lo), bflo(hi_), bfhi(hi_)};
                sts16(slab + (j >> 2) * 8192 + 4096 + (j & 3) * 16, __builtin_bit_cast(u32x4, v));
#pragma unroll
                for (int e = 0; e < 4; e += 2) {
                    const u32 dx = pk2(add1(v[e], nm), add1(v[e + 1], nm)), xr = pk2(mul1(bflo(dx), ri), mul1(bfhi(dx), ri));
                    xn[4 * j + e] = bflo(xr); xn[4 * j + e + 1] = bfhi(xr);
                }
            }
            if (kw == 0) *(LAS float*)(lds0 + L_R2 + lane * 4) = -1.4426950408889634f * ri * ri;
            else *(LAS float*)(lds0 + L_B2F + lane * 4) = -1.4426950408889634f * bflo(*(const LAS u16*)(lds0 + L_B2 + lane * 2));
        }
        const char* yb = ws + WS_YT + (size_t)(q.D0 + 32 * kw) * TP + q.T0 * 4;
        const u32 vo = (u32)(gg * TP + bb * 16);
        u32x4 y[8];
        __builtin_amdgcn_s_setprio(2);
        _Pragma("clang loop unroll(disable)") for (int spin = 0; spin < SPIN_CAP; ++spin) {
#pragma unroll
            for (int i = 0; i < 8; ++i) y[i] = gld16c<0>(vo, yb + i * 4 * TP);
            asm volatile("s_waitcnt vmcnt(0)" : "+v"(y[0]), "+v"(y[1]), "+v"(y[2]), "+v"(y[3]), "+v"(y[4]), "+v"(y[5]), "+v"(y[6]), "+v"(y[7]) :: "memory");
            u32 bad = 0u;
#pragma unroll
            for (int i = 0; i < 8; ++i) bad = chk(y[i].w, tlo, chk(y[i].y, tlo, bad));
            if (!__any(bad != 0u)) break;
            __builtin_amdgcn_s_sleep(8);
        }
        const u32 ys = lds0 + L_CONV + kw * CONV_CH + TOEP_CH + U_SZ;
#pragma unroll
        for (int i = 0; i < 8; ++i) sts8(ys + (4 * i + gg) * 144 + bb * 8, u32x2{y[i].x, y[i].z});
        const int qq = (lane & 15) >> 2, pp = lane & 3;
        const u32 a = ys + qq * 144 + gg * 32 + pp * 8;
        u32x2 t[8];
        const float nln2 = -0.6931471805599453f;
#pragma unroll
        for (int j = 0; j < 8; ++j) t[j] = ldtr(a + j * 4 * 144);
        SB();
#pragma unroll
        for (int j = 0; j < 8; ++j)
        {
            const u32 s01 = pk2(mul1(bflo(t[j].x), xn[4 * j]), mul1(bfhi(t[j].x), xn[4 * j + 1])), s23 = pk2(mul1(bflo(t[j].y), xn[4 * j + 2]), mul1(bfhi(t[j].y), xn[4 * j + 3]));
            sts16(slab + (j >> 2) * 8192 + (j & 3) * 16, __builtin_bit_cast(u32x4, f32x4{mul1(bflo(s01), nln2), mul1(bfhi(s01), nln2), mul1(bflo(s23), nln2), mul1(bfhi(s23), nln2)}));
        }
        if (q.b == 0 && kw == 1) {
            _Pragma("clang loop unroll(disable)") for (int spin = 0; spin < SPIN_CAP; ++spin) {
                u32x4 f0 = gld16c<0>(lane * 16u, ws + WS_ALL), f1 = gld16c<1024>((lane < 48 ? lane : 0) * 16u, ws + WS_ALL);
                asm volatile("s_waitcnt vmcnt(0)" : "+v"(f0), "+v"(f1) :: "memory");
                bool ok = f0.x == tlo && f0.y == thi && f0.z == tlo && f0.w == thi && f1.x == tlo && f1.y == thi && f1.z == tlo && f1.w == thi;
                if (__all(ok)) break;
                __builtin_amdgcn_s_sleep(2);
            }
            if (lane == 0) { const u64 e1 = epoch + 1; gst8c<0>(0u, ws + WS_EPOCH, u32x2{(u32)e1, (u32)(e1 >> 32)}); }
        }
    }
    __builtin_amdgcn_s_setprio(0);
    BARL();
    final_epilogue(p, q, lds0);
}

DI float dpp_xor_sum8(float v) {
    v += __uint_as_float(__builtin_amdgcn_mov_dpp(__float_as_uint(v), 0xB1, 0xf, 0xf, true));
    v += __uint_as_float(__builtin_amdgcn_mov_dpp(__float_as_uint(v), 0x4E, 0xf, 0xf, true));
    v += __uint_as_float(__builtin_amdgcn_mov_dpp(__float_as_uint(v), 0x141, 0xf, 0xf, true));
    return v;
}
DI void body_help(const Params& p, const int wave) {
    const u32 lds0 = (u32)(size_t)(LAS char*)smem;
    const int lane = threadIdx.x & 63, r8 = lane >> 3, c8 = lane & 7;
    const int xcd = blockIdx.x & 7, j = blockIdx.x >> 3;
    char* ws = p.ws;
    u32x2 ep = gld8c(0u, ws + WS_EPOCH);
    u32x4 d[14];
    const int ctile = (xcd >> 1) * 4 + j;
    const bool cols = wave >= 4;
    if (!cols) {
        const char* xb = (const char*)p.x + (size_t)(256 * xcd + 64 * j + 16 * wave) * 896;
#pragma unroll
        for (int i = 0; i < 14; ++i) d[i] = gld16((u32)((r8 + 8 * (i / 7)) * 896 + (i % 7) * 128 + c8 * 16), xb);
    } else {
        const char* wb = (const char*)p.w1 + (size_t)(112 * (wave - 4)) * 3584 + ((xcd & 1) * 14 + (ctile < 14 ? ctile : 0)) * 128;
#pragma unroll
        for (int i = 0; i < 14; ++i) d[i] = gld16((u32)((r8 + 8 * i) * 3584 + c8 * 16), wb);
    }
    asm volatile("s_waitcnt vmcnt(0)" : "+v"(ep), "+v"(d[0]), "+v"(d[1]), "+v"(d[2]), "+v"(d[3]), "+v"(d[4]), "+v"(d[5]), "+v"(d[6]), "+v"(d[7]), "+v"(d[8]), "+v"(d[9]),
                 "+v"(d[10]), "+v"(d[11]), "+v"(d[12]), "+v"(d[13]) :: "memory");
    const u32 tlo = (u32)((((u64)ep.y << 32) | ep.x) + TAG_SALT);
    if (!cols) {
#pragma unroll
        for (int h = 0; h < 2; ++h) {
            float s0 = 0.f, s1 = 0.f, q0 = 0.f, q1 = 0.f;
#pragma unroll
            for (int i = 0; i < 7; ++i)
#pragma unroll
                for (int e = 0; e < 4; e += 2) {
                    s0 = dot2(d[7 * h + i][e], 0x3f803f80u, s0); q0 = dot2(d[7 * h + i][e], d[7 * h + i][e], q0);
                    s1 = dot2(d[7 * h + i][e + 1], 0x3f803f80u, s1); q1 = dot2(d[7 * h + i][e + 1], d[7 * h + i][e + 1], q1);
                }
            const float mean = dpp_xor_sum8(s0 + s1) * (1.f / 448.f);
            const float var = fmaxf(dpp_xor_sum8(q0 + q1) * (1.f / 448.f) - mean * mean, 0.f);
            const float sd = __builtin_amdgcn_sqrtf(rbf(rbf(var) + 1e-5f));
            if (c8 == 0) gst8c<0>((u32)(WS_RG + (256 * xcd + 64 * j + 16 * wave + 8 * h + r8) * 8), ws, u32x2{pk2(mean, sd), tlo});
        }
    } else {
        float a[8] = {};
        u32 klo = 0x00003f80u, khi = 0x3f800000u; PIN(klo); PIN(khi);
#pragma unroll
        for (int i = 0; i < 14; ++i)
#pragma unroll
            for (int e = 0; e < 4; ++e) { a[2 * e] = dot2(d[i][e], klo, a[2 * e]); a[2 * e + 1] = dot2(d[i][e], khi, a[2 * e + 1]); }
        const u32 o = lds0 + ((wave - 4) * 8 + r8) * 272 + c8 * 32;
        sts16(o, __builtin_bit_cast(u32x4, f32x4{a[0], a[1], a[2], a[3]})); sts16(o + 16, __builtin_bit_cast(u32x4, f32x4{a[4], a[5], a[6], a[7]}));
    }
    BARL();
    if (wave == 4 && ctile < 14) {
        float t0 = 0.f, t1 = 0.f;
#pragma unroll
        for (int i = 0; i < 32; i += 2) { t0 += *(const LAS float*)(lds0 + i * 272 + lane * 4); t1 += *(const LAS float*)(lds0 + (i + 1) * 272 + lane * 4); }
        gst8c<0>((u32)(WS_CG + (((xcd & 1) * 14 + ctile) * 64 + lane) * 8), ws, u32x2{pk2(t0 + t1, (t0 + t1) - rbf(t0 + t1)), tlo});
    }
    BARL();
    {
        const int crot = (2 * xcd) % 14;
        setm0(lds0 + 16384 + wave * 4096);
        for (int c = 0; c < 14; ++c) {
            int cc = c + crot; if (cc >= 14) cc -= 14;
            const char* sb = (const char*)p.w2 + (size_t)cc * 114688 + (j * 8 + wave) * 3584;
            if (lane < 32) dmai<3072>(lane * 16u, sb);
            dmai<0>(lane * 16u, sb); dmai<1024>(lane * 16u, sb); dmai<2048>(lane * 16u, sb);
            WAITV(8);
        }
        WAITV(0);
    }
}

__global__ __launch_bounds__(512, 2) void fused_kernel(Params p) {
    {
        u64 pc; asm volatile("s_getpc_b64 %0" : "=s"(pc));
        if (threadIdx.x < CODE_LINES) touch((const char*)(pc & ~127ull) + threadIdx.x * 128, (u32)(size_t)(LAS char*)smem + L_B2F);
    }
    const int wave = __builtin_amdgcn_readfirstlane(threadIdx.x >> 6);
    if (blockIdx.x < NHELP) { body_help(p, wave); return; }
    if (threadIdx.x < 32) *(LAS u32*)((u32)(size_t)(LAS char*)smem + L_FLAG + threadIdx.x * 4) = 0u;
    else if (threadIdx.x < 64) *(LAS u32*)((u32)(size_t)(LAS char*)smem + F_CONS + ((threadIdx.x - 32) >> 2) * 144 + (threadIdx.x & 3) * 4) = 0u;
    BARL();
    if (wave < 4) body_g(p, wave); else body_c(p, wave - 4);
}

extern "C" void kernel_launch(void* const* d_in, const int* in_sizes, int n_in,
                              void* d_out, int out_size, void* d_ws, size_t ws_size,
                              hipStream_t stream) {
    Params p;
    p.x = (const u16*)d_in[0]; p.k = (const u16*)d_in[1]; p.v = (const u16*)d_in[2]; p.shk = (const u16*)d_in[3];
    p.ssmk = (const u16*)d_in[4]; p.ssmd = (const u16*)d_in[5]; p.w1 = (const u16*)d_in[7]; p.b1 = (const u16*)d_in[8];
    p.w2 = (const u16*)d_in[9]; p.b2 = (const u16*)d_in[10];
    p.out = (u16*)d_out; p.ws = (char*)d_ws;
    (void)hipFuncSetAttribute((const void*)fused_kernel, hipFuncAttributeMaxDynamicSharedMemorySize, LDS_BYTES);
    fused_kernel<<<NWG + NHELP, 512, LDS_BYTES, stream>>>(p);
}
```

```cpp
#include <hip/hip_runtime.h>
#include <stdint.h>
#pragma clang diagnostic ignored "-Wint-to-pointer-cast"

typedef unsigned short u16;
typedef unsigned int u32;
typedef unsigned long long u64;
typedef u32 u32x4 __attribute__((ext_vector_type(4)));
typedef u32 u32x2 __attribute__((ext_vector_type(2)));
typedef float f32x4 __attribute__((ext_vector_type(4)));
typedef float f32x2 __attribute__((ext_vector_type(2)));
typedef float f32x16 __attribute__((ext_vector_type(16)));
typedef __bf16 bf16x8 __attribute__((ext_vector_type(8)));
typedef __bf16 bf16x2 __attribute__((ext_vector_type(2)));
typedef short s16x4 __attribute__((ext_vector_type(4)));
#define LAS __attribute__((address_space(3)))
#define GAS __attribute__((address_space(1)))
#define DI __device__ __forceinline__

constexpr int S_ = 2048, D_ = 448;
constexpr int NHELP = 32, NWG = 224;
constexpr int CODE_LINES = 264;
constexpr u32 WS_EPOCH = 0, WS_ALL = 2048, WS_RG = 4096, WS_CG = WS_RG + 16384;
constexpr u32 TP = 8192 + 128;
constexpr u32 WS_H = 1u << 20, WS_KVT = 9u << 20, WS_YT = 13u << 20;
constexpr u32 L_PIECE = 16384, L_W = 65536, L_LOC0 = 131072, LDS_BYTES = 163840;
constexpr u32 KVRAW_SZ = 5632, L_KVS = L_LOC0 + 4 * KVRAW_SZ, L_B2 = L_KVS + 9216, L_FLAG = L_B2 + 128, L_ROWF = L_FLAG + 128, L_B2F = L_ROWF + 256, L_R2 = L_B2F + 256;
static_assert(L_R2 + 256 <= LDS_BYTES, "lds");
constexpr u32 F_TOEP = L_FLAG, F_HCNT = L_FLAG + 16, F_YCNT = L_FLAG + 28, F_W = L_FLAG + 48, F_LAND = L_FLAG + 64, F_XCH = L_FLAG + 96, F_KVS = L_FLAG + 124;
constexpr u32 F_CONS = L_KVS + 128;
constexpr u32 L_CONV = 16384, CONV_CH = 65536, TOEP_CP = 4160, TOEP_CH = 4 * TOEP_CP, U_SZ = 4608;
constexpr u64 TAG_SALT = 0x9E3779B97F4A7C15ull;
constexpr int SPIN_CAP = 1 << 15;

DI void dma16(u32 voff, const void* sbase, u32 lds) {
    asm volatile("s_mov_b32 m0, %2\n\ts_nop 0\n\tglobal_load_lds_dwordx4 %0, %1" :: "v"(voff), "s"(sbase), "s"(lds) : "memory");
}
DI void setm0(u32 lds) { asm volatile("s_mov_b32 m0, %0\n\ts_nop 0" :: "s"(lds) : "memory"); }
template <int OFF> DI void dmai(u32 voff, const void* sbase) { asm volatile("global_load_lds_dwordx4 %0, %1 offset:%2" :: "v"(voff), "s"(sbase), "i"(OFF) : "memory"); }
DI void bdma16(u32 voff, u32x4 srd, u32 lds) {
    asm volatile("s_mov_b32 m0, %2\n\ts_nop 0\n\tbuffer_load_dwordx4 %0, %1, 0 offen lds" :: "v"(voff), "s"(srd), "s"(lds) : "memory");
}
#define SB() __builtin_amdgcn_sched_barrier(0)
template <int OFF> DI u32x2 alds8(u32 a) { u32x2 r; asm volatile("ds_read_b64 %0, %1 offset:%2" : "=v"(r) : "v"(a), "i"(OFF)); return r; }
template <int OFF> DI u32x4 alds16(u32 a) { u32x4 r; asm volatile("ds_read_b128 %0, %1 offset:%2" : "=v"(r) : "v"(a), "i"(OFF)); return r; }
DI u32x4 gld16(u32 voff, const void* sbase) { u32x4 r; asm volatile("global_load_dwordx4 %0, %1, %2" : "=v"(r) : "v"(voff), "s"(sbase) : "memory"); return r; }
template <int OFF> DI u32x4 gld16c(u32 voff, const void* sbase) { u32x4 r; asm volatile("global_load_dwordx4 %0, %1, %2 offset:%3 sc1" : "=v"(r) : "v"(voff), "s"(sbase), "i"(OFF) : "memory"); return r; }
DI u32x2 gld8c(u32 voff, const void* sbase) { u32x2 r; asm volatile("global_load_dwordx2 %0, %1, %2 sc1" : "=v"(r) : "v"(voff), "s"(sbase) : "memory"); return r; }
DI void touch(const void* addr, u32 lds) { asm volatile("s_mov_b32 m0, %1\n\ts_nop 0\n\tglobal_load_lds_dword %0, off" :: "v"(addr), "s"(lds) : "memory"); }
DI u32 gld2(u32 voff, const void* sbase) { u32 r; asm volatile("global_load_ushort %0, %1, %2" : "=v"(r) : "v"(voff), "s"(sbase) : "memory"); return r; }
DI u32x4 bld16(u32 voff, u32x4 srd) { u32x4 r; asm volatile("buffer_load_dwordx4 %0, %1, %2, 0 offen" : "=v"(r) : "v"(voff), "s"(srd) : "memory"); return r; }
template <int OFF> DI u32x4 gld16o(u32 voff, const void* sbase) { u32x4 r; asm volatile("global_load_dwordx4 %0, %1, %2 offset:%3" : "=v"(r) : "v"(voff), "s"(sbase), "i"(OFF) : "memory"); return r; }
template <int OFF> DI void gst16c(u32 voff, void* sbase, u32x4 v) { asm volatile("global_store_dwordx4 %0, %1, %2 offset:%3 sc1\n\ts_nop 1" :: "v"(voff), "v"(v), "s"(sbase), "i"(OFF) : "memory"); }
template <int OFF> DI void gst8c(u32 voff, void* sbase, u32x2 v) { asm volatile("global_store_dwordx2 %0, %1, %2 offset:%3 sc1" :: "v"(voff), "v"(v), "s"(sbase), "i"(OFF) : "memory"); }
#define WAITV(n) asm volatile("s_waitcnt vmcnt(%0)" :: "i"(n) : "memory")
#define BARL() asm volatile("s_waitcnt lgkmcnt(0)\n\ts_barrier" ::: "memory")
#define PIN(x) asm volatile("" : "+v"(x))

DI u32x4 mksrd(const void* p, u32 bytes) { u64 a = (u64)p; return u32x4{(u32)a, (u32)(a >> 32) & 0xffffu, bytes, 0x00020000u}; }
DI float bflo(u32 x) { return __uint_as_float(x << 16); }
DI float bfhi(u32 x) { return __uint_as_float(x & 0xffff0000u); }
DI u32 pk2(float a, float b) { bf16x2 v = {(__bf16)a, (__bf16)b}; return __builtin_bit_cast(u32, v); }
DI float rbf(float a) { return bflo(pk2(a, a)); }
DI float relu(float a) { int i = __float_as_int(a); return __int_as_float(i > 0 ? i : 0); }
DI float mul1(float a, float b) { float r; asm("v_mul_f32 %0, %1, %2" : "=v"(r) : "v"(a), "v"(b)); return r; }
DI float add1(float a, float b) { float r; asm("v_add_f32 %0, %1, %2" : "=v"(r) : "v"(a), "v"(b)); return r; }
DI float dot2(u32 a, u32 b, float c) { return __builtin_amdgcn_fdot2_f32_bf16(__builtin_bit_cast(bf16x2, a), __builtin_bit_cast(bf16x2, b), c, false); }
DI u32x4 lds16(u32 a) { return *(const LAS u32x4*)a; }
DI u32x2 lds8(u32 a) { return *(const LAS u32x2*)a; }
DI void sts16(u32 a, u32x4 v) { *(LAS u32x4*)a = v; }
DI void sts8(u32 a, u32x2 v) { *(LAS u32x2*)a = v; }
DI u32x2 ldtr(u32 a) { return __builtin_bit_cast(u32x2, __builtin_amdgcn_ds_read_tr16_b64_v4i16((LAS s16x4*)a)); }
DI u32x4 cat(u32x2 a, u32x2 b) { return u32x4{a.x, a.y, b.x, b.y}; }
DI f32x16 mfma32(u32x4 a, u32x4 b, f32x16 c) { return __builtin_amdgcn_mfma_f32_32x32x16_bf16(__builtin_bit_cast(bf16x8, a), __builtin_bit_cast(bf16x8, b), c, 0, 0, 0); }
DI f32x4 mfma16(u32x4 a, u32x4 b, f32x4 c) { return __builtin_amdgcn_mfma_f32_16x16x32_bf16(__builtin_bit_cast(bf16x8, a), __builtin_bit_cast(bf16x8, b), c, 0, 0, 0); }

struct Params {
    const u16 *x, *k, *v, *shk, *ssmk, *ssmd, *w1, *b1, *w2, *b2;
    u16* out; char* ws;
};

template <int SL> DI void probe(u32 voff, const void* sbase, u32 tag) {
    _Pragma("clang loop unroll(disable)") for (int spin = 0; spin < SPIN_CAP; ++spin) {
        u32x2 f = gld8c(voff, sbase);
        asm volatile("s_waitcnt vmcnt(0)" : "+v"(f) :: "memory");
        if (__all(f.y == tag)) break;
        __builtin_amdgcn_s_sleep(SL);
    }
}
DI bool lds_arrive_last(u32 addr, u32 n) {
    u32 old = 0;
    if ((threadIdx.x & 63) == 0) old = __hip_atomic_fetch_add((LAS u32*)addr, 1u, __ATOMIC_RELAXED, __HIP_MEMORY_SCOPE_WORKGROUP);
    return (u32)__builtin_amdgcn_readfirstlane(old) == n - 1;
}
DI void lds_wait(u32 addr) {
    _Pragma("clang loop unroll(disable)") for (int spin = 0; spin < (1 << 22); ++spin) {
        if (__builtin_amdgcn_readfirstlane(*(volatile LAS u32*)addr)) break;
        __builtin_amdgcn_s_sleep(1);
    }
    asm volatile("" ::: "memory");
}
DI void lds_add(u32 addr) { asm volatile("" ::: "memory"); __hip_atomic_fetch_add((LAS u32*)addr, 1u, __ATOMIC_RELAXED, __HIP_MEMORY_SCOPE_WORKGROUP); asm volatile("" ::: "memory"); }
DI u32 lds_peek(u32 addr) { return *(volatile LAS u32*)addr; }
DI void lds_wait_ge(u32 addr, u32 n, u32 first) {
    u32 v = first;
    _Pragma("clang loop unroll(disable)") for (int spin = 0; spin < (1 << 22) && (u32)__builtin_amdgcn_readfirstlane(v) < n; ++spin) { __builtin_amdgcn_s_sleep(1); v = lds_peek(addr); }
    asm volatile("" ::: "memory");
}
template <int t> DI void cons_set(u32 lds0, int g) { asm volatile("s_waitcnt lgkmcnt(0)\n\tds_write_b32 %0, %1 offset:%2" :: "v"(lds0 + 131072 + g * 4), "v"(1u), "i"(F_CONS + t * 144 - 131072) : "memory"); }
DI void cons_wait(u32 lds0, int t) {
    _Pragma("clang loop unroll(disable)") for (int spin = 0; spin < (1 << 22); ++spin) {
        const u32x4 v = *(volatile LAS u32x4*)(lds0 + F_CONS + t * 144);
        if (__builtin_amdgcn_readfirstlane(v.x & v.y & v.z & v.w)) break;
        __builtin_amdgcn_s_sleep(1);
    }
    asm volatile("" ::: "memory");
}
DI void lds_set(u32 addr) { asm volatile("" ::: "memory"); if ((threadIdx.x & 63) == 0) *(volatile LAS u32*)addr = 1u; }
constexpr u32 loc_of(int c) { return (c % 5) == 0 ? L_LOC0 : (c % 5) == 1 ? 3 * L_PIECE : (c % 5) == 2 ? L_W + 3 * L_PIECE : (c % 5) == 3 ? 0u : L_W; }

constexpr int HD = 4, HS = HD + 1;
constexpr int WS_ = 2;
constexpr int g2_younger(int c) {
    const int n = c + 1;
    if (n >= 14) return 63;
    bool sh = false, sw = n < 5; int ah = 0, aw = sw ? 1000 : 0;
    for (int i = 0; i < HD; ++i) { if (sh) ah += 4; if (i == n) sh = true; }
    for (int i = 0; i < c; ++i) {
        if (i + HD < 14) { if (sh) ah += 4; if (sw) aw += 4; if (i + HD == n) sh = true; }
        if (i + 5 < 14) { if (sh) ah += 4; if (sw) aw += 4; if (i + 5 == n) sw = true; }
    }
    return ah < aw ? ah : aw;
}

struct G2Ctx { const char* w2b; const char* hb; int crot; u32 voffw[4], ldsg, vlane, ta[3][2], smask; };
template <int C> DI int g2_chunk(const G2Ctx& x) {
    int c = C + x.crot; if (c >= 14) c -= 14;
    return c;
}
template <int C> DI const char* g2_wsrc(const G2Ctx& x) { return x.w2b + (size_t)g2_chunk<C>(x) * (128 * 896); }
template <int C> DI u32 g2_loc(const G2Ctx& x) { return loc_of(C) + x.ldsg; }
template <int C, int N> DI void g2_w1(const G2Ctx& x, const char* sb) { if constexpr (N == 0) setm0(g2_loc<C>(x)); dmai<N * 1024>(x.voffw[N], sb); }
template <int C> DI void g2_issue_w(const G2Ctx& x) { const char* sb = g2_wsrc<C>(x); g2_w1<C, 0>(x, sb); g2_w1<C, 1>(x, sb); g2_w1<C, 2>(x, sb); g2_w1<C, 3>(x, sb); }
template <int C> DI const char* g2_hsrc(const G2Ctx& x) { return x.hb + g2_chunk<C>(x) * 8192; }
template <int N> DI u32x4 g2_h1(const G2Ctx& x, const char* sb) { return gld16c<(N & 1) * 1024>(x.vlane + (N >> 1) * 112 * 1024, sb); }
template <int C> DI void g2_issue_h(const G2Ctx& x, u32x4 (&hf)[HS][4]) {
    const char* sb = g2_hsrc<C>(x);
    hf[C % HS][0] = g2_h1<0>(x, sb); hf[C % HS][1] = g2_h1<1>(x, sb); hf[C % HS][2] = g2_h1<2>(x, sb); hf[C % HS][3] = g2_h1<3>(x, sb);
}
template <int C> DI void g2_read(const G2Ctx& x, u32x4 (&wf)[WS_][4]) {
    const u32 a0 = x.ta[loc_of(C) >> 16][0], a1 = x.ta[loc_of(C) >> 16][1];
    constexpr u32 lo = loc_of(C) & 0xffffu;
#pragma unroll
    for (int ks = 0; ks < 2; ++ks) {
        wf[C % WS_][ks * 2] = cat(ldtr(a0 + lo + ks * 2048), ldtr(a0 + lo + ks * 2048 + 1024));
        wf[C % WS_][ks * 2 + 1] = cat(ldtr(a1 + lo + ks * 2048), ldtr(a1 + lo + ks * 2048 + 1024));
    }
}
template <int C, int I> DI void g2_rd(const G2Ctx& x, u32x4 (&wf)[WS_][4]) {
    if constexpr (C < 14) {
        const u32 a = x.ta[loc_of(C) >> 16][I & 1]; constexpr u32 lo = (loc_of(C) & 0xffffu) + (I >> 1) * 2048;
        wf[C % WS_][I] = cat(ldtr(a + lo), ldtr(a + lo + 1024));
    }
}
DI u32 chk(u32 d, u32 m, u32 bad) { return __builtin_amdgcn_bitop3_b32(d, m, bad, 0xBE); }
template <int C> DI void g2_retry(const G2Ctx& x, u32x4 (&hf)[HS][4], u32 bad) {
    if (__builtin_expect(!__any((bad & 0x80008000u) != 0u), 1)) return;
    _Pragma("clang loop unroll(disable)") for (int spin = 0; spin < SPIN_CAP; ++spin) {
        __builtin_amdgcn_s_sleep(8);
        g2_issue_h<C>(x, hf);
        asm volatile("s_waitcnt vmcnt(0)" : "+v"(hf[C % HS][0]), "+v"(hf[C % HS][1]), "+v"(hf[C % HS][2]), "+v"(hf[C % HS][3]) :: "memory");
        bad = 0u;
#pragma unroll
        for (int i = 0; i < 16; ++i) { hf[C % HS][i >> 2][i & 3] ^= x.smask; bad |= hf[C % HS][i >> 2][i & 3]; }
        if (!__any((bad & 0x80008000u) != 0u)) break;
    }
}
template <int C> DI void g2_step(const G2Ctx& x, u32x4 (&hf)[HS][4], f32x16 (&acc)[2][2], u32x4 (&wf)[WS_][4]) {
    constexpr int N = (C + 1 < 14 ? C + 1 : C) % HS;
    asm volatile("s_waitcnt vmcnt(%4) lgkmcnt(0)" : "+v"(hf[N][0]), "+v"(hf[N][1]), "+v"(hf[N][2]), "+v"(hf[N][3]) : "i"(g2_younger(C)) : "memory");
    SB();
    constexpr bool DH = C + HD < 14, DW = C + 5 < 14, DC = C + 1 < 14;
    const char* hs = g2_hsrc<(DH ? C + HD : 0)>(x);
    const char* wsrc = g2_wsrc<(DW ? C + 5 : 0)>(x);
    u32 bad = 0u;
#define G2M(i) acc[((i) >> 1) & 1][(i) & 1] = mfma32(wf[C % WS_][((i) >> 2) * 2 + (((i) >> 1) & 1)], hf[C % HS][((i) & 1) * 2 + ((i) >> 2)], acc[((i) >> 1) & 1][(i) & 1]); \
               if constexpr (DC) { hf[N][(i) >> 1][((i) & 1) * 2] ^= x.smask; hf[N][(i) >> 1][((i) & 1) * 2 + 1] ^= x.smask; bad = bad | hf[N][(i) >> 1][((i) & 1) * 2] | hf[N][(i) >> 1][((i) & 1) * 2 + 1]; }
    G2M(0); g2_rd<C + 1, 0>(x, wf); if constexpr (DH) hf[(C + HD) % HS][0] = g2_h1<0>(x, hs); SB();
    G2M(1); g2_rd<C + 1, 1>(x, wf); if constexpr (DH) hf[(C + HD) % HS][1] = g2_h1<1>(x, hs); SB();
    G2M(2); g2_rd<C + 1, 2>(x, wf); if constexpr (DH) hf[(C + HD) % HS][2] = g2_h1<2>(x, hs); SB();
    G2M(3); g2_rd<C + 1, 3>(x, wf); if constexpr (DH) hf[(C + HD) % HS][3] = g2_h1<3>(x, hs); SB();
    G2M(4); if constexpr (DW) g2_w1<C + 5, 0>(x, wsrc); SB();
    G2M(5); if constexpr (DW) g2_w1<C + 5, 1>(x, wsrc); SB();
    G2M(6); if constexpr (DW) g2_w1<C + 5, 2>(x, wsrc); SB();
    G2M(7); if constexpr (DW) g2_w1<C + 5, 3>(x, wsrc); SB();
#undef G2M
    if constexpr (DC) { g2_retry<C + 1>(x, hf, bad); g2_step<C + 1>(x, hf, acc, wf); }
}

struct CF { u32x2 a0[8], a1[8]; u32x4 b[8]; };
template <int S, int R> DI void cv_ld(CF& f, u32 aA, u32 aB) {
    f.a0[R] = alds8<R * 512 + S * 64>(aA); f.a1[R] = alds8<R * 512 + S * 64 + 8>(aA);
    f.b[R] = alds16<(224 - 32 * R + 4 * S) * 16>(aB);
}
DI void cv_wait(CF& f) {
    asm volatile("s_waitcnt lgkmcnt(0)" : "+v"(f.a0[0]), "+v"(f.a0[1]), "+v"(f.a0[2]), "+v"(f.a0[3]), "+v"(f.a0[4]), "+v"(f.a0[5]), "+v"(f.a0[6]), "+v"(f.a0[7]),
                 "+v"(f.a1[0]), "+v"(f.a1[1]), "+v"(f.a1[2]), "+v"(f.a1[3]), "+v"(f.a1[4]), "+v"(f.a1[5]), "+v"(f.a1[6]), "+v"(f.a1[7]),
                 "+v"(f.b[0]), "+v"(f.b[1]), "+v"(f.b[2]), "+v"(f.b[3]), "+v"(f.b[4]), "+v"(f.b[5]), "+v"(f.b[6]), "+v"(f.b[7]));
}
constexpr int cv_r(int m) { int r = 0; while (m >= 8 - r) { m -= 8 - r; ++r; } return r; }
constexpr int cv_c(int m) { int r = 0; while (m >= 8 - r) { m -= 8 - r; ++r; } return m; }
template <int S, int M> DI void cv_mm(CF& f, CF& n, u32 aA, u32 aB, f32x4 (&acc)[8]) {
    constexpr int r = cv_r(M), c = cv_c(M);
    acc[r + c] = mfma16(cat(f.a0[r], f.a1[r]), f.b[c], acc[r + c]);
    if constexpr ((M & 3) == 0 && M / 4 < 8) cv_ld<S + 1, M / 4>(n, aA, aB);
    SB();
    if constexpr (M + 1 < 36) cv_mm<S, M + 1>(f, n, aA, aB, acc);
}
DI void conv_body(u32 aA, u32 aB, f32x4 (&acc)[8]) {
    CF f, n;
    asm volatile("s_waitcnt lgkmcnt(0)" ::: "memory");
    cv_ld<0, 0>(f, aA, aB); cv_ld<0, 1>(f, aA, aB); cv_ld<0, 2>(f, aA, aB); cv_ld<0, 3>(f, aA, aB);
    cv_ld<0, 4>(f, aA, aB); cv_ld<0, 5>(f, aA, aB); cv_ld<0, 6>(f, aA, aB); cv_ld<0, 7>(f, aA, aB);
    _Pragma("clang loop unroll(disable)") for (int it = 0; it < 2; ++it) {
        cv_wait(f); SB();
        cv_mm<0, 0>(f, n, aA, aB, acc);
        cv_wait(n); SB();
        cv_mm<1, 0>(n, f, aA, aB, acc);
        aA += 128; aB += 128;
    }
    cv_wait(f);
    asm volatile("" ::: "memory");
}

struct Geo { int b, xcd, fh2, RB, CT, tb64, db, rot, crot; u32 R0, F0, T0, D0; };
DI Geo make_geo() {
    Geo g; g.b = blockIdx.x - NHELP; g.xcd = g.b & 7; const int bi = g.b >> 3;
    const int rq = g.xcd >> 1, rb = bi / 7, ct = bi - rb * 7;
    g.fh2 = g.xcd & 1; g.RB = 4 * rq + rb; g.CT = 7 * g.fh2 + ct; g.R0 = 128 * g.RB; g.F0 = 128 * g.CT;
    g.tb64 = 2 * g.RB + g.fh2; g.db = ct; g.T0 = 64 * g.tb64; g.D0 = 64 * g.db;
    g.rot = g.xcd >= 7 ? 0 : g.xcd; g.crot = g.CT;
    return g;
}
DI void make_g2(G2Ctx& g2, const Params& p, const Geo& q, int g, int lane, u32 lds0) {
    g2.w2b = (const char*)p.w2 + (size_t)(32 * g) * 896 + q.D0 * 2;
    g2.hb = p.ws + WS_H + (size_t)(q.tb64 * 2 * 112 + 2 * g) * 1024;
    g2.crot = q.crot;
#pragma unroll
    for (int n = 0; n < 4; ++n) g2.voffw[n] = (u32)((lane >> 3) * 896 + 16 * ((lane & 7) ^ (((lane >> 4) & 1) << 2)) + n * (8 * 896 - 1024));
    g2.ldsg = lds0 + g * 4096; g2.vlane = lane * 16u;
}
DI u32 slab_base(u32 lds0, int cw) { return lds0 + L_CONV + (cw >> 1) * CONV_CH + (cw & 1) * 8192; }
DI void final_epilogue(const Params& p, const Geo& q, u32 lds0) {
    int tz = threadIdx.x; PIN(tz);
    const int t = tz >> 3, c = tz & 7;
    const u32 pb = lds0 + ((t >> 5) ? 0u : 3 * L_PIECE) + ((t >> 4) & 1) * L_W + (t & 15) * 256;
    const u32 o0 = pb + 16 * ((2 * c) ^ (t & 7)), o1 = o0 ^ 16;
    f32x4 pa[4], pb_[4];
#pragma unroll
    for (int g = 0; g < 4; ++g) { pa[g] = __builtin_bit_cast(f32x4, lds16(o0 + g * 4096)); pb_[g] = __builtin_bit_cast(f32x4, lds16(o1 + g * 4096)); }
    const float r2 = *(const LAS float*)(lds0 + L_R2 + t * 4);
    const f32x4 b0 = __builtin_bit_cast(f32x4, lds16(lds0 + L_B2F + c * 32)), b1 = __builtin_bit_cast(f32x4, lds16(lds0 + L_B2F + c * 32 + 16));
    const u32 sb = slab_base(lds0, c >> 1) + t * 64 + (c & 1) * 32;
    const f32x4 sv0 = __builtin_bit_cast(f32x4, lds16(sb)), sv1 = __builtin_bit_cast(f32x4, lds16(sb + 16));
    const f32x4 xv0 = __builtin_bit_cast(f32x4, lds16(sb + 4096)), xv1 = __builtin_bit_cast(f32x4, lds16(sb + 4096 + 16));
    SB();
    const f32x4 s0 = (pa[0] + pa[1]) + (pa[2] + pa[3]), s1 = (pb_[0] + pb_[1]) + (pb_[2] + pb_[3]);
    f32x4 z0 = s0 * r2 + b0, z1 = s1 * r2 + b1, e0, e1;
#pragma unroll
    for (int j = 0; j < 4; ++j) { e0[j] = __builtin_amdgcn_exp2f(z0[j]); e1[j] = __builtin_amdgcn_exp2f(z1[j]); }
    e0 += 1.f; e1 += 1.f;
#pragma unroll
    for (int j = 0; j < 4; ++j) { e0[j] = __builtin_amdgcn_rcpf(e0[j]); e1[j] = __builtin_amdgcn_rcpf(e1[j]); }
    z0 = z0 * e0; z1 = z1 * e1;
    z0 = z0 * sv0 + xv0; z1 = z1 * sv1 + xv1;
    const u32x4 res = {pk2(z0[0], z0[1]), pk2(z0[2], z0[3]), pk2(z1[0], z1[1]), pk2(z1[2], z1[3])};
    *(GAS u32x4*)((GAS char*)p.out + (size_t)(q.T0 + t) * 896 + q.D0 * 2 + c * 16) = res;
}

extern __shared__ __attribute__((aligned(16))) char smem[];

struct CL { const char *xb, *wb; u32 vx[4], vw[4], sl; int rot; };
struct CSrc { const char *xs, *wsrc; };
template <int ST_> DI CSrc c_src(const CL& c) { int kc = ST_ + c.rot; if (kc >= 7) kc -= 7; return CSrc{c.xb + kc * 128, c.wb + (size_t)kc * 64 * 3584}; }
template <int ST_, int N> DI void c_dma1(const CL& c, const CSrc& s) {
    if constexpr (N == 0) setm0(c.sl + (ST_ & 3) * L_PIECE);
    if constexpr (N == 4) setm0(c.sl + (ST_ & 3) * L_PIECE + L_W);
    if constexpr (N < 4) dmai<N * 1024>(c.vx[N], s.xs); else dmai<(N - 4) * 1024>(c.vw[N - 4], s.wsrc);
}
template <int ST_> DI void c_dma(const CL& c) {
    const CSrc s = c_src<ST_>(c);
    c_dma1<ST_, 0>(c, s); c_dma1<ST_, 1>(c, s); c_dma1<ST_, 2>(c, s); c_dma1<ST_, 3>(c, s);
    c_dma1<ST_, 4>(c, s); c_dma1<ST_, 5>(c, s); c_dma1<ST_, 6>(c, s); c_dma1<ST_, 7>(c, s);
}
DI void make_cl(CL& c, const Params& p, const Geo& q, int cw, int lane, u32 lds0) {
    const int r8 = lane >> 3, c8 = lane & 7, r4 = lane >> 4, c16 = lane & 15;
#pragma unroll
    for (int n = 0; n < 4; ++n) {
        c.vx[n] = (u32)((32 * cw + r8) * 896 + 16 * (c8 ^ (((n & 1) << 2) | (r8 >> 1))) + n * (8 * 896 - 1024));
        c.vw[n] = (u32)((16 * cw + r4) * 3584 + 16 * (c16 ^ (r4 << 2)) + n * (4 * 3584 - 1024));
    }
    c.xb = (const char*)p.x + (size_t)q.R0 * 896; c.wb = (const char*)p.w1 + q.F0 * 2;
    c.sl = lds0 + cw * 4096; c.rot = q.rot;
}

template <int ST_> DI void l_dma(const CL& c) {
    const CSrc s = c_src<ST_>(c);
    const u32 sl = c.sl + (ST_ & 3) * L_PIECE;
    setm0(sl); dmai<0>(c.vx[0], s.xs); dmai<1024>(c.vx[1], s.xs); dmai<2048>(c.vx[2], s.xs); dmai<3072>(c.vx[3], s.xs);
    setm0(sl + L_W); dmai<0>(c.vw[0], s.wsrc); dmai<1024>(c.vw[1], s.wsrc); dmai<2048>(c.vw[2], s.wsrc); dmai<3072>(c.vw[3], s.wsrc);
    const char* x2 = s.xs + 32 * 896; const char* w2 = s.wsrc + 16 * 3584;
    setm0(sl + 4096); dmai<0>(c.vx[0], x2); dmai<1024>(c.vx[1], x2); dmai<2048>(c.vx[2], x2); dmai<3072>(c.vx[3], x2);
    setm0(sl + L_W + 4096); dmai<0>(c.vw[0], w2); dmai<1024>(c.vw[1], w2); dmai<2048>(c.vw[2], w2); dmai<3072>(c.vw[3], w2);
}

struct Fr { u32x4 x[4][2], w[4][2]; };
struct GA { u32 ax[4], aw[2]; };
template <int K0> DI void g1_mma(const Fr& f, f32x16 (&acc)[2][2]) {
#pragma unroll
    for (int k = K0; k < K0 + 2; ++k) {
        acc[0][0] = mfma32(f.w[k][0], f.x[k][0], acc[0][0]); acc[1][0] = mfma32(f.w[k][1], f.x[k][0], acc[1][0]);
        acc[0][1] = mfma32(f.w[k][0], f.x[k][1], acc[0][1]); acc[1][1] = mfma32(f.w[k][1], f.x[k][1], acc[1][1]);
    }
    SB();
}

template <int ST_, int I> DI void g1_rd(const GA& a, Fr& f) {
    constexpr u32 so = (ST_ & 3) * L_PIECE; constexpr int k = I >> 1, h = I & 1;
    f.w[k][h] = cat(ldtr(a.aw[h] + so + k * 4096), ldtr(a.aw[h] + so + k * 4096 + 1024));
    f.x[k][h] = lds16(a.ax[k] + so + h * 4096);
}
template <int K0, int I> DI void g1_m1(const Fr& f, f32x16 (&acc)[2][2]) {
    constexpr int k = K0 + (I >> 2), mt = I & 1, st = (I >> 1) & 1;
    acc[mt][st] = mfma32(f.w[k][mt], f.x[k][st], acc[mt][st]);
}
template <int ST_, bool CARRY> DI void g1_stage(const GA& a, const Fr& fp, Fr& fn, f32x16 (&acc)[2][2], u32 lds0, int g, u32& pk) {
    if constexpr (CARRY) { g1_m1<2, 0>(fp, acc); SB(); cons_set<ST_ - 1>(lds0, g); SB(); }
    lds_wait_ge(lds0 + F_LAND + 4 * ST_, ST_ == 0 ? 256 : (ST_ == 3 || ST_ == 6) ? 128 : 64, pk);
    if constexpr (CARRY) {
        g1_rd<ST_, 0>(a, fn); SB();
        g1_m1<2, 1>(fp, acc); SB(); g1_m1<2, 2>(fp, acc); g1_rd<ST_, 1>(a, fn); SB();
        g1_m1<2, 3>(fp, acc); SB(); g1_m1<2, 4>(fp, acc); g1_rd<ST_, 2>(a, fn); SB();
        g1_m1<2, 5>(fp, acc); g1_rd<ST_, 3>(a, fn); SB(); g1_m1<2, 6>(fp, acc); SB();
        g1_m1<2, 7>(fp, acc); SB();
    } else { g1_rd<ST_, 0>(a, fn); g1_rd<ST_, 1>(a, fn); g1_rd<ST_, 2>(a, fn); g1_rd<ST_, 3>(a, fn); SB(); }
    g1_m1<0, 0>(fn, acc); g1_rd<ST_, 4>(a, fn); SB(); g1_m1<0, 1>(fn, acc); SB();
    g1_m1<0, 2>(fn, acc); g1_rd<ST_, 5>(a, fn); SB(); g1_m1<0, 3>(fn, acc); g1_rd<ST_, 6>(a, fn); SB();
    g1_m1<0, 4>(fn, acc); g1_rd<ST_, 7>(a, fn); SB(); g1_m1<0, 5>(fn, acc); pk = lds_peek(lds0 + F_LAND + 4 * ST_ + 4); SB();
    g1_m1<0, 6>(fn, acc); SB(); g1_m1<0, 7>(fn, acc); SB();
}

DI void body_g(const Params& p, const int g) {
    const u32 lds0 = (u32)(size_t)(LAS char*)smem;
    const int lane = threadIdx.x & 63;
    const Geo q = make_geo();
    char* ws = p.ws;
    const int hi = lane >> 5, s31 = lane & 31;
    const int r8 = lane >> 3, c8 = lane & 7;
    const u32 raw = lds0 + L_LOC0 + g * KVRAW_SZ;
    {
        const u32 vkv = (u32)(r8 * 896 + c8 * 16);
        bdma16((u32)(((int)q.T0 + 16 * g - 8) * 896 + (int)q.D0 * 2) + vkv, mksrd(p.k, S_ * D_ * 2), raw);
        const char* kb = (const char*)p.k + (size_t)(q.T0 + 16 * g) * 896 + q.D0 * 2;
        const char* vb = (const char*)p.v + (size_t)(q.T0 + 16 * g) * 896 + q.D0 * 2;
        dmai<1024>(vkv, kb - 1024); dmai<2048>(vkv, kb + 8 * 896 - 2048); dmai<3072>(vkv, vb - 3072);
        setm0(raw + 4096);
        dmai<0>(vkv, vb + 8 * 896);
        if (lane < 32) dmai<1024>(lane * 16u, (const char*)p.shk + q.D0 * 8 - 1024);
        CL c; make_cl(c, p, q, g, lane, lds0);
        c_dma<0>(c);
    }
    u32x2 ep = gld8c(0u, ws + WS_EPOCH);
    const int fh = g & 1, sh = g >> 1;
    GA ga;
    {
        const int blk = (lane >> 4) & 1, qq = (lane & 15) >> 2, pp = lane & 3;
        const u32 Z = (u32)(hi ^ ((s31 >> 1) & 7));
#pragma unroll
        for (int k = 0; k < 4; ++k) ga.ax[k] = lds0 + (64 * sh + s31) * 128 + 16 * ((2 * k) ^ Z);
        ga.aw[0] = lds0 + L_W + (8 * hi + qq) * 256 + 16 * ((8 * fh + 2 * blk + (pp >> 1)) ^ (qq << 2)) + 8 * (pp & 1);
        ga.aw[1] = ga.aw[0] ^ 64;
    }
    WAITV(9);
    {
        u32x4 wr[4], kr[5], vr[2];
#pragma unroll
        for (int e = 0; e < 4; ++e) wr[e] = lds16(raw + 5120 + c8 * 64 + e * 16);
#pragma unroll
        for (int j = 0; j < 5; ++j) kr[j] = lds16(raw + (5 + 2 * r8 + j) * 128 + c8 * 16);
        vr[0] = lds16(raw + 3072 + 2 * r8 * 128 + c8 * 16); vr[1] = lds16(raw + 3072 + (2 * r8 + 1) * 128 + c8 * 16);
        SB();
        u32x4 o0, o1;
#pragma unroll
        for (int e = 0; e < 4; ++e) {
            u32 pl[4], ph[4];
#pragma unroll
            for (int j = 0; j < 4; ++j) { pl[j] = __builtin_amdgcn_perm(kr[j][e], kr[j + 1][e], 0x05040100u); ph[j] = __builtin_amdgcn_perm(kr[j][e], kr[j + 1][e], 0x07060302u); }
            f32x2 a = {dot2(pl[0], wr[e].y, dot2(pl[2], wr[e].x, 0.f)), dot2(ph[0], wr[e].w, dot2(ph[2], wr[e].z, 0.f))};
            f32x2 b = {dot2(pl[1], wr[e].y, dot2(pl[3], wr[e].x, 0.f)), dot2(ph[1], wr[e].w, dot2(ph[3], wr[e].z, 0.f))};
            const u32 ar = pk2(a.x, a.y), br = pk2(b.x, b.y);
            a = f32x2{bflo(ar), bfhi(ar)} * f32x2{bflo(vr[0][e]), bfhi(vr[0][e])}; b = f32x2{bflo(br), bfhi(br)} * f32x2{bflo(vr[1][e]), bfhi(vr[1][e])};
            o0[e] = pk2(a.x, a.y); o1[e] = pk2(b.x, b.y);
        }
        sts16(lds0 + L_KVS + (16 * g + 2 * r8) * 144 + c8 * 16, o0);
        sts16(lds0 + L_KVS + (16 * g + 2 * r8 + 1) * 144 + c8 * 16, o1);
        asm volatile("s_waitcnt lgkmcnt(0)" ::: "memory");
        lds_add(lds0 + F_KVS);
    }
    f32x16 acc[2][2] = {};
    Fr fa, fb;
    WAITV(1);
    lds_add(lds0 + F_LAND);
    u32 pk = lds_peek(lds0 + F_LAND);
    g1_stage<0, false>(ga, fb, fa, acc, lds0, g, pk);
    g1_stage<1, true>(ga, fa, fb, acc, lds0, g, pk);
    g1_stage<2, true>(ga, fb, fa, acc, lds0, g, pk);
    asm volatile("s_waitcnt vmcnt(0)" : "+v"(ep) :: "memory");
    const u64 epoch = ((u64)(u32)__builtin_amdgcn_readfirstlane(ep.y) << 32) | (u32)__builtin_amdgcn_readfirstlane(ep.x);
    const u64 tag = epoch + TAG_SALT;
    const u32 tlo = (u32)tag, thi = (u32)(tag >> 32);
    const u32 smask = ((lane ^ (u32)epoch) & 1u) ? 0x80008000u : 0u;
    g1_stage<3, true>(ga, fa, fb, acc, lds0, g, pk);
    const u32 rgo = WS_RG + (q.R0 + 64 * sh + lane) * 8, cgo = WS_CG + (q.F0 + 64 * fh + lane) * 8;
    u32x2 rg = gld8c(rgo, ws), cg = gld8c(cgo, ws);
    u32 b1v = gld2((u32)((64 * fh + lane) * 2), (const char*)p.b1 + q.F0 * 2);
    G2Ctx g2; make_g2(g2, p, q, g, lane, lds0);
    {
        const int blk = (lane >> 4) & 1, qq = (lane & 15) >> 2, pp = lane & 3;
        const u32 t0 = g2.ldsg + (4 * hi + qq) * 128 + 16 * ((2 * blk + (pp >> 1)) ^ ((qq >> 1) << 2)) + 8 * (pp & 1);
#pragma unroll
        for (int i = 0; i < 3; ++i) { g2.ta[i][0] = t0 + i * 65536; g2.ta[i][1] = (t0 ^ 64) + i * 65536; PIN(g2.ta[i][0]); PIN(g2.ta[i][1]); }
#pragma unroll
        for (int i = 0; i < 4; ++i) PIN(g2.voffw[i]);
    }
    g2.smask = smask;
    g1_stage<4, true>(ga, fb, fa, acc, lds0, g, pk);
    g1_stage<5, true>(ga, fa, fb, acc, lds0, g, pk);
    g1_stage<6, true>(ga, fb, fa, acc, lds0, g, pk);
    cons_set<6>(lds0, g);
    asm volatile("s_waitcnt vmcnt(0)" : "+v"(rg), "+v"(cg), "+v"(b1v) :: "memory");
    _Pragma("clang loop unroll(disable)") for (int spin = 0; spin < SPIN_CAP && !__all(rg.y == tlo && cg.y == tlo); ++spin) {
        rg = gld8c(rgo, ws); cg = gld8c(cgo, ws);
        asm volatile("s_waitcnt vmcnt(0)" : "+v"(rg), "+v"(cg) :: "memory");
    }
    if (fh == 0 && sh == q.fh2) *(LAS u32*)(lds0 + L_ROWF + lane * 4) = rg.x;
    g1_mma<2>(fa, acc);
    {
        u32x4 ax[2], bx[2];
        const auto rr = __builtin_amdgcn_permlane32_swap(rg.x, rg.x, false, false);
        const auto cc = __builtin_amdgcn_permlane32_swap(cg.x, cg.x, false, false);
        const auto bb = __builtin_amdgcn_permlane32_swap(b1v, b1v, false, false);
#pragma unroll
        for (int i = 0; i < 2; ++i) {
            const u32 nm = __builtin_amdgcn_perm(rr[i], rr[i], 0x01000100u) ^ 0x80008000u;
            ax[i] = u32x4{cc[i], bb[i], 0u, 0u};
            bx[i] = u32x4{hi ? 0u : nm, hi ? 0u : (rr[i] >> 16), 0u, 0u};
        }
#pragma unroll
        for (int st = 0; st < 2; ++st)
#pragma unroll
            for (int mt = 0; mt < 2; ++mt) acc[mt][st] = mfma32(ax[mt], bx[st], acc[mt][st]);
#pragma unroll
        for (int st = 0; st < 2; ++st) {
            char* hb = ws + WS_H + (size_t)((4 * q.RB + 2 * sh + st) * 112 + q.CT * 8 + fh * 4) * 1024;
#pragma unroll
            for (int mt = 0; mt < 2; ++mt)
#pragma unroll
                for (int t = 0; t < 2; ++t) {
                    u32x4 hv;
#pragma unroll
                    for (int j = 0; j < 4; ++j) {
                        f32x2 a = {acc[mt][st][8 * t + 2 * j], acc[mt][st][8 * t + 2 * j + 1]};
                        f32x2 u = {relu(a.x), relu(a.y)};
                        u = f32x2{mul1(u.x, u.x), mul1(u.y, u.y)};
                        hv[j] = (pk2(u.x, u.y) & 0x7fff7fffu) | smask;
                    }
                    if (mt == 0 && t == 0) gst16c<0>(lane * 16u, hb, hv);
                    if (mt == 0 && t == 1) gst16c<1024>(lane * 16u, hb, hv);
                    if (mt == 1 && t == 0) gst16c<2048>(lane * 16u, hb, hv);
                    if (mt == 1 && t == 1) gst16c<3072>(lane * 16u, hb, hv);
                    SB();
                }
        }
    }
    u32x4 wf[WS_][4];
    cons_wait(lds0, 4);
    asm volatile("s_waitcnt lgkmcnt(0)" ::: "memory");
    {
        u32x4 hf[HS][4];
        if (lds_arrive_last(lds0 + F_HCNT, 4)) { if (lane == 0) gst8c<0>(WS_ALL + q.b * 8, ws, u32x2{tlo, thi}); }
        g2_issue_w<2>(g2); g2_issue_w<3>(g2); g2_issue_w<4>(g2);
        WAITV(0);
        g2_issue_h<0>(g2, hf); g2_issue_h<1>(g2, hf); g2_issue_h<2>(g2, hf); g2_issue_h<3>(g2, hf);
        lds_wait(lds0 + F_W);
        g2_read<0>(g2, wf);
        asm volatile("s_waitcnt vmcnt(%4)" : "+v"(hf[0][0]), "+v"(hf[0][1]), "+v"(hf[0][2]), "+v"(hf[0][3]) : "i"(4 * (HD - 1)) : "memory");
        {
            u32 bad = 0u;
#pragma unroll
            for (int i = 0; i < 16; ++i) { hf[0][i >> 2][i & 3] ^= smask; bad |= hf[0][i >> 2][i & 3]; }
            g2_retry<0>(g2, hf, bad);
        }
        f32x16 a2[2][2] = {};
        g2_step<0>(g2, hf, a2, wf);
        const u32 pw = g2.ldsg + (s31 >> 4) * L_W + (s31 & 15) * 256;
#pragma unroll
        for (int s2 = 0; s2 < 2; ++s2)
#pragma unroll
            for (int dt = 0; dt < 2; ++dt)
#pragma unroll
                for (int gr = 0; gr < 4; ++gr) {
                    const u32 a = pw + (s2 ? 0u : 3 * L_PIECE) + 16 * ((8 * dt + 2 * gr + hi) ^ (s31 & 7));
                    sts16(a, __builtin_bit_cast(u32x4, f32x4{a2[dt][s2][4 * gr], a2[dt][s2][4 * gr + 1], a2[dt][s2][4 * gr + 2], a2[dt][s2][4 * gr + 3]}));
                }
    }
    BARL();
    final_epilogue(p, q, lds0);
}

DI void body_c(const Params& p, const int cw) {
    const u32 lds0 = (u32)(size_t)(LAS char*)smem;
    const int lane = threadIdx.x & 63;
    const Geo q = make_geo();
    char* ws = p.ws;
    const int hi = lane >> 5, s31 = lane & 31;
    const int half = cw & 1, chl = cw >> 1, chan = 2 * q.b + chl;
    u32x2 ep;
    u32x4 xs[4]; u32 dv;
    if (cw < 2) {
        CL c, c2; make_cl(c, p, q, 0, lane, lds0); c2 = c; c2.xb += 64 * 896; c2.wb += 32 * 3584; c2.sl += 8192;
        ep = gld8c(0u, ws + WS_EPOCH);
        if (cw == 0) {
            l_dma<1>(c); l_dma<1>(c2);
            WAITV(0); lds_add(lds0 + F_LAND + 4);
            cons_wait(lds0, 0); l_dma<4>(c); l_dma<4>(c2);
            WAITV(0); lds_add(lds0 + F_LAND + 16);
        } else {
            l_dma<2>(c); l_dma<2>(c2);
            WAITV(0); lds_add(lds0 + F_LAND + 8);
            cons_wait(lds0, 1); l_dma<5>(c); l_dma<5>(c2);
            WAITV(0); lds_add(lds0 + F_LAND + 20);
        }
        asm volatile("s_waitcnt vmcnt(0)" : "+v"(ep) :: "memory");
        const u32 tg32 = (u32)((((u64)ep.y << 32) | ep.x) + TAG_SALT);
        if (cw == 1) {
            G2Ctx g2; make_g2(g2, p, q, 0, lane, lds0);
            cons_wait(lds0, 3);
#define W4(C) do { const char* sb = g2_wsrc<C>(g2); _Pragma("unroll") for (int i = 0; i < 4; ++i) { setm0(g2_loc<C>(g2) + i * 4096); \
                dmai<0>(g2.voffw[0], sb + i * 32 * 896); dmai<1024>(g2.voffw[1], sb + i * 32 * 896); dmai<2048>(g2.voffw[2], sb + i * 32 * 896); dmai<3072>(g2.voffw[3], sb + i * 32 * 896); } } while (0)
            W4(0); W4(1);
#undef W4
            dv = gld2(0u, (const char*)p.ssmd + chan * 2);
            WAITV(1);
            lds_set(lds0 + F_W);
            asm volatile("s_waitcnt vmcnt(0)" : "+v"(dv) :: "memory");
        } else {
            const char* kb2 = ws + WS_KVT + (size_t)(2 * q.b) * TP;
            u32x4 u[16];
            probe<2>((u32)(hi * TP + s31 * 256), kb2, tg32);
            _Pragma("clang loop unroll(disable)") for (int spin = 0; spin < SPIN_CAP; ++spin) {
#pragma unroll
                for (int c = 0; c < 4; ++c) {
                    const char* sb = kb2 + (c >> 1) * TP + (c & 1) * 4096;
                    u[4 * c] = gld16c<0>(lane * 16u, sb); u[4 * c + 1] = gld16c<1024>(lane * 16u, sb); u[4 * c + 2] = gld16c<2048>(lane * 16u, sb); u[4 * c + 3] = gld16c<3072>(lane * 16u, sb);
                }
                asm volatile("s_waitcnt vmcnt(0)" : "+v"(u[0]), "+v"(u[1]), "+v"(u[2]), "+v"(u[3]), "+v"(u[4]), "+v"(u[5]), "+v"(u[6]), "+v"(u[7]), "+v"(u[8]), "+v"(u[9]), "+v"(u[10]),
                             "+v"(u[11]), "+v"(u[12]), "+v"(u[13]), "+v"(u[14]), "+v"(u[15]) :: "memory");
                u32 bad = 0u;
#pragma unroll
                for (int i = 0; i < 16; ++i) bad = chk(u[i].w, tg32, chk(u[i].y, tg32, bad));
                if (!__any(bad != 0u)) break;
                __builtin_amdgcn_s_sleep(4);
            }
            dv = gld2(0u, (const char*)p.ssmd + chan * 2);
            cons_wait(lds0, 5); cons_wait(lds0, 6);
            const u32 uq_ = lds0 + L_CONV + TOEP_CH + lane * 8;
#pragma unroll
            for (int i = 0; i < 16; ++i) sts8(uq_ + (i >> 3) * CONV_CH + (i & 7) * 512, u32x2{u[i].x, u[i].z});
            sts8(uq_ + 4096, u32x2{0u, 0u}); sts8(uq_ + CONV_CH + 4096, u32x2{0u, 0u});
            asm volatile("s_waitcnt lgkmcnt(0)" ::: "memory");
            lds_add(lds0 + F_TOEP); lds_add(lds0 + F_TOEP + 4);
            asm volatile("s_waitcnt vmcnt(0)" : "+v"(dv) :: "memory");
        }
    } else {
        const int kw = cw - 2;
        if (kw == 0 && lane < 8) dma16(lane * 16u, (const char*)p.b2 + q.D0 * 2, lds0 + L_B2);
        if (kw == 1) {
            const char* a = ws + WS_RG;
            if (lane == 1) a = ws + WS_H + (size_t)(4 * q.RB * 112 + q.CT * 8) * 1024;
            if (lane == 2) a = ws + WS_H + (size_t)((4 * q.RB + 3) * 112 + q.CT * 8) * 1024;
            if (lane == 3) a = ws + WS_KVT + (size_t)q.D0 * TP + 8192;
            if (lane == 4) a = ws + WS_KVT + (size_t)(q.D0 + 63) * TP + 8192;
            if (lane == 5) a = ws + WS_KVT + (size_t)(2 * q.b) * TP + 8192;
            if (lane == 6) a = ws + WS_YT + (size_t)q.D0 * TP + 8192;
            if (lane == 7) a = ws + WS_YT + (size_t)(q.D0 + 63) * TP + 8192;
            if (lane == 8) a = ws + WS_YT + (size_t)(2 * q.b) * TP + 8192;
            if (lane == 9) a = (const char*)p.out + (size_t)q.T0 * 896;
            if (lane == 10) a = (const char*)p.ssmk + (size_t)(2 * q.b) * 4096;
            if (lane == 11) a = (const char*)p.ssmd;
            if (lane == 12) a = (const char*)p.b1;
            if (lane >= 16 && lane < 16 + 25) a = (const char*)p.w2 + (size_t)(lane - 16) * 65536 + q.D0 * 2;
            touch(a, lds0 + L_B2F);
        }
        ep = gld8c(0u, ws + WS_EPOCH);
        CL c; make_cl(c, p, q, 2 * kw, lane, lds0);
        l_dma<3>(c);
        lds_wait_ge(lds0 + F_KVS, 256, lds_peek(lds0 + F_KVS));
        asm volatile("s_waitcnt vmcnt(16)" : "+v"(ep) :: "memory");
        const u32 tg32 = (u32)((((u64)ep.y << 32) | ep.x) + TAG_SALT);
        {
            const int grp = lane >> 4, qq = (lane & 15) >> 2, pp = lane & 3;
            const u32 a = lds0 + L_KVS + (16 * grp + 4 * pp + 3 - qq) * 144 + kw * 64;
            const u32 vo = (u32)((q.D0 + 32 * kw + (lane & 3)) * TP + (2044 - (int)q.T0 - 4 * (lane >> 2)) * 4);
            u32x2 tr[8];
#pragma unroll
            for (int i = 0; i < 8; ++i) tr[i] = ldtr(a + 8 * i);
            SB();
#pragma unroll
            for (int i = 0; i < 8; ++i) gst16c<0>(vo, ws + WS_KVT + i * 4 * TP, u32x4{tr[i].x, tg32, tr[i].y, tg32});
        }
        WAITV(8);
        lds_add(lds0 + F_LAND + 12);
        cons_wait(lds0, 2);
        l_dma<6>(c);
        u32x4 sk[5];
        const u32x4 sksrd = mksrd((const char*)p.ssmk + (size_t)(2 * q.b + kw) * 4096, 4096);
#pragma unroll
        for (int r = 0; r < 5; ++r) sk[r] = bld16((u32)((lane - 1 + 63 * r) * 16), sksrd);
        { const char* xsl = (const char*)p.x + (size_t)q.T0 * 896 + (q.D0 + 32 * kw) * 2;
          xs[0] = gld16o<0>((u32)(lane * 896), xsl); xs[1] = gld16o<16>((u32)(lane * 896), xsl); xs[2] = gld16o<32>((u32)(lane * 896), xsl); xs[3] = gld16o<48>((u32)(lane * 896), xsl); }
        dv = gld2(0u, (const char*)p.ssmd + chan * 2);
        WAITV(10);
        lds_add(lds0 + F_LAND + 24);
        asm volatile("s_waitcnt vmcnt(0)" : "+v"(sk[0]), "+v"(sk[1]), "+v"(sk[2]), "+v"(sk[3]), "+v"(sk[4]), "+v"(xs[0]), "+v"(xs[1]), "+v"(xs[2]), "+v"(xs[3]), "+v"(dv) :: "memory");
        u32x4 tw[5][4];
#pragma unroll
        for (int r = 0; r < 5; ++r) {
            const u32x4 B = sk[r];
            u32x4 A;
#pragma unroll
            for (int j = 0; j < 4; ++j) A[j] = __builtin_amdgcn_update_dpp(0u, B[j], 0x138  , 0xf, 0xf, false);
            const u32 e0 = __builtin_amdgcn_alignbit(A.y, A.x, 16), e1 = __builtin_amdgcn_alignbit(A.z, A.y, 16), e2 = __builtin_amdgcn_alignbit(A.w, A.z, 16),
                      e3 = __builtin_amdgcn_alignbit(B.x, A.w, 16), e4 = __builtin_amdgcn_alignbit(B.y, B.x, 16);
            tw[r][0] = u32x4{e0, e1, e2, e3}; tw[r][1] = u32x4{A.y, A.z, A.w, B.x}; tw[r][2] = u32x4{e1, e2, e3, e4}; tw[r][3] = u32x4{A.z, A.w, B.x, B.y};
            PIN(tw[r][0]); PIN(tw[r][1]); PIN(tw[r][2]); PIN(tw[r][3]);
        }
        cons_wait(lds0, 5); cons_wait(lds0, 6);
        const u32 tq = lds0 + L_CONV + kw * CONV_CH + lane * 16;
#pragma unroll
        for (int r = 0; r < 5; ++r) {
            if ((lane > 0 || r == 0) && (r < 4 || lane < 6)) {
#pragma unroll
                for (int c = 0; c < 4; ++c) sts16(tq + r * 1008 + c * TOEP_CP, tw[r][c]);
            }
        }
        asm volatile("s_waitcnt lgkmcnt(0)" ::: "memory");
        lds_add(lds0 + F_TOEP + kw * 4);
    }
    const u64 epoch = ((u64)(u32)__builtin_amdgcn_readfirstlane(ep.y) << 32) | (u32)__builtin_amdgcn_readfirstlane(ep.x);
    const u64 tag = epoch + TAG_SALT;
    const u32 tlo = (u32)tag, thi = (u32)(tag >> 32);
    const u32 tbase = lds0 + L_CONV + chl * CONV_CH;
    const u32 ub = tbase + TOEP_CH;
    lds_wait_ge(lds0 + F_TOEP + chl * 4, 128, lds_peek(lds0 + F_TOEP + chl * 4));
    const int bb = lane & 15, gg = lane >> 4;
    const u32 aA = tbase + (bb & 3) * TOEP_CP + ((bb >> 2) + 2 * gg) * 8 + half * 256;
    const u32 aB = ub + (30 - 2 * bb + gg) * 16 + half * 256;
    f32x4 ac8[8] = {};
    __builtin_amdgcn_s_setprio(2);
    conv_body(aA, aB, ac8);
    f32x4 acc[4];
    {
        const u32 xo = ub + U_SZ + lane * 16;
#pragma unroll
        for (int i = 0; i < 4; ++i) { const f32x4 give = half ? ac8[i] : ac8[4 + i]; acc[i] = half ? ac8[4 + i] : ac8[i]; sts16(xo + half * 4096 + i * 1024, __builtin_bit_cast(u32x4, give)); }
        asm volatile("s_waitcnt lgkmcnt(0)" ::: "memory");
        lds_set(lds0 + F_XCH + cw * 4);
        lds_wait(lds0 + F_XCH + (cw ^ 1) * 4);
        f32x4 got[4];
#pragma unroll
        for (int i = 0; i < 4; ++i) got[i] = __builtin_bit_cast(f32x4, lds16(xo + (half ^ 1) * 4096 + i * 1024));
        SB();
#pragma unroll
        for (int i = 0; i < 4; ++i) acc[i] = f32x4{add1(acc[i][0], got[i][0]), add1(acc[i][1], got[i][1]), add1(acc[i][2], got[i][2]), add1(acc[i][3], got[i][3])};
    }
    {
        const float dd = bflo(dv);
        char* yb = ws + WS_YT + (size_t)chan * TP;
        u32x2 uq[4];
#pragma unroll
        for (int i = 0; i < 4; ++i) uq[i] = lds8(ub + (2044 - (256 * (4 * half + i) + 16 * bb + 4 * gg)) * 2);
        SB();
#pragma unroll
        for (int i = 0; i < 4; ++i) {
            const int qt = 4 * half + i;
            const int t = 256 * qt + 16 * bb + 4 * gg;
            const u32x2 uu = uq[i];
            const u32 c01 = pk2(acc[i][0], acc[i][1]), c23 = pk2(acc[i][2], acc[i][3]);
            const u32 d01 = pk2(mul1(bfhi(uu.y), dd), mul1(bflo(uu.y), dd)), d23 = pk2(mul1(bfhi(uu.x), dd), mul1(bflo(uu.x), dd));
            float y0 = add1(bflo(d01), bflo(c01)), y1 = add1(bfhi(d01), bfhi(c01));
            float y2 = add1(bflo(d23), bflo(c23)), y3 = add1(bfhi(d23), bfhi(c23));
            gst16c<0>((u32)(t * 4), yb, u32x4{pk2(y0, y1), tlo, pk2(y2, y3), tlo});
        }
        lds_arrive_last(lds0 + F_YCNT, 4);
    }
    if (cw >= 2) {
        const int kw = cw - 2;
        lds_wait_ge(lds0 + F_YCNT, 4, lds_peek(lds0 + F_YCNT)); lds_wait_ge(lds0 + F_HCNT, 4, lds_peek(lds0 + F_HCNT));
        __builtin_amdgcn_s_setprio(0);
        const u32 slab = slab_base(lds0, 2 * kw) + lane * 64;
        float xn[32];
        {
            const u32 rf = *(const LAS u32*)(lds0 + L_ROWF + lane * 4);
            const float nm = -bflo(rf); float ri = __builtin_amdgcn_rcpf(bfhi(rf));
            asm volatile("s_nop 1" : "+v"(ri));
#pragma unroll
            for (int j = 0; j < 8; ++j) {
                const u32 lo = xs[j >> 1][(j & 1) * 2], hi_ = xs[j >> 1][(j & 1) * 2 + 1];
                const f32x4 v = {bflo(lo), bfhi(lo), bflo(hi_), bfhi(hi_)};
                sts16(slab + (j >> 2) * 8192 + 4096 + (j & 3) * 16, __builtin_bit_cast(u32x4, v));
#pragma unroll
                for (int e = 0; e < 4; e += 2) {
                    const u32 dx = pk2(add1(v[e], nm), add1(v[e + 1], nm)), xr = pk2(mul1(bflo(dx), ri), mul1(bfhi(dx), ri));
                    xn[4 * j + e] = bflo(xr); xn[4 * j + e + 1] = bfhi(xr);
                }
            }
            if (kw == 0) *(LAS float*)(lds0 + L_R2 + lane * 4) = -1.4426950408889634f * ri * ri;
            else *(LAS float*)(lds0 + L_B2F + lane * 4) = -1.4426950408889634f * bflo(*(const LAS u16*)(lds0 + L_B2 + lane * 2));
        }
        const char* yb = ws + WS_YT + (size_t)(q.D0 + 32 * kw) * TP + q.T0 * 4;
        const u32 vo = (u32)(gg * TP + bb * 16);
        u32x4 y[8];
        __builtin_amdgcn_s_setprio(2);
        _Pragma("clang loop unroll(disable)") for (int spin = 0; spin < SPIN_CAP; ++spin) {
#pragma unroll
            for (int i = 0; i < 8; ++i) y[i] = gld16c<0>(vo, yb + i * 4 * TP);
            asm volatile("s_waitcnt vmcnt(0)" : "+v"(y[0]), "+v"(y[1]), "+v"(y[2]), "+v"(y[3]), "+v"(y[4]), "+v"(y[5]), "+v"(y[6]), "+v"(y[7]) :: "memory");
            u32 bad = 0u;
#pragma unroll
            for (int i = 0; i < 8; ++i) bad = chk(y[i].w, tlo, chk(y[i].y, tlo, bad));
            if (!__any(bad != 0u)) break;
            __builtin_amdgcn_s_sleep(8);
        }
        const u32 ys = lds0 + L_CONV + kw * CONV_CH + TOEP_CH + U_SZ;
#pragma unroll
        for (int i = 0; i < 8; ++i) sts8(ys + (4 * i + gg) * 144 + bb * 8, u32x2{y[i].x, y[i].z});
        const int qq = (lane & 15) >> 2, pp = lane & 3;
        const u32 a = ys + qq * 144 + gg * 32 + pp * 8;
        u32x2 t[8];
        const float nln2 = -0.6931471805599453f;
#pragma unroll
        for (int j = 0; j < 8; ++j) t[j] = ldtr(a + j * 4 * 144);
        SB();
#pragma unroll
        for (int j = 0; j < 8; ++j)
        {
            const u32 s01 = pk2(mul1(bflo(t[j].x), xn[4 * j]), mul1(bfhi(t[j].x), xn[4 * j + 1])), s23 = pk2(mul1(bflo(t[j].y), xn[4 * j + 2]), mul1(bfhi(t[j].y), xn[4 * j + 3]));
            sts16(slab + (j >> 2) * 8192 + (j & 3) * 16, __builtin_bit_cast(u32x4, f32x4{mul1(bflo(s01), nln2), mul1(bfhi(s01), nln2), mul1(bflo(s23), nln2), mul1(bfhi(s23), nln2)}));
        }
        if (q.b == 0 && kw == 1) {
            _Pragma("clang loop unroll(disable)") for (int spin = 0; spin < SPIN_CAP; ++spin) {
                u32x4 f0 = gld16c<0>(lane * 16u, ws + WS_ALL), f1 = gld16c<1024>((lane < 48 ? lane : 0) * 16u, ws + WS_ALL);
                asm volatile("s_waitcnt vmcnt(0)" : "+v"(f0), "+v"(f1) :: "memory");
                bool ok = f0.x == tlo && f0.y == thi && f0.z == tlo && f0.w == thi && f1.x == tlo && f1.y == thi && f1.z == tlo && f1.w == thi;
                if (__all(ok)) break;
                __builtin_amdgcn_s_sleep(2);
            }
            if (lane == 0) { const u64 e1 = epoch + 1; gst8c<0>(0u, ws + WS_EPOCH, u32x2{(u32)e1, (u32)(e1 >> 32)}); }
        }
    }
    __builtin_amdgcn_s_setprio(0);
    BARL();
    final_epilogue(p, q, lds0);
}

DI float dpp_xor_sum8(float v) {
    v += __uint_as_float(__builtin_amdgcn_mov_dpp(__float_as_uint(v), 0xB1, 0xf, 0xf, true));
    v += __uint_as_float(__builtin_amdgcn_mov_dpp(__float_as_uint(v), 0x4E, 0xf, 0xf, true));
    v += __uint_as_float(__builtin_amdgcn_mov_dpp(__float_as_uint(v), 0x141, 0xf, 0xf, true));
    return v;
}
DI void body_help(const Params& p, const int wave) {
    const u32 lds0 = (u32)(size_t)(LAS char*)smem;
    const int lane = threadIdx.x & 63, r8 = lane >> 3, c8 = lane & 7;
    const int xcd = blockIdx.x & 7, j = blockIdx.x >> 3;
    char* ws = p.ws;
    u32x2 ep = gld8c(0u, ws + WS_EPOCH);
    u32x4 d[14];
    const int ctile = (xcd >> 1) * 4 + j;
    const bool cols = wave >= 4;
    if (!cols) {
        const char* xb = (const char*)p.x + (size_t)(256 * xcd + 64 * j + 16 * wave) * 896;
#pragma unroll
        for (int i = 0; i < 14; ++i) d[i] = gld16((u32)((r8 + 8 * (i / 7)) * 896 + (i % 7) * 128 + c8 * 16), xb);
    } else {
        const char* wb = (const char*)p.w1 + (size_t)(112 * (wave - 4)) * 3584 + ((xcd & 1) * 14 + (ctile < 14 ? ctile : 0)) * 128;
#pragma unroll
        for (int i = 0; i < 14; ++i) d[i] = gld16((u32)((r8 + 8 * i) * 3584 + c8 * 16), wb);
    }
    asm volatile("s_waitcnt vmcnt(0)" : "+v"(ep), "+v"(d[0]), "+v"(d[1]), "+v"(d[2]), "+v"(d[3]), "+v"(d[4]), "+v"(d[5]), "+v"(d[6]), "+v"(d[7]), "+v"(d[8]), "+v"(d[9]),
                 "+v"(d[10]), "+v"(d[11]), "+v"(d[12]), "+v"(d[13]) :: "memory");
    const u32 tlo = (u32)((((u64)ep.y << 32) | ep.x) + TAG_SALT);
    if (!cols) {
#pragma unroll
        for (int h = 0; h < 2; ++h) {
            float s0 = 0.f, s1 = 0.f, q0 = 0.f, q1 = 0.f;
#pragma unroll
            for (int i = 0; i < 7; ++i)
#pragma unroll
                for (int e = 0; e < 4; e += 2) {
                    s0 = dot2(d[7 * h + i][e], 0x3f803f80u, s0); q0 = dot2(d[7 * h + i][e], d[7 * h + i][e], q0);
                    s1 = dot2(d[7 * h + i][e + 1], 0x3f803f80u, s1); q1 = dot2(d[7 * h + i][e + 1], d[7 * h + i][e + 1], q1);
                }
            const float mean = dpp_xor_sum8(s0 + s1) * (1.f / 448.f);
            const float var = fmaxf(dpp_xor_sum8(q0 + q1) * (1.f / 448.f) - mean * mean, 0.f);
            const float sd = __builtin_amdgcn_sqrtf(rbf(rbf(var) + 1e-5f));
            if (c8 == 0) gst8c<0>((u32)(WS_RG + (256 * xcd + 64 * j + 16 * wave + 8 * h + r8) * 8), ws, u32x2{pk2(mean, sd), tlo});
        }
    } else {
        float a[8] = {};
        u32 klo = 0x00003f80u, khi = 0x3f800000u; PIN(klo); PIN(khi);
#pragma unroll
        for (int i = 0; i < 14; ++i)
#pragma unroll
            for (int e = 0; e < 4; ++e) { a[2 * e] = dot2(d[i][e], klo, a[2 * e]); a[2 * e + 1] = dot2(d[i][e], khi, a[2 * e + 1]); }
        const u32 o = lds0 + ((wave - 4) * 8 + r8) * 272 + c8 * 32;
        sts16(o, __builtin_bit_cast(u32x4, f32x4{a[0], a[1], a[2], a[3]})); sts16(o + 16, __builtin_bit_cast(u32x4, f32x4{a[4], a[5], a[6], a[7]}));
    }
    BARL();
    if (wave == 4 && ctile < 14) {
        float t0 = 0.f, t1 = 0.f;
#pragma unroll
        for (int i = 0; i < 32; i += 2) { t0 += *(const LAS float*)(lds0 + i * 272 + lane * 4); t1 += *(const LAS float*)(lds0 + (i + 1) * 272 + lane * 4); }
        gst8c<0>((u32)(WS_CG + (((xcd & 1) * 14 + ctile) * 64 + lane) * 8), ws, u32x2{pk2(t0 + t1, (t0 + t1) - rbf(t0 + t1)), tlo});
    }
    BARL();
    {
        const int crot = (2 * xcd) % 14;
        setm0(lds0 + 16384 + wave * 4096);
        for (int c = 0; c < 14; ++c) {
            int cc = c + crot; if (cc >= 14) cc -= 14;
            const char* sb = (const char*)p.w2 + (size_t)cc * 114688 + (j * 8 + wave) * 3584;
            if (lane < 32) dmai<3072>(lane * 16u, sb);
            dmai<0>(lane * 16u, sb); dmai<1024>(lane * 16u, sb); dmai<2048>(lane * 16u, sb);
            WAITV(8);
        }
        WAITV(0);
    }
}

__global__ __launch_bounds__(512, 2) void fused_kernel(Params p) {
    {
        u64 pc; asm volatile("s_getpc_b64 %0" : "=s"(pc));
        if (threadIdx.x < CODE_LINES) touch((const char*)(pc & ~127ull) + threadIdx.x * 128, (u32)(size_t)(LAS char*)smem + L_B2F);
    }
    const int wave = __builtin_amdgcn_readfirstlane(threadIdx.x >> 6);
    if (blockIdx.x < NHELP) { body_help(p, wave); return; }
    if (threadIdx.x < 32) *(LAS u32*)((u32)(size_t)(LAS char*)smem + L_FLAG + threadIdx.x * 4) = 0u;
    else if (threadIdx.x < 64) *(LAS u32*)((u32)(size_t)(LAS char*)smem + F_CONS + ((threadIdx.x - 32) >> 2) * 144 + (threadIdx.x & 3) * 4) = 0u;
    BARL();
    if (wave < 4) body_g(p, wave); else body_c(p, wave - 4);
}

extern "C" void kernel_launch(void* const* d_in, const int* in_sizes, int n_in,
                              void* d_out, int out_size, void* d_ws, size_t ws_size,
                              hipStream_t stream) {
    Params p;
    p.x = (const u16*)d_in[0]; p.k = (const u16*)d_in[1]; p.v = (const u16*)d_in[2]; p.shk = (const u16*)d_in[3];
    p.ssmk = (const u16*)d_in[4]; p.ssmd = (const u16*)d_in[5]; p.w1 = (const u16*)d_in[7]; p.b1 = (const u16*)d_in[8];
    p.w2 = (const u16*)d_in[9]; p.b2 = (const u16*)d_in[10];
    p.out = (u16*)d_out; p.ws = (char*)d_ws;
    (void)hipFuncSetAttribute((const void*)fused_kernel, hipFuncAttributeMaxDynamicSharedMemorySize, LDS_BYTES);
    fused_kernel<<<NWG + NHELP, 512, LDS_BYTES, stream>>>(p);
}
```

```cpp
#include <hip/hip_runtime.h>
#include <stdint.h>
#pragma clang diagnostic ignored "-Wint-to-pointer-cast"

typedef unsigned short u16;
typedef unsigned int u32;
typedef unsigned long long u64;
typedef u32 u32x4 __attribute__((ext_vector_type(4)));
typedef u32 u32x2 __attribute__((ext_vector_type(2)));
typedef float f32x4 __attribute__((ext_vector_type(4)));
typedef float f32x2 __attribute__((ext_vector_type(2)));
typedef float f32x16 __attribute__((ext_vector_type(16)));
typedef __bf16 bf16x8 __attribute__((ext_vector_type(8)));
typedef __bf16 bf16x2 __attribute__((ext_vector_type(2)));
typedef short s16x4 __attribute__((ext_vector_type(4)));
#define LAS __attribute__((address_space(3)))
#define GAS __attribute__((address_space(1)))
#define DI __device__ __forceinline__

constexpr int S_ = 2048, D_ = 448;
constexpr int NHELP = 32, NWG = 224;
constexpr int CODE_LINES = 264;
constexpr u32 WS_EPOCH = 0, WS_ALL = 2048, WS_RG = 4096, WS_CG = WS_RG + 16384;
constexpr u32 TP = 8192 + 128;
constexpr u32 WS_H = 1u << 20, WS_KVT = 9u << 20, WS_YT = 13u << 20;
constexpr u32 L_PIECE = 16384, L_W = 65536, L_LOC0 = 131072, LDS_BYTES = 163840;
constexpr u32 KVRAW_SZ = 5632, L_KVS = L_LOC0 + 4 * KVRAW_SZ, L_B2 = L_KVS + 9216, L_FLAG = L_B2 + 128, L_ROWF = L_FLAG + 128, L_B2F = L_ROWF + 256, L_R2 = L_B2F + 256;
static_assert(L_R2 + 256 <= LDS_BYTES, "lds");
constexpr u32 F_TOEP = L_FLAG, F_HCNT = L_FLAG + 16, F_YCNT = L_FLAG + 28, F_W = L_FLAG + 48, F_LAND = L_FLAG + 64, F_XCH = L_FLAG + 96, F_KVS = L_FLAG + 124;
constexpr u32 F_CONS = L_KVS + 128;
constexpr u32 L_CONV = 16384, CONV_CH = 65536, TOEP_CP = 4160, TOEP_CH = 4 * TOEP_CP, U_SZ = 4608;
constexpr u64 TAG_SALT = 0x9E3779B97F4A7C15ull;
constexpr int SPIN_CAP = 1 << 15;

DI void dma16(u32 voff, const void* sbase, u32 lds) {
    asm volatile("s_mov_b32 m0, %2\n\ts_nop 0\n\tglobal_load_lds_dwordx4 %0, %1" :: "v"(voff), "s"(sbase), "s"(lds) : "memory");
}
DI void setm0(u32 lds) { asm volatile("s_mov_b32 m0, %0\n\ts_nop 0" :: "s"(lds) : "memory"); }
template <int OFF> DI void dmai(u32 voff, const void* sbase) { asm volatile("global_load_lds_dwordx4 %0, %1 offset:%2" :: "v"(voff), "s"(sbase), "i"(OFF) : "memory"); }
DI void bdma16(u32 voff, u32x4 srd, u32 lds) {
    asm volatile("s_mov_b32 m0, %2\n\ts_nop 0\n\tbuffer_load_dwordx4 %0, %1, 0 offen lds" :: "v"(voff), "s"(srd), "s"(lds) : "memory");
}
#define SB() __builtin_amdgcn_sched_barrier(0)
template <int OFF> DI u32x2 alds8(u32 a) { u32x2 r; asm volatile("ds_read_b64 %0, %1 offset:%2" : "=v"(r) : "v"(a), "i"(OFF)); return r; }
template <int OFF> DI u32x4 alds16(u32 a) { u32x4 r; asm volatile("ds_read_b128 %0, %1 offset:%2" : "=v"(r) : "v"(a), "i"(OFF)); return r; }
DI u32x4 gld16(u32 voff, const void* sbase) { u32x4 r; asm volatile("global_load_dwordx4 %0, %1, %2" : "=v"(r) : "v"(voff), "s"(sbase) : "memory"); return r; }
template <int OFF> DI u32x4 gld16c(u32 voff, const void* sbase) { u32x4 r; asm volatile("global_load_dwordx4 %0, %1, %2 offset:%3 sc1" : "=v"(r) : "v"(voff), "s"(sbase), "i"(OFF) : "memory"); return r; }
DI u32x2 gld8c(u32 voff, const void* sbase) { u32x2 r; asm volatile("global_load_dwordx2 %0, %1, %2 sc1" : "=v"(r) : "v"(voff), "s"(sbase) : "memory"); return r; }
DI void touch(const void* addr, u32 lds) { asm volatile("s_mov_b32 m0, %1\n\ts_nop 0\n\tglobal_load_lds_dword %0, off" :: "v"(addr), "s"(lds) : "memory"); }
DI u32 gld2(u32 voff, const void* sbase) { u32 r; asm volatile("global_load_ushort %0, %1, %2" : "=v"(r) : "v"(voff), "s"(sbase) : "memory"); return r; }
DI u32x4 bld16(u32 voff, u32x4 srd) { u32x4 r; asm volatile("buffer_load_dwordx4 %0, %1, %2, 0 offen" : "=v"(r) : "v"(voff), "s"(srd) : "memory"); return r; }
template <int OFF> DI u32x4 gld16o(u32 voff, const void* sbase) { u32x4 r; asm volatile("global_load_dwordx4 %0, %1, %2 offset:%3" : "=v"(r) : "v"(voff), "s"(sbase), "i"(OFF) : "memory"); return r; }
template <int OFF> DI void gst16c(u32 voff, void* sbase, u32x4 v) { asm volatile("global_store_dwordx4 %0, %1, %2 offset:%3 sc1\n\ts_nop 1" :: "v"(voff), "v"(v), "s"(sbase), "i"(OFF) : "memory"); }
template <int OFF> DI void gst8c(u32 voff, void* sbase, u32x2 v) { asm volatile("global_store_dwordx2 %0, %1, %2 offset:%3 sc1" :: "v"(voff), "v"(v), "s"(sbase), "i"(OFF) : "memory"); }
#define WAITV(n) asm volatile("s_waitcnt vmcnt(%0)" :: "i"(n) : "memory")
#define BARL() asm volatile("s_waitcnt lgkmcnt(0)\n\ts_barrier" ::: "memory")
#define PIN(x) asm volatile("" : "+v"(x))

DI u32x4 mksrd(const void* p, u32 bytes) { u64 a = (u64)p; return u32x4{(u32)a, (u32)(a >> 32) & 0xffffu, bytes, 0x00020000u}; }
DI float bflo(u32 x) { return __uint_as_float(x << 16); }
DI float bfhi(u32 x) { return __uint_as_float(x & 0xffff0000u); }
DI u32 pk2(float a, float b) { bf16x2 v = {(__bf16)a, (__bf16)b}; return __builtin_bit_cast(u32, v); }
DI float rbf(float a) { return bflo(pk2(a, a)); }
DI float relu(float a) { int i = __float_as_int(a); return __int_as_float(i > 0 ? i : 0); }
DI float mul1(float a, float b) { float r; asm("v_mul_f32 %0, %1, %2" : "=v"(r) : "v"(a), "v"(b)); return r; }
DI float add1(float a, float b) { float r; asm("v_add_f32 %0, %1, %2" : "=v"(r) : "v"(a), "v"(b)); return r; }
DI float dot2(u32 a, u32 b, float c) { return __builtin_amdgcn_fdot2_f32_bf16(__builtin_bit_cast(bf16x2, a), __builtin_bit_cast(bf16x2, b), c, false); }
DI u32x4 lds16(u32 a) { return *(const LAS u32x4*)a; }
DI u32x2 lds8(u32 a) { return *(const LAS u32x2*)a; }
DI void sts16(u32 a, u32x4 v) { *(LAS u32x4*)a = v; }
DI void sts8(u32 a, u32x2 v) { *(LAS u32x2*)a = v; }
DI u32x2 ldtr(u32 a) { return __builtin_bit_cast(u32x2, __builtin_amdgcn_ds_read_tr16_b64_v4i16((LAS s16x4*)a)); }
DI u32x4 cat(u32x2 a, u32x2 b) { return u32x4{a.x, a.y, b.x, b.y}; }
DI f32x16 mfma32(u32x4 a, u32x4 b, f32x16 c) { return __builtin_amdgcn_mfma_f32_32x32x16_bf16(__builtin_bit_cast(bf16x8, a), __builtin_bit_cast(bf16x8, b), c, 0, 0, 0); }
DI f32x4 mfma16(u32x4 a, u32x4 b, f32x4 c) { return __builtin_amdgcn_mfma_f32_16x16x32_bf16(__builtin_bit_cast(bf16x8, a), __builtin_bit_cast(bf16x8, b), c, 0, 0, 0); }

struct Params {
    const u16 *x, *k, *v, *shk, *ssmk, *ssmd, *w1, *b1, *w2, *b2;
    u16* out; char* ws;
};

template <int SL> DI void probe(u32 voff, const void* sbase, u32 tag) {
    _Pragma("clang loop unroll(disable)") for (int spin = 0; spin < SPIN_CAP; ++spin) {
        u32x2 f = gld8c(voff, sbase);
        asm volatile("s_waitcnt vmcnt(0)" : "+v"(f) :: "memory");
        if (__all(f.y == tag)) break;
        __builtin_amdgcn_s_sleep(SL);
    }
}
DI bool lds_arrive_last(u32 addr, u32 n) {
    u32 old = 0;
    if ((threadIdx.x & 63) == 0) old = __hip_atomic_fetch_add((LAS u32*)addr, 1u, __ATOMIC_RELAXED, __HIP_MEMORY_SCOPE_WORKGROUP);
    return (u32)__builtin_amdgcn_readfirstlane(old) == n - 1;
}
DI void lds_wait(u32 addr) {
    _Pragma("clang loop unroll(disable)") for (int spin = 0; spin < (1 << 22); ++spin) {
        if (__builtin_amdgcn_readfirstlane(*(volatile LAS u32*)addr)) break;
        __builtin_amdgcn_s_sleep(1);
    }
    asm volatile("" ::: "memory");
}
DI void lds_add(u32 addr) { asm volatile("" ::: "memory"); __hip_atomic_fetch_add((LAS u32*)addr, 1u, __ATOMIC_RELAXED, __HIP_MEMORY_SCOPE_WORKGROUP); asm volatile("" ::: "memory"); }
DI u32 lds_peek(u32 addr) { return *(volatile LAS u32*)addr; }
DI void lds_wait_ge(u32 addr, u32 n, u32 first) {
    u32 v = first;
    _Pragma("clang loop unroll(disable)") for (int spin = 0; spin < (1 << 22) && (u32)__builtin_amdgcn_readfirstlane(v) < n; ++spin) { __builtin_amdgcn_s_sleep(1); v = lds_peek(addr); }
    asm volatile("" ::: "memory");
}
template <int t> DI void cons_set(u32 lds0, int g) { asm volatile("s_waitcnt lgkmcnt(0)\n\tds_write_b32 %0, %1 offset:%2" :: "v"(lds0 + 131072 + g * 4), "v"(1u), "i"(F_CONS + t * 144 - 131072) : "memory"); }
DI void cons_wait(u32 lds0, int t) {
    _Pragma("clang loop unroll(disable)") for (int spin = 0; spin < (1 << 22); ++spin) {
        const u32x4 v = *(volatile LAS u32x4*)(lds0 + F_CONS + t * 144);
        if (__builtin_amdgcn_readfirstlane(v.x & v.y & v.z & v.w)) break;
        __builtin_amdgcn_s_sleep(1);
    }
    asm volatile("" ::: "memory");
}
DI void lds_set(u32 addr) { asm volatile("" ::: "memory"); if ((threadIdx.x & 63) == 0) *(volatile LAS u32*)addr = 1u; }
constexpr u32 loc_of(int c) { return (c % 5) == 0 ? L_LOC0 : (c % 5) == 1 ? 3 * L_PIECE : (c % 5) == 2 ? L_W + 3 * L_PIECE : (c % 5) == 3 ? 0u : L_W; }

constexpr int HD = 4, HS = HD + 1;
constexpr int WS_ = 2;
constexpr int g2_younger(int c) {
    const int n = c + 1;
    if (n >= 14) return 63;
    bool sh = false, sw = n < 5; int ah = 0, aw = sw ? 1000 : 0;
    for (int i = 0; i < HD; ++i) { if (sh) ah += 4; if (i == n) sh = true; }
    for (int i = 0; i < c; ++i) {
        if (i + HD < 14) { if (sh) ah += 4; if (sw) aw += 4; if (i + HD == n) sh = true; }
        if (i + 5 < 14) { if (sh) ah += 4; if (sw) aw += 4; if (i + 5 == n) sw = true; }
    }
    return ah < aw ? ah : aw;
}

struct G2Ctx { const char* w2b; const char* hb; int crot; u32 voffw[4], ldsg, vlane, ta[3][2], smask; };
template <int C> DI int g2_chunk(const G2Ctx& x) {
    int c = C + x.crot; if (c >= 14) c -= 14;
    return c;
}
template <int C> DI const char* g2_wsrc(const G2Ctx& x) { return x.w2b + (size_t)g2_chunk<C>(x) * (128 * 896); }
template <int C> DI u32 g2_loc(const G2Ctx& x) { return loc_of(C) + x.ldsg; }
template <int C, int N> DI void g2_w1(const G2Ctx& x, const char* sb) { if constexpr (N == 0) setm0(g2_loc<C>(x)); dmai<N * 1024>(x.voffw[N], sb); }
template <int C> DI void g2_issue_w(const G2Ctx& x) { const char* sb = g2_wsrc<C>(x); g2_w1<C, 0>(x, sb); g2_w1<C, 1>(x, sb); g2_w1<C, 2>(x, sb); g2_w1<C, 3>(x, sb); }
template <int C> DI const char* g2_hsrc(const G2Ctx& x) { return x.hb + g2_chunk<C>(x) * 8192; }
template <int N> DI u32x4 g2_h1(const G2Ctx& x, const char* sb) { return gld16c<(N & 1) * 1024>(x.vlane + (N >> 1) * 112 * 1024, sb); }
template <int C> DI void g2_issue_h(const G2Ctx& x, u32x4 (&hf)[HS][4]) {
    const char* sb = g2_hsrc<C>(x);
    hf[C % HS][0] = g2_h1<0>(x, sb); hf[C % HS][1] = g2_h1<1>(x, sb); hf[C % HS][2] = g2_h1<2>(x, sb); hf[C % HS][3] = g2_h1<3>(x, sb);
}
template <int C> DI void g2_read(const G2Ctx& x, u32x4 (&wf)[WS_][4]) {
    const u32 a0 = x.ta[loc_of(C) >> 16][0], a1 = x.ta[loc_of(C) >> 16][1];
    constexpr u32 lo = loc_of(C) & 0xffffu;
#pragma unroll
    for (int ks = 0; ks < 2; ++ks) {
        wf[C % WS_][ks * 2] = cat(ldtr(a0 + lo + ks * 2048), ldtr(a0 + lo + ks * 2048 + 1024));
        wf[C % WS_][ks * 2 + 1] = cat(ldtr(a1 + lo + ks * 2048), ldtr(a1 + lo + ks * 2048 + 1024));
    }
}
template <int C, int I> DI void g2_rd(const G2Ctx& x, u32x4 (&wf)[WS_][4]) {
    if constexpr (C < 14) {
        const u32 a = x.ta[loc_of(C) >> 16][I & 1]; constexpr u32 lo = (loc_of(C) & 0xffffu) + (I >> 1) * 2048;
        wf[C % WS_][I] = cat(ldtr(a + lo), ldtr(a + lo + 1024));
    }
}
DI u32 chk(u32 d, u32 m, u32 bad) { return __builtin_amdgcn_bitop3_b32(d, m, bad, 0xBE); }
template <int C> DI void g2_retry(const G2Ctx& x, u32x4 (&hf)[HS][4], u32 bad) {
    if (__builtin_expect(!__any((bad & 0x80008000u) != 0u), 1)) return;
    _Pragma("clang loop unroll(disable)") for (int spin = 0; spin < SPIN_CAP; ++spin) {
        __builtin_amdgcn_s_sleep(8);
        g2_issue_h<C>(x, hf);
        asm volatile("s_waitcnt vmcnt(0)" : "+v"(hf[C % HS][0]), "+v"(hf[C % HS][1]), "+v"(hf[C % HS][2]), "+v"(hf[C % HS][3]) :: "memory");
        bad = 0u;
#pragma unroll
        for (int i = 0; i < 16; ++i) { hf[C % HS][i >> 2][i & 3] ^= x.smask; bad |= hf[C % HS][i >> 2][i & 3]; }
        if (!__any((bad & 0x80008000u) != 0u)) break;
    }
}
template <int C> DI void g2_step(const G2Ctx& x, u32x4 (&hf)[HS][4], f32x16 (&acc)[2][2], u32x4 (&wf)[WS_][4]) {
    constexpr int N = (C + 1 < 14 ? C + 1 : C) % HS;
    asm volatile("s_waitcnt vmcnt(%4) lgkmcnt(0)" : "+v"(hf[N][0]), "+v"(hf[N][1]), "+v"(hf[N][2]), "+v"(hf[N][3]) : "i"(g2_younger(C)) : "memory");
    SB();
    constexpr bool DH = C + HD < 14, DW = C + 5 < 14, DC = C + 1 < 14;
    const char* hs = g2_hsrc<(DH ? C + HD : 0)>(x);
    const char* wsrc = g2_wsrc<(DW ? C + 5 : 0)>(x);
    u32 bad = 0u;
#define G2M(i) acc[((i) >> 1) & 1][(i) & 1] = mfma32(wf[C % WS_][((i) >> 2) * 2 + (((i) >> 1) & 1)], hf[C % HS][((i) & 1) * 2 + ((i) >> 2)], acc[((i) >> 1) & 1][(i) & 1]); \
               if constexpr (DC) { hf[N][(i) >> 1][((i) & 1) * 2] ^= x.smask; hf[N][(i) >> 1][((i) & 1) * 2 + 1] ^= x.smask; bad = bad | hf[N][(i) >> 1][((i) & 1) * 2] | hf[N][(i) >> 1][((i) & 1) * 2 + 1]; }
    G2M(0); g2_rd<C + 1, 0>(x, wf); if constexpr (DH) hf[(C + HD) % HS][0] = g2_h1<0>(x, hs); SB();
    G2M(1); g2_rd<C + 1, 1>(x, wf); if constexpr (DH) hf[(C + HD) % HS][1] = g2_h1<1>(x, hs); SB();
    G2M(2); g2_rd<C + 1, 2>(x, wf); if constexpr (DH) hf[(C + HD) % HS][2] = g2_h1<2>(x, hs); SB();
    G2M(3); g2_rd<C + 1, 3>(x, wf); if constexpr (DH) hf[(C + HD) % HS][3] = g2_h1<3>(x, hs); SB();
    G2M(4); if constexpr (DW) g2_w1<C + 5, 0>(x, wsrc); SB();
    G2M(5); if constexpr (DW) g2_w1<C + 5, 1>(x, wsrc); SB();
    G2M(6); if constexpr (DW) g2_w1<C + 5, 2>(x, wsrc); SB();
    G2M(7); if constexpr (DW) g2_w1<C + 5, 3>(x, wsrc); SB();
#undef G2M
    if constexpr (DC) { g2_retry<C + 1>(x, hf, bad); g2_step<C + 1>(x, hf, acc, wf); }
}

struct CF { u32x2 a0[8], a1[8]; u32x4 b[8]; };
template <int S, int R> DI void cv_ld(CF& f, u32 aA, u32 aB) {
    f.a0[R] = alds8<R * 512 + S * 64>(aA); f.a1[R] = alds8<R * 512 + S * 64 + 8>(aA);
    f.b[R] = alds16<(224 - 32 * R + 4 * S) * 16>(aB);
}
DI void cv_wait(CF& f) {
    asm volatile("s_waitcnt lgkmcnt(0)" : "+v"(f.a0[0]), "+v"(f.a0[1]), "+v"(f.a0[2]), "+v"(f.a0[3]), "+v"(f.a0[4]), "+v"(f.a0[5]), "+v"(f.a0[6]), "+v"(f.a0[7]),
                 "+v"(f.a1[0]), "+v"(f.a1[1]), "+v"(f.a1[2]), "+v"(f.a1[3]), "+v"(f.a1[4]), "+v"(f.a1[5]), "+v"(f.a1[6]), "+v"(f.a1[7]),
                 "+v"(f.b[0]), "+v"(f.b[1]), "+v"(f.b[2]), "+v"(f.b[3]), "+v"(f.b[4]), "+v"(f.b[5]), "+v"(f.b[6]), "+v"(f.b[7]));
}
constexpr int cv_r(int m) { int r = 0; while (m >= 8 - r) { m -= 8 - r; ++r; } return r; }
constexpr int cv_c(int m) { int r = 0; while (m >= 8 - r) { m -= 8 - r; ++r; } return m; }
template <int S, int M> DI void cv_mm(CF& f, CF& n, u32 aA, u32 aB, f32x4 (&acc)[8]) {
    constexpr int r = cv_r(M), c = cv_c(M);
    acc[r + c] = mfma16(cat(f.a0[r], f.a1[r]), f.b[c], acc[r + c]);
    if constexpr ((M & 3) == 0 && M / 4 < 8) cv_ld<S + 1, M / 4>(n, aA, aB);
    SB();
    if constexpr (M + 1 < 36) cv_mm<S, M + 1>(f, n, aA, aB, acc);
}
DI void conv_body(u32 aA, u32 aB, f32x4 (&acc)[8]) {
    CF f, n;
    asm volatile("s_waitcnt lgkmcnt(0)" ::: "memory");
    cv_ld<0, 0>(f, aA, aB); cv_ld<0, 1>(f, aA, aB); cv_ld<0, 2>(f, aA, aB); cv_ld<0, 3>(f, aA, aB);
    cv_ld<0, 4>(f, aA, aB); cv_ld<0, 5>(f, aA, aB); cv_ld<0, 6>(f, aA, aB); cv_ld<0, 7>(f, aA, aB);
    _Pragma("clang loop unroll(disable)") for (int it = 0; it < 2; ++it) {
        cv_wait(f); SB();
        cv_mm<0, 0>(f, n, aA, aB, acc);
        cv_wait(n); SB();
        cv_mm<1, 0>(n, f, aA, aB, acc);
        aA += 128; aB += 128;
    }
    cv_wait(f);
    asm volatile("" ::: "memory");
}

struct Geo { int b, xcd, fh2, RB, CT, tb64, db, rot, crot; u32 R0, F0, T0, D0; };
DI Geo make_geo() {
    Geo g; g.b = blockIdx.x - NHELP; g.xcd = g.b & 7; const int bi = g.b >> 3;
    const int rq = g.xcd >> 1, rb = bi / 7, ct = bi - rb * 7;
    g.fh2 = g.xcd & 1; g.RB = 4 * rq + rb; g.CT = 7 * g.fh2 + ct; g.R0 = 128 * g.RB; g.F0 = 128 * g.CT;
    g.tb64 = 2 * g.RB + g.fh2; g.db = ct; g.T0 = 64 * g.tb64; g.D0 = 64 * g.db;
    g.rot = g.xcd >= 7 ? 0 : g.xcd; g.crot = g.CT;
    return g;
}
DI void make_g2(G2Ctx& g2, const Params& p, const Geo& q, int g, int lane, u32 lds0) {
    g2.w2b = (const char*)p.w2 + (size_t)(32 * g) * 896 + q.D0 * 2;
    g2.hb = p.ws + WS_H + (size_t)(q.tb64 * 2 * 112 + 2 * g) * 1024;
    g2.crot = q.crot;
#pragma unroll
    for (int n = 0; n < 4; ++n) g2.voffw[n] = (u32)((lane >> 3) * 896 + 16 * ((lane & 7) ^ (((lane >> 4) & 1) << 2)) + n * (8 * 896 - 1024));
    g2.ldsg = lds0 + g * 4096; g2.vlane = lane * 16u;
}
DI u32 slab_base(u32 lds0, int cw) { return lds0 + L_CONV + (cw >> 1) * CONV_CH + (cw & 1) * 8192; }
DI void final_epilogue(const Params& p, const Geo& q, u32 lds0) {
    int tz = threadIdx.x; PIN(tz);
    const int t = tz >> 3, c = tz & 7;
    const u32 pb = lds0 + ((t >> 5) ? 0u : 3 * L_PIECE) + ((t >> 4) & 1) * L_W + (t & 15) * 256;
    const u32 o0 = pb + 16 * ((2 * c) ^ (t & 7)), o1 = o0 ^ 16;
    f32x4 pa[4], pb_[4];
#pragma unroll
    for (int g = 0; g < 4; ++g) { pa[g] = __builtin_bit_cast(f32x4, lds16(o0 + g * 4096)); pb_[g] = __builtin_bit_cast(f32x4, lds16(o1 + g * 4096)); }
    const float r2 = *(const LAS float*)(lds0 + L_R2 + t * 4);
    const f32x4 b0 = __builtin_bit_cast(f32x4, lds16(lds0 + L_B2F + c * 32)), b1 = __builtin_bit_cast(f32x4, lds16(lds0 + L_B2F + c * 32 + 16));
    const u32 sb = slab_base(lds0, c >> 1) + t * 64 + (c & 1) * 32;
    const f32x4 sv0 = __builtin_bit_cast(f32x4, lds16(sb)), sv1 = __builtin_bit_cast(f32x4, lds16(sb + 16));
    const f32x4 xv0 = __builtin_bit_cast(f32x4, lds16(sb + 4096)), xv1 = __builtin_bit_cast(f32x4, lds16(sb + 4096 + 16));
    SB();
    const f32x4 s0 = (pa[0] + pa[1]) + (pa[2] + pa[3]), s1 = (pb_[0] + pb_[1]) + (pb_[2] + pb_[3]);
    f32x4 z0 = s0 * r2 + b0, z1 = s1 * r2 + b1, e0, e1;
#pragma unroll
    for (int j = 0; j < 4; ++j) { e0[j] = __builtin_amdgcn_exp2f(z0[j]); e1[j] = __builtin_amdgcn_exp2f(z1[j]); }
    e0 += 1.f; e1 += 1.f;
#pragma unroll
    for (int j = 0; j < 4; ++j) { e0[j] = __builtin_amdgcn_rcpf(e0[j]); e1[j] = __builtin_amdgcn_rcpf(e1[j]); }
    z0 = z0 * e0; z1 = z1 * e1;
    z0 = z0 * sv0 + xv0; z1 = z1 * sv1 + xv1;
    const u32x4 res = {pk2(z0[0], z0[1]), pk2(z0[2], z0[3]), pk2(z1[0], z1[1]), pk2(z1[2], z1[3])};
    *(GAS u32x4*)((GAS char*)p.out + (size_t)(q.T0 + t) * 896 + q.D0 * 2 + c * 16) = res;
}

extern __shared__ __attribute__((aligned(16))) char smem[];

struct CL { const char *xb, *wb; u32 vx[4], vw[4], sl; int rot; };
struct CSrc { const char *xs, *wsrc; };
template <int ST_> DI CSrc c_src(const CL& c) { int kc = ST_ + c.rot; if (kc >= 7) kc -= 7; return CSrc{c.xb + kc * 128, c.wb + (size_t)kc * 64 * 3584}; }
template <int ST_, int N> DI void c_dma1(const CL& c, const CSrc& s) {
    if constexpr (N == 0) setm0(c.sl + (ST_ & 3) * L_PIECE);
    if constexpr (N == 4) setm0(c.sl + (ST_ & 3) * L_PIECE + L_W);
    if constexpr (N < 4) dmai<N * 1024>(c.vx[N], s.xs); else dmai<(N - 4) * 1024>(c.vw[N - 4], s.wsrc);
}
template <int ST_> DI void c_dma(const CL& c) {
    const CSrc s = c_src<ST_>(c);
    c_dma1<ST_, 0>(c, s); c_dma1<ST_, 1>(c, s); c_dma1<ST_, 2>(c, s); c_dma1<ST_, 3>(c, s);
    c_dma1<ST_, 4>(c, s); c_dma1<ST_, 5>(c, s); c_dma1<ST_, 6>(c, s); c_dma1<ST_, 7>(c, s);
}
DI void make_cl(CL& c, const Params& p, const Geo& q, int cw, int lane, u32 lds0) {
    const int r8 = lane >> 3, c8 = lane & 7, r4 = lane >> 4, c16 = lane & 15;
#pragma unroll
    for (int n = 0; n < 4; ++n) {
        c.vx[n] = (u32)((32 * cw + r8) * 896 + 16 * (c8 ^ (((n & 1) << 2) | (r8 >> 1))) + n * (8 * 896 - 1024));
        c.vw[n] = (u32)((16 * cw + r4) * 3584 + 16 * (c16 ^ (r4 << 2)) + n * (4 * 3584 - 1024));
    }
    c.xb = (const char*)p.x + (size_t)q.R0 * 896; c.wb = (const char*)p.w1 + q.F0 * 2;
    c.sl = lds0 + cw * 4096; c.rot = q.rot;
}

template <int ST_> DI void l_dma(const CL& c) {
    const CSrc s = c_src<ST_>(c);
    const u32 sl = c.sl + (ST_ & 3) * L_PIECE;
    setm0(sl); dmai<0>(c.vx[0], s.xs); dmai<1024>(c.vx[1], s.xs); dmai<2048>(c.vx[2], s.xs); dmai<3072>(c.vx[3], s.xs);
    setm0(sl + L_W); dmai<0>(c.vw[0], s.wsrc); dmai<1024>(c.vw[1], s.wsrc); dmai<2048>(c.vw[2], s.wsrc); dmai<3072>(c.vw[3], s.wsrc);
    const char* x2 = s.xs + 32 * 896; const char* w2 = s.wsrc + 16 * 3584;
    setm0(sl + 4096); dmai<0>(c.vx[0], x2); dmai<1024>(c.vx[1], x2); dmai<2048>(c.vx[2], x2); dmai<3072>(c.vx[3], x2);
    setm0(sl + L_W + 4096); dmai<0>(c.vw[0], w2); dmai<1024>(c.vw[1], w2); dmai<2048>(c.vw[2], w2); dmai<3072>(c.vw[3], w2);
}

struct Fr { u32x4 x[4][2], w[4][2]; };
struct GA { u32 ax[4], aw[2]; };
template <int K0> DI void g1_mma(const Fr& f, f32x16 (&acc)[2][2]) {
#pragma unroll
    for (int k = K0; k < K0 + 2; ++k) {
        acc[0][0] = mfma32(f.w[k][0], f.x[k][0], acc[0][0]); acc[1][0] = mfma32(f.w[k][1], f.x[k][0], acc[1][0]);
        acc[0][1] = mfma32(f.w[k][0], f.x[k][1], acc[0][1]); acc[1][1] = mfma32(f.w[k][1], f.x[k][1], acc[1][1]);
    }
    SB();
}

template <int ST_, int I> DI void g1_rd(const GA& a, Fr& f) {
    constexpr u32 so = (ST_ & 3) * L_PIECE; constexpr int k = I >> 1, h = I & 1;
    f.w[k][h] = cat(ldtr(a.aw[h] + so + k * 4096), ldtr(a.aw[h] + so + k * 4096 + 1024));
    f.x[k][h] = lds16(a.ax[k] + so + h * 4096);
}
template <int K0, int I> DI void g1_m1(const Fr& f, f32x16 (&acc)[2][2]) {
    constexpr int k = K0 + (I >> 2), mt = I & 1, st = (I >> 1) & 1;
    acc[mt][st] = mfma32(f.w[k][mt], f.x[k][st], acc[mt][st]);
}
template <int ST_, bool CARRY> DI void g1_stage(const GA& a, const Fr& fp, Fr& fn, f32x16 (&acc)[2][2], u32 lds0, int g, u32& pk) {
    if constexpr (CARRY) { g1_m1<2, 0>(fp, acc); SB(); cons_set<ST_ - 1>(lds0, g); SB(); }
    lds_wait_ge(lds0 + F_LAND + 4 * ST_, ST_ == 0 ? 256 : (ST_ == 3 || ST_ == 6) ? 128 : 64, pk);
    if constexpr (CARRY) {
        g1_rd<ST_, 0>(a, fn); SB();
        g1_m1<2, 1>(fp, acc); SB(); g1_m1<2, 2>(fp, acc); g1_rd<ST_, 1>(a, fn); SB();
        g1_m1<2, 3>(fp, acc); SB(); g1_m1<2, 4>(fp, acc); g1_rd<ST_, 2>(a, fn); SB();
        g1_m1<2, 5>(fp, acc); g1_rd<ST_, 3>(a, fn); SB(); g1_m1<2, 6>(fp, acc); SB();
        g1_m1<2, 7>(fp, acc); SB();
    } else { g1_rd<ST_, 0>(a, fn); g1_rd<ST_, 1>(a, fn); g1_rd<ST_, 2>(a, fn); g1_rd<ST_, 3>(a, fn); SB(); }
    g1_m1<0, 0>(fn, acc); g1_rd<ST_, 4>(a, fn); SB(); g1_m1<0, 1>(fn, acc); SB();
    g1_m1<0, 2>(fn, acc); g1_rd<ST_, 5>(a, fn); SB(); g1_m1<0, 3>(fn, acc); g1_rd<ST_, 6>(a, fn); SB();
    g1_m1<0, 4>(fn, acc); g1_rd<ST_, 7>(a, fn); SB(); g1_m1<0, 5>(fn, acc); pk = lds_peek(lds0 + F_LAND + 4 * ST_ + 4); SB();
    g1_m1<0, 6>(fn, acc); SB(); g1_m1<0, 7>(fn, acc); SB();
}

DI void body_g(const Params& p, const int g) {
    const u32 lds0 = (u32)(size_t)(LAS char*)smem;
    const int lane = threadIdx.x & 63;
    const Geo q = make_geo();
    char* ws = p.ws;
    const int hi = lane >> 5, s31 = lane & 31;
    const int r8 = lane >> 3, c8 = lane & 7;
    const u32 raw = lds0 + L_LOC0 + g * KVRAW_SZ;
    {
        const u32 vkv = (u32)(r8 * 896 + c8 * 16);
        bdma16((u32)(((int)q.T0 + 16 * g - 8) * 896 + (int)q.D0 * 2) + vkv, mksrd(p.k, S_ * D_ * 2), raw);
        const char* kb = (const char*)p.k + (size_t)(q.T0 + 16 * g) * 896 + q.D0 * 2;
        const char* vb = (const char*)p.v + (size_t)(q.T0 + 16 * g) * 896 + q.D0 * 2;
        dmai<1024>(vkv, kb - 1024); dmai<2048>(vkv, kb + 8 * 896 - 2048); dmai<3072>(vkv, vb - 3072);
        setm0(raw + 4096);
        dmai<0>(vkv, vb + 8 * 896);
        if (lane < 32) dmai<1024>(lane * 16u, (const char*)p.shk + q.D0 * 8 - 1024);
        CL c; make_cl(c, p, q, g, lane, lds0);
        c_dma<0>(c);
    }
    u32x2 ep = gld8c(0u, ws + WS_EPOCH);
    const int fh = g & 1, sh = g >> 1;
    GA ga;
    {
        const int blk = (lane >> 4) & 1, qq = (lane & 15) >> 2, pp = lane & 3;
        const u32 Z = (u32)(hi ^ ((s31 >> 1) & 7));
#pragma unroll
        for (int k = 0; k < 4; ++k) ga.ax[k] = lds0 + (64 * sh + s31) * 128 + 16 * ((2 * k) ^ Z);
        ga.aw[0] = lds0 + L_W + (8 * hi + qq) * 256 + 16 * ((8 * fh + 2 * blk + (pp >> 1)) ^ (qq << 2)) + 8 * (pp & 1);
        ga.aw[1] = ga.aw[0] ^ 64;
    }
    WAITV(9);
    {
        u32x4 wr[4], kr[5], vr[2];
#pragma unroll
        for (int e = 0; e < 4; ++e) wr[e] = lds16(raw + 5120 + c8 * 64 + e * 16);
#pragma unroll
        for (int j = 0; j < 5; ++j) kr[j] = lds16(raw + (5 + 2 * r8 + j) * 128 + c8 * 16);
        vr[0] = lds16(raw + 3072 + 2 * r8 * 128 + c8 * 16); vr[1] = lds16(raw + 3072 + (2 * r8 + 1) * 128 + c8 * 16);
        SB();
        u32x4 o0, o1;
#pragma unroll
        for (int e = 0; e < 4; ++e) {
            u32 pl[4], ph[4];
#pragma unroll
            for (int j = 0; j < 4; ++j) { pl[j] = __builtin_amdgcn_perm(kr[j][e], kr[j + 1][e], 0x05040100u); ph[j] = __builtin_amdgcn_perm(kr[j][e], kr[j + 1][e], 0x07060302u); }
            f32x2 a = {dot2(pl[0], wr[e].y, dot2(pl[2], wr[e].x, 0.f)), dot2(ph[0], wr[e].w, dot2(ph[2], wr[e].z, 0.f))};
            f32x2 b = {dot2(pl[1], wr[e].y, dot2(pl[3], wr[e].x, 0.f)), dot2(ph[1], wr[e].w, dot2(ph[3], wr[e].z, 0.f))};
            const u32 ar = pk2(a.x, a.y), br = pk2(b.x, b.y);
            a = f32x2{bflo(ar), bfhi(ar)} * f32x2{bflo(vr[0][e]), bfhi(vr[0][e])}; b = f32x2{bflo(br), bfhi(br)} * f32x2{bflo(vr[1][e]), bfhi(vr[1][e])};
            o0[e] = pk2(a.x, a.y); o1[e] = pk2(b.x, b.y);
        }
        sts16(lds0 + L_KVS + (16 * g + 2 * r8) * 144 + c8 * 16, o0);
        sts16(lds0 + L_KVS + (16 * g + 2 * r8 + 1) * 144 + c8 * 16, o1);
        asm volatile("s_waitcnt lgkmcnt(0)" ::: "memory");
        lds_add(lds0 + F_KVS);
    }
    f32x16 acc[2][2] = {};
    Fr fa, fb;
    WAITV(1);
    lds_add(lds0 + F_LAND);
    u32 pk = lds_peek(lds0 + F_LAND);
    g1_stage<0, false>(ga, fb, fa, acc, lds0, g, pk);
    g1_stage<1, true>(ga, fa, fb, acc, lds0, g, pk);
    g1_stage<2, true>(ga, fb, fa, acc, lds0, g, pk);
    asm volatile("s_waitcnt vmcnt(0)" : "+v"(ep) :: "memory");
    const u64 epoch = ((u64)(u32)__builtin_amdgcn_readfirstlane(ep.y) << 32) | (u32)__builtin_amdgcn_readfirstlane(ep.x);
    const u64 tag = epoch + TAG_SALT;
    const u32 tlo = (u32)tag, thi = (u32)(tag >> 32);
    const u32 smask = ((lane ^ (u32)epoch) & 1u) ? 0x80008000u : 0u;
    g1_stage<3, true>(ga, fa, fb, acc, lds0, g, pk);
    const u32 rgo = WS_RG + (q.R0 + 64 * sh + lane) * 8, cgo = WS_CG + (q.F0 + 64 * fh + lane) * 8;
    u32x2 rg = gld8c(rgo, ws), cg = gld8c(cgo, ws);
    u32 b1v = gld2((u32)((64 * fh + lane) * 2), (const char*)p.b1 + q.F0 * 2);
    G2Ctx g2; make_g2(g2, p, q, g, lane, lds0);
    {
        const int blk = (lane >> 4) & 1, qq = (lane & 15) >> 2, pp = lane & 3;
        const u32 t0 = g2.ldsg + (4 * hi + qq) * 128 + 16 * ((2 * blk + (pp >> 1)) ^ ((qq >> 1) << 2)) + 8 * (pp & 1);
#pragma unroll
        for (int i = 0; i < 3; ++i) { g2.ta[i][0] = t0 + i * 65536; g2.ta[i][1] = (t0 ^ 64) + i * 65536; PIN(g2.ta[i][0]); PIN(g2.ta[i][1]); }
#pragma unroll
        for (int i = 0; i < 4; ++i) PIN(g2.voffw[i]);
    }
    g2.smask = smask;
    g1_stage<4, true>(ga, fb, fa, acc, lds0, g, pk);
    g1_stage<5, true>(ga, fa, fb, acc, lds0, g, pk);
    g1_stage<6, true>(ga, fb, fa, acc, lds0, g, pk);
    cons_set<6>(lds0, g);
    asm volatile("s_waitcnt vmcnt(0)" : "+v"(rg), "+v"(cg), "+v"(b1v) :: "memory");
    _Pragma("clang loop unroll(disable)") for (int spin = 0; spin < SPIN_CAP && !__all(rg.y == tlo && cg.y == tlo); ++spin) {
        rg = gld8c(rgo, ws); cg = gld8c(cgo, ws);
        asm volatile("s_waitcnt vmcnt(0)" : "+v"(rg), "+v"(cg) :: "memory");
    }
    if (fh == 0 && sh == q.fh2) *(LAS u32*)(lds0 + L_ROWF + lane * 4) = rg.x;
    g1_mma<2>(fa, acc);
    {
        u32x4 ax[2], bx[2];
        const auto rr = __builtin_amdgcn_permlane32_swap(rg.x, rg.x, false, false);
        const auto cc = __builtin_amdgcn_permlane32_swap(cg.x, cg.x, false, false);
        const auto bb = __builtin_amdgcn_permlane32_swap(b1v, b1v, false, false);
#pragma unroll
        for (int i = 0; i < 2; ++i) {
            const u32 nm = __builtin_amdgcn_perm(rr[i], rr[i], 0x01000100u) ^ 0x80008000u;
            ax[i] = u32x4{cc[i], bb[i], 0u, 0u};
            bx[i] = u32x4{hi ? 0u : nm, hi ? 0u : (rr[i] >> 16), 0u, 0u};
        }
#pragma unroll
        for (int st = 0; st < 2; ++st)
#pragma unroll
            for (int mt = 0; mt < 2; ++mt) acc[mt][st] = mfma32(ax[mt], bx[st], acc[mt][st]);
#pragma unroll
        for (int st = 0; st < 2; ++st) {
            char* hb = ws + WS_H + (size_t)((4 * q.RB + 2 * sh + st) * 112 + q.CT * 8 + fh * 4) * 1024;
#pragma unroll
            for (int mt = 0; mt < 2; ++mt)
#pragma unroll
                for (int t = 0; t < 2; ++t) {
                    u32x4 hv;
#pragma unroll
                    for (int j = 0; j < 4; ++j) {
                        f32x2 a = {acc[mt][st][8 * t + 2 * j], acc[mt][st][8 * t + 2 * j + 1]};
                        f32x2 u = {relu(a.x), relu(a.y)};
                        u = f32x2{mul1(u.x, u.x), mul1(u.y, u.y)};
                        hv[j] = (pk2(u.x, u.y) & 0x7fff7fffu) | smask;
                    }
                    if (mt == 0 && t == 0) gst16c<0>(lane * 16u, hb, hv);
                    if (mt == 0 && t == 1) gst16c<1024>(lane * 16u, hb, hv);
                    if (mt == 1 && t == 0) gst16c<2048>(lane * 16u, hb, hv);
                    if (mt == 1 && t == 1) gst16c<3072>(lane * 16u, hb, hv);
                    SB();
                }
        }
    }
    u32x4 wf[WS_][4];
    cons_wait(lds0, 4);
    asm volatile("s_waitcnt lgkmcnt(0)" ::: "memory");
    {
        u32x4 hf[HS][4];
        if (lds_arrive_last(lds0 + F_HCNT, 4)) { if (lane == 0) gst8c<0>(WS_ALL + q.b * 8, ws, u32x2{tlo, thi}); }
        g2_issue_w<2>(g2); g2_issue_w<3>(g2); g2_issue_w<4>(g2);
        WAITV(0);
        g2_issue_h<0>(g2, hf); g2_issue_h<1>(g2, hf); g2_issue_h<2>(g2, hf); g2_issue_h<3>(g2, hf);
        lds_wait(lds0 + F_W);
        g2_read<0>(g2, wf);
        asm volatile("s_waitcnt vmcnt(%4)" : "+v"(hf[0][0]), "+v"(hf[0][1]), "+v"(hf[0][2]), "+v"(hf[0][3]) : "i"(4 * (HD - 1)) : "memory");
        {
            u32 bad = 0u;
#pragma unroll
            for (int i = 0; i < 16; ++i) { hf[0][i >> 2][i & 3] ^= smask; bad |= hf[0][i >> 2][i & 3]; }
            g2_retry<0>(g2, hf, bad);
        }
        f32x16 a2[2][2] = {};
        g2_step<0>(g2, hf, a2, wf);
        const u32 pw = g2.ldsg + (s31 >> 4) * L_W + (s31 & 15) * 256;
#pragma unroll
        for (int s2 = 0; s2 < 2; ++s2)
#pragma unroll
            for (int dt = 0; dt < 2; ++dt)
#pragma unroll
                for (int gr = 0; gr < 4; ++gr) {
                    const u32 a = pw + (s2 ? 0u : 3 * L_PIECE) + 16 * ((8 * dt + 2 * gr + hi) ^ (s31 & 7));
                    sts16(a, __builtin_bit_cast(u32x4, f32x4{a2[dt][s2][4 * gr], a2[dt][s2][4 * gr + 1], a2[dt][s2][4 * gr + 2], a2[dt][s2][4 * gr + 3]}));
                }
    }
    BARL();
    final_epilogue(p, q, lds0);
}

DI void body_c(const Params& p, const int cw) {
    const u32 lds0 = (u32)(size_t)(LAS char*)smem;
    const int lane = threadIdx.x & 63;
    const Geo q = make_geo();
    char* ws = p.ws;
    const int hi = lane >> 5, s31 = lane & 31;
    const int half = cw & 1, chl = cw >> 1, chan = 2 * q.b + chl;
    u32x2 ep;
    u32x4 xs[4]; u32 dv;
    if (cw < 2) {
        CL c, c2; make_cl(c, p, q, 0, lane, lds0); c2 = c; c2.xb += 64 * 896; c2.wb += 32 * 3584; c2.sl += 8192;
        ep = gld8c(0u, ws + WS_EPOCH);
        if (cw == 0) {
            l_dma<1>(c); l_dma<1>(c2);
            WAITV(0); lds_add(lds0 + F_LAND + 4);
            cons_wait(lds0, 0); l_dma<4>(c); l_dma<4>(c2);
            WAITV(0); lds_add(lds0 + F_LAND + 16);
        } else {
            l_dma<2>(c); l_dma<2>(c2);
            WAITV(0); lds_add(lds0 + F_LAND + 8);
            cons_wait(lds0, 1); l_dma<5>(c); l_dma<5>(c2);
            WAITV(0); lds_add(lds0 + F_LAND + 20);
        }
        asm volatile("s_waitcnt vmcnt(0)" : "+v"(ep) :: "memory");
        const u32 tg32 = (u32)((((u64)ep.y << 32) | ep.x) + TAG_SALT);
        if (cw == 1) {
            G2Ctx g2; make_g2(g2, p, q, 0, lane, lds0);
            cons_wait(lds0, 3);
#define W4(C) do { const char* sb = g2_wsrc<C>(g2); _Pragma("unroll") for (int i = 0; i < 4; ++i) { setm0(g2_loc<C>(g2) + i * 4096); \
                dmai<0>(g2.voffw[0], sb + i * 32 * 896); dmai<1024>(g2.voffw[1], sb + i * 32 * 896); dmai<2048>(g2.voffw[2], sb + i * 32 * 896); dmai<3072>(g2.voffw[3], sb + i * 32 * 896); } } while (0)
            W4(0); W4(1);
#undef W4
            dv = gld2(0u, (const char*)p.ssmd + chan * 2);
            WAITV(1);
            lds_set(lds0 + F_W);
            asm volatile("s_waitcnt vmcnt(0)" : "+v"(dv) :: "memory");
        } else {
            const char* kb2 = ws + WS_KVT + (size_t)(2 * q.b) * TP;
            u32x4 u[16];
            probe<2>((u32)(hi * TP + s31 * 256), kb2, tg32);
            _Pragma("clang loop unroll(disable)") for (int spin = 0; spin < SPIN_CAP; ++spin) {
#pragma unroll
                for (int c = 0; c < 4; ++c) {
                    const char* sb = kb2 + (c >> 1) * TP + (c & 1) * 4096;
                    u[4 * c] = gld16c<0>(lane * 16u, sb); u[4 * c + 1] = gld16c<1024>(lane * 16u, sb); u[4 * c + 2] = gld16c<2048>(lane * 16u, sb); u[4 * c + 3] = gld16c<3072>(lane * 16u, sb);
                }
                asm volatile("s_waitcnt vmcnt(0)" : "+v"(u[0]), "+v"(u[1]), "+v"(u[2]), "+v"(u[3]), "+v"(u[4]), "+v"(u[5]), "+v"(u[6]), "+v"(u[7]), "+v"(u[8]), "+v"(u[9]), "+v"(u[10]),
                             "+v"(u[11]), "+v"(u[12]), "+v"(u[13]), "+v"(u[14]), "+v"(u[15]) :: "memory");
                u32 bad = 0u;
#pragma unroll
                for (int i = 0; i < 16; ++i) bad = chk(u[i].w, tg32, chk(u[i].y, tg32, bad));
                if (!__any(bad != 0u)) break;
                __builtin_amdgcn_s_sleep(4);
            }
            dv = gld2(0u, (const char*)p.ssmd + chan * 2);
            cons_wait(lds0, 5); cons_wait(lds0, 6);
            const u32 uq_ = lds0 + L_CONV + TOEP_CH + lane * 8;
#pragma unroll
            for (int i = 0; i < 16; ++i) sts8(uq_ + (i >> 3) * CONV_CH + (i & 7) * 512, u32x2{u[i].x, u[i].z});
            sts8(uq_ + 4096, u32x2{0u, 0u}); sts8(uq_ + CONV_CH + 4096, u32x2{0u, 0u});
            asm volatile("s_waitcnt lgkmcnt(0)" ::: "memory");
            lds_add(lds0 + F_TOEP); lds_add(lds0 + F_TOEP + 4);
            asm volatile("s_waitcnt vmcnt(0)" : "+v"(dv) :: "memory");
        }
    } else {
        const int kw = cw - 2;
        if (kw == 0 && lane < 8) dma16(lane * 16u, (const char*)p.b2 + q.D0 * 2, lds0 + L_B2);
        ep = gld8c(0u, ws + WS_EPOCH);
        CL c; make_cl(c, p, q, 2 * kw, lane, lds0);
        l_dma<3>(c);
        lds_wait_ge(lds0 + F_KVS, 256, lds_peek(lds0 + F_KVS));
        asm volatile("s_waitcnt vmcnt(16)" : "+v"(ep) :: "memory");
        const u32 tg32 = (u32)((((u64)ep.y << 32) | ep.x) + TAG_SALT);
        {
            const int grp = lane >> 4, qq = (lane & 15) >> 2, pp = lane & 3;
            const u32 a = lds0 + L_KVS + (16 * grp + 4 * pp + 3 - qq) * 144 + kw * 64;
            const u32 vo = (u32)((q.D0 + 32 * kw + (lane & 3)) * TP + (2044 - (int)q.T0 - 4 * (lane >> 2)) * 4);
            u32x2 tr[8];
#pragma unroll
            for (int i = 0; i < 8; ++i) tr[i] = ldtr(a + 8 * i);
            SB();
#pragma unroll
            for (int i = 0; i < 8; ++i) gst16c<0>(vo, ws + WS_KVT + i * 4 * TP, u32x4{tr[i].x, tg32, tr[i].y, tg32});
        }
        WAITV(8);
        lds_add(lds0 + F_LAND + 12);
        cons_wait(lds0, 2);
        l_dma<6>(c);
        u32x4 sk[5];
        const u32x4 sksrd = mksrd((const char*)p.ssmk + (size_t)(2 * q.b + kw) * 4096, 4096);
#pragma unroll
        for (int r = 0; r < 5; ++r) sk[r] = bld16((u32)((lane - 1 + 63 * r) * 16), sksrd);
        { const char* xsl = (const char*)p.x + (size_t)q.T0 * 896 + (q.D0 + 32 * kw) * 2;
          xs[0] = gld16o<0>((u32)(lane * 896), xsl); xs[1] = gld16o<16>((u32)(lane * 896), xsl); xs[2] = gld16o<32>((u32)(lane * 896), xsl); xs[3] = gld16o<48>((u32)(lane * 896), xsl); }
        dv = gld2(0u, (const char*)p.ssmd + chan * 2);
        WAITV(10);
        lds_add(lds0 + F_LAND + 24);
        asm volatile("s_waitcnt vmcnt(0)" : "+v"(sk[0]), "+v"(sk[1]), "+v"(sk[2]), "+v"(sk[3]), "+v"(sk[4]), "+v"(xs[0]), "+v"(xs[1]), "+v"(xs[2]), "+v"(xs[3]), "+v"(dv) :: "memory");
        u32x4 tw[5][4];
#pragma unroll
        for (int r = 0; r < 5; ++r) {
            const u32x4 B = sk[r];
            u32x4 A;
#pragma unroll
            for (int j = 0; j < 4; ++j) A[j] = __builtin_amdgcn_update_dpp(0u, B[j], 0x138  , 0xf, 0xf, false);
            const u32 e0 = __builtin_amdgcn_alignbit(A.y, A.x, 16), e1 = __builtin_amdgcn_alignbit(A.z, A.y, 16), e2 = __builtin_amdgcn_alignbit(A.w, A.z, 16),
                      e3 = __builtin_amdgcn_alignbit(B.x, A.w, 16), e4 = __builtin_amdgcn_alignbit(B.y, B.x, 16);
            tw[r][0] = u32x4{e0, e1, e2, e3}; tw[r][1] = u32x4{A.y, A.z, A.w, B.x}; tw[r][2] = u32x4{e1, e2, e3, e4}; tw[r][3] = u32x4{A.z, A.w, B.x, B.y};
            PIN(tw[r][0]); PIN(tw[r][1]); PIN(tw[r][2]); PIN(tw[r][3]);
        }
        cons_wait(lds0, 5); cons_wait(lds0, 6);
        const u32 tq = lds0 + L_CONV + kw * CONV_CH + lane * 16;
#pragma unroll
        for (int r = 0; r < 5; ++r) {
            if ((lane > 0 || r == 0) && (r < 4 || lane < 6)) {
#pragma unroll
                for (int c = 0; c < 4; ++c) sts16(tq + r * 1008 + c * TOEP_CP, tw[r][c]);
            }
        }
        asm volatile("s_waitcnt lgkmcnt(0)" ::: "memory");
        lds_add(lds0 + F_TOEP + kw * 4);
    }
    const u64 epoch = ((u64)(u32)__builtin_amdgcn_readfirstlane(ep.y) << 32) | (u32)__builtin_amdgcn_readfirstlane(ep.x);
    const u64 tag = epoch + TAG_SALT;
    const u32 tlo = (u32)tag, thi = (u32)(tag >> 32);
    const u32 tbase = lds0 + L_CONV + chl * CONV_CH;
    const u32 ub = tbase + TOEP_CH;
    lds_wait_ge(lds0 + F_TOEP + chl * 4, 128, lds_peek(lds0 + F_TOEP + chl * 4));
    const int bb = lane & 15, gg = lane >> 4;
    const u32 aA = tbase + (bb & 3) * TOEP_CP + ((bb >> 2) + 2 * gg) * 8 + half * 256;
    const u32 aB = ub + (30 - 2 * bb + gg) * 16 + half * 256;
    f32x4 ac8[8] = {};
    __builtin_amdgcn_s_setprio(2);
    conv_body(aA, aB, ac8);
    f32x4 acc[4];
    {
        const u32 xo = ub + U_SZ + lane * 16;
#pragma unroll
        for (int i = 0; i < 4; ++i) { const f32x4 give = half ? ac8[i] : ac8[4 + i]; acc[i] = half ? ac8[4 + i] : ac8[i]; sts16(xo + half * 4096 + i * 1024, __builtin_bit_cast(u32x4, give)); }
        asm volatile("s_waitcnt lgkmcnt(0)" ::: "memory");
        lds_set(lds0 + F_XCH + cw * 4);
        lds_wait(lds0 + F_XCH + (cw ^ 1) * 4);
        f32x4 got[4];
#pragma unroll
        for (int i = 0; i < 4; ++i) got[i] = __builtin_bit_cast(f32x4, lds16(xo + (half ^ 1) * 4096 + i * 1024));
        SB();
#pragma unroll
        for (int i = 0; i < 4; ++i) acc[i] = f32x4{add1(acc[i][0], got[i][0]), add1(acc[i][1], got[i][1]), add1(acc[i][2], got[i][2]), add1(acc[i][3], got[i][3])};
    }
    {
        const float dd = bflo(dv);
        char* yb = ws + WS_YT + (size_t)chan * TP;
        u32x2 uq[4];
#pragma unroll
        for (int i = 0; i < 4; ++i) uq[i] = lds8(ub + (2044 - (256 * (4 * half + i) + 16 * bb + 4 * gg)) * 2);
        SB();
#pragma unroll
        for (int i = 0; i < 4; ++i) {
            const int qt = 4 * half + i;
            const int t = 256 * qt + 16 * bb + 4 * gg;
            const u32x2 uu = uq[i];
            const u32 c01 = pk2(acc[i][0], acc[i][1]), c23 = pk2(acc[i][2], acc[i][3]);
            const u32 d01 = pk2(mul1(bfhi(uu.y), dd), mul1(bflo(uu.y), dd)), d23 = pk2(mul1(bfhi(uu.x), dd), mul1(bflo(uu.x), dd));
            float y0 = add1(bflo(d01), bflo(c01)), y1 = add1(bfhi(d01), bfhi(c01));
            float y2 = add1(bflo(d23), bflo(c23)), y3 = add1(bfhi(d23), bfhi(c23));
            gst16c<0>((u32)(t * 4), yb, u32x4{pk2(y0, y1), tlo, pk2(y2, y3), tlo});
        }
        lds_arrive_last(lds0 + F_YCNT, 4);
    }
    if (cw >= 2) {
        const int kw = cw - 2;
        lds_wait_ge(lds0 + F_YCNT, 4, lds_peek(lds0 + F_YCNT)); lds_wait_ge(lds0 + F_HCNT, 4, lds_peek(lds0 + F_HCNT));
        __builtin_amdgcn_s_setprio(0);
        const u32 slab = slab_base(lds0, 2 * kw) + lane * 64;
        float xn[32];
        {
            const u32 rf = *(const LAS u32*)(lds0 + L_ROWF + lane * 4);
            const float nm = -bflo(rf); float ri = __builtin_amdgcn_rcpf(bfhi(rf));
            asm volatile("s_nop 1" : "+v"(ri));
#pragma unroll
            for (int j = 0; j < 8; ++j) {
                const u32 lo = xs[j >> 1][(j & 1) * 2], hi_ = xs[j >> 1][(j & 1) * 2 + 1];
                const f32x4 v = {bflo(lo), bfhi(lo), bflo(hi_), bfhi(hi_)};
                sts16(slab + (j >> 2) * 8192 + 4096 + (j & 3) * 16, __builtin_bit_cast(u32x4, v));
#pragma unroll
                for (int e = 0; e < 4; e += 2) {
                    const u32 dx = pk2(add1(v[e], nm), add1(v[e + 1], nm)), xr = pk2(mul1(bflo(dx), ri), mul1(bfhi(dx), ri));
                    xn[4 * j + e] = bflo(xr); xn[4 * j + e + 1] = bfhi(xr);
                }
            }
            if (kw == 0) *(LAS float*)(lds0 + L_R2 + lane * 4) = -1.4426950408889634f * ri * ri;
            else *(LAS float*)(lds0 + L_B2F + lane * 4) = -1.4426950408889634f * bflo(*(const LAS u16*)(lds0 + L_B2 + lane * 2));
        }
        const char* yb = ws + WS_YT + (size_t)(q.D0 + 32 * kw) * TP + q.T0 * 4;
        const u32 vo = (u32)(gg * TP + bb * 16);
        u32x4 y[8];
        __builtin_amdgcn_s_setprio(2);
        _Pragma("clang loop unroll(disable)") for (int spin = 0; spin < SPIN_CAP; ++spin) {
#pragma unroll
            for (int i = 0; i < 8; ++i) y[i] = gld16c<0>(vo, yb + i * 4 * TP);
            asm volatile("s_waitcnt vmcnt(0)" : "+v"(y[0]), "+v"(y[1]), "+v"(y[2]), "+v"(y[3]), "+v"(y[4]), "+v"(y[5]), "+v"(y[6]), "+v"(y[7]) :: "memory");
            u32 bad = 0u;
#pragma unroll
            for (int i = 0; i < 8; ++i) bad = chk(y[i].w, tlo, chk(y[i].y, tlo, bad));
            if (!__any(bad != 0u)) break;
            __builtin_amdgcn_s_sleep(8);
        }
        const u32 ys = lds0 + L_CONV + kw * CONV_CH + TOEP_CH + U_SZ;
#pragma unroll
        for (int i = 0; i < 8; ++i) sts8(ys + (4 * i + gg) * 144 + bb * 8, u32x2{y[i].x, y[i].z});
        const int qq = (lane & 15) >> 2, pp = lane & 3;
        const u32 a = ys + qq * 144 + gg * 32 + pp * 8;
        u32x2 t[8];
        const float nln2 = -0.6931471805599453f;
#pragma unroll
        for (int j = 0; j < 8; ++j) t[j] = ldtr(a + j * 4 * 144);
        SB();
#pragma unroll
        for (int j = 0; j < 8; ++j)
        {
            const u32 s01 = pk2(mul1(bflo(t[j].x), xn[4 * j]), mul1(bfhi(t[j].x), xn[4 * j + 1])), s23 = pk2(mul1(bflo(t[j].y), xn[4 * j + 2]), mul1(bfhi(t[j].y), xn[4 * j + 3]));
            sts16(slab + (j >> 2) * 8192 + (j & 3) * 16, __builtin_bit_cast(u32x4, f32x4{mul1(bflo(s01), nln2), mul1(bfhi(s01), nln2), mul1(bflo(s23), nln2), mul1(bfhi(s23), nln2)}));
        }
        if (q.b == 0 && kw == 1) {
            _Pragma("clang loop unroll(disable)") for (int spin = 0; spin < SPIN_CAP; ++spin) {
                u32x4 f0 = gld16c<0>(lane * 16u, ws + WS_ALL), f1 = gld16c<1024>((lane < 48 ? lane : 0) * 16u, ws + WS_ALL);
                asm volatile("s_waitcnt vmcnt(0)" : "+v"(f0), "+v"(f1) :: "memory");
                bool ok = f0.x == tlo && f0.y == thi && f0.z == tlo && f0.w == thi && f1.x == tlo && f1.y == thi && f1.z == tlo && f1.w == thi;
                if (__all(ok)) break;
                __builtin_amdgcn_s_sleep(2);
            }
            if (lane == 0) { const u64 e1 = epoch + 1; gst8c<0>(0u, ws + WS_EPOCH, u32x2{(u32)e1, (u32)(e1 >> 32)}); }
        }
    }
    __builtin_amdgcn_s_setprio(0);
    BARL();
    final_epilogue(p, q, lds0);
}

DI float dpp_xor_sum8(float v) {
    v += __uint_as_float(__builtin_amdgcn_mov_dpp(__float_as_uint(v), 0xB1, 0xf, 0xf, true));
    v += __uint_as_float(__builtin_amdgcn_mov_dpp(__float_as_uint(v), 0x4E, 0xf, 0xf, true));
    v += __uint_as_float(__builtin_amdgcn_mov_dpp(__float_as_uint(v), 0x141, 0xf, 0xf, true));
    return v;
}
DI void body_help(const Params& p, const int wave) {
    const u32 lds0 = (u32)(size_t)(LAS char*)smem;
    const int lane = threadIdx.x & 63, r8 = lane >> 3, c8 = lane & 7;
    const int xcd = blockIdx.x & 7, j = blockIdx.x >> 3;
    char* ws = p.ws;
    u32x2 ep = gld8c(0u, ws + WS_EPOCH);
    u32x4 d[14];
    const int ctile = (xcd >> 1) * 4 + j;
    const bool cols = wave >= 4;
    if (!cols) {
        const char* xb = (const char*)p.x + (size_t)(256 * xcd + 64 * j + 16 * wave) * 896;
#pragma unroll
        for (int i = 0; i < 14; ++i) d[i] = gld16((u32)((r8 + 8 * (i / 7)) * 896 + (i % 7) * 128 + c8 * 16), xb);
    } else {
        const char* wb = (const char*)p.w1 + (size_t)(112 * (wave - 4)) * 3584 + ((xcd & 1) * 14 + (ctile < 14 ? ctile : 0)) * 128;
#pragma unroll
        for (int i = 0; i < 14; ++i) d[i] = gld16((u32)((r8 + 8 * i) * 3584 + c8 * 16), wb);
    }
    asm volatile("s_waitcnt vmcnt(0)" : "+v"(ep), "+v"(d[0]), "+v"(d[1]), "+v"(d[2]), "+v"(d[3]), "+v"(d[4]), "+v"(d[5]), "+v"(d[6]), "+v"(d[7]), "+v"(d[8]), "+v"(d[9]),
                 "+v"(d[10]), "+v"(d[11]), "+v"(d[12]), "+v"(d[13]) :: "memory");
    const u32 tlo = (u32)((((u64)ep.y << 32) | ep.x) + TAG_SALT);
    if (!cols) {
#pragma unroll
        for (int h = 0; h < 2; ++h) {
            float s0 = 0.f, s1 = 0.f, q0 = 0.f, q1 = 0.f;
#pragma unroll
            for (int i = 0; i < 7; ++i)
#pragma unroll
                for (int e = 0; e < 4; e += 2) {
                    s0 = dot2(d[7 * h + i][e], 0x3f803f80u, s0); q0 = dot2(d[7 * h + i][e], d[7 * h + i][e], q0);
                    s1 = dot2(d[7 * h + i][e + 1], 0x3f803f80u, s1); q1 = dot2(d[7 * h + i][e + 1], d[7 * h + i][e + 1], q1);
                }
            const float mean = dpp_xor_sum8(s0 + s1) * (1.f / 448.f);
            const float var = fmaxf(dpp_xor_sum8(q0 + q1) * (1.f / 448.f) - mean * mean, 0.f);
            const float sd = __builtin_amdgcn_sqrtf(rbf(rbf(var) + 1e-5f));
            if (c8 == 0) gst8c<0>((u32)(WS_RG + (256 * xcd + 64 * j + 16 * wave + 8 * h + r8) * 8), ws, u32x2{pk2(mean, sd), tlo});
        }
    } else {
        float a[8] = {};
        u32 klo = 0x00003f80u, khi = 0x3f800000u; PIN(klo); PIN(khi);
#pragma unroll
        for (int i = 0; i < 14; ++i)
#pragma unroll
            for (int e = 0; e < 4; ++e) { a[2 * e] = dot2(d[i][e], klo, a[2 * e]); a[2 * e + 1] = dot2(d[i][e], khi, a[2 * e + 1]); }
        const u32 o = lds0 + ((wave - 4) * 8 + r8) * 272 + c8 * 32;
        sts16(o, __builtin_bit_cast(u32x4, f32x4{a[0], a[1], a[2], a[3]})); sts16(o + 16, __builtin_bit_cast(u32x4, f32x4{a[4], a[5], a[6], a[7]}));
    }
    BARL();
    if (wave == 4 && ctile < 14) {
        float t0 = 0.f, t1 = 0.f;
#pragma unroll
        for (int i = 0; i < 32; i += 2) { t0 += *(const LAS float*)(lds0 + i * 272 + lane * 4); t1 += *(const LAS float*)(lds0 + (i + 1) * 272 + lane * 4); }
        gst8c<0>((u32)(WS_CG + (((xcd & 1) * 14 + ctile) * 64 + lane) * 8), ws, u32x2{pk2(t0 + t1, (t0 + t1) - rbf(t0 + t1)), tlo});
    }
    BARL();
    {
        const int crot = (2 * xcd) % 14;
        setm0(lds0 + 16384 + wave * 4096);
        for (int c = 0; c < 14; ++c) {
            int cc = c + crot; if (cc >= 14) cc -= 14;
            const char* sb = (const char*)p.w2 + (size_t)cc * 114688 + (j * 8 + wave) * 3584;
            if (lane < 32) dmai<3072>(lane * 16u, sb);
            dmai<0>(lane * 16u, sb); dmai<1024>(lane * 16u, sb); dmai<2048>(lane * 16u, sb);
            WAITV(8);
        }
        WAITV(0);
    }
}

__global__ __launch_bounds__(512, 2) void fused_kernel(Params p) {
    {
        u64 pc; asm volatile("s_getpc_b64 %0" : "=s"(pc));
        if (threadIdx.x < CODE_LINES) touch((const char*)(pc & ~127ull) + threadIdx.x * 128, (u32)(size_t)(LAS char*)smem + L_B2F);
    }
    const int wave = __builtin_amdgcn_readfirstlane(threadIdx.x >> 6);
    if (blockIdx.x < NHELP) { body_help(p, wave); return; }
    if (threadIdx.x < 32) *(LAS u32*)((u32)(size_t)(LAS char*)smem + L_FLAG + threadIdx.x * 4) = 0u;
    else if (threadIdx.x < 64) *(LAS u32*)((u32)(size_t)(LAS char*)smem + F_CONS + ((threadIdx.x - 32) >> 2) * 144 + (threadIdx.x & 3) * 4) = 0u;
    BARL();
    if (wave < 4) body_g(p, wave); else body_c(p, wave - 4);
}

extern "C" void kernel_launch(void* const* d_in, const int* in_sizes, int n_in,
                              void* d_out, int out_size, void* d_ws, size_t ws_size,
                              hipStream_t stream) {
    Params p;
    p.x = (const u16*)d_in[0]; p.k = (const u16*)d_in[1]; p.v = (const u16*)d_in[2]; p.shk = (const u16*)d_in[3];
    p.ssmk = (const u16*)d_in[4]; p.ssmd = (const u16*)d_in[5]; p.w1 = (const u16*)d_in[7]; p.b1 = (const u16*)d_in[8];
    p.w2 = (const u16*)d_in[9]; p.b2 = (const u16*)d_in[10];
    p.out = (u16*)d_out; p.ws = (char*)d_ws;
    (void)hipFuncSetAttribute((const void*)fused_kernel, hipFuncAttributeMaxDynamicSharedMemorySize, LDS_BYTES);
    fused_kernel<<<NWG + NHELP, 512, LDS_BYTES, stream>>>(p);
}
```
